```python
import torch

def _softmax(x, axis=-1):
    return torch.softmax(x, dim=axis)

def _gelu(x):
    return torch.nn.functional.gelu(x, approximate='tanh')

def _silu(x):
    return torch.nn.functional.silu(x)

def _sigmoid(x):
    return torch.sigmoid(x)

def _layernorm(x, axis=-1, eps=1e-05):
    m = x.mean(dim=axis, keepdim=True)
    v = x.var(dim=axis, keepdim=True, unbiased=False)
    return (x - m) / torch.sqrt(v + eps)

def _scatter_add(tgt, idx, src, axis=0):
    return tgt.clone().index_add_(axis, idx, src)

def _segment_sum(src, seg, n):
    out = torch.zeros((n,) + src.shape[1:], dtype=src.dtype)
    return out.index_add_(0, seg, src)

def _pad(x, pw):
    flat = tuple((v for pair in reversed(pw) for v in pair))
    return torch.nn.functional.pad(x, flat)
kernel_name = 'standalone__neighborhood_nat__vision'

def setup_inputs():

    _w = lambda a: (lambda t: t.to(torch.bfloat16) if t.is_floating_point() else t)(torch.as_tensor(a))
    H = W = 224
    C = 192
    return {'x': _w((lambda *s: torch.randn(*s, dtype=torch.bfloat16) * 0.2)(1, H, W, C)), 'wqkv': _w((lambda *s: torch.randn(*s, dtype=torch.bfloat16) * 0.2)(C, 3 * C)), 'wo': _w((lambda *s: torch.randn(*s, dtype=torch.bfloat16) * 0.05)(C, C))}

def reference(x, wqkv, wo):
    B, H, W, C = x.shape
    ks, dil = (7, 2)
    qkv = x @ wqkv
    q, k, v = (qkv[..., :C].clone(), qkv[..., C:2 * C].clone(), qkv[..., 2 * C:].clone())
    half = (ks - 1) // 2 * dil
    ir = torch.arange(H)
    ic = torch.arange(W)
    off = torch.arange(ks) * dil
    r0 = torch.clip(ir - half, 0, H - 1 - (ks - 1) * dil)
    c0 = torch.clip(ic - half, 0, W - 1 - (ks - 1) * dil)
    ri = r0[:, None] + off[None, :]
    ci = c0[:, None] + off[None, :]
    nidx = (ri[:, None, :, None] * W + ci[None, :, None, :]).reshape(1, H * W * ks * ks, 1)
    kf = k.reshape(B, H * W, C)
    vf = v.reshape(B, H * W, C)
    kn = (lambda x, i, axis: torch.take_along_dim(x, i, dim=axis))(kf, nidx, 1).reshape(B, H * W, ks * ks, C)
    vn = (lambda x, i, axis: torch.take_along_dim(x, i, dim=axis))(vf, nidx, 1).reshape(B, H * W, ks * ks, C)
    qf = q.reshape(B, H * W, C)
    scale = C ** (-0.5)
    logits = torch.einsum('bnc,bnkc->bnk', qf, kn) * scale
    p = _softmax(logits, axis=-1)
    o = torch.einsum('bnk,bnkc->bnc', p, vn) @ wo
    return o.reshape(B, H, W, C)
```

```cpp
#include <hip/hip_runtime.h>
#include <stdint.h>
#include <utility>
#include <type_traits>

typedef unsigned short u16;
typedef unsigned long long u64;
typedef __attribute__((ext_vector_type(8))) __bf16 bf16x8_t;
typedef __attribute__((ext_vector_type(2))) __bf16 bf16x2_t;
typedef __attribute__((ext_vector_type(4))) float f32x4;
typedef __attribute__((ext_vector_type(2))) float f32x2;
typedef __attribute__((ext_vector_type(4))) unsigned u32x4;
typedef __attribute__((ext_vector_type(2))) unsigned u32x2;
typedef __attribute__((ext_vector_type(4))) short s16x4;
typedef __attribute__((address_space(3))) unsigned lds_u32;

#define DEVINL __device__ __forceinline__
#define LAMBDA_INL __attribute__((always_inline))
#define IC(n) std::integral_constant<int, (n)>{}
#define SCHED __builtin_amdgcn_sched_barrier(0)
#define MEMBAR asm volatile("" ::: "memory")

static constexpr int IMW = 224, CH = 192, PIXB = 384, XBYTES = IMW * IMW * PIXB;
static constexpr int ROWB = 7680;
static constexpr int LDS_TOT = 163840;
static constexpr int SLOT_B = 138240, SLOT_C = 145920, SLOT_A = 153600, CQ = 159744;
static constexpr int TAB1 = 161792, TAB2 = TAB1 + 832, FLG = TAB2 + 832;
static constexpr int F_XC = 8;
static constexpr int F_HL = 10;
static constexpr int F_M1 = 12;
static constexpr int F_QF = 16;
static constexpr int F_QP = 32;
static constexpr int F_ZF = 48;
static constexpr int F_PF = 64;
static constexpr int ALL4 = 0x01010101;

static constexpr int PD = 6;
static constexpr int NB = 16, ZD = 3;
static constexpr int CPF = 34500;
static constexpr int T1C = 6000;
static constexpr int XQ = 4;
static constexpr int G2LOW = 7;

constexpr int G1ORD(int j) { constexpr int o[13] = {12, 0, 3, 2, 5, 6, 9, 1, 8, 4, 11, 7, 10}; return o[j]; }
constexpr int QSLOT(int grp) {
    constexpr int s[13] = {SLOT_A, SLOT_A, SLOT_C, SLOT_B, SLOT_A, SLOT_A, SLOT_B, SLOT_A, SLOT_A, SLOT_C, SLOT_A, SLOT_A, CQ};
    return s[grp];
}
constexpr int QPREV(int grp) {
    constexpr int s[13] = {-1, 5, -1, -1, 1, 0, 3, 4, 10, 2, 7, 8, -1};
    return s[grp];
}
constexpr int HRC(int r) { return ((r << 1) & 4) | ((r >> 2) & 1) | (2 * (r & 1)); }
constexpr unsigned HXT(int r0) { unsigned t = 0; for (int k = 0; k < 7; ++k) t |= (unsigned)HRC(r0 + 2 * k) << (4 * k); return t; }
constexpr int G2B(int j) { constexpr int o[7] = {0, 1, 6, 7, 2, 8, 12}; return o[(j - 1) >> 1]; }

template <int... Is, class F>
DEVINL void static_for_impl(std::integer_sequence<int, Is...>, F&& f) { (f(std::integral_constant<int, Is>{}), ...); }
template <int N, class F>
DEVINL void static_for(F&& f) { static_for_impl(std::make_integer_sequence<int, N>{}, f); }

DEVINL f32x4 mfma16(u32x4 a, u32x4 b, f32x4 c) {
    return __builtin_amdgcn_mfma_f32_16x16x32_bf16(__builtin_bit_cast(bf16x8_t, a), __builtin_bit_cast(bf16x8_t, b), c, 0, 0, 0);
}
DEVINL void mma(f32x4& c, const u32x4& a, const u32x4& b) { asm volatile("v_mfma_f32_16x16x32_bf16 %0, %1, %2, %0" : "+v"(c) : "v"(a), "v"(b)); }
DEVINL void mma0(f32x4& c, const u32x4& a, const u32x4& b) { asm volatile("v_mfma_f32_16x16x32_bf16 %0, %1, %2, 0" : "=v"(c) : "v"(a), "v"(b)); }
DEVINL void mmac(f32x4& d, const u32x4& a, const u32x4& b, const f32x4& c) { asm volatile("v_mfma_f32_16x16x32_bf16 %0, %1, %2, %3" : "=v"(d) : "v"(a), "v"(b), "v"(c)); }
DEVINL void mmah(f32x4& c, const u32x2& a, const u32x2& b) { asm volatile("v_mfma_f32_16x16x16_bf16 %0, %1, %2, %0" : "+v"(c) : "v"(a), "v"(b)); }
template <bool FIRST> DEVINL void mmf(f32x4& c, const u32x4& a, const u32x4& b) { if constexpr (FIRST) mma0(c, a, b); else mma(c, a, b); }
DEVINL unsigned cvt2(float lo, float hi) { unsigned r; asm volatile("v_cvt_pk_bf16_f32 %0, %1, %2" : "=v"(r) : "v"(lo), "v"(hi)); return r; }
DEVINL u32x4 cvt8(const f32x4& a, const f32x4& b) { return (u32x4){cvt2(a[0], a[1]), cvt2(a[2], a[3]), cvt2(b[0], b[1]), cvt2(b[2], b[3])}; }
DEVINL float max3(float a, float b, float c) { float r; asm("v_max3_f32 %0, %1, %2, %3" : "=v"(r) : "v"(a), "v"(b), "v"(c)); return r; }
DEVINL float max2(float a, float b) { float r; asm("v_max_f32 %0, %1, %2" : "=v"(r) : "v"(a), "v"(b)); return r; }
template <int N> DEVINL void nops() { asm volatile("s_nop %0" :: "i"(N - 1)); }
template <class T> DEVINL void use(const T& v) { asm volatile("" :: "v"(v)); }
template <class T> DEVINL void pin(T& v) { asm volatile("" : "+v"(v)); }
DEVINL unsigned xad(unsigned s, unsigned x, unsigned a) { unsigned r; asm volatile("v_xad_u32 %0, %1, %2, %3" : "=v"(r) : "s"(s), "v"(x), "v"(a)); return r; }
DEVINL unsigned pk2(float lo, float hi) {
    f32x2 f = {lo, hi};
    return __builtin_bit_cast(unsigned, __builtin_convertvector(f, bf16x2_t));
}
DEVINL u32x4 pk8(f32x4 a, f32x4 b) { return (u32x4){pk2(a[0], a[1]), pk2(a[2], a[3]), pk2(b[0], b[1]), pk2(b[2], b[3])}; }
DEVINL u32x2 trread(const char* p) {
    s16x4 t = __builtin_amdgcn_ds_read_tr16_b64_v4i16((__attribute__((address_space(3))) s16x4*)(p));
    return __builtin_bit_cast(u32x2, t);
}
template <int LOFF> DEVINL void dmax(u32x4 rs, unsigned cpart, unsigned hx, unsigned soff, unsigned lds) {
    unsigned t;
    asm volatile("s_add_u32 m0, %5, %6\n\tv_xor_b32 %0, %3, %1\n\tbuffer_load_dwordx4 %0, %2, %4 offen lds"
                 : "=&v"(t) : "v"(cpart), "s"(rs), "s"(hx), "s"(soff), "s"(lds), "i"(LOFF) : "memory", "scc");
}
DEVINL u32x4 xload(u32x4 rs, unsigned cpart, unsigned hx, unsigned soff) {
    unsigned t; u32x4 r;
    asm volatile("v_xor_b32 %1, %4, %2\n\tbuffer_load_dwordx4 %0, %1, %3, %5 offen" : "=v"(r), "=&v"(t) : "v"(cpart), "s"(rs), "s"(hx), "s"(soff) : "memory");
    return r;
}
template <int OFF> DEVINL u32x4 gload16(const char* sbase, unsigned voff) {
    u32x4 r;
    asm volatile("global_load_dwordx4 %0, %1, %2 offset:%3" : "=v"(r) : "v"(voff), "s"(sbase), "i"(OFF) : "memory");
    return r;
}
template <int OFF> DEVINL u32x4 gload16s(const char* sbase, unsigned voff) {
    u32x4 r;
    asm volatile("global_load_dwordx4 %0, %1, %2 offset:%3 sc1" : "=v"(r) : "v"(voff), "s"(sbase), "i"(OFF) : "memory");
    return r;
}
template <int OFF> DEVINL void gstore16(char* sbase, unsigned voff, u32x4 v) {
    asm volatile("global_store_dwordx4 %0, %1, %2 offset:%3 sc1\n\ts_nop 1" :: "v"(voff), "v"(v), "s"(sbase), "i"(OFF) : "memory");
}
DEVINL void gstore8(char* sbase, unsigned voff, u64 v) {
    asm volatile("global_store_dwordx2 %0, %1, %2 sc1\n\ts_nop 1" :: "v"(voff), "v"(v), "s"(sbase) : "memory");
}
DEVINL float xr16(float x, float& other) {
    unsigned u = __float_as_uint(x);
    auto r = __builtin_amdgcn_permlane16_swap(u, u, false, false);
    other = __uint_as_float(r[1]);
    return __uint_as_float(r[0]);
}
DEVINL float xr32(float x, float& other) {
    unsigned u = __float_as_uint(x);
    auto r = __builtin_amdgcn_permlane32_swap(u, u, false, false);
    other = __uint_as_float(r[1]);
    return __uint_as_float(r[0]);
}
DEVINL float xmax(float x) { float o, a = xr16(x, o); a = max2(a, o); a = xr32(a, o); return max2(a, o); }
DEVINL float xsum(float x) { float o, a = xr16(x, o); a = a + o; a = xr32(a, o); return a + o; }
DEVINL void wgbar() { MEMBAR; __builtin_amdgcn_s_barrier(); MEMBAR; }
template <int N> DEVINL void waitv() { asm volatile("s_waitcnt vmcnt(%0)" :: "i"(N) : "memory"); }

template <int IDX> DEVINL unsigned fl_ld(unsigned fb) { return __hip_atomic_load((lds_u32*)(size_t)(fb + 4 * IDX), __ATOMIC_RELAXED, __HIP_MEMORY_SCOPE_WORKGROUP); }
template <int IDX> DEVINL void fl_set(unsigned fbw, unsigned one) { asm volatile("ds_write_b8 %0, %1 offset:%2" :: "v"(fbw), "v"(one), "i"(4 * IDX) : "memory"); }
DEVINL unsigned rfl(unsigned v) { unsigned s; asm volatile("v_readfirstlane_b32 %0, %1" : "=s"(s) : "v"(v)); return s; }
template <int IDX, int NEED> DEVINL void fl_chk(unsigned fb, unsigned s) {
    unsigned n, v;
    asm volatile("s_cmp_ge_u32 %0, %5\n\ts_cbranch_scc1 2f\n\ts_mov_b32 %1, 0x100000\n"
                 "1:\n\tds_read_b32 %2, %3 offset:%4\n\ts_waitcnt lgkmcnt(0)\n\ts_sub_u32 %1, %1, 1\n\ts_cbranch_scc1 2f\n\t"
                 "v_readfirstlane_b32 %0, %2\n\ts_cmp_ge_u32 %0, %5\n\ts_cbranch_scc1 2f\n\ts_sleep 1\n\ts_branch 1b\n"
                 "2:" : "+s"(s), "=&s"(n), "=&v"(v) : "v"(fb), "i"(4 * IDX), "i"(NEED) : "memory", "scc");
}
template <int IDX, int NEED> DEVINL void fl_wait(unsigned fb) { fl_chk<IDX, NEED>(fb, 0u); }

DEVINL int h2f(int x) { return ((x << 1) & 4) | ((x >> 2) & 1); }
DEVINL int pixc(int par, int t, int i) {
    const int k = 14 * t - 6 + i, d = k >= 0 ? 2 * k : 2 * k + 7;
    return par ? 223 - d : d;
}

DEVINL void fold2_slow(const u16* __restrict__ wqkv, const u16* __restrict__ wo, char* __restrict__ dst, unsigned off, int f, int lane) {
    const int rho = lane & 15, g = lane >> 4;
    const int m = f / 12, s = (f % 12) >> 1, d = f & 1;
    const int c0 = 32 * s + rho, c1 = c0 + 16;
    const int cn = 32 * m + 8 * (rho >> 2) + 4 * d + (rho & 3);
    f32x4 a0 = {0.f, 0.f, 0.f, 0.f}, a1 = {0.f, 0.f, 0.f, 0.f};
#pragma unroll 1
    for (int t = 0; t < 6; ++t) {
        u32x4 b;
#pragma unroll
        for (int k = 0; k < 4; ++k)
            b[k] = (unsigned)wo[(size_t)(32 * t + 8 * g + 2 * k) * CH + cn] | ((unsigned)wo[(size_t)(32 * t + 8 * g + 2 * k + 1) * CH + cn] << 16);
        a0 = mfma16(*(const u32x4*)(wqkv + (size_t)c0 * (3 * CH) + 2 * CH + 32 * t + 8 * g), b, a0);
        a1 = mfma16(*(const u32x4*)(wqkv + (size_t)c1 * (3 * CH) + 2 * CH + 32 * t + 8 * g), b, a1);
    }
    gstore16<0>(dst, off, pk8(a0, a1));
}
DEVINL u32x4 fold1_slow(const u16* __restrict__ wqkv, int f, int lane) {
    const int rho = lane & 15, g = lane >> 4, pr = 8 * (rho >> 2) + (rho & 3);
    const int w = f / 18, s = (f % 18) / 3, b = f % 3;
    const int cb = b < 2 ? 3 * w + (w & 1) + b : 3 * w + ((w & 1) ? 0 : 2);
    const u16* pa = wqkv + (size_t)(32 * s + pr) * (3 * CH) + 8 * g;
    const u16* pb = wqkv + (size_t)(32 * (cb >> 1) + 4 * (cb & 1) + pr) * (3 * CH) + CH + 8 * g;
    f32x4 a0 = {0.f, 0.f, 0.f, 0.f}, a1 = {0.f, 0.f, 0.f, 0.f};
#pragma unroll 1
    for (int t = 0; t < 6; ++t) {
        const u32x4 bb = *(const u32x4*)(pb + 32 * t);
        a0 = mfma16(*(const u32x4*)(pa + 32 * t), bb, a0);
        a1 = mfma16(*(const u32x4*)(pa + 4 * 3 * CH + 32 * t), bb, a1);
    }
    return pk8(a0, a1);
}
static constexpr unsigned WS_M1 = 96 * 1024;
static constexpr unsigned WS_FLAG1 = 512 * 1024;
static constexpr unsigned WS_FLAGS = 256 * 1024;
static constexpr unsigned WS_EPOCH = 320 * 1024;
static constexpr unsigned WS_DELEG = 352 * 1024;
static constexpr unsigned WS_ACKS = 384 * 1024;
static constexpr unsigned WS_PRIV = 1024 * 1024;
static constexpr unsigned WS_USED = WS_PRIV + 1024 * 24 * 1024;
static constexpr int NPROD = 72;
DEVINL u64 mix64(u64 z) {
    z += 0x9e3779b97f4a7c15ull;
    z = (z ^ (z >> 30)) * 0xbf58476d1ce4e5b9ull;
    z = (z ^ (z >> 27)) * 0x94d049bb133111ebull;
    return z ^ (z >> 31);
}

struct Att {
    unsigned sT[7];
    unsigned linS, linP, swS, swP;
    unsigned dlin, dsw, zw;
    f32x4 mk[4];
    int t0v;
    unsigned ztab, zflag;
};
DEVINL void lane_terms(int k, int cell, int low, int S0, int L0, bool o, unsigned& lin, unsigned& sw) {
    const int s = S0 + 2 * (cell ^ (cell >> 1)) + (k & 1), e = (k >> 1) & 1;
    lin = (unsigned)(__mul24(s, o ? PIXB : ROWB) + __mul24(L0 + e, o ? ROWB : PIXB));
    sw = (unsigned)(16 * (h2f(s) ^ (2 * (o ? e : (k & 1))) ^ low));
    pin(lin); pin(sw);
}
DEVINL void att_addrS(Att& at, int lane, int S0, int L0, bool o) { lane_terms(lane & 15, (lane >> 2) & 3, lane >> 4, S0, L0, o, at.linS, at.swS); }
DEVINL void att_addrP(Att& at, int lane, int S0, int L0, bool o) {
    lane_terms(lane >> 2, lane >> 4, (lane >> 1) & 1, S0, L0, o, at.linP, at.swP);
    at.linP += 8 * (lane & 1); pin(at.linP);
}
DEVINL void att_tiles(Att& at, int L0, bool o) {
#pragma unroll
    for (int t = 0; t < 7; ++t) at.sT[t] = (unsigned)(2 * t * (o ? ROWB : PIXB) + 16 * h2f(L0 + 2 * t));
}
DEVINL void att_masks(Att& at, int g, int sq, int lq) {
    const int rb = ((sq + 7) & 7) - 2 * (g ^ (g >> 1)), pinv = (lq + 7) & 7;
    at.t0v = lq >> 1;
    const float r0 = rb == 0 ? -1e30f : 0.f, r1 = rb == 1 ? -1e30f : 0.f;
#pragma unroll
    for (int tm = 0; tm < 4; ++tm) {
        const bool c0 = pinv == 2 * tm, c1 = pinv == 2 * tm + 1;
        at.mk[tm] = (f32x4){c0 ? -1e30f : r0, c0 ? -1e30f : r1, c1 ? -1e30f : r0, c1 ? -1e30f : r1};
        pin(at.mk[tm]);
    }
    pin(at.t0v);
}
template <int NT, int NR, bool GATE, bool KEEP, class H>
DEVINL void att_rounds(Att& at, char* smem, unsigned g16, unsigned one, u32x4 (&qq)[NR][6], f32x4 (&accZ)[4][3], u32x4 (&zl)[6], H&& hook) {
    constexpr int NK = (NT + 1) / 2, NS = 6 * NT, NP = 12 * NK, NO = NS + NP, NOPS = NR * NO, BL = 3 * NK;
    u32x4 fr[NB], Pf[3];
    u32x2 Pf3;
    f32x4 accS[7];
    unsigned aS[7], aP[2], zt, gv = 0, gs = 0;
    auto zput = [&](auto kc) LAMBDA_INL {
        constexpr int k = decltype(kc)::value;
        SCHED;
        MEMBAR;
        *(u32x4*)(smem + 128 * (k % 3) + at.zw) = zl[k];
        if constexpr (k == 0) { at.zw ^= 64u; pin(at.zw); }
        SCHED;
    };
    auto load = [&](auto oc) LAMBDA_INL {
        constexpr int O = decltype(oc)::value, i = O / NO, n = O % NO;
        if constexpr (n < NS) {
            constexpr int ch = (n % (3 * NT)) / NT, t = n % NT;
            if constexpr (n == 0 && i > 0) { at.linS += at.dlin; at.swS ^= at.dsw ^ 64u; }
            if constexpr (n == 3 * NT) at.swS ^= 64u;
            if constexpr (ch == 0) aS[t] = xad(at.sT[t], at.swS, at.linS);
            fr[O % NB] = *(const u32x4*)(smem + 128 * ch + aS[t]);
            if constexpr (n == NS - 8) zt = *(const unsigned*)(smem + 128 * i + at.ztab);
        } else {
            constexpr int m = n - NS, cbp = m / BL, tau = (m % BL) / 3, ch = m % 3;
            if constexpr (m == 0 && i > 0) { at.linP += at.dlin; at.swP ^= at.dsw ^ 96u; }
            if constexpr (m > 0 && m % BL == 0) at.swP ^= (cbp == 2 ? 96u : 32u);
            if constexpr (ch == 0) {
                aP[0] = xad(at.sT[2 * tau], at.swP, at.linP);
                if constexpr (2 * tau + 1 < NT) aP[1] = xad(at.sT[2 * tau + 1], at.swP, at.linP);
            }
            if constexpr (2 * tau + 1 < NT) {
                const u32x2 lo = trread(smem + 128 * ch + aP[0]), hi = trread(smem + 128 * ch + aP[1]);
                fr[O % NB] = (u32x4){lo[0], lo[1], hi[0], hi[1]};
            } else if constexpr (ch == 0) {
#pragma unroll
                for (int c = 0; c < 3; ++c) { const u32x2 lo = trread(smem + 128 * c + aP[0]); fr[(O + c) % NB][0] = lo[0]; fr[(O + c) % NB][1] = lo[1]; }
            }
        }
        SCHED;
    };
    static_for<NOPS + PD + (KEEP ? 0 : ZD)>([&](auto sc) LAMBDA_INL {
        constexpr int S = decltype(sc)::value, J = S - PD;
        hook(sc);
        if constexpr (J >= 0 && J < NOPS && J % NO == NS) {
            bool sl[3];
#pragma unroll
            for (int t = 0; t < 3; ++t) sl[t] = t < at.t0v;
            at.zw = ((zt & 0x3fffu) << 4) ^ g16; pin(at.zw); nops<6>();
            SCHED;
            if constexpr (NT == 7) {
#pragma unroll
                for (int t = 0; t < 3; ++t)
#pragma unroll
                    for (int r = 0; r < 4; ++r) accS[t][r] = sl[t] ? accS[t + 4][r] : accS[t][r];
            }
            float ma = max3(accS[3][0], accS[3][1], accS[3][2]), mb = max3(accS[0][0], accS[0][1], accS[0][2]);
            ma = max3(ma, accS[3][3], accS[0][3]);
            mb = max3(mb, accS[1][0], accS[1][1]); ma = max3(ma, accS[1][2], accS[1][3]);
            mb = max3(mb, accS[2][0], accS[2][1]); ma = max3(ma, accS[2][2], accS[2][3]);
            const float mx = xmax(max2(ma, mb));
            const float kappa = 0.10411754112863712f;
            const float mneg = -mx * kappa;
#pragma unroll
            for (int t = 0; t < 4; ++t)
#pragma unroll
                for (int r = 0; r < 4; ++r) accS[t][r] = __builtin_amdgcn_exp2f(__builtin_fmaf(accS[t][r], kappa, mneg));
            const f32x4 s4 = (accS[0] + accS[1]) + (accS[2] + accS[3]);
            const float inv = __builtin_amdgcn_rcpf(xsum((s4[0] + s4[1]) + (s4[2] + s4[3])));
            unsigned pc[4][2];
#pragma unroll
            for (int t = 0; t < 4; ++t) { accS[t] *= inv; pin(accS[t]); }
            SCHED;
#pragma unroll
            for (int t = 0; t < 4; ++t) { pc[t][0] = pk2(accS[t][0], accS[t][1]); pc[t][1] = pk2(accS[t][2], accS[t][3]); }
            if constexpr (NT == 7) {
                Pf[0] = (u32x4){sl[0] ? 0u : pc[0][0], sl[0] ? 0u : pc[0][1], sl[1] ? 0u : pc[1][0], sl[1] ? 0u : pc[1][1]};
                Pf[1] = (u32x4){sl[2] ? 0u : pc[2][0], sl[2] ? 0u : pc[2][1], pc[3][0], pc[3][1]};
                Pf[2] = (u32x4){sl[0] ? pc[0][0] : 0u, sl[0] ? pc[0][1] : 0u, sl[1] ? pc[1][0] : 0u, sl[1] ? pc[1][1] : 0u};
                Pf3 = (u32x2){sl[2] ? pc[2][0] : 0u, sl[2] ? pc[2][1] : 0u};
                pin(Pf[2]); pin(Pf3);
            } else {
                Pf[0] = (u32x4){pc[0][0], pc[0][1], pc[1][0], pc[1][1]};
                Pf[1] = (u32x4){pc[2][0], pc[2][1], pc[3][0], pc[3][1]};
            }
            pin(Pf[0]); pin(Pf[1]);
            SCHED;
        }
        {
            constexpr int i = S / NO, n = S % NO;
            constexpr bool win = i > 0 && i < NR && n < 16;
            if constexpr (win) {
                if constexpr (n < 4) { load(IC(i * NO + 2 * n)); load(IC(i * NO + 2 * n + 1)); }
                if constexpr (n == 4) zput(IC(0));
                if constexpr (n == 7 || n == 8) { load(IC(i * NO + 3 * n - 13)); load(IC(i * NO + 3 * n - 12)); load(IC(i * NO + 3 * n - 11)); }
                if constexpr (n == 9) zput(IC(5));
                if constexpr (n == 11) zput(IC(4));
                if constexpr (n == 13) zput(IC(3));
                if constexpr (n == 15) { fl_set<F_ZF + 2 * i - 2>(at.zflag, one); load(IC(i * NO + 14)); load(IC(i * NO + 15)); }
            } else if constexpr (S < NOPS) load(sc);
            if constexpr (GATE && S == NOPS - 22) gv = fl_ld<F_PF + 2 * NR - 3>(at.zflag);
            if constexpr (GATE && S == NOPS - 17) gs = rfl(gv);
            if constexpr (GATE && S == NOPS - 14) fl_chk<F_PF + 2 * NR - 3, 1>(at.zflag, gs);
            if constexpr (NT == 7 && i < NR && n == NO - 14 && !KEEP) zput(IC(2));
            if constexpr (NT == 7 && i == 0 && n == NO - 2) fl_set<F_PF>(at.zflag, one);
            if constexpr (NT == 7 && i < NR && n == NO - 2 && !KEEP) zput(IC(1));
            if constexpr (NT == 7 && S == NOPS + 1 && !KEEP) zput(IC(0));
            if constexpr (NT == 4 && S == NOPS) zput(IC(2));
            if constexpr (NT == 4 && S == NOPS + 3) zput(IC(1));
            if constexpr (NT == 4 && S == NOPS + 5) zput(IC(0));
        }
        if constexpr (J >= 0 && J < NOPS) {
            constexpr int i = J / NO, n = J % NO;
            if constexpr (n < NS) {
                constexpr int nr = n - n % 3 + 2 - n % 3, Jr = J - n + nr;
                constexpr int sp = nr / (3 * NT), ch = (nr % (3 * NT)) / NT, t = nr % NT;
                if constexpr (nr < NT) mmac(accS[t], fr[Jr % NB], qq[i][0], at.mk[t & 3]);
                else mma(accS[t], fr[Jr % NB], qq[i][2 * ch + sp]);
                if constexpr (n == NT + 4) { use(at.mk[0]); use(at.mk[1]); use(at.mk[2]); use(at.mk[3]); }
            } else {
                constexpr int m = n - NS, cbp = m / BL, tau = (m % BL) / 3, ch = 2 - m % 3, Jr = J - m % 3 + ch;
                if constexpr (2 * tau + 1 < NT) mmf<tau == 0>(accZ[cbp][ch], fr[Jr % NB], Pf[tau]);
                else mmah(accZ[cbp][ch], (u32x2){fr[Jr % NB][0], fr[Jr % NB][1]}, Pf3);
            }
        } else if constexpr (J == NOPS) nops<6>();
        constexpr int Jp = J - ZD;
        if constexpr (Jp >= 0 && Jp < NOPS) {
            constexpr int i = Jp / NO, n = Jp % NO;
            if constexpr (n >= NS) {
                constexpr int m = n - NS, cbp = m / BL, tau = (m % BL) / 3, ch = 2 - m % 3;
                if constexpr ((cbp == 1 || (cbp == 3 && i == NR - 1)) && tau == NK - 1) {
                    zl[3 * (cbp >> 1) + ch] = cvt8(accZ[cbp - 1][ch], accZ[cbp][ch]);
                    if constexpr (cbp == 3) zput(IC(3 + ch));
                    if constexpr (cbp == 3 && ch == 0) fl_set<F_ZF + 2 * NR - 2>(at.zflag, one);
                }
            }
        }
        if constexpr (J >= NO && J < NOPS && J % NO >= 1 && J % NO < 7) {
            constexpr int ch = 2 - (J % NO - 1) / 2, h = (J % NO - 1) & 1;
            zl[3 + ch][2 * h] = cvt2(accZ[2 + h][ch][0], accZ[2 + h][ch][1]); zl[3 + ch][2 * h + 1] = cvt2(accZ[2 + h][ch][2], accZ[2 + h][ch][3]);
        }
        SCHED;
    });
    static_assert(PD == 6, "window schedule");
}

__global__ __launch_bounds__(512, 2) void na_main(const u16* __restrict__ x, const u16* __restrict__ wqkv,
                                                  const u16* __restrict__ wo, char* __restrict__ ws,
                                                  u16* __restrict__ out) {
    __shared__ __attribute__((aligned(1024))) char smem[LDS_TOT];

    u64 tev = __builtin_amdgcn_s_memrealtime();
    {
        const int ln_ = threadIdx.x & 63, wv_ = __builtin_amdgcn_readfirstlane(threadIdx.x >> 6), b_ = blockIdx.x;
        const unsigned lb_ = (unsigned)(size_t)smem;
        unsigned jk_;
        u64 pc_;
        asm volatile("s_getpc_b64 %0" : "=s"(pc_));
        { const unsigned off = min((unsigned)(((b_ >> 3) * 8 + wv_) * 64 + (ln_ >> 4) * 16384), (unsigned)CPF);
          asm volatile("global_load_dword %0, %1, %2" : "=v"(jk_) : "v"(off), "s"(pc_) : "memory"); }
        const int xc_ = b_ & 7, cl_ = xc_ >> 1, tl_ = (xc_ & 1) * 32 + (b_ >> 3);
        const unsigned base_ = (unsigned)(((28 * (tl_ >> 3) + (cl_ >> 1)) * 224 + 28 * (tl_ & 7) + (cl_ & 1)) * 384);
        auto dm_ = [&](const void* sb, unsigned voff, unsigned l) LAMBDA_INL {
            asm volatile("s_mov_b32 m0, %2\n\ts_nop 0\n\tglobal_load_lds_dwordx4 %0, %1" "" :: "v"(voff), "s"(sb), "s"(l) : "memory");
        };
        auto oa_ = [&](int j) LAMBDA_INL -> unsigned {
            int n = min(8 * j + (ln_ >> 3), 195), r = (n * 147) >> 11, c = n - 14 * r;
            return base_ + (unsigned)(r * 2 * 224 * 384 + c * 768 + (ln_ & 7) * 16);
        };
        auto ob_ = [&](int j) LAMBDA_INL -> unsigned {
            int l = min(8 * j + (ln_ >> 3), 587), n = (l * 683) >> 11, t = l - 3 * n, r = (n * 147) >> 11, c = n - 14 * r;
            return base_ + (unsigned)(r * 2 * 224 * 384 + c * 768 + t * 128 + (ln_ & 7) * 16);
        };

        waitv<0>();
        use(jk_);
        wgbar();
    }
    const int tid = threadIdx.x;
    const int lane = tid & 63;
    const int wave = __builtin_amdgcn_readfirstlane(tid >> 6);
    const int bid = blockIdx.x;
    const unsigned ldsbase = (unsigned)(size_t)smem;
    const int i16 = lane & 15, g = lane >> 4;
    const unsigned lane16 = lane * 16, g16 = (unsigned)g * 16;
    const f32x4 zero4 = {0.f, 0.f, 0.f, 0.f};

    unsigned jkc;
    {
        u64 pc0;
        asm volatile("s_getpc_b64 %0" : "=s"(pc0));
        const unsigned off = min((unsigned)(((bid >> 3) * 8 + wave) * 64 + (lane >> 4) * 16384), (unsigned)CPF);
        asm volatile("global_load_dword %0, %1, %2" : "=v"(jkc) : "v"(off), "s"(pc0) : "memory");
    }
    const u64 cstart = __builtin_amdgcn_s_memtime();
    if (wave == 1 || wave == 2) *(unsigned*)(smem + FLG + 4 * lane + (wave & 1) * 128) = 0u;
    asm volatile("s_waitcnt lgkmcnt(0)" ::: "memory");
    wgbar();
    const bool prod1 = wave == 0 && bid < NPROD, prod2 = wave == 0 && bid >= NPROD && bid < 2 * NPROD;
    asm volatile("" :: "s"(x), "s"(wqkv), "s"(wo), "s"(ws), "s"(out));
    u64 ep = 0;
    if (wave < 4 || wave == 7) asm volatile("global_load_dwordx2 %0, %1, %2 sc1" : "=v"(ep) : "v"(WS_EPOCH), "s"(ws) : "memory");
    const int pr_ = 8 * (i16 >> 2) + (i16 & 3);
    u64 m1tag = 0;
    if (prod1) {
        u32x4 fra[12], fro[6];
        const int w = (bid * 57) >> 10, r = bid - 18 * w, s = (r * 11) >> 5, b = r - 3 * s;
        const int cb = 3 * w + (b < 2 ? (w & 1) + b : (w & 1) ? 0 : 2);
        const unsigned ao = (unsigned)((32 * s + pr_) * (3 * PIXB) + 16 * g), bo = (unsigned)((32 * (cb >> 1) + 4 * (cb & 1) + pr_) * (3 * PIXB) + PIXB + 16 * g);
        static_for<6>([&](auto tc) LAMBDA_INL {
            constexpr int t = decltype(tc)::value;
            fro[t] = gload16<64 * t>((const char*)wqkv, bo);
            fra[2 * t] = gload16<64 * t>((const char*)wqkv, ao);
            fra[2 * t + 1] = gload16<64 * t>((const char*)wqkv + 4 * 3 * PIXB, ao);
        });
        waitv<18>();
        pin(ep);
        const u64 tg1 = mix64(ep);
        SCHED;
        waitv<0>();
#pragma unroll
            for (int i = 0; i < 12; ++i) pin(fra[i]);
#pragma unroll
            for (int i = 0; i < 6; ++i) pin(fro[i]);
            f32x4 pa0 = zero4, pa1 = zero4;
#pragma unroll
            for (int t = 0; t < 6; ++t) { pa0 = mfma16(fra[2 * t], fro[t], pa0); pa1 = mfma16(fra[2 * t + 1], fro[t], pa1); }
            gstore16<0>(ws, WS_M1 + (unsigned)bid * 1024u + lane16, pk8(pa0, pa1));
            m1tag = tg1;
    }
    SCHED;

    const int xcd = bid & 7;
    const int cls = xcd >> 1, tile = (xcd & 1) * 32 + (bid >> 3);
    const int p = cls >> 1, q = cls & 1;
    const int ta = tile >> 3, tb = tile & 7;
    const bool br = ta == 0, bc = tb == 0;
    const int qc4 = i16 >> 2;
    const int Sb = ((qc4 + 1) >> 1) & 1, Lq = 2 * (i16 & 3) + (qc4 & 1);
    const int a = wave & 3;
    const bool o = a & 1, rev = a == 1 || a == 2;
    const int L0 = a >= 2 ? 6 : 0;
    const bool clS = a == 0 ? br : a == 3 ? bc : false, clL = a == 0 ? bc : a == 1 ? br : false;
    const int S00 = rev ? 12 : clS ? 6 : 0, dS = rev ? -2 : clS ? 0 : 2;
    Att at;
    f32x4 accZ[4][3];
    u32x4 zl[6];

    unsigned fb, fbw, one;
    auto set_fb = [&]() LAMBDA_INL { fb = ldsbase + FLG; fbw = fb + (unsigned)a; one = 1; pin(fb); pin(fbw); pin(one); };
    const unsigned ti4 = (unsigned)i16 * 4 + TAB1, lq16 = lane16 + SLOT_B;
    auto finish = [&]() LAMBDA_INL {
        while ((long long)(__builtin_amdgcn_s_memrealtime() - tev) < 4000ll) __builtin_amdgcn_s_sleep(1);
        __builtin_amdgcn_endpgm();
        __builtin_unreachable();
    };

    if (wave < 4) {
        const __amdgpu_buffer_rsrc_t rws = __builtin_amdgcn_make_buffer_rsrc(ws, 0, WS_USED, 0x20000);
        u32x4 fra[12], fro[12];
        if (prod2) {
            const int f = bid - NPROD;
            const unsigned oo = (unsigned)(pr_ * PIXB + 64 * (f / 12) + 16 * g);
            const unsigned ao = (unsigned)((32 * ((f % 12) >> 1) + i16) * (3 * PIXB) + 2 * PIXB + 16 * g);
            static_for<6>([&](auto tc) LAMBDA_INL {
                constexpr int t = decltype(tc)::value;
                fro[2 * t] = gload16<0>((const char*)wo + 32 * t * PIXB, oo);
                fro[2 * t + 1] = gload16<4 * PIXB>((const char*)wo + 32 * t * PIXB, oo);
                fra[2 * t] = gload16<64 * t>((const char*)wqkv, ao);
                fra[2 * t + 1] = gload16<64 * t>((const char*)wqkv + 16 * 3 * PIXB, ao);
            });
        }
        SCHED;
        {
            const bool ctr = g == 3;
            const int sc = (rev ? 12 - 2 * g : 2 * g) + Sb, lc = L0 + Lq;
            int r = o ? lc : sc, c = o ? sc : lc;
            r = ctr ? 6 + Sb : r; c = ctr ? 6 + (Lq & 1) : c;
            const int oi = r < (br ? 6 : 0) ? (r >> 1) + 6 - 3 * (r & 1) : r + 3;
            const int oj = c < (bc ? 6 : 0) ? (c >> 1) + 6 - 3 * (c & 1) : c + 3;
            const unsigned xa = (unsigned)(__mul24(oi, ROWB) + __mul24(oj, PIXB) + 16 * (h2f(oi ^ oj) ^ (2 * (oi & 1))));
            const int zs = sc + (rev ? 6 : 0), zl = 2 * L0 + Lq;
            const int zr = o ? zl : zs, zc = o ? zs : zl;
            const int zmv = (wave == 2 && bc) ? -8 * (ROWB / 16) - 12 * 24 : (wave == 3 && br) ? -12 * (ROWB / 16) + 8 * 24 : 0;
            int za = __mul24(zr, ROWB / 16) + __mul24(zc, 24) + (h2f(zr ^ zc) ^ (2 * (zr & 1))) + (Lq == 0 ? zmv : 0);
            za = ctr ? CQ / 16 + 24 * qc4 + 4 * (qc4 & 1) + (qc4 >> 1) : za;
            const int ur = 14 * ta + r, uc = 14 * tb + c;
            const int dr = max(ur, 2 * ur - 6), dc = max(uc, 2 * uc - 6);
            const int pix = __mul24(dr, p ? -IMW : IMW) + (q ? -dc : dc) + (p ? 223 * IMW : 0) + (q ? 223 : 0);
            const unsigned e = (unsigned)(ctr ? 192 + i16 : 48 * wave + lane) * 4;
            *(unsigned*)(smem + TAB1 + e) = xa;
            *(unsigned*)(smem + TAB2 + e) = (unsigned)za | ((unsigned)pix << 14);
        }
        set_fb();
        const int cbP = 3 * wave + (wave & 1), cbS = 3 * wave + ((wave & 1) ? 0 : 2);
        u32x4 m1[6][3];
        if (prod2) waitv<24>(); else waitv<0>();
        if (prod1 && lane < 8) gstore8(ws, WS_FLAG1 + (unsigned)((lane * NPROD + bid) * 128), m1tag);
        use(jkc);
        pin(ep);
        const u64 tg = mix64(ep);
        const u32x2 tagv = {(unsigned)tg, (unsigned)(tg >> 32)};
        att_addrS(at, lane, S00 + dS, L0, o); att_addrP(at, lane, S00 + dS, L0, o);
        att_masks(at, g, clS ? 0 : Sb, clL ? max(Lq, 6) : Lq);
        fl_set<F_M1>(fbw, one);
        u32x4 xb[2][6];
        f32x4 accQ[2][3];
        unsigned xt, xa0, xa1, fv = 0, fv2 = 0, fv3 = 0, fs = 0, fs2 = 0, fs3 = 0;
        auto ldt1 = [&](auto jc) LAMBDA_INL { xt = *(const unsigned*)(smem + 64 * G1ORD(decltype(jc)::value) + ti4); };
        auto setx = [&]() LAMBDA_INL { xa0 = xt ^ g16; xa1 = xa0 ^ 64u; pin(xa0); pin(xa1); };
        auto ldx = [&](auto jc, auto sc) LAMBDA_INL {
            constexpr int j = decltype(jc)::value, s = decltype(sc)::value;
            xb[j & 1][s] = *(const u32x4*)(smem + 128 * (s >> 1) + ((s & 1) ? xa1 : xa0));
        };
        fl_wait<F_M1, ALL4>(fb);
        ldt1(IC(0)); setx(); ldt1(IC(1));
        if (prod2) {
            waitv<0>();
#pragma unroll
            for (int i = 0; i < 12; ++i) { pin(fra[i]); pin(fro[i]); }
            const int el = 4 * (bid & 1) + (i16 & 3);
            const unsigned on = (g == (i16 >> 2)) ? (0x3f80u << (16 * (el & 1))) : 0u;
            const u32x4 psel = {(el >> 1) == 0 ? on : 0u, (el >> 1) == 1 ? on : 0u, (el >> 1) == 2 ? on : 0u, (el >> 1) == 3 ? on : 0u};
            f32x4 pa0 = zero4, pa1 = zero4;
#pragma unroll
            for (int t = 0; t < 6; ++t) {
                const u32x4 b = pk8(mfma16(fro[2 * t], psel, zero4), mfma16(fro[2 * t + 1], psel, zero4));
                pa0 = mfma16(fra[2 * t], b, pa0);
                pa1 = mfma16(fra[2 * t + 1], b, pa1);
            }
            gstore16<0>(ws, (unsigned)(bid - NPROD) * 1024u + lane16, pk8(pa0, pa1));
        }
        unsigned m1a = WS_M1 + (unsigned)(18 * wave * 1024) + lane16;
        {
            const unsigned f1o = WS_FLAG1 + (unsigned)(((bid & 7) * NPROD + 18 * wave + min(lane, 17)) * 128);
            while (__builtin_amdgcn_s_memtime() - cstart < (u64)T1C) __builtin_amdgcn_s_sleep(1);
            const u64 t0 = __builtin_amdgcn_s_memrealtime();
            bool ok = false;
            for (;;) {
                const u32x2 v = __builtin_amdgcn_raw_buffer_load_b64(rws, f1o, 0, 16);
                if (__all(v[0] == tagv[0] && v[1] == tagv[1])) { ok = true; break; }
                if (__builtin_amdgcn_s_memrealtime() - t0 > 20000ull) break;
            }
            if (!ok) {
                m1a = WS_PRIV + (unsigned)(bid * 4 + wave) * 24576u + lane16;
                for (int f = 0; f < 18; ++f) gstore16<0>(ws, m1a + f * 1024, fold1_slow(wqkv, 18 * wave + f, lane));
                waitv<0>();
            }
            MEMBAR;
        }
        if (prod2) { waitv<0>(); if (lane < 8) gstore8(ws, WS_FLAGS + (unsigned)(bid - NPROD) * 8u + (unsigned)lane * 1024u, tg); }
        static_for<18>([&](auto ic) LAMBDA_INL {
            constexpr int i = decltype(ic)::value;
            m1[i / 3][i % 3] = __builtin_amdgcn_raw_buffer_load_b128(rws, m1a + 1024 * (i & 3), 4096 * (i >> 2), 16);
            if constexpr (i == 14) fv = fl_ld<F_XC>(fb);
            if constexpr (i == 16) fs = rfl(fv);
            SCHED;
        });
        __builtin_amdgcn_s_setprio(2);

        fl_chk<F_XC, ALL4>(fb, fs);
        static_for<6>([&](auto sc) LAMBDA_INL { ldx(IC(0), sc); });
        SCHED;
        unsigned qwP, qwS;
        u32x2 qv[13][3];
        unsigned fgo, m2a, m2b;
        u32x2 mflag;
        u32x4 m2[2][12];
        auto wrq = [&](auto jc, auto bc_) LAMBDA_INL {
            constexpr int j = decltype(jc)::value, bi = decltype(bc_)::value;
            MEMBAR;
            *(u32x2*)(smem + (j == 0 ? 0 : QSLOT(G1ORD(j)) - SLOT_B) + (bi == 1 ? 8 : 0) + (bi < 2 ? qwP : qwS)) = qv[j][bi];
            MEMBAR;
        };
        u32x4 qm[1][6];
        u32x2 hlv;
        unsigned hl0 = 0, hl1 = 0;
        auto pullm = [&](auto gc) LAMBDA_INL {
            constexpr int grp = decltype(gc)::value;
            if (wave == grp / 3) {
                fl_chk<F_QF + grp, ALL4>(fb, fs2);
#pragma unroll
                for (int m = 0; m < 6; ++m) qm[0][m] = *(const u32x4*)(smem + (QSLOT(grp) - SLOT_B) + 1024 * m + lq16);
                fl_set<F_QP + grp>(fb, one);
            }
        };
        auto m2_ready = [&]() LAMBDA_INL {
            m2a = (unsigned)(12 * wave * 1024) + lane16; m2b = (unsigned)(12 * (4 + (wave >> 1)) * 1024) + lane16;
            if (!__all(mflag[0] == tagv[0] && mflag[1] == tagv[1])) {
                const u64 t0 = __builtin_amdgcn_s_memrealtime();
                bool ok = false;
                for (;;) {
                    mflag = __builtin_amdgcn_raw_buffer_load_b64(rws, fgo, 0, 16);
                    if (__all(mflag[0] == tagv[0] && mflag[1] == tagv[1])) { ok = true; break; }
                    if (__builtin_amdgcn_s_memrealtime() - t0 > 20000ull) break;
                    __builtin_amdgcn_s_sleep(4);
                }
                if (!ok) {
                    m2a = WS_PRIV + (unsigned)(bid * 4 + wave) * 24576u + lane16; m2b = m2a + 12288;
                    for (int f = 0; f < 24; ++f) fold2_slow(wqkv, wo, ws, m2a + f * 1024, f < 12 ? 12 * wave + f : 12 * (4 + (wave >> 1)) + f - 12, lane);
                    waitv<0>();
                }
            }
            MEMBAR;
        };
        static_for<14>([&](auto jc) LAMBDA_INL {
            constexpr int j = decltype(jc)::value;
            static_for<18>([&](auto nc) LAMBDA_INL {
                constexpr int n = decltype(nc)::value, s = n / 3, bi = n % 3;
                if constexpr (j == 0) {
                    if constexpr (n == 0) setx();
                    if constexpr (n == 15) {
                        qwP = (unsigned)(16 * (lane >> 2) + (cbP >> 1) * 256 + CQ); qwS = (unsigned)(16 * (lane >> 2) + (cbS >> 1) * 256 + 8 * (cbS & 1) + CQ);
                        pin(qwP); pin(qwS);
                    }
                }
                if constexpr (j == 1 && n == 16) { qwP = lq16 + (unsigned)((cbP >> 1) * 1024); qwS = lq16 + (unsigned)((cbS >> 1) * 1024 + 8 * (cbS & 1)); pin(qwP); pin(qwS); }
                if constexpr (j == 2 && n == 16) {
                    const int lf = min(lane, 23);
                    fgo = WS_FLAGS + (unsigned)((bid & 7) * 1024 + 96 * wave) + (unsigned)(lf * 8 + (lf >= 12 ? 96 * (3 + (wave >> 1) - wave) : 0)); pin(fgo);
                }
                if constexpr (j == 3 && n == 8) fv3 = fl_ld<F_XC + 1>(fb);
                if constexpr (j == 3 && n == 16) fs3 = rfl(fv3);
                if constexpr (j == 4 && n == 0) { fl_chk<F_XC + 1, ALL4>(fb, fs3); }
                if constexpr (j + 1 < 13 && n >= 1 && n < 7) ldx(IC(j + 1), IC(n >= 1 && n < 7 ? n - 1 : 0));
                if constexpr (j + 2 < 13 && n == 7) ldt1(IC(j + 2 < 13 ? j + 2 : 0));
                if constexpr (j + 2 < 13 && n == 12) setx();
                if constexpr (j >= 1) {
                    constexpr int pj = j - 1, grp = G1ORD(pj);
                    if constexpr (n >= 5 && n < 11) qv[pj][(n - 5) >> 1][(n - 5) & 1] = cvt2(accQ[pj & 1][(n - 5) >> 1][2 * ((n - 5) & 1)], accQ[pj & 1][(n - 5) >> 1][2 * ((n - 5) & 1) + 1]);
                    if constexpr (pj != 8 && pj != 10 && pj != 12) {
                        constexpr int need = QPREV(grp) >= 0 ? F_QP + QPREV(grp) : -1;
                        if constexpr (need >= 0 && n == 4) fv = fl_ld<(need >= 0 ? need : 0)>(fb);
                        if constexpr (need >= 0 && n == 9) fs = rfl(fv);
                        if constexpr (need >= 0 && n == 11) fl_chk<(need >= 0 ? need : 0), 1>(fb, fs);
                        if constexpr (n == 11) wrq(IC(pj), IC(0));
                        if constexpr (n == 13) wrq(IC(pj), IC(1));
                        if constexpr (n == 15) wrq(IC(pj), IC(2));
                        if constexpr (n == 17) fl_set<F_QF + grp>(fbw, one);
                    }
                }
                if constexpr (j == 9 || j == 11) {
                    constexpr int grp = G1ORD(j - 2);
                    if constexpr (n == 8) fv2 = fl_ld<F_QF + grp>(fb);
                    if constexpr (n == 11) fs2 = rfl(fv2);
                    if constexpr (n == 14) pullm(IC(grp));
                }
                if constexpr (j == 12 && n == 2) hlv = *(const u32x2*)(smem + FLG + 4 * F_HL);
                if constexpr (j == 12 && n == 12) { hl0 = rfl(hlv[0]); hl1 = rfl(hlv[1]); }
                if constexpr (j == 12 && n == 14) { att_tiles(at, L0, o); at.ztab = ti4 + (unsigned)(832 + 192 * wave + 64); at.zflag = fb + (unsigned)(12 * wave + 4); pin(at.ztab); pin(at.zflag); }
                SCHED;
                if constexpr (j < 13) mmf<s == 0>(accQ[j & 1][bi], m1[s][bi], xb[j & 1][s]);
                else if constexpr (n == 0) nops<8>();
                SCHED;
            });
        });
        __builtin_amdgcn_s_setprio(1);
        const __amdgpu_buffer_rsrc_t rout = __builtin_amdgcn_make_buffer_rsrc(out, 0, XBYTES, 0x20000);
        const unsigned oc = g16 + 64u * (unsigned)wave;
        const int osh = 64 * (4 + (wave >> 1) - wave);
        const unsigned odd3 = (wave & 1) ? 3u : 0u;
        const unsigned tiq[2] = {ti4 + 832 + 64 * (3 - odd3), ti4 + 832 + 64 * odd3};
        const unsigned fbq[2] = {fb + 4 * (3 - odd3), fb + 4 * odd3};
        u32x4 zb[2][6], ov;
        f32x4 accO[2][2], accH[2];
        unsigned t2e, po[3], z0, z1;
        auto ldt2 = [&](auto jc) LAMBDA_INL {
            constexpr int j = decltype(jc)::value;
            if constexpr (j == 13) t2e = *(const unsigned*)(smem + 832 + 64 * 12 + ti4);
            else t2e = *(const unsigned*)(smem + 64 * G2B(j) + tiq[j & 1]);
        };
        auto zfb = [&](auto jc) LAMBDA_INL -> unsigned { constexpr int j = decltype(jc)::value; if constexpr (j == 13) return fb; else return fbq[j & 1]; };
        auto setz = [&](auto jc) LAMBDA_INL {
            constexpr int j = decltype(jc)::value;
            z0 = ((t2e & 0x3fffu) << 4) ^ g16; z1 = z0 ^ 64u; po[j % 3] = __umul24(t2e >> 14, PIXB) + oc;
            pin(z0); pin(z1); pin(po[j % 3]);
        };
        auto ldz = [&](auto jc, auto sc) LAMBDA_INL {
            constexpr int j = decltype(jc)::value, s = decltype(sc)::value;
            zb[j & 1][s] = *(const u32x4*)(smem + 128 * (s >> 1) + ((s & 1) ? z1 : z0));
        };
        auto zout = [&](auto kc) LAMBDA_INL {
            constexpr int k = decltype(kc)::value;
            MEMBAR;
            *(u32x4*)(smem + 128 * (k % 3) + (k >= 3 ? at.zw ^ 64u : at.zw)) = zl[k];
            MEMBAR;
        };
        fs2 = 0;
        pullm(IC(7));
        fl_chk<F_HL, ALL4>(fb, hl0);
        if (wave == 3) fl_chk<F_HL + 1, 0x01010000>(fb, hl1);
        att_rounds<7, 1, false, true>(at, smem, g16, one, qm, accZ, zl, [&](auto sc) LAMBDA_INL {
            constexpr int S = decltype(sc)::value;
            if constexpr (S == 6) {
                if (wave == 3) { fl_wait<F_QP + 7, 1>(fb); wrq(IC(12), IC(0)); wrq(IC(12), IC(1)); wrq(IC(12), IC(2)); fl_set<F_QF + 10>(fbw, one); }
                fs2 = 0;
                pullm(IC(10));
                fv = fl_ld<F_QP + 7>(fb);
            }
            if constexpr (S == 9) fs = rfl(fv);
            if constexpr (S == 11) { if (wave != 3) { fl_chk<F_QP + 7, 1>(fb, fs); wrq(IC(12), IC(0)); wrq(IC(12), IC(1)); wrq(IC(12), IC(2)); fl_set<F_QF + 10>(fbw, one); } }
            if constexpr (S == 24) fv = fl_ld<F_QP + 10>(fb);
            if constexpr (S == 29) fs = rfl(fv);
            if constexpr (S == 31) { fl_chk<F_QP + 10, 1>(fb, fs); wrq(IC(8), IC(0)); }
            if constexpr (S == 33) wrq(IC(8), IC(1));
            if constexpr (S == 35) wrq(IC(8), IC(2));
            if constexpr (S == 37) fl_set<F_QF + 8>(fbw, one);
            if constexpr (S == 16) { mflag = __builtin_amdgcn_raw_buffer_load_b64(rws, fgo, 0, 16); }
            if constexpr (S == 42 + PD) {
                m2_ready();
                static_for<6>([&](auto ic) LAMBDA_INL { constexpr int i = decltype(ic)::value; m2[0][i] = __builtin_amdgcn_raw_buffer_load_b128(rws, m2a + 1024 * (i & 3), 4096 * (i >> 2), 16); });
            }
            if constexpr (S >= 50 && S < 86 && S % 2 == 0) {
                constexpr int i = (S - 50) / 2 + 6, h = i / 12, k = i % 12;
                m2[h][k] = __builtin_amdgcn_raw_buffer_load_b128(rws, (h ? m2b : m2a) + 1024 * (k & 3), 4096 * (k >> 2), 16);
            }
            if constexpr (S == 57) fv = fl_ld<F_QP + 8>(fb);
            if constexpr (S == 61) fs = rfl(fv);
            if constexpr (S == 64) { fl_chk<F_QP + 8, 1>(fb, fs); wrq(IC(10), IC(0)); }
            if constexpr (S == 65) wrq(IC(10), IC(1));
            if constexpr (S == 66) wrq(IC(10), IC(2));
            if constexpr (S == 67) fl_set<F_QF + 11>(fbw, one);
            if constexpr (S == 79) { ldt2(IC(1)); fv = fl_ld<F_ZF + G2B(1)>(fbq[1]); }
            if constexpr (S == 84) { fs = rfl(fv); setz(IC(1)); }
            if constexpr (S == 90) fl_chk<F_ZF + G2B(1), 1>(fbq[1], fs);
            if constexpr (S >= 90 && S < 96) ldz(IC(1), IC(S >= 90 && S < 96 ? S - 90 : 0));
            if constexpr (S == 95) { ldt2(IC(2)); fv = fl_ld<F_ZF + G2B(2)>(fbq[0]); }
            SCHED;
        });
        static_for<13>([&](auto jc0) LAMBDA_INL {
            constexpr int j = decltype(jc0)::value + 1;
            if constexpr (j == G2LOW) __builtin_amdgcn_s_setprio(0);
            constexpr bool sh = (j & 1) && j < 13, more = j < 13;
            constexpr int jn = more ? j + 1 : 1, jn2 = j + 2 <= 13 ? j + 2 : 1, NM = sh ? 24 : 12;
            constexpr int L = sh ? 4 : 0;
            static_for<NM>([&](auto nc) LAMBDA_INL {
                constexpr int n = decltype(nc)::value, k = n % 12;
                if constexpr ((j == 2 || j == 4) && n == 1) setz(IC(jn));
                if constexpr (j == 2 && n == 5) fv = fl_ld<F_ZF + G2B(3)>(zfb(IC(3)));
                if constexpr (j == 2 && n == 11) fs = rfl(fv);
                if constexpr (j == 4 && n == 3) fv2 = fl_ld<F_PF - 1>(at.zflag);
                if constexpr (j == 4 && n == 11) fs2 = rfl(fv2);
                if constexpr (more && j != 2 && j != 4) {
                    if constexpr (n == L) fs = rfl(fv);
                    if constexpr (n == L + 1) setz(IC(jn));
                    if constexpr (n == L + 2) fl_chk<F_ZF + G2B(jn), 1>(zfb(IC(jn)), fs);
                    if constexpr (n >= L + 3 && n < L + 9) ldz(IC(jn), IC(n >= L + 3 && n < L + 9 ? n - L - 3 : 0));
                    if constexpr (j + 2 <= 13 && n == (sh ? 18 : 11)) { ldt2(IC(jn2)); fv = fl_ld<F_ZF + G2B(jn2)>(zfb(IC(jn2))); }
                }
                if constexpr (j == 1 && n >= 7 && n < 13) {
                    constexpr int ch = (n >= 7 ? (n - 7) / 2 : 0), h = (n - 7) & 1;
                    zl[3 + ch][2 * h] = cvt2(accZ[2 + h][ch][0], accZ[2 + h][ch][1]); zl[3 + ch][2 * h + 1] = cvt2(accZ[2 + h][ch][2], accZ[2 + h][ch][3]);
                }
                if constexpr (j == 1 && (n == 13 || n == 16 || n == 21)) { if (wave < 2) { zout(IC(n == 13 ? 0 : n == 16 ? 2 : 4)); zout(IC(n == 13 ? 1 : n == 16 ? 3 : 5)); } }
                if constexpr (j == 2 && n == 0) { if (wave < 2) fl_set<F_ZF>(at.zflag, one); }
                if constexpr (j >= 2) {
                    constexpr bool psh = (j - 1) & 1;
                    if constexpr (n >= 6 && n < 10) {
                        if constexpr (psh) ov[n - 6] = cvt2(accH[(n - 6) >> 1][2 * (n & 1)], accH[(n - 6) >> 1][2 * (n & 1) + 1]);
                        else ov[n - 6] = cvt2(accO[0][(n - 6) >> 1][2 * (n & 1)], accO[0][(n - 6) >> 1][2 * (n & 1) + 1]);
                    }
                    if constexpr (n == 10) __builtin_amdgcn_raw_buffer_store_b128(ov, rout, po[(j - 1) % 3], psh ? osh : 0, 16);
                }
                if constexpr (sh) {
                    if constexpr (n >= 19 && n < 23) ov[n - 19] = cvt2(accO[1][(n - 19) >> 1][2 * ((n - 19) & 1)], accO[1][(n - 19) >> 1][2 * ((n - 19) & 1) + 1]);
                    if constexpr (n == 23) __builtin_amdgcn_raw_buffer_store_b128(ov, rout, po[j % 3], 0, 16);
                }
                SCHED;
                if constexpr (n < 12) mmf<(k < 2)>(accO[j & 1][k & 1], m2[0][k], zb[j & 1][k >> 1]);
                else mmf<(k < 2)>(accH[k & 1], m2[1][k], zb[j & 1][k >> 1]);
                SCHED;
            });
            if constexpr (j == 2 || j == 4) {
                if constexpr (j == 4) {
                    if (wave >= 2) {
                        fl_chk<F_PF - 1, 1>(at.zflag, fs2);
                        static_for<6>([&](auto kc) LAMBDA_INL { zout(kc); });
                        fl_set<F_ZF>(at.zflag, one);
                    }
                    fs = 0;
                }
                fl_chk<F_ZF + G2B(jn), 1>(zfb(IC(jn)), fs);
                static_for<6>([&](auto sc) LAMBDA_INL { ldz(IC(jn), sc); });
                ldt2(IC(jn2)); fv = fl_ld<F_ZF + G2B(jn2)>(zfb(IC(jn2)));
                SCHED;
            }
        });
        if (wave & 1) {
            static_for<12>([&](auto nc) LAMBDA_INL { constexpr int n = decltype(nc)::value; mmf<(n < 2)>(accH[n & 1], m2[1][n], zb[1][n >> 1]); SCHED; });
            __builtin_amdgcn_raw_buffer_store_b128(cvt8(accO[1][0], accO[1][1]), rout, po[13 % 3], 0, 16);
            nops<4>();
            __builtin_amdgcn_raw_buffer_store_b128(cvt8(accH[0], accH[1]), rout, po[13 % 3], osh, 16);
        } else {
            nops<8>();
            __builtin_amdgcn_raw_buffer_store_b128(cvt8(accO[1][0], accO[1][1]), rout, po[13 % 3], 0, 16);
        }
        finish();
    } else {
        const bool w7 = wave == 7;
        const u32x4 rsx = {(unsigned)(size_t)x, (unsigned)((size_t)x >> 32) & 0xffffu, (unsigned)XBYTES, 0x20000u};
        const int half = a & 1, rpar = a >> 1;
        Att atc;
        unsigned cp[4];
        auto setup_cp = [&](auto nc) LAMBDA_INL {
            constexpr int n = decltype(nc)::value;
            const int lam = (n < 3 ? 4 + 24 * half + 8 * n : half ? 0 : 52) + (lane >> 3);
            const int col = (lam * 43) >> 7, chn = lam - 3 * col;
            const int pc = pixc(q, tb, col);
            unsigned v = (unsigned)(__mul24(pc, PIXB) + chn * 128 + 16 * ((lane & 7) ^ h2f(col)));
            cp[n] = (unsigned)pc < 224u ? v : 0x40000000u;
            pin(cp[n]);
        };
        const int rs1 = p ? -2 * IMW * PIXB : 2 * IMW * PIXB;
        const int rs0 = (p ? 223 * IMW * PIXB : 0) + (14 * ta - 6) * rs1;
        int rsw0 = rs0 + (3 + rpar) * rs1;
        const int rfix0 = br ? 7 * (rs1 / 2) : 0, rfix1 = rpar ? 0 : rfix0;
        unsigned hxt = rpar ? HXT(4) : HXT(3);
        const unsigned lown = ldsbase + (unsigned)(512 + 3072 * half), lside = ldsbase + (half ? 0u : 6656u);
        const unsigned lownw = lown + (unsigned)((3 + rpar) * ROWB), lsidew = lside + (unsigned)((3 + rpar) * ROWB);
        auto dma_row = [&](auto kc, auto wc) LAMBDA_INL {
            constexpr int k = decltype(kc)::value, what = decltype(wc)::value;
            unsigned so = (unsigned)(rsw0 + 2 * k * rs1 + (k == 0 ? rfix0 : k == 1 ? rfix1 : 0)), hx = (k == 0 ? hxt << 4 : hxt >> (4 * k - 4)) & 0x70u;
            asm volatile("" : "+s"(so), "+s"(hx));
            auto thr = [&]() LAMBDA_INL { if constexpr (k >= 2) waitv<XQ>(); };
            if constexpr (what & 1) { thr(); dmax<15360 * k>(rsx, cp[0], hx, so, lownw); thr(); dmax<15360 * k + 1024>(rsx, cp[1], hx, so, lownw); thr(); dmax<15360 * k + 2048>(rsx, cp[2], hx, so, lownw); }
            if constexpr (what & 2) { waitv<XQ>(); dmax<15360 * k>(rsx, cp[3], hx, so, lsidew); }
            SCHED;
        };
        auto dma_halo = [&](auto rc) LAMBDA_INL {
            constexpr int r = decltype(rc)::value;
            const unsigned so = (r < 3 && br) ? 0x40000000u : (unsigned)(rs0 + r * rs1), hx = 16 * HRC(r);
            dmax<ROWB * r>(rsx, cp[0], hx, so, lown); dmax<ROWB * r + 1024>(rsx, cp[1], hx, so, lown); dmax<ROWB * r + 2048>(rsx, cp[2], hx, so, lown);
            dmax<ROWB * r>(rsx, cp[3], hx, so, lside);
            SCHED;
        };

        u64 tag = 0;
        u32x4 dlg, ack0, ack1;
        setup_cp(IC(0)); setup_cp(IC(1)); setup_cp(IC(2));
        dma_row(IC(0), IC(1));
        dma_row(IC(1), IC(1));
        setup_cp(IC(3)); set_fb();
        dma_row(IC(2), IC(1));
        att_addrS(at, lane, S00, L0, o);
        dma_row(IC(3), IC(1));
        att_addrP(at, lane, S00, L0, o);
        dma_row(IC(4), IC(1)); dma_row(IC(5), IC(1));
        waitv<6>();
        use(jkc);
        fl_set<F_XC>(fbw, one);
        dma_row(IC(6), IC(1));
        asm volatile("" : "+s"(rsw0), "+s"(hxt));
        static_for<7>([&](auto kc) LAMBDA_INL { dma_row(kc, IC(2)); });
        waitv<7>();
        fl_set<F_XC + 1>(fbw, one);
        u32x4 qf[2][6], qcf[1][6];
        const unsigned fba = fb + 12 * a;
        auto pull = [&](auto ic, unsigned slot) LAMBDA_INL {
            constexpr int i = decltype(ic)::value;
            fl_wait<F_QF + 2 * i, ALL4>(fba);
            const unsigned b = lq16 + slot;
#pragma unroll
            for (int m = 0; m < 6; ++m) qf[i][m] = *(const u32x4*)(smem + 1024 * m + b);
            fl_set<F_QP + 2 * i>(fba, one);
            SCHED;
        };
        if (a < 2) {
            dma_halo(IC(2)); dma_halo(IC(1)); dma_halo(IC(0));
            att_masks(at, g, clS ? 0 : Sb, clL ? max(Lq, 6) : Lq);
            if (a == 0) { att_addrS(atc, lane, 6, 6, false); att_addrP(atc, lane, 6, 6, false); att_masks(atc, g, Sb, Lq & 1); }
            waitv<0>();
            fl_set<F_HL>(fbw, one);
            pull(IC(0), a == 0 ? QSLOT(0) - SLOT_B : QSLOT(3) - SLOT_B);
            pull(IC(1), a == 0 ? QSLOT(2) - SLOT_B : QSLOT(5) - SLOT_B);
        } else {
            dma_halo(IC(17));
            u32x4 xr[8];
            static_for<8>([&](auto ic) LAMBDA_INL { constexpr int i = decltype(ic)::value, r = 18 + i / 4; xr[i] = xload(rsx, cp[i % 4], 16 * HRC(r), (unsigned)(rs0 + r * rs1)); });
            att_masks(at, g, clS ? 0 : Sb, clL ? max(Lq, 6) : Lq);
            waitv<0>();
            fl_set<F_HL>(fbw, one);
            if (w7) {
                pin(ep);
                const u64 tg = mix64(ep);
                tag = ((u64)(unsigned)__builtin_amdgcn_readfirstlane((unsigned)(tg >> 32)) << 32) | (unsigned)__builtin_amdgcn_readfirstlane((unsigned)tg);
                fl_wait<F_M1, ALL4>(fb);
                if (lane == 0) gstore8(ws, WS_ACKS + (unsigned)bid * 8u, tag);
                waitv<0>();
                dlg = gload16s<0>(ws, WS_DELEG);
                waitv<0>();
                pin(dlg);
            }
            const unsigned wo18 = lane16 + (lown - ldsbase) + 18 * ROWB, ws18 = lane16 + (lside - ldsbase) + 18 * ROWB;
            auto put = [&](auto ic) LAMBDA_INL {
                constexpr int i = decltype(ic)::value;
                pin(xr[i]);
                *(u32x4*)(smem + ROWB * (i / 4) + (i % 4 < 3 ? 1024 * (i % 4) : 0) + (i % 4 < 3 ? wo18 : ws18)) = xr[i];
                MEMBAR;
            };
            __builtin_amdgcn_s_setprio(3);
            if (a == 2) pull(IC(0), QSLOT(6) - SLOT_B); else fl_wait<F_QP + 6, 1>(fb);
            static_for<4>([&](auto ic) LAMBDA_INL { put(ic); });
            if (a == 3) pull(IC(0), QSLOT(9) - SLOT_B); else fl_wait<F_QP + 9, 1>(fb);
            static_for<4>([&](auto ic) LAMBDA_INL { put(IC(4 + decltype(ic)::value)); });
            fl_set<F_HL + 1>(fbw, one);
        }
        __builtin_amdgcn_s_setprio(1);
        att_tiles(at, L0, o);
        at.dlin = (unsigned)(2 * dS * (o ? PIXB : ROWB)); at.dsw = dS ? 16u : 0u;
        at.ztab = ti4 + (unsigned)(832 + 192 * a); at.zflag = fba;
        fl_wait<F_HL, ALL4>(fb);
        if (a >= 2) fl_wait<F_HL + 1, 0x01010000>(fb);
        att_rounds<7, 2, true, false>(at, smem, g16, one, qf, accZ, zl, [&](auto sc) LAMBDA_INL {
            constexpr int S = decltype(sc)::value;
            if constexpr (S == 49) { if (w7 && bid == 0 && lane == 0) gstore8(ws, WS_DELEG, tag); }
            if constexpr (S == 139) { if (a < 2) __builtin_amdgcn_s_setprio(3); }
            if constexpr (S == 63) { if (a == 2) pull(IC(1), SLOT_A - SLOT_B); }
            if constexpr (S == 87) { if (a == 3) pull(IC(1), SLOT_A - SLOT_B); }
        });
        if (a == 0) {
#pragma unroll
            for (int m = 0; m < 6; ++m) qcf[0][m] = *(const u32x4*)(smem + CQ + 256 * m + 16 * (lane >> 2));
            att_tiles(atc, 6, false);
            atc.ztab = ti4 + (unsigned)(832 + 64 * 12); atc.zflag = fb + 48;
            att_rounds<4, 1, false, false>(atc, smem, g16, one, qcf, accZ, zl, [&](auto) LAMBDA_INL {});
        }
        if (w7) {
            waitv<0>();
            pin(dlg);
            const unsigned tl = (unsigned)tag, th = (unsigned)(tag >> 32);
            if (bid == 0 || __builtin_amdgcn_readfirstlane(dlg[0] == tl && dlg[1] == th)) {
                ack0 = gload16s<0>(ws, WS_ACKS + lane16); ack1 = gload16s<1024>(ws, WS_ACKS + lane16);
                waitv<0>();
                pin(ack0); pin(ack1);
                const bool good = ack0[0] == tl && ack0[1] == th && ack0[2] == tl && ack0[3] == th &&
                                  ack1[0] == tl && ack1[1] == th && ack1[2] == tl && ack1[3] == th;
                if (__all(good) && lane == 0) gstore8(ws, WS_EPOCH, tag);
            }
        }
        finish();
    }
}

extern "C" void kernel_launch(void* const* d_in, const int* in_sizes, int n_in,
                              void* d_out, int out_size, void* d_ws, size_t ws_size,
                              hipStream_t stream) {
    const u16* x = (const u16*)d_in[0];
    const u16* wqkv = (const u16*)d_in[1];
    const u16* wo = (const u16*)d_in[2];
    na_main<<<256, 512, 0, stream>>>(x, wqkv, wo, (char*)d_ws, (u16*)d_out);
}
```

```cpp
#include <hip/hip_runtime.h>
#include <stdint.h>
#include <utility>
#include <type_traits>

typedef unsigned short u16;
typedef unsigned long long u64;
typedef __attribute__((ext_vector_type(8))) __bf16 bf16x8_t;
typedef __attribute__((ext_vector_type(2))) __bf16 bf16x2_t;
typedef __attribute__((ext_vector_type(4))) float f32x4;
typedef __attribute__((ext_vector_type(2))) float f32x2;
typedef __attribute__((ext_vector_type(4))) unsigned u32x4;
typedef __attribute__((ext_vector_type(2))) unsigned u32x2;
typedef __attribute__((ext_vector_type(4))) short s16x4;
typedef __attribute__((address_space(3))) unsigned lds_u32;

#define DEVINL __device__ __forceinline__
#define LAMBDA_INL __attribute__((always_inline))
#define IC(n) std::integral_constant<int, (n)>{}
#define SCHED __builtin_amdgcn_sched_barrier(0)
#define MEMBAR asm volatile("" ::: "memory")

static constexpr int IMW = 224, CH = 192, PIXB = 384, XBYTES = IMW * IMW * PIXB;
static constexpr int ROWB = 7680;
static constexpr int LDS_TOT = 163840;
static constexpr int SLOT_B = 138240, SLOT_C = 145920, SLOT_A = 153600, CQ = 159744;
static constexpr int TAB1 = 161792, TAB2 = TAB1 + 832, FLG = TAB2 + 832;
static constexpr int F_XC = 8;
static constexpr int F_HL = 10;
static constexpr int F_M1 = 12;
static constexpr int F_QF = 16;
static constexpr int F_QP = 32;
static constexpr int F_ZF = 48;
static constexpr int F_PF = 64;
static constexpr int ALL4 = 0x01010101;

static constexpr int PD = 6;
static constexpr int NB = 16, ZD = 3;
static constexpr int CPF = 34500;
static constexpr int T1C = 6000;
static constexpr int XQ = 4;
static constexpr int G2LOW = 7;

constexpr int G1ORD(int j) { constexpr int o[13] = {12, 0, 3, 2, 5, 6, 9, 1, 8, 4, 11, 7, 10}; return o[j]; }
constexpr int QSLOT(int grp) {
    constexpr int s[13] = {SLOT_A, SLOT_A, SLOT_C, SLOT_B, SLOT_A, SLOT_A, SLOT_B, SLOT_A, SLOT_A, SLOT_C, SLOT_A, SLOT_A, CQ};
    return s[grp];
}
constexpr int QPREV(int grp) {
    constexpr int s[13] = {-1, 5, -1, -1, 1, 0, 3, 4, 10, 2, 7, 8, -1};
    return s[grp];
}
constexpr int HRC(int r) { return ((r << 1) & 4) | ((r >> 2) & 1) | (2 * (r & 1)); }
constexpr unsigned HXT(int r0) { unsigned t = 0; for (int k = 0; k < 7; ++k) t |= (unsigned)HRC(r0 + 2 * k) << (4 * k); return t; }
constexpr int G2B(int j) { constexpr int o[7] = {0, 1, 6, 7, 2, 8, 12}; return o[(j - 1) >> 1]; }

template <int... Is, class F>
DEVINL void static_for_impl(std::integer_sequence<int, Is...>, F&& f) { (f(std::integral_constant<int, Is>{}), ...); }
template <int N, class F>
DEVINL void static_for(F&& f) { static_for_impl(std::make_integer_sequence<int, N>{}, f); }

DEVINL f32x4 mfma16(u32x4 a, u32x4 b, f32x4 c) {
    return __builtin_amdgcn_mfma_f32_16x16x32_bf16(__builtin_bit_cast(bf16x8_t, a), __builtin_bit_cast(bf16x8_t, b), c, 0, 0, 0);
}
DEVINL void mma(f32x4& c, const u32x4& a, const u32x4& b) { asm volatile("v_mfma_f32_16x16x32_bf16 %0, %1, %2, %0" : "+v"(c) : "v"(a), "v"(b)); }
DEVINL void mma0(f32x4& c, const u32x4& a, const u32x4& b) { asm volatile("v_mfma_f32_16x16x32_bf16 %0, %1, %2, 0" : "=v"(c) : "v"(a), "v"(b)); }
DEVINL void mmac(f32x4& d, const u32x4& a, const u32x4& b, const f32x4& c) { asm volatile("v_mfma_f32_16x16x32_bf16 %0, %1, %2, %3" : "=v"(d) : "v"(a), "v"(b), "v"(c)); }
DEVINL void mmah(f32x4& c, const u32x2& a, const u32x2& b) { asm volatile("v_mfma_f32_16x16x16_bf16 %0, %1, %2, %0" : "+v"(c) : "v"(a), "v"(b)); }
template <bool FIRST> DEVINL void mmf(f32x4& c, const u32x4& a, const u32x4& b) { if constexpr (FIRST) mma0(c, a, b); else mma(c, a, b); }
DEVINL unsigned cvt2(float lo, float hi) { unsigned r; asm volatile("v_cvt_pk_bf16_f32 %0, %1, %2" : "=v"(r) : "v"(lo), "v"(hi)); return r; }
DEVINL u32x4 cvt8(const f32x4& a, const f32x4& b) { return (u32x4){cvt2(a[0], a[1]), cvt2(a[2], a[3]), cvt2(b[0], b[1]), cvt2(b[2], b[3])}; }
DEVINL float max3(float a, float b, float c) { float r; asm("v_max3_f32 %0, %1, %2, %3" : "=v"(r) : "v"(a), "v"(b), "v"(c)); return r; }
DEVINL float max2(float a, float b) { float r; asm("v_max_f32 %0, %1, %2" : "=v"(r) : "v"(a), "v"(b)); return r; }
template <int N> DEVINL void nops() { asm volatile("s_nop %0" :: "i"(N - 1)); }
template <class T> DEVINL void use(const T& v) { asm volatile("" :: "v"(v)); }
template <class T> DEVINL void pin(T& v) { asm volatile("" : "+v"(v)); }
DEVINL unsigned xad(unsigned s, unsigned x, unsigned a) { unsigned r; asm volatile("v_xad_u32 %0, %1, %2, %3" : "=v"(r) : "s"(s), "v"(x), "v"(a)); return r; }
DEVINL unsigned pk2(float lo, float hi) {
    f32x2 f = {lo, hi};
    return __builtin_bit_cast(unsigned, __builtin_convertvector(f, bf16x2_t));
}
DEVINL u32x4 pk8(f32x4 a, f32x4 b) { return (u32x4){pk2(a[0], a[1]), pk2(a[2], a[3]), pk2(b[0], b[1]), pk2(b[2], b[3])}; }
DEVINL u32x2 trread(const char* p) {
    s16x4 t = __builtin_amdgcn_ds_read_tr16_b64_v4i16((__attribute__((address_space(3))) s16x4*)(p));
    return __builtin_bit_cast(u32x2, t);
}
template <int LOFF> DEVINL void dmax(u32x4 rs, unsigned cpart, unsigned hx, unsigned soff, unsigned lds) {
    unsigned t;
    asm volatile("s_add_u32 m0, %5, %6\n\tv_xor_b32 %0, %3, %1\n\tbuffer_load_dwordx4 %0, %2, %4 offen lds"
                 : "=&v"(t) : "v"(cpart), "s"(rs), "s"(hx), "s"(soff), "s"(lds), "i"(LOFF) : "memory", "scc");
}
DEVINL u32x4 xload(u32x4 rs, unsigned cpart, unsigned hx, unsigned soff) {
    unsigned t; u32x4 r;
    asm volatile("v_xor_b32 %1, %4, %2\n\tbuffer_load_dwordx4 %0, %1, %3, %5 offen" : "=v"(r), "=&v"(t) : "v"(cpart), "s"(rs), "s"(hx), "s"(soff) : "memory");
    return r;
}
template <int OFF> DEVINL u32x4 gload16(const char* sbase, unsigned voff) {
    u32x4 r;
    asm volatile("global_load_dwordx4 %0, %1, %2 offset:%3" : "=v"(r) : "v"(voff), "s"(sbase), "i"(OFF) : "memory");
    return r;
}
template <int OFF> DEVINL u32x4 gload16s(const char* sbase, unsigned voff) {
    u32x4 r;
    asm volatile("global_load_dwordx4 %0, %1, %2 offset:%3 sc1" : "=v"(r) : "v"(voff), "s"(sbase), "i"(OFF) : "memory");
    return r;
}
template <int OFF> DEVINL void gstore16(char* sbase, unsigned voff, u32x4 v) {
    asm volatile("global_store_dwordx4 %0, %1, %2 offset:%3 sc1\n\ts_nop 1" :: "v"(voff), "v"(v), "s"(sbase), "i"(OFF) : "memory");
}
DEVINL void gstore8(char* sbase, unsigned voff, u64 v) {
    asm volatile("global_store_dwordx2 %0, %1, %2 sc1\n\ts_nop 1" :: "v"(voff), "v"(v), "s"(sbase) : "memory");
}
DEVINL float xr16(float x, float& other) {
    unsigned u = __float_as_uint(x);
    auto r = __builtin_amdgcn_permlane16_swap(u, u, false, false);
    other = __uint_as_float(r[1]);
    return __uint_as_float(r[0]);
}
DEVINL float xr32(float x, float& other) {
    unsigned u = __float_as_uint(x);
    auto r = __builtin_amdgcn_permlane32_swap(u, u, false, false);
    other = __uint_as_float(r[1]);
    return __uint_as_float(r[0]);
}
DEVINL float xmax(float x) { float o, a = xr16(x, o); a = max2(a, o); a = xr32(a, o); return max2(a, o); }
DEVINL float xsum(float x) { float o, a = xr16(x, o); a = a + o; a = xr32(a, o); return a + o; }
DEVINL void wgbar() { MEMBAR; __builtin_amdgcn_s_barrier(); MEMBAR; }
template <int N> DEVINL void waitv() { asm volatile("s_waitcnt vmcnt(%0)" :: "i"(N) : "memory"); }

template <int IDX> DEVINL unsigned fl_ld(unsigned fb) { return __hip_atomic_load((lds_u32*)(size_t)(fb + 4 * IDX), __ATOMIC_RELAXED, __HIP_MEMORY_SCOPE_WORKGROUP); }
template <int IDX> DEVINL void fl_set(unsigned fbw, unsigned one) { asm volatile("ds_write_b8 %0, %1 offset:%2" :: "v"(fbw), "v"(one), "i"(4 * IDX) : "memory"); }
DEVINL unsigned rfl(unsigned v) { unsigned s; asm volatile("v_readfirstlane_b32 %0, %1" : "=s"(s) : "v"(v)); return s; }
template <int IDX, int NEED> DEVINL void fl_chk(unsigned fb, unsigned s) {
    unsigned n, v;
    asm volatile("s_cmp_ge_u32 %0, %5\n\ts_cbranch_scc1 2f\n\ts_mov_b32 %1, 0x100000\n"
                 "1:\n\tds_read_b32 %2, %3 offset:%4\n\ts_waitcnt lgkmcnt(0)\n\ts_sub_u32 %1, %1, 1\n\ts_cbranch_scc1 2f\n\t"
                 "v_readfirstlane_b32 %0, %2\n\ts_cmp_ge_u32 %0, %5\n\ts_cbranch_scc1 2f\n\ts_sleep 1\n\ts_branch 1b\n"
                 "2:" : "+s"(s), "=&s"(n), "=&v"(v) : "v"(fb), "i"(4 * IDX), "i"(NEED) : "memory", "scc");
}
template <int IDX, int NEED> DEVINL void fl_wait(unsigned fb) { fl_chk<IDX, NEED>(fb, 0u); }

DEVINL int h2f(int x) { return ((x << 1) & 4) | ((x >> 2) & 1); }
DEVINL int pixc(int par, int t, int i) {
    const int k = 14 * t - 6 + i, d = k >= 0 ? 2 * k : 2 * k + 7;
    return par ? 223 - d : d;
}

DEVINL void fold2_slow(const u16* __restrict__ wqkv, const u16* __restrict__ wo, char* __restrict__ dst, unsigned off, int f, int lane) {
    const int rho = lane & 15, g = lane >> 4;
    const int m = f / 12, s = (f % 12) >> 1, d = f & 1;
    const int c0 = 32 * s + rho, c1 = c0 + 16;
    const int cn = 32 * m + 8 * (rho >> 2) + 4 * d + (rho & 3);
    f32x4 a0 = {0.f, 0.f, 0.f, 0.f}, a1 = {0.f, 0.f, 0.f, 0.f};
#pragma unroll 1
    for (int t = 0; t < 6; ++t) {
        u32x4 b;
#pragma unroll
        for (int k = 0; k < 4; ++k)
            b[k] = (unsigned)wo[(size_t)(32 * t + 8 * g + 2 * k) * CH + cn] | ((unsigned)wo[(size_t)(32 * t + 8 * g + 2 * k + 1) * CH + cn] << 16);
        a0 = mfma16(*(const u32x4*)(wqkv + (size_t)c0 * (3 * CH) + 2 * CH + 32 * t + 8 * g), b, a0);
        a1 = mfma16(*(const u32x4*)(wqkv + (size_t)c1 * (3 * CH) + 2 * CH + 32 * t + 8 * g), b, a1);
    }
    gstore16<0>(dst, off, pk8(a0, a1));
}
DEVINL u32x4 fold1_slow(const u16* __restrict__ wqkv, int f, int lane) {
    const int rho = lane & 15, g = lane >> 4, pr = 8 * (rho >> 2) + (rho & 3);
    const int w = f / 18, s = (f % 18) / 3, b = f % 3;
    const int cb = b < 2 ? 3 * w + (w & 1) + b : 3 * w + ((w & 1) ? 0 : 2);
    const u16* pa = wqkv + (size_t)(32 * s + pr) * (3 * CH) + 8 * g;
    const u16* pb = wqkv + (size_t)(32 * (cb >> 1) + 4 * (cb & 1) + pr) * (3 * CH) + CH + 8 * g;
    f32x4 a0 = {0.f, 0.f, 0.f, 0.f}, a1 = {0.f, 0.f, 0.f, 0.f};
#pragma unroll 1
    for (int t = 0; t < 6; ++t) {
        const u32x4 bb = *(const u32x4*)(pb + 32 * t);
        a0 = mfma16(*(const u32x4*)(pa + 32 * t), bb, a0);
        a1 = mfma16(*(const u32x4*)(pa + 4 * 3 * CH + 32 * t), bb, a1);
    }
    return pk8(a0, a1);
}
static constexpr unsigned WS_M1 = 96 * 1024;
static constexpr unsigned WS_FLAG1 = 512 * 1024;
static constexpr unsigned WS_FLAGS = 256 * 1024;
static constexpr unsigned WS_EPOCH = 320 * 1024;
static constexpr unsigned WS_DELEG = 352 * 1024;
static constexpr unsigned WS_ACKS = 384 * 1024;
static constexpr unsigned WS_PRIV = 1024 * 1024;
static constexpr unsigned WS_USED = WS_PRIV + 1024 * 24 * 1024;
static constexpr int NPROD = 72;
DEVINL u64 mix64(u64 z) {
    z += 0x9e3779b97f4a7c15ull;
    z = (z ^ (z >> 30)) * 0xbf58476d1ce4e5b9ull;
    z = (z ^ (z >> 27)) * 0x94d049bb133111ebull;
    return z ^ (z >> 31);
}

struct Att {
    unsigned sT[7];
    unsigned linS, linP, swS, swP;
    unsigned dlin, dsw, zw;
    f32x4 mk[4];
    int t0v;
    unsigned ztab, zflag;
};
DEVINL void lane_terms(int k, int cell, int low, int S0, int L0, bool o, unsigned& lin, unsigned& sw) {
    const int s = S0 + 2 * (cell ^ (cell >> 1)) + (k & 1), e = (k >> 1) & 1;
    lin = (unsigned)(__mul24(s, o ? PIXB : ROWB) + __mul24(L0 + e, o ? ROWB : PIXB));
    sw = (unsigned)(16 * (h2f(s) ^ (2 * (o ? e : (k & 1))) ^ low));
    pin(lin); pin(sw);
}
DEVINL void att_addrS(Att& at, int lane, int S0, int L0, bool o) { lane_terms(lane & 15, (lane >> 2) & 3, lane >> 4, S0, L0, o, at.linS, at.swS); }
DEVINL void att_addrP(Att& at, int lane, int S0, int L0, bool o) {
    lane_terms(lane >> 2, lane >> 4, (lane >> 1) & 1, S0, L0, o, at.linP, at.swP);
    at.linP += 8 * (lane & 1); pin(at.linP);
}
DEVINL void att_tiles(Att& at, int L0, bool o) {
#pragma unroll
    for (int t = 0; t < 7; ++t) at.sT[t] = (unsigned)(2 * t * (o ? ROWB : PIXB) + 16 * h2f(L0 + 2 * t));
}
DEVINL void att_masks(Att& at, int g, int sq, int lq) {
    const int rb = ((sq + 7) & 7) - 2 * (g ^ (g >> 1)), pinv = (lq + 7) & 7;
    at.t0v = lq >> 1;
    const float r0 = rb == 0 ? -1e30f : 0.f, r1 = rb == 1 ? -1e30f : 0.f;
#pragma unroll
    for (int tm = 0; tm < 4; ++tm) {
        const bool c0 = pinv == 2 * tm, c1 = pinv == 2 * tm + 1;
        at.mk[tm] = (f32x4){c0 ? -1e30f : r0, c0 ? -1e30f : r1, c1 ? -1e30f : r0, c1 ? -1e30f : r1};
        pin(at.mk[tm]);
    }
    pin(at.t0v);
}
template <int NT, int NR, bool GATE, bool KEEP, class H>
DEVINL void att_rounds(Att& at, char* smem, unsigned g16, unsigned one, u32x4 (&qq)[NR][6], f32x4 (&accZ)[4][3], u32x4 (&zl)[6], H&& hook) {
    constexpr int NK = (NT + 1) / 2, NS = 6 * NT, NP = 12 * NK, NO = NS + NP, NOPS = NR * NO, BL = 3 * NK;
    u32x4 fr[NB], Pf[3];
    u32x2 Pf3;
    f32x4 accS[7];
    unsigned aS[7], aP[2], zt, gv = 0, gs = 0;
    auto zput = [&](auto kc) LAMBDA_INL {
        constexpr int k = decltype(kc)::value;
        SCHED;
        MEMBAR;
        *(u32x4*)(smem + 128 * (k % 3) + at.zw) = zl[k];
        if constexpr (k == 0) { at.zw ^= 64u; pin(at.zw); }
        SCHED;
    };
    auto load = [&](auto oc) LAMBDA_INL {
        constexpr int O = decltype(oc)::value, i = O / NO, n = O % NO;
        if constexpr (n < NS) {
            constexpr int ch = (n % (3 * NT)) / NT, t = n % NT;
            if constexpr (n == 0 && i > 0) { at.linS += at.dlin; at.swS ^= at.dsw ^ 64u; }
            if constexpr (n == 3 * NT) at.swS ^= 64u;
            if constexpr (ch == 0) aS[t] = xad(at.sT[t], at.swS, at.linS);
            fr[O % NB] = *(const u32x4*)(smem + 128 * ch + aS[t]);
            if constexpr (n == NS - 8) zt = *(const unsigned*)(smem + 128 * i + at.ztab);
        } else {
            constexpr int m = n - NS, cbp = m / BL, tau = (m % BL) / 3, ch = m % 3;
            if constexpr (m == 0 && i > 0) { at.linP += at.dlin; at.swP ^= at.dsw ^ 96u; }
            if constexpr (m > 0 && m % BL == 0) at.swP ^= (cbp == 2 ? 96u : 32u);
            if constexpr (ch == 0) {
                aP[0] = xad(at.sT[2 * tau], at.swP, at.linP);
                if constexpr (2 * tau + 1 < NT) aP[1] = xad(at.sT[2 * tau + 1], at.swP, at.linP);
            }
            if constexpr (2 * tau + 1 < NT) {
                const u32x2 lo = trread(smem + 128 * ch + aP[0]), hi = trread(smem + 128 * ch + aP[1]);
                fr[O % NB] = (u32x4){lo[0], lo[1], hi[0], hi[1]};
            } else if constexpr (ch == 0) {
#pragma unroll
                for (int c = 0; c < 3; ++c) { const u32x2 lo = trread(smem + 128 * c + aP[0]); fr[(O + c) % NB][0] = lo[0]; fr[(O + c) % NB][1] = lo[1]; }
            }
        }
        SCHED;
    };
    static_for<NOPS + PD + (KEEP ? 0 : ZD)>([&](auto sc) LAMBDA_INL {
        constexpr int S = decltype(sc)::value, J = S - PD;
        hook(sc);
        if constexpr (J >= 0 && J < NOPS && J % NO == NS) {
            bool sl[3];
#pragma unroll
            for (int t = 0; t < 3; ++t) sl[t] = t < at.t0v;
            at.zw = ((zt & 0x3fffu) << 4) ^ g16; pin(at.zw); nops<6>();
            SCHED;
            if constexpr (NT == 7) {
#pragma unroll
                for (int t = 0; t < 3; ++t)
#pragma unroll
                    for (int r = 0; r < 4; ++r) accS[t][r] = sl[t] ? accS[t + 4][r] : accS[t][r];
            }
            float ma = max3(accS[3][0], accS[3][1], accS[3][2]), mb = max3(accS[0][0], accS[0][1], accS[0][2]);
            ma = max3(ma, accS[3][3], accS[0][3]);
            mb = max3(mb, accS[1][0], accS[1][1]); ma = max3(ma, accS[1][2], accS[1][3]);
            mb = max3(mb, accS[2][0], accS[2][1]); ma = max3(ma, accS[2][2], accS[2][3]);
            const float mx = xmax(max2(ma, mb));
            const float kappa = 0.10411754112863712f;
            const float mneg = -mx * kappa;
#pragma unroll
            for (int t = 0; t < 4; ++t)
#pragma unroll
                for (int r = 0; r < 4; ++r) accS[t][r] = __builtin_amdgcn_exp2f(__builtin_fmaf(accS[t][r], kappa, mneg));
            const f32x4 s4 = (accS[0] + accS[1]) + (accS[2] + accS[3]);
            const float inv = __builtin_amdgcn_rcpf(xsum((s4[0] + s4[1]) + (s4[2] + s4[3])));
            unsigned pc[4][2];
#pragma unroll
            for (int t = 0; t < 4; ++t) { accS[t] *= inv; pin(accS[t]); }
            SCHED;
#pragma unroll
            for (int t = 0; t < 4; ++t) { pc[t][0] = pk2(accS[t][0], accS[t][1]); pc[t][1] = pk2(accS[t][2], accS[t][3]); }
            if constexpr (NT == 7) {
                Pf[0] = (u32x4){sl[0] ? 0u : pc[0][0], sl[0] ? 0u : pc[0][1], sl[1] ? 0u : pc[1][0], sl[1] ? 0u : pc[1][1]};
                Pf[1] = (u32x4){sl[2] ? 0u : pc[2][0], sl[2] ? 0u : pc[2][1], pc[3][0], pc[3][1]};
                Pf[2] = (u32x4){sl[0] ? pc[0][0] : 0u, sl[0] ? pc[0][1] : 0u, sl[1] ? pc[1][0] : 0u, sl[1] ? pc[1][1] : 0u};
                Pf3 = (u32x2){sl[2] ? pc[2][0] : 0u, sl[2] ? pc[2][1] : 0u};
                pin(Pf[2]); pin(Pf3);
            } else {
                Pf[0] = (u32x4){pc[0][0], pc[0][1], pc[1][0], pc[1][1]};
                Pf[1] = (u32x4){pc[2][0], pc[2][1], pc[3][0], pc[3][1]};
            }
            pin(Pf[0]); pin(Pf[1]);
            SCHED;
        }
        {
            constexpr int i = S / NO, n = S % NO;
            constexpr bool win = i > 0 && i < NR && n < 16;
            if constexpr (win) {
                if constexpr (n < 4) { load(IC(i * NO + 2 * n)); load(IC(i * NO + 2 * n + 1)); }
                if constexpr (n == 4) zput(IC(0));
                if constexpr (n == 7 || n == 8) { load(IC(i * NO + 3 * n - 13)); load(IC(i * NO + 3 * n - 12)); load(IC(i * NO + 3 * n - 11)); }
                if constexpr (n == 9) zput(IC(5));
                if constexpr (n == 11) zput(IC(4));
                if constexpr (n == 13) zput(IC(3));
                if constexpr (n == 15) { fl_set<F_ZF + 2 * i - 2>(at.zflag, one); load(IC(i * NO + 14)); load(IC(i * NO + 15)); }
            } else if constexpr (S < NOPS) load(sc);
            if constexpr (GATE && S == NOPS - 22) gv = fl_ld<F_PF + 2 * NR - 3>(at.zflag);
            if constexpr (GATE && S == NOPS - 17) gs = rfl(gv);
            if constexpr (GATE && S == NOPS - 14) fl_chk<F_PF + 2 * NR - 3, 1>(at.zflag, gs);
            if constexpr (NT == 7 && i < NR && n == NO - 14 && !KEEP) zput(IC(2));
            if constexpr (NT == 7 && i == 0 && n == NO - 2) fl_set<F_PF>(at.zflag, one);
            if constexpr (NT == 7 && i < NR && n == NO - 2 && !KEEP) zput(IC(1));
            if constexpr (NT == 7 && S == NOPS + 1 && !KEEP) zput(IC(0));
            if constexpr (NT == 4 && S == NOPS) zput(IC(2));
            if constexpr (NT == 4 && S == NOPS + 3) zput(IC(1));
            if constexpr (NT == 4 && S == NOPS + 5) zput(IC(0));
        }
        if constexpr (J >= 0 && J < NOPS) {
            constexpr int i = J / NO, n = J % NO;
            if constexpr (n < NS) {
                constexpr int nr = n - n % 3 + 2 - n % 3, Jr = J - n + nr;
                constexpr int sp = nr / (3 * NT), ch = (nr % (3 * NT)) / NT, t = nr % NT;
                if constexpr (nr < NT) mmac(accS[t], fr[Jr % NB], qq[i][0], at.mk[t & 3]);
                else mma(accS[t], fr[Jr % NB], qq[i][2 * ch + sp]);
                if constexpr (n == NT + 4) { use(at.mk[0]); use(at.mk[1]); use(at.mk[2]); use(at.mk[3]); }
            } else {
                constexpr int m = n - NS, cbp = m / BL, tau = (m % BL) / 3, ch = 2 - m % 3, Jr = J - m % 3 + ch;
                if constexpr (2 * tau + 1 < NT) mmf<tau == 0>(accZ[cbp][ch], fr[Jr % NB], Pf[tau]);
                else mmah(accZ[cbp][ch], (u32x2){fr[Jr % NB][0], fr[Jr % NB][1]}, Pf3);
            }
        } else if constexpr (J == NOPS) nops<6>();
        constexpr int Jp = J - ZD;
        if constexpr (Jp >= 0 && Jp < NOPS) {
            constexpr int i = Jp / NO, n = Jp % NO;
            if constexpr (n >= NS) {
                constexpr int m = n - NS, cbp = m / BL, tau = (m % BL) / 3, ch = 2 - m % 3;
                if constexpr ((cbp == 1 || (cbp == 3 && i == NR - 1)) && tau == NK - 1) {
                    zl[3 * (cbp >> 1) + ch] = cvt8(accZ[cbp - 1][ch], accZ[cbp][ch]);
                    if constexpr (cbp == 3) zput(IC(3 + ch));
                    if constexpr (cbp == 3 && ch == 0) fl_set<F_ZF + 2 * NR - 2>(at.zflag, one);
                }
            }
        }
        if constexpr (J >= NO && J < NOPS && J % NO >= 1 && J % NO < 7) {
            constexpr int ch = 2 - (J % NO - 1) / 2, h = (J % NO - 1) & 1;
            zl[3 + ch][2 * h] = cvt2(accZ[2 + h][ch][0], accZ[2 + h][ch][1]); zl[3 + ch][2 * h + 1] = cvt2(accZ[2 + h][ch][2], accZ[2 + h][ch][3]);
        }
        SCHED;
    });
    static_assert(PD == 6, "window schedule");
}

__global__ __launch_bounds__(512, 2) void na_main(const u16* __restrict__ x, const u16* __restrict__ wqkv,
                                                  const u16* __restrict__ wo, char* __restrict__ ws,
                                                  u16* __restrict__ out) {
    __shared__ __attribute__((aligned(1024))) char smem[LDS_TOT];

    u64 tev ;
    {
        const int ln_ = threadIdx.x & 63, wv_ = __builtin_amdgcn_readfirstlane(threadIdx.x >> 6), b_ = blockIdx.x;
        const unsigned lb_ = (unsigned)(size_t)smem;
        unsigned jk_;
        u64 pc_;
        asm volatile("s_getpc_b64 %0" : "=s"(pc_));
        { const unsigned off = min((unsigned)(((b_ >> 3) * 8 + wv_) * 64 + (ln_ >> 4) * 16384), (unsigned)CPF);
          asm volatile("global_load_dword %0, %1, %2" : "=v"(jk_) : "v"(off), "s"(pc_) : "memory"); }
        const int xc_ = b_ & 7, cl_ = xc_ >> 1, tl_ = (xc_ & 1) * 32 + (b_ >> 3);
        const unsigned base_ = (unsigned)(((28 * (tl_ >> 3) + (cl_ >> 1)) * 224 + 28 * (tl_ & 7) + (cl_ & 1)) * 384);
        auto dm_ = [&](const void* sb, unsigned voff, unsigned l) LAMBDA_INL {
            asm volatile("s_mov_b32 m0, %2\n\ts_nop 0\n\tglobal_load_lds_dwordx4 %0, %1" "" :: "v"(voff), "s"(sb), "s"(l) : "memory");
        };
        auto oa_ = [&](int j) LAMBDA_INL -> unsigned {
            int n = min(8 * j + (ln_ >> 3), 195), r = (n * 147) >> 11, c = n - 14 * r;
            return base_ + (unsigned)(r * 2 * 224 * 384 + c * 768 + (ln_ & 7) * 16);
        };
        auto ob_ = [&](int j) LAMBDA_INL -> unsigned {
            int l = min(8 * j + (ln_ >> 3), 587), n = (l * 683) >> 11, t = l - 3 * n, r = (n * 147) >> 11, c = n - 14 * r;
            return base_ + (unsigned)(r * 2 * 224 * 384 + c * 768 + t * 128 + (ln_ & 7) * 16);
        };
        const u64 t00_ = __builtin_amdgcn_s_memrealtime(); tev = t00_;

        const u64 c0_ = __builtin_amdgcn_s_memtime();
        constexpr unsigned PW_ = 32u << 20;
        u64 ep_;
        asm volatile("global_load_dwordx2 %0, %1, %2 sc1" : "=v"(ep_) : "v"(WS_EPOCH), "s"(ws) : "memory");
        const bool pd_ = wv_ == 0 && b_ < 72;
        u32x4 fa_[12], fo_[6];
        if (pd_) {
            const int i16_ = ln_ & 15, g_ = ln_ >> 4;
            const int s = b_ / 12, cb = b_ % 12;
            const unsigned ao = (unsigned)((32 * s + i16_) * (3 * PIXB) + 16 * g_), bo = (unsigned)((16 * cb + i16_) * (3 * PIXB) + PIXB + 16 * g_);
            static_for<6>([&](auto tc) LAMBDA_INL {
                constexpr int t = decltype(tc)::value;
                fo_[t] = gload16<64 * t>((const char*)wqkv, bo);
                fa_[2 * t] = gload16<64 * t>((const char*)wqkv, ao);
                fa_[2 * t + 1] = gload16<64 * t>((const char*)wqkv + 16 * 3 * PIXB, ao);
            });
        }
        SCHED;
        unsigned o[4]; for (int i = 0; i < 3; ++i) o[i] = oa_(wv_ + 8 * i); o[3] = oa_(24);
        auto xi_ = [&](int k) LAMBDA_INL { const int t = k / 3, i = k % 3; dm_((const char*)x + 128 * t, o[i], lb_ + 51200 * t + 1024 * (wv_ + 8 * i)); };
        auto m1_ = [&]() LAMBDA_INL {
            for (int i = 0; i < 9; ++i)
                asm volatile("s_mov_b32 m0, %2\n\ts_nop 0\n\tglobal_load_lds_dwordx4 %0, %1 sc1" :: "v"(PW_ + (unsigned)ln_ * 16 + (unsigned)(wv_ + 8 * i) * 1024), "s"(ws), "s"(lb_ + 51200 * (i / 3) + 25600 + 1024 * (wv_ + 8 * (i % 3))) : "memory");
        };
        if (!pd_) { for (int k = 0; k < 4; ++k) xi_(k); waitv<4>(); }
        else waitv<18>();
        pin(ep_);
        const u64 tg_ = mix64(ep_ ^ 0x1234);
        if (pd_) {
            waitv<0>();
            for (int i = 0; i < 12; ++i) pin(fa_[i]);
            for (int i = 0; i < 6; ++i) pin(fo_[i]);
            f32x4 pa0 = {0.f, 0.f, 0.f, 0.f}, pa1 = pa0;
            for (int t = 0; t < 6; ++t) { pa0 = mfma16(fa_[2 * t], fo_[t], pa0); pa1 = mfma16(fa_[2 * t + 1], fo_[t], pa1); }
            u32x4 dv = pk8(pa0, pa1);
            dv[0] = (unsigned)tg_; dv[1] = (unsigned)(tg_ >> 32);
            gstore16<0>(ws, PW_ + (unsigned)b_ * 1024u + (unsigned)ln_ * 16, dv);

            for (int k = 0; k < 4; ++k) xi_(k);
        }
        wgbar();
        for (int k = 4; k < 9; ++k) xi_(k);
        while (__builtin_amdgcn_s_memtime() - c0_ < (u64)6000) __builtin_amdgcn_s_sleep(1);

        m1_();
        for (int k = 9; k < 9; ++k) xi_(k);
        if (wv_ < 3) dm_((const char*)x + 128 * wv_, o[3], lb_ + 51200 * wv_ + 1024 * 24);
        int tries_ = 0;
        for (;;) {
            waitv<0>();
            bool ok = true;
            for (int i = 0; i < 9; ++i) {
                const u32x2 v = *(const u32x2*)(smem + 51200 * (i / 3) + 25600 + 1024 * (wv_ + 8 * (i % 3)) + ln_ * 16);
                ok = ok && v[0] == (unsigned)tg_ && v[1] == (unsigned)(tg_ >> 32);
            }
            if (__all(ok) || ++tries_ > 200) break;
            m1_();
        }
        tev = t00_ + 100 * tries_;

        waitv<0>();
        use(jk_);
        wgbar();
    }
    const int tid = threadIdx.x;
    const int lane = tid & 63;
    const int wave = __builtin_amdgcn_readfirstlane(tid >> 6);
    const int bid = blockIdx.x;
    const unsigned ldsbase = (unsigned)(size_t)smem;
    const int i16 = lane & 15, g = lane >> 4;
    const unsigned lane16 = lane * 16, g16 = (unsigned)g * 16;
    const f32x4 zero4 = {0.f, 0.f, 0.f, 0.f};

    unsigned jkc;
    {
        u64 pc0;
        asm volatile("s_getpc_b64 %0" : "=s"(pc0));
        const unsigned off = min((unsigned)(((bid >> 3) * 8 + wave) * 64 + (lane >> 4) * 16384), (unsigned)CPF);
        asm volatile("global_load_dword %0, %1, %2" : "=v"(jkc) : "v"(off), "s"(pc0) : "memory");
    }
    const u64 cstart = __builtin_amdgcn_s_memtime();
    if (wave == 1 || wave == 2) *(unsigned*)(smem + FLG + 4 * lane + (wave & 1) * 128) = 0u;
    asm volatile("s_waitcnt lgkmcnt(0)" ::: "memory");
    wgbar();
    const bool prod1 = wave == 0 && bid < NPROD, prod2 = wave == 0 && bid >= NPROD && bid < 2 * NPROD;
    asm volatile("" :: "s"(x), "s"(wqkv), "s"(wo), "s"(ws), "s"(out));
    u64 ep = 0;
    if (wave < 4 || wave == 7) asm volatile("global_load_dwordx2 %0, %1, %2 sc1" : "=v"(ep) : "v"(WS_EPOCH), "s"(ws) : "memory");
    const int pr_ = 8 * (i16 >> 2) + (i16 & 3);
    u64 m1tag = 0;
    if (prod1) {
        u32x4 fra[12], fro[6];
        const int w = (bid * 57) >> 10, r = bid - 18 * w, s = (r * 11) >> 5, b = r - 3 * s;
        const int cb = 3 * w + (b < 2 ? (w & 1) + b : (w & 1) ? 0 : 2);
        const unsigned ao = (unsigned)((32 * s + pr_) * (3 * PIXB) + 16 * g), bo = (unsigned)((32 * (cb >> 1) + 4 * (cb & 1) + pr_) * (3 * PIXB) + PIXB + 16 * g);
        static_for<6>([&](auto tc) LAMBDA_INL {
            constexpr int t = decltype(tc)::value;
            fro[t] = gload16<64 * t>((const char*)wqkv, bo);
            fra[2 * t] = gload16<64 * t>((const char*)wqkv, ao);
            fra[2 * t + 1] = gload16<64 * t>((const char*)wqkv + 4 * 3 * PIXB, ao);
        });
        waitv<18>();
        pin(ep);
        const u64 tg1 = mix64(ep);
        SCHED;
        waitv<0>();
#pragma unroll
            for (int i = 0; i < 12; ++i) pin(fra[i]);
#pragma unroll
            for (int i = 0; i < 6; ++i) pin(fro[i]);
            f32x4 pa0 = zero4, pa1 = zero4;
#pragma unroll
            for (int t = 0; t < 6; ++t) { pa0 = mfma16(fra[2 * t], fro[t], pa0); pa1 = mfma16(fra[2 * t + 1], fro[t], pa1); }
            gstore16<0>(ws, WS_M1 + (unsigned)bid * 1024u + lane16, pk8(pa0, pa1));
            m1tag = tg1;
    }
    SCHED;

    const int xcd = bid & 7;
    const int cls = xcd >> 1, tile = (xcd & 1) * 32 + (bid >> 3);
    const int p = cls >> 1, q = cls & 1;
    const int ta = tile >> 3, tb = tile & 7;
    const bool br = ta == 0, bc = tb == 0;
    const int qc4 = i16 >> 2;
    const int Sb = ((qc4 + 1) >> 1) & 1, Lq = 2 * (i16 & 3) + (qc4 & 1);
    const int a = wave & 3;
    const bool o = a & 1, rev = a == 1 || a == 2;
    const int L0 = a >= 2 ? 6 : 0;
    const bool clS = a == 0 ? br : a == 3 ? bc : false, clL = a == 0 ? bc : a == 1 ? br : false;
    const int S00 = rev ? 12 : clS ? 6 : 0, dS = rev ? -2 : clS ? 0 : 2;
    Att at;
    f32x4 accZ[4][3];
    u32x4 zl[6];

    unsigned fb, fbw, one;
    auto set_fb = [&]() LAMBDA_INL { fb = ldsbase + FLG; fbw = fb + (unsigned)a; one = 1; pin(fb); pin(fbw); pin(one); };
    const unsigned ti4 = (unsigned)i16 * 4 + TAB1, lq16 = lane16 + SLOT_B;
    auto finish = [&]() LAMBDA_INL {
        while ((long long)(__builtin_amdgcn_s_memrealtime() - tev) < 4000ll) __builtin_amdgcn_s_sleep(1);
        __builtin_amdgcn_endpgm();
        __builtin_unreachable();
    };

    if (wave < 4) {
        const __amdgpu_buffer_rsrc_t rws = __builtin_amdgcn_make_buffer_rsrc(ws, 0, WS_USED, 0x20000);
        u32x4 fra[12], fro[12];
        if (prod2) {
            const int f = bid - NPROD;
            const unsigned oo = (unsigned)(pr_ * PIXB + 64 * (f / 12) + 16 * g);
            const unsigned ao = (unsigned)((32 * ((f % 12) >> 1) + i16) * (3 * PIXB) + 2 * PIXB + 16 * g);
            static_for<6>([&](auto tc) LAMBDA_INL {
                constexpr int t = decltype(tc)::value;
                fro[2 * t] = gload16<0>((const char*)wo + 32 * t * PIXB, oo);
                fro[2 * t + 1] = gload16<4 * PIXB>((const char*)wo + 32 * t * PIXB, oo);
                fra[2 * t] = gload16<64 * t>((const char*)wqkv, ao);
                fra[2 * t + 1] = gload16<64 * t>((const char*)wqkv + 16 * 3 * PIXB, ao);
            });
        }
        SCHED;
        {
            const bool ctr = g == 3;
            const int sc = (rev ? 12 - 2 * g : 2 * g) + Sb, lc = L0 + Lq;
            int r = o ? lc : sc, c = o ? sc : lc;
            r = ctr ? 6 + Sb : r; c = ctr ? 6 + (Lq & 1) : c;
            const int oi = r < (br ? 6 : 0) ? (r >> 1) + 6 - 3 * (r & 1) : r + 3;
            const int oj = c < (bc ? 6 : 0) ? (c >> 1) + 6 - 3 * (c & 1) : c + 3;
            const unsigned xa = (unsigned)(__mul24(oi, ROWB) + __mul24(oj, PIXB) + 16 * (h2f(oi ^ oj) ^ (2 * (oi & 1))));
            const int zs = sc + (rev ? 6 : 0), zl = 2 * L0 + Lq;
            const int zr = o ? zl : zs, zc = o ? zs : zl;
            const int zmv = (wave == 2 && bc) ? -8 * (ROWB / 16) - 12 * 24 : (wave == 3 && br) ? -12 * (ROWB / 16) + 8 * 24 : 0;
            int za = __mul24(zr, ROWB / 16) + __mul24(zc, 24) + (h2f(zr ^ zc) ^ (2 * (zr & 1))) + (Lq == 0 ? zmv : 0);
            za = ctr ? CQ / 16 + 24 * qc4 + 4 * (qc4 & 1) + (qc4 >> 1) : za;
            const int ur = 14 * ta + r, uc = 14 * tb + c;
            const int dr = max(ur, 2 * ur - 6), dc = max(uc, 2 * uc - 6);
            const int pix = __mul24(dr, p ? -IMW : IMW) + (q ? -dc : dc) + (p ? 223 * IMW : 0) + (q ? 223 : 0);
            const unsigned e = (unsigned)(ctr ? 192 + i16 : 48 * wave + lane) * 4;
            *(unsigned*)(smem + TAB1 + e) = xa;
            *(unsigned*)(smem + TAB2 + e) = (unsigned)za | ((unsigned)pix << 14);
        }
        set_fb();
        const int cbP = 3 * wave + (wave & 1), cbS = 3 * wave + ((wave & 1) ? 0 : 2);
        u32x4 m1[6][3];
        if (prod2) waitv<24>(); else waitv<0>();
        if (prod1 && lane < 8) gstore8(ws, WS_FLAG1 + (unsigned)((lane * NPROD + bid) * 128), m1tag);
        use(jkc);
        pin(ep);
        const u64 tg = mix64(ep);
        const u32x2 tagv = {(unsigned)tg, (unsigned)(tg >> 32)};
        att_addrS(at, lane, S00 + dS, L0, o); att_addrP(at, lane, S00 + dS, L0, o);
        att_masks(at, g, clS ? 0 : Sb, clL ? max(Lq, 6) : Lq);
        fl_set<F_M1>(fbw, one);
        u32x4 xb[2][6];
        f32x4 accQ[2][3];
        unsigned xt, xa0, xa1, fv = 0, fv2 = 0, fv3 = 0, fs = 0, fs2 = 0, fs3 = 0;
        auto ldt1 = [&](auto jc) LAMBDA_INL { xt = *(const unsigned*)(smem + 64 * G1ORD(decltype(jc)::value) + ti4); };
        auto setx = [&]() LAMBDA_INL { xa0 = xt ^ g16; xa1 = xa0 ^ 64u; pin(xa0); pin(xa1); };
        auto ldx = [&](auto jc, auto sc) LAMBDA_INL {
            constexpr int j = decltype(jc)::value, s = decltype(sc)::value;
            xb[j & 1][s] = *(const u32x4*)(smem + 128 * (s >> 1) + ((s & 1) ? xa1 : xa0));
        };
        fl_wait<F_M1, ALL4>(fb);
        ldt1(IC(0)); setx(); ldt1(IC(1));
        if (prod2) {
            waitv<0>();
#pragma unroll
            for (int i = 0; i < 12; ++i) { pin(fra[i]); pin(fro[i]); }
            const int el = 4 * (bid & 1) + (i16 & 3);
            const unsigned on = (g == (i16 >> 2)) ? (0x3f80u << (16 * (el & 1))) : 0u;
            const u32x4 psel = {(el >> 1) == 0 ? on : 0u, (el >> 1) == 1 ? on : 0u, (el >> 1) == 2 ? on : 0u, (el >> 1) == 3 ? on : 0u};
            f32x4 pa0 = zero4, pa1 = zero4;
#pragma unroll
            for (int t = 0; t < 6; ++t) {
                const u32x4 b = pk8(mfma16(fro[2 * t], psel, zero4), mfma16(fro[2 * t + 1], psel, zero4));
                pa0 = mfma16(fra[2 * t], b, pa0);
                pa1 = mfma16(fra[2 * t + 1], b, pa1);
            }
            gstore16<0>(ws, (unsigned)(bid - NPROD) * 1024u + lane16, pk8(pa0, pa1));
        }
        unsigned m1a = WS_M1 + (unsigned)(18 * wave * 1024) + lane16;
        {
            const unsigned f1o = WS_FLAG1 + (unsigned)(((bid & 7) * NPROD + 18 * wave + min(lane, 17)) * 128);
            while (__builtin_amdgcn_s_memtime() - cstart < (u64)T1C) __builtin_amdgcn_s_sleep(1);
            const u64 t0 = __builtin_amdgcn_s_memrealtime();
            bool ok = false;
            for (;;) {
                const u32x2 v = __builtin_amdgcn_raw_buffer_load_b64(rws, f1o, 0, 16);
                if (__all(v[0] == tagv[0] && v[1] == tagv[1])) { ok = true; break; }
                if (__builtin_amdgcn_s_memrealtime() - t0 > 20000ull) break;
            }
            if (!ok) {
                m1a = WS_PRIV + (unsigned)(bid * 4 + wave) * 24576u + lane16;
                for (int f = 0; f < 18; ++f) gstore16<0>(ws, m1a + f * 1024, fold1_slow(wqkv, 18 * wave + f, lane));
                waitv<0>();
            }
            MEMBAR;
        }
        if (prod2) { waitv<0>(); if (lane < 8) gstore8(ws, WS_FLAGS + (unsigned)(bid - NPROD) * 8u + (unsigned)lane * 1024u, tg); }
        static_for<18>([&](auto ic) LAMBDA_INL {
            constexpr int i = decltype(ic)::value;
            m1[i / 3][i % 3] = __builtin_amdgcn_raw_buffer_load_b128(rws, m1a + 1024 * (i & 3), 4096 * (i >> 2), 16);
            if constexpr (i == 14) fv = fl_ld<F_XC>(fb);
            if constexpr (i == 16) fs = rfl(fv);
            SCHED;
        });
        __builtin_amdgcn_s_setprio(2);

        fl_chk<F_XC, ALL4>(fb, fs);
        static_for<6>([&](auto sc) LAMBDA_INL { ldx(IC(0), sc); });
        SCHED;
        unsigned qwP, qwS;
        u32x2 qv[13][3];
        unsigned fgo, m2a, m2b;
        u32x2 mflag;
        u32x4 m2[2][12];
        auto wrq = [&](auto jc, auto bc_) LAMBDA_INL {
            constexpr int j = decltype(jc)::value, bi = decltype(bc_)::value;
            MEMBAR;
            *(u32x2*)(smem + (j == 0 ? 0 : QSLOT(G1ORD(j)) - SLOT_B) + (bi == 1 ? 8 : 0) + (bi < 2 ? qwP : qwS)) = qv[j][bi];
            MEMBAR;
        };
        u32x4 qm[1][6];
        u32x2 hlv;
        unsigned hl0 = 0, hl1 = 0;
        auto pullm = [&](auto gc) LAMBDA_INL {
            constexpr int grp = decltype(gc)::value;
            if (wave == grp / 3) {
                fl_chk<F_QF + grp, ALL4>(fb, fs2);
#pragma unroll
                for (int m = 0; m < 6; ++m) qm[0][m] = *(const u32x4*)(smem + (QSLOT(grp) - SLOT_B) + 1024 * m + lq16);
                fl_set<F_QP + grp>(fb, one);
            }
        };
        auto m2_ready = [&]() LAMBDA_INL {
            m2a = (unsigned)(12 * wave * 1024) + lane16; m2b = (unsigned)(12 * (4 + (wave >> 1)) * 1024) + lane16;
            if (!__all(mflag[0] == tagv[0] && mflag[1] == tagv[1])) {
                const u64 t0 = __builtin_amdgcn_s_memrealtime();
                bool ok = false;
                for (;;) {
                    mflag = __builtin_amdgcn_raw_buffer_load_b64(rws, fgo, 0, 16);
                    if (__all(mflag[0] == tagv[0] && mflag[1] == tagv[1])) { ok = true; break; }
                    if (__builtin_amdgcn_s_memrealtime() - t0 > 20000ull) break;
                    __builtin_amdgcn_s_sleep(4);
                }
                if (!ok) {
                    m2a = WS_PRIV + (unsigned)(bid * 4 + wave) * 24576u + lane16; m2b = m2a + 12288;
                    for (int f = 0; f < 24; ++f) fold2_slow(wqkv, wo, ws, m2a + f * 1024, f < 12 ? 12 * wave + f : 12 * (4 + (wave >> 1)) + f - 12, lane);
                    waitv<0>();
                }
            }
            MEMBAR;
        };
        static_for<14>([&](auto jc) LAMBDA_INL {
            constexpr int j = decltype(jc)::value;
            static_for<18>([&](auto nc) LAMBDA_INL {
                constexpr int n = decltype(nc)::value, s = n / 3, bi = n % 3;
                if constexpr (j == 0) {
                    if constexpr (n == 0) setx();
                    if constexpr (n == 15) {
                        qwP = (unsigned)(16 * (lane >> 2) + (cbP >> 1) * 256 + CQ); qwS = (unsigned)(16 * (lane >> 2) + (cbS >> 1) * 256 + 8 * (cbS & 1) + CQ);
                        pin(qwP); pin(qwS);
                    }
                }
                if constexpr (j == 1 && n == 16) { qwP = lq16 + (unsigned)((cbP >> 1) * 1024); qwS = lq16 + (unsigned)((cbS >> 1) * 1024 + 8 * (cbS & 1)); pin(qwP); pin(qwS); }
                if constexpr (j == 2 && n == 16) {
                    const int lf = min(lane, 23);
                    fgo = WS_FLAGS + (unsigned)((bid & 7) * 1024 + 96 * wave) + (unsigned)(lf * 8 + (lf >= 12 ? 96 * (3 + (wave >> 1) - wave) : 0)); pin(fgo);
                }
                if constexpr (j == 3 && n == 8) fv3 = fl_ld<F_XC + 1>(fb);
                if constexpr (j == 3 && n == 16) fs3 = rfl(fv3);
                if constexpr (j == 4 && n == 0) { fl_chk<F_XC + 1, ALL4>(fb, fs3); }
                if constexpr (j + 1 < 13 && n >= 1 && n < 7) ldx(IC(j + 1), IC(n >= 1 && n < 7 ? n - 1 : 0));
                if constexpr (j + 2 < 13 && n == 7) ldt1(IC(j + 2 < 13 ? j + 2 : 0));
                if constexpr (j + 2 < 13 && n == 12) setx();
                if constexpr (j >= 1) {
                    constexpr int pj = j - 1, grp = G1ORD(pj);
                    if constexpr (n >= 5 && n < 11) qv[pj][(n - 5) >> 1][(n - 5) & 1] = cvt2(accQ[pj & 1][(n - 5) >> 1][2 * ((n - 5) & 1)], accQ[pj & 1][(n - 5) >> 1][2 * ((n - 5) & 1) + 1]);
                    if constexpr (pj != 8 && pj != 10 && pj != 12) {
                        constexpr int need = QPREV(grp) >= 0 ? F_QP + QPREV(grp) : -1;
                        if constexpr (need >= 0 && n == 4) fv = fl_ld<(need >= 0 ? need : 0)>(fb);
                        if constexpr (need >= 0 && n == 9) fs = rfl(fv);
                        if constexpr (need >= 0 && n == 11) fl_chk<(need >= 0 ? need : 0), 1>(fb, fs);
                        if constexpr (n == 11) wrq(IC(pj), IC(0));
                        if constexpr (n == 13) wrq(IC(pj), IC(1));
                        if constexpr (n == 15) wrq(IC(pj), IC(2));
                        if constexpr (n == 17) fl_set<F_QF + grp>(fbw, one);
                    }
                }
                if constexpr (j == 9 || j == 11) {
                    constexpr int grp = G1ORD(j - 2);
                    if constexpr (n == 8) fv2 = fl_ld<F_QF + grp>(fb);
                    if constexpr (n == 11) fs2 = rfl(fv2);
                    if constexpr (n == 14) pullm(IC(grp));
                }
                if constexpr (j == 12 && n == 2) hlv = *(const u32x2*)(smem + FLG + 4 * F_HL);
                if constexpr (j == 12 && n == 12) { hl0 = rfl(hlv[0]); hl1 = rfl(hlv[1]); }
                if constexpr (j == 12 && n == 14) { att_tiles(at, L0, o); at.ztab = ti4 + (unsigned)(832 + 192 * wave + 64); at.zflag = fb + (unsigned)(12 * wave + 4); pin(at.ztab); pin(at.zflag); }
                SCHED;
                if constexpr (j < 13) mmf<s == 0>(accQ[j & 1][bi], m1[s][bi], xb[j & 1][s]);
                else if constexpr (n == 0) nops<8>();
                SCHED;
            });
        });
        __builtin_amdgcn_s_setprio(1);
        const __amdgpu_buffer_rsrc_t rout = __builtin_amdgcn_make_buffer_rsrc(out, 0, XBYTES, 0x20000);
        const unsigned oc = g16 + 64u * (unsigned)wave;
        const int osh = 64 * (4 + (wave >> 1) - wave);
        const unsigned odd3 = (wave & 1) ? 3u : 0u;
        const unsigned tiq[2] = {ti4 + 832 + 64 * (3 - odd3), ti4 + 832 + 64 * odd3};
        const unsigned fbq[2] = {fb + 4 * (3 - odd3), fb + 4 * odd3};
        u32x4 zb[2][6], ov;
        f32x4 accO[2][2], accH[2];
        unsigned t2e, po[3], z0, z1;
        auto ldt2 = [&](auto jc) LAMBDA_INL {
            constexpr int j = decltype(jc)::value;
            if constexpr (j == 13) t2e = *(const unsigned*)(smem + 832 + 64 * 12 + ti4);
            else t2e = *(const unsigned*)(smem + 64 * G2B(j) + tiq[j & 1]);
        };
        auto zfb = [&](auto jc) LAMBDA_INL -> unsigned { constexpr int j = decltype(jc)::value; if constexpr (j == 13) return fb; else return fbq[j & 1]; };
        auto setz = [&](auto jc) LAMBDA_INL {
            constexpr int j = decltype(jc)::value;
            z0 = ((t2e & 0x3fffu) << 4) ^ g16; z1 = z0 ^ 64u; po[j % 3] = __umul24(t2e >> 14, PIXB) + oc;
            pin(z0); pin(z1); pin(po[j % 3]);
        };
        auto ldz = [&](auto jc, auto sc) LAMBDA_INL {
            constexpr int j = decltype(jc)::value, s = decltype(sc)::value;
            zb[j & 1][s] = *(const u32x4*)(smem + 128 * (s >> 1) + ((s & 1) ? z1 : z0));
        };
        auto zout = [&](auto kc) LAMBDA_INL {
            constexpr int k = decltype(kc)::value;
            MEMBAR;
            *(u32x4*)(smem + 128 * (k % 3) + (k >= 3 ? at.zw ^ 64u : at.zw)) = zl[k];
            MEMBAR;
        };
        fs2 = 0;
        pullm(IC(7));
        fl_chk<F_HL, ALL4>(fb, hl0);
        if (wave == 3) fl_chk<F_HL + 1, 0x01010000>(fb, hl1);
        att_rounds<7, 1, false, true>(at, smem, g16, one, qm, accZ, zl, [&](auto sc) LAMBDA_INL {
            constexpr int S = decltype(sc)::value;
            if constexpr (S == 6) {
                if (wave == 3) { fl_wait<F_QP + 7, 1>(fb); wrq(IC(12), IC(0)); wrq(IC(12), IC(1)); wrq(IC(12), IC(2)); fl_set<F_QF + 10>(fbw, one); }
                fs2 = 0;
                pullm(IC(10));
                fv = fl_ld<F_QP + 7>(fb);
            }
            if constexpr (S == 9) fs = rfl(fv);
            if constexpr (S == 11) { if (wave != 3) { fl_chk<F_QP + 7, 1>(fb, fs); wrq(IC(12), IC(0)); wrq(IC(12), IC(1)); wrq(IC(12), IC(2)); fl_set<F_QF + 10>(fbw, one); } }
            if constexpr (S == 24) fv = fl_ld<F_QP + 10>(fb);
            if constexpr (S == 29) fs = rfl(fv);
            if constexpr (S == 31) { fl_chk<F_QP + 10, 1>(fb, fs); wrq(IC(8), IC(0)); }
            if constexpr (S == 33) wrq(IC(8), IC(1));
            if constexpr (S == 35) wrq(IC(8), IC(2));
            if constexpr (S == 37) fl_set<F_QF + 8>(fbw, one);
            if constexpr (S == 16) { mflag = __builtin_amdgcn_raw_buffer_load_b64(rws, fgo, 0, 16); }
            if constexpr (S == 42 + PD) {
                m2_ready();
                static_for<6>([&](auto ic) LAMBDA_INL { constexpr int i = decltype(ic)::value; m2[0][i] = __builtin_amdgcn_raw_buffer_load_b128(rws, m2a + 1024 * (i & 3), 4096 * (i >> 2), 16); });
            }
            if constexpr (S >= 50 && S < 86 && S % 2 == 0) {
                constexpr int i = (S - 50) / 2 + 6, h = i / 12, k = i % 12;
                m2[h][k] = __builtin_amdgcn_raw_buffer_load_b128(rws, (h ? m2b : m2a) + 1024 * (k & 3), 4096 * (k >> 2), 16);
            }
            if constexpr (S == 57) fv = fl_ld<F_QP + 8>(fb);
            if constexpr (S == 61) fs = rfl(fv);
            if constexpr (S == 64) { fl_chk<F_QP + 8, 1>(fb, fs); wrq(IC(10), IC(0)); }
            if constexpr (S == 65) wrq(IC(10), IC(1));
            if constexpr (S == 66) wrq(IC(10), IC(2));
            if constexpr (S == 67) fl_set<F_QF + 11>(fbw, one);
            if constexpr (S == 79) { ldt2(IC(1)); fv = fl_ld<F_ZF + G2B(1)>(fbq[1]); }
            if constexpr (S == 84) { fs = rfl(fv); setz(IC(1)); }
            if constexpr (S == 90) fl_chk<F_ZF + G2B(1), 1>(fbq[1], fs);
            if constexpr (S >= 90 && S < 96) ldz(IC(1), IC(S >= 90 && S < 96 ? S - 90 : 0));
            if constexpr (S == 95) { ldt2(IC(2)); fv = fl_ld<F_ZF + G2B(2)>(fbq[0]); }
            SCHED;
        });
        static_for<13>([&](auto jc0) LAMBDA_INL {
            constexpr int j = decltype(jc0)::value + 1;
            if constexpr (j == G2LOW) __builtin_amdgcn_s_setprio(0);
            constexpr bool sh = (j & 1) && j < 13, more = j < 13;
            constexpr int jn = more ? j + 1 : 1, jn2 = j + 2 <= 13 ? j + 2 : 1, NM = sh ? 24 : 12;
            constexpr int L = sh ? 4 : 0;
            static_for<NM>([&](auto nc) LAMBDA_INL {
                constexpr int n = decltype(nc)::value, k = n % 12;
                if constexpr ((j == 2 || j == 4) && n == 1) setz(IC(jn));
                if constexpr (j == 2 && n == 5) fv = fl_ld<F_ZF + G2B(3)>(zfb(IC(3)));
                if constexpr (j == 2 && n == 11) fs = rfl(fv);
                if constexpr (j == 4 && n == 3) fv2 = fl_ld<F_PF - 1>(at.zflag);
                if constexpr (j == 4 && n == 11) fs2 = rfl(fv2);
                if constexpr (more && j != 2 && j != 4) {
                    if constexpr (n == L) fs = rfl(fv);
                    if constexpr (n == L + 1) setz(IC(jn));
                    if constexpr (n == L + 2) fl_chk<F_ZF + G2B(jn), 1>(zfb(IC(jn)), fs);
                    if constexpr (n >= L + 3 && n < L + 9) ldz(IC(jn), IC(n >= L + 3 && n < L + 9 ? n - L - 3 : 0));
                    if constexpr (j + 2 <= 13 && n == (sh ? 18 : 11)) { ldt2(IC(jn2)); fv = fl_ld<F_ZF + G2B(jn2)>(zfb(IC(jn2))); }
                }
                if constexpr (j == 1 && n >= 7 && n < 13) {
                    constexpr int ch = (n >= 7 ? (n - 7) / 2 : 0), h = (n - 7) & 1;
                    zl[3 + ch][2 * h] = cvt2(accZ[2 + h][ch][0], accZ[2 + h][ch][1]); zl[3 + ch][2 * h + 1] = cvt2(accZ[2 + h][ch][2], accZ[2 + h][ch][3]);
                }
                if constexpr (j == 1 && (n == 13 || n == 16 || n == 21)) { if (wave < 2) { zout(IC(n == 13 ? 0 : n == 16 ? 2 : 4)); zout(IC(n == 13 ? 1 : n == 16 ? 3 : 5)); } }
                if constexpr (j == 2 && n == 0) { if (wave < 2) fl_set<F_ZF>(at.zflag, one); }
                if constexpr (j >= 2) {
                    constexpr bool psh = (j - 1) & 1;
                    if constexpr (n >= 6 && n < 10) {
                        if constexpr (psh) ov[n - 6] = cvt2(accH[(n - 6) >> 1][2 * (n & 1)], accH[(n - 6) >> 1][2 * (n & 1) + 1]);
                        else ov[n - 6] = cvt2(accO[0][(n - 6) >> 1][2 * (n & 1)], accO[0][(n - 6) >> 1][2 * (n & 1) + 1]);
                    }
                    if constexpr (n == 10) __builtin_amdgcn_raw_buffer_store_b128(ov, rout, po[(j - 1) % 3], psh ? osh : 0, 16);
                }
                if constexpr (sh) {
                    if constexpr (n >= 19 && n < 23) ov[n - 19] = cvt2(accO[1][(n - 19) >> 1][2 * ((n - 19) & 1)], accO[1][(n - 19) >> 1][2 * ((n - 19) & 1) + 1]);
                    if constexpr (n == 23) __builtin_amdgcn_raw_buffer_store_b128(ov, rout, po[j % 3], 0, 16);
                }
                SCHED;
                if constexpr (n < 12) mmf<(k < 2)>(accO[j & 1][k & 1], m2[0][k], zb[j & 1][k >> 1]);
                else mmf<(k < 2)>(accH[k & 1], m2[1][k], zb[j & 1][k >> 1]);
                SCHED;
            });
            if constexpr (j == 2 || j == 4) {
                if constexpr (j == 4) {
                    if (wave >= 2) {
                        fl_chk<F_PF - 1, 1>(at.zflag, fs2);
                        static_for<6>([&](auto kc) LAMBDA_INL { zout(kc); });
                        fl_set<F_ZF>(at.zflag, one);
                    }
                    fs = 0;
                }
                fl_chk<F_ZF + G2B(jn), 1>(zfb(IC(jn)), fs);
                static_for<6>([&](auto sc) LAMBDA_INL { ldz(IC(jn), sc); });
                ldt2(IC(jn2)); fv = fl_ld<F_ZF + G2B(jn2)>(zfb(IC(jn2)));
                SCHED;
            }
        });
        if (wave & 1) {
            static_for<12>([&](auto nc) LAMBDA_INL { constexpr int n = decltype(nc)::value; mmf<(n < 2)>(accH[n & 1], m2[1][n], zb[1][n >> 1]); SCHED; });
            __builtin_amdgcn_raw_buffer_store_b128(cvt8(accO[1][0], accO[1][1]), rout, po[13 % 3], 0, 16);
            nops<4>();
            __builtin_amdgcn_raw_buffer_store_b128(cvt8(accH[0], accH[1]), rout, po[13 % 3], osh, 16);
        } else {
            nops<8>();
            __builtin_amdgcn_raw_buffer_store_b128(cvt8(accO[1][0], accO[1][1]), rout, po[13 % 3], 0, 16);
        }
        finish();
    } else {
        const bool w7 = wave == 7;
        const u32x4 rsx = {(unsigned)(size_t)x, (unsigned)((size_t)x >> 32) & 0xffffu, (unsigned)XBYTES, 0x20000u};
        const int half = a & 1, rpar = a >> 1;
        Att atc;
        unsigned cp[4];
        auto setup_cp = [&](auto nc) LAMBDA_INL {
            constexpr int n = decltype(nc)::value;
            const int lam = (n < 3 ? 4 + 24 * half + 8 * n : half ? 0 : 52) + (lane >> 3);
            const int col = (lam * 43) >> 7, chn = lam - 3 * col;
            const int pc = pixc(q, tb, col);
            unsigned v = (unsigned)(__mul24(pc, PIXB) + chn * 128 + 16 * ((lane & 7) ^ h2f(col)));
            cp[n] = (unsigned)pc < 224u ? v : 0x40000000u;
            pin(cp[n]);
        };
        const int rs1 = p ? -2 * IMW * PIXB : 2 * IMW * PIXB;
        const int rs0 = (p ? 223 * IMW * PIXB : 0) + (14 * ta - 6) * rs1;
        int rsw0 = rs0 + (3 + rpar) * rs1;
        const int rfix0 = br ? 7 * (rs1 / 2) : 0, rfix1 = rpar ? 0 : rfix0;
        unsigned hxt = rpar ? HXT(4) : HXT(3);
        const unsigned lown = ldsbase + (unsigned)(512 + 3072 * half), lside = ldsbase + (half ? 0u : 6656u);
        const unsigned lownw = lown + (unsigned)((3 + rpar) * ROWB), lsidew = lside + (unsigned)((3 + rpar) * ROWB);
        auto dma_row = [&](auto kc, auto wc) LAMBDA_INL {
            constexpr int k = decltype(kc)::value, what = decltype(wc)::value;
            unsigned so = (unsigned)(rsw0 + 2 * k * rs1 + (k == 0 ? rfix0 : k == 1 ? rfix1 : 0)), hx = (k == 0 ? hxt << 4 : hxt >> (4 * k - 4)) & 0x70u;
            asm volatile("" : "+s"(so), "+s"(hx));
            auto thr = [&]() LAMBDA_INL { if constexpr (k >= 2) waitv<XQ>(); };
            if constexpr (what & 1) { thr(); dmax<15360 * k>(rsx, cp[0], hx, so, lownw); thr(); dmax<15360 * k + 1024>(rsx, cp[1], hx, so, lownw); thr(); dmax<15360 * k + 2048>(rsx, cp[2], hx, so, lownw); }
            if constexpr (what & 2) { waitv<XQ>(); dmax<15360 * k>(rsx, cp[3], hx, so, lsidew); }
            SCHED;
        };
        auto dma_halo = [&](auto rc) LAMBDA_INL {
            constexpr int r = decltype(rc)::value;
            const unsigned so = (r < 3 && br) ? 0x40000000u : (unsigned)(rs0 + r * rs1), hx = 16 * HRC(r);
            dmax<ROWB * r>(rsx, cp[0], hx, so, lown); dmax<ROWB * r + 1024>(rsx, cp[1], hx, so, lown); dmax<ROWB * r + 2048>(rsx, cp[2], hx, so, lown);
            dmax<ROWB * r>(rsx, cp[3], hx, so, lside);
            SCHED;
        };

        u64 tag = 0;
        u32x4 dlg, ack0, ack1;
        setup_cp(IC(0)); setup_cp(IC(1)); setup_cp(IC(2));
        dma_row(IC(0), IC(1));
        dma_row(IC(1), IC(1));
        setup_cp(IC(3)); set_fb();
        dma_row(IC(2), IC(1));
        att_addrS(at, lane, S00, L0, o);
        dma_row(IC(3), IC(1));
        att_addrP(at, lane, S00, L0, o);
        dma_row(IC(4), IC(1)); dma_row(IC(5), IC(1));
        waitv<6>();
        use(jkc);
        fl_set<F_XC>(fbw, one);
        dma_row(IC(6), IC(1));
        asm volatile("" : "+s"(rsw0), "+s"(hxt));
        static_for<7>([&](auto kc) LAMBDA_INL { dma_row(kc, IC(2)); });
        waitv<7>();
        fl_set<F_XC + 1>(fbw, one);
        u32x4 qf[2][6], qcf[1][6];
        const unsigned fba = fb + 12 * a;
        auto pull = [&](auto ic, unsigned slot) LAMBDA_INL {
            constexpr int i = decltype(ic)::value;
            fl_wait<F_QF + 2 * i, ALL4>(fba);
            const unsigned b = lq16 + slot;
#pragma unroll
            for (int m = 0; m < 6; ++m) qf[i][m] = *(const u32x4*)(smem + 1024 * m + b);
            fl_set<F_QP + 2 * i>(fba, one);
            SCHED;
        };
        if (a < 2) {
            dma_halo(IC(2)); dma_halo(IC(1)); dma_halo(IC(0));
            att_masks(at, g, clS ? 0 : Sb, clL ? max(Lq, 6) : Lq);
            if (a == 0) { att_addrS(atc, lane, 6, 6, false); att_addrP(atc, lane, 6, 6, false); att_masks(atc, g, Sb, Lq & 1); }
            waitv<0>();
            fl_set<F_HL>(fbw, one);
            pull(IC(0), a == 0 ? QSLOT(0) - SLOT_B : QSLOT(3) - SLOT_B);
            pull(IC(1), a == 0 ? QSLOT(2) - SLOT_B : QSLOT(5) - SLOT_B);
        } else {
            dma_halo(IC(17));
            u32x4 xr[8];
            static_for<8>([&](auto ic) LAMBDA_INL { constexpr int i = decltype(ic)::value, r = 18 + i / 4; xr[i] = xload(rsx, cp[i % 4], 16 * HRC(r), (unsigned)(rs0 + r * rs1)); });
            att_masks(at, g, clS ? 0 : Sb, clL ? max(Lq, 6) : Lq);
            waitv<0>();
            fl_set<F_HL>(fbw, one);
            if (w7) {
                pin(ep);
                const u64 tg = mix64(ep);
                tag = ((u64)(unsigned)__builtin_amdgcn_readfirstlane((unsigned)(tg >> 32)) << 32) | (unsigned)__builtin_amdgcn_readfirstlane((unsigned)tg);
                fl_wait<F_M1, ALL4>(fb);
                if (lane == 0) gstore8(ws, WS_ACKS + (unsigned)bid * 8u, tag);
                waitv<0>();
                dlg = gload16s<0>(ws, WS_DELEG);
                waitv<0>();
                pin(dlg);
            }
            const unsigned wo18 = lane16 + (lown - ldsbase) + 18 * ROWB, ws18 = lane16 + (lside - ldsbase) + 18 * ROWB;
            auto put = [&](auto ic) LAMBDA_INL {
                constexpr int i = decltype(ic)::value;
                pin(xr[i]);
                *(u32x4*)(smem + ROWB * (i / 4) + (i % 4 < 3 ? 1024 * (i % 4) : 0) + (i % 4 < 3 ? wo18 : ws18)) = xr[i];
                MEMBAR;
            };
            __builtin_amdgcn_s_setprio(3);
            if (a == 2) pull(IC(0), QSLOT(6) - SLOT_B); else fl_wait<F_QP + 6, 1>(fb);
            static_for<4>([&](auto ic) LAMBDA_INL { put(ic); });
            if (a == 3) pull(IC(0), QSLOT(9) - SLOT_B); else fl_wait<F_QP + 9, 1>(fb);
            static_for<4>([&](auto ic) LAMBDA_INL { put(IC(4 + decltype(ic)::value)); });
            fl_set<F_HL + 1>(fbw, one);
        }
        __builtin_amdgcn_s_setprio(1);
        att_tiles(at, L0, o);
        at.dlin = (unsigned)(2 * dS * (o ? PIXB : ROWB)); at.dsw = dS ? 16u : 0u;
        at.ztab = ti4 + (unsigned)(832 + 192 * a); at.zflag = fba;
        fl_wait<F_HL, ALL4>(fb);
        if (a >= 2) fl_wait<F_HL + 1, 0x01010000>(fb);
        att_rounds<7, 2, true, false>(at, smem, g16, one, qf, accZ, zl, [&](auto sc) LAMBDA_INL {
            constexpr int S = decltype(sc)::value;
            if constexpr (S == 49) { if (w7 && bid == 0 && lane == 0) gstore8(ws, WS_DELEG, tag); }
            if constexpr (S == 139) { if (a < 2) __builtin_amdgcn_s_setprio(3); }
            if constexpr (S == 63) { if (a == 2) pull(IC(1), SLOT_A - SLOT_B); }
            if constexpr (S == 87) { if (a == 3) pull(IC(1), SLOT_A - SLOT_B); }
        });
        if (a == 0) {
#pragma unroll
            for (int m = 0; m < 6; ++m) qcf[0][m] = *(const u32x4*)(smem + CQ + 256 * m + 16 * (lane >> 2));
            att_tiles(atc, 6, false);
            atc.ztab = ti4 + (unsigned)(832 + 64 * 12); atc.zflag = fb + 48;
            att_rounds<4, 1, false, false>(atc, smem, g16, one, qcf, accZ, zl, [&](auto) LAMBDA_INL {});
        }
        if (w7) {
            waitv<0>();
            pin(dlg);
            const unsigned tl = (unsigned)tag, th = (unsigned)(tag >> 32);
            if (bid == 0 || __builtin_amdgcn_readfirstlane(dlg[0] == tl && dlg[1] == th)) {
                ack0 = gload16s<0>(ws, WS_ACKS + lane16); ack1 = gload16s<1024>(ws, WS_ACKS + lane16);
                waitv<0>();
                pin(ack0); pin(ack1);
                const bool good = ack0[0] == tl && ack0[1] == th && ack0[2] == tl && ack0[3] == th &&
                                  ack1[0] == tl && ack1[1] == th && ack1[2] == tl && ack1[3] == th;
                if (__all(good) && lane == 0) gstore8(ws, WS_EPOCH, tag);
            }
        }
        finish();
    }
}

extern "C" void kernel_launch(void* const* d_in, const int* in_sizes, int n_in,
                              void* d_out, int out_size, void* d_ws, size_t ws_size,
                              hipStream_t stream) {
    const u16* x = (const u16*)d_in[0];
    const u16* wqkv = (const u16*)d_in[1];
    const u16* wo = (const u16*)d_in[2];
    na_main<<<256, 512, 0, stream>>>(x, wqkv, wo, (char*)d_ws, (u16*)d_out);
}
```

```cpp
#include <hip/hip_runtime.h>
#include <stdint.h>
#include <utility>
#include <type_traits>

typedef unsigned short u16;
typedef unsigned long long u64;
typedef __attribute__((ext_vector_type(8))) __bf16 bf16x8_t;
typedef __attribute__((ext_vector_type(2))) __bf16 bf16x2_t;
typedef __attribute__((ext_vector_type(4))) float f32x4;
typedef __attribute__((ext_vector_type(2))) float f32x2;
typedef __attribute__((ext_vector_type(4))) unsigned u32x4;
typedef __attribute__((ext_vector_type(2))) unsigned u32x2;
typedef __attribute__((ext_vector_type(4))) short s16x4;
typedef __attribute__((address_space(3))) unsigned lds_u32;

#define DEVINL __device__ __forceinline__
#define LAMBDA_INL __attribute__((always_inline))
#define IC(n) std::integral_constant<int, (n)>{}
#define SCHED __builtin_amdgcn_sched_barrier(0)
#define MEMBAR asm volatile("" ::: "memory")

static constexpr int IMW = 224, CH = 192, PIXB = 384, XBYTES = IMW * IMW * PIXB;
static constexpr int ROWB = 7680;
static constexpr int LDS_TOT = 163840;
static constexpr int SLOT_B = 138240, SLOT_C = 145920, SLOT_A = 153600, CQ = 159744;
static constexpr int TAB1 = 161792, TAB2 = TAB1 + 832, FLG = TAB2 + 832;
static constexpr int F_XC = 8;
static constexpr int F_HL = 10;
static constexpr int F_M1 = 12;
static constexpr int F_QF = 16;
static constexpr int F_QP = 32;
static constexpr int F_ZF = 48;
static constexpr int F_PF = 64;
static constexpr int ALL4 = 0x01010101;

static constexpr int PD = 6;
static constexpr int NB = 16, ZD = 3;
static constexpr int CPF = 34500;
static constexpr int T1C = 6000;
static constexpr int XQ = 4;
static constexpr int G2LOW = 7;

constexpr int G1ORD(int j) { constexpr int o[13] = {12, 0, 3, 2, 5, 6, 9, 1, 8, 4, 11, 7, 10}; return o[j]; }
constexpr int QSLOT(int grp) {
    constexpr int s[13] = {SLOT_A, SLOT_A, SLOT_C, SLOT_B, SLOT_A, SLOT_A, SLOT_B, SLOT_A, SLOT_A, SLOT_C, SLOT_A, SLOT_A, CQ};
    return s[grp];
}
constexpr int QPREV(int grp) {
    constexpr int s[13] = {-1, 5, -1, -1, 1, 0, 3, 4, 10, 2, 7, 8, -1};
    return s[grp];
}
constexpr int HRC(int r) { return ((r << 1) & 4) | ((r >> 2) & 1) | (2 * (r & 1)); }
constexpr unsigned HXT(int r0) { unsigned t = 0; for (int k = 0; k < 7; ++k) t |= (unsigned)HRC(r0 + 2 * k) << (4 * k); return t; }
constexpr int G2B(int j) { constexpr int o[7] = {0, 1, 6, 7, 2, 8, 12}; return o[(j - 1) >> 1]; }

template <int... Is, class F>
DEVINL void static_for_impl(std::integer_sequence<int, Is...>, F&& f) { (f(std::integral_constant<int, Is>{}), ...); }
template <int N, class F>
DEVINL void static_for(F&& f) { static_for_impl(std::make_integer_sequence<int, N>{}, f); }

DEVINL f32x4 mfma16(u32x4 a, u32x4 b, f32x4 c) {
    return __builtin_amdgcn_mfma_f32_16x16x32_bf16(__builtin_bit_cast(bf16x8_t, a), __builtin_bit_cast(bf16x8_t, b), c, 0, 0, 0);
}
DEVINL void mma(f32x4& c, const u32x4& a, const u32x4& b) { asm volatile("v_mfma_f32_16x16x32_bf16 %0, %1, %2, %0" : "+v"(c) : "v"(a), "v"(b)); }
DEVINL void mma0(f32x4& c, const u32x4& a, const u32x4& b) { asm volatile("v_mfma_f32_16x16x32_bf16 %0, %1, %2, 0" : "=v"(c) : "v"(a), "v"(b)); }
DEVINL void mmac(f32x4& d, const u32x4& a, const u32x4& b, const f32x4& c) { asm volatile("v_mfma_f32_16x16x32_bf16 %0, %1, %2, %3" : "=v"(d) : "v"(a), "v"(b), "v"(c)); }
DEVINL void mmah(f32x4& c, const u32x2& a, const u32x2& b) { asm volatile("v_mfma_f32_16x16x16_bf16 %0, %1, %2, %0" : "+v"(c) : "v"(a), "v"(b)); }
template <bool FIRST> DEVINL void mmf(f32x4& c, const u32x4& a, const u32x4& b) { if constexpr (FIRST) mma0(c, a, b); else mma(c, a, b); }
DEVINL unsigned cvt2(float lo, float hi) { unsigned r; asm volatile("v_cvt_pk_bf16_f32 %0, %1, %2" : "=v"(r) : "v"(lo), "v"(hi)); return r; }
DEVINL u32x4 cvt8(const f32x4& a, const f32x4& b) { return (u32x4){cvt2(a[0], a[1]), cvt2(a[2], a[3]), cvt2(b[0], b[1]), cvt2(b[2], b[3])}; }
DEVINL float max3(float a, float b, float c) { float r; asm("v_max3_f32 %0, %1, %2, %3" : "=v"(r) : "v"(a), "v"(b), "v"(c)); return r; }
DEVINL float max2(float a, float b) { float r; asm("v_max_f32 %0, %1, %2" : "=v"(r) : "v"(a), "v"(b)); return r; }
template <int N> DEVINL void nops() { asm volatile("s_nop %0" :: "i"(N - 1)); }
template <class T> DEVINL void use(const T& v) { asm volatile("" :: "v"(v)); }
template <class T> DEVINL void pin(T& v) { asm volatile("" : "+v"(v)); }
DEVINL unsigned xad(unsigned s, unsigned x, unsigned a) { unsigned r; asm volatile("v_xad_u32 %0, %1, %2, %3" : "=v"(r) : "s"(s), "v"(x), "v"(a)); return r; }
DEVINL unsigned pk2(float lo, float hi) {
    f32x2 f = {lo, hi};
    return __builtin_bit_cast(unsigned, __builtin_convertvector(f, bf16x2_t));
}
DEVINL u32x4 pk8(f32x4 a, f32x4 b) { return (u32x4){pk2(a[0], a[1]), pk2(a[2], a[3]), pk2(b[0], b[1]), pk2(b[2], b[3])}; }
DEVINL u32x2 trread(const char* p) {
    s16x4 t = __builtin_amdgcn_ds_read_tr16_b64_v4i16((__attribute__((address_space(3))) s16x4*)(p));
    return __builtin_bit_cast(u32x2, t);
}
template <int LOFF> DEVINL void dmax(u32x4 rs, unsigned cpart, unsigned hx, unsigned soff, unsigned lds) {
    unsigned t;
    asm volatile("s_add_u32 m0, %5, %6\n\tv_xor_b32 %0, %3, %1\n\tbuffer_load_dwordx4 %0, %2, %4 offen lds"
                 : "=&v"(t) : "v"(cpart), "s"(rs), "s"(hx), "s"(soff), "s"(lds), "i"(LOFF) : "memory", "scc");
}
DEVINL u32x4 xload(u32x4 rs, unsigned cpart, unsigned hx, unsigned soff) {
    unsigned t; u32x4 r;
    asm volatile("v_xor_b32 %1, %4, %2\n\tbuffer_load_dwordx4 %0, %1, %3, %5 offen" : "=v"(r), "=&v"(t) : "v"(cpart), "s"(rs), "s"(hx), "s"(soff) : "memory");
    return r;
}
template <int OFF> DEVINL u32x4 gload16(const char* sbase, unsigned voff) {
    u32x4 r;
    asm volatile("global_load_dwordx4 %0, %1, %2 offset:%3" : "=v"(r) : "v"(voff), "s"(sbase), "i"(OFF) : "memory");
    return r;
}
template <int OFF> DEVINL u32x4 gload16s(const char* sbase, unsigned voff) {
    u32x4 r;
    asm volatile("global_load_dwordx4 %0, %1, %2 offset:%3 sc1" : "=v"(r) : "v"(voff), "s"(sbase), "i"(OFF) : "memory");
    return r;
}
template <int OFF> DEVINL void gstore16(char* sbase, unsigned voff, u32x4 v) {
    asm volatile("global_store_dwordx4 %0, %1, %2 offset:%3 sc1\n\ts_nop 1" :: "v"(voff), "v"(v), "s"(sbase), "i"(OFF) : "memory");
}
DEVINL void gstore8(char* sbase, unsigned voff, u64 v) {
    asm volatile("global_store_dwordx2 %0, %1, %2 sc1\n\ts_nop 1" :: "v"(voff), "v"(v), "s"(sbase) : "memory");
}
DEVINL float xr16(float x, float& other) {
    unsigned u = __float_as_uint(x);
    auto r = __builtin_amdgcn_permlane16_swap(u, u, false, false);
    other = __uint_as_float(r[1]);
    return __uint_as_float(r[0]);
}
DEVINL float xr32(float x, float& other) {
    unsigned u = __float_as_uint(x);
    auto r = __builtin_amdgcn_permlane32_swap(u, u, false, false);
    other = __uint_as_float(r[1]);
    return __uint_as_float(r[0]);
}
DEVINL float xmax(float x) { float o, a = xr16(x, o); a = max2(a, o); a = xr32(a, o); return max2(a, o); }
DEVINL float xsum(float x) { float o, a = xr16(x, o); a = a + o; a = xr32(a, o); return a + o; }
DEVINL void wgbar() { MEMBAR; __builtin_amdgcn_s_barrier(); MEMBAR; }
template <int N> DEVINL void waitv() { asm volatile("s_waitcnt vmcnt(%0)" :: "i"(N) : "memory"); }

template <int IDX> DEVINL unsigned fl_ld(unsigned fb) { return __hip_atomic_load((lds_u32*)(size_t)(fb + 4 * IDX), __ATOMIC_RELAXED, __HIP_MEMORY_SCOPE_WORKGROUP); }
template <int IDX> DEVINL void fl_set(unsigned fbw, unsigned one) { asm volatile("ds_write_b8 %0, %1 offset:%2" :: "v"(fbw), "v"(one), "i"(4 * IDX) : "memory"); }
DEVINL unsigned rfl(unsigned v) { unsigned s; asm volatile("v_readfirstlane_b32 %0, %1" : "=s"(s) : "v"(v)); return s; }
template <int IDX, int NEED> DEVINL void fl_chk(unsigned fb, unsigned s) {
    unsigned n, v;
    asm volatile("s_cmp_ge_u32 %0, %5\n\ts_cbranch_scc1 2f\n\ts_mov_b32 %1, 0x100000\n"
                 "1:\n\tds_read_b32 %2, %3 offset:%4\n\ts_waitcnt lgkmcnt(0)\n\ts_sub_u32 %1, %1, 1\n\ts_cbranch_scc1 2f\n\t"
                 "v_readfirstlane_b32 %0, %2\n\ts_cmp_ge_u32 %0, %5\n\ts_cbranch_scc1 2f\n\ts_sleep 1\n\ts_branch 1b\n"
                 "2:" : "+s"(s), "=&s"(n), "=&v"(v) : "v"(fb), "i"(4 * IDX), "i"(NEED) : "memory", "scc");
}
template <int IDX, int NEED> DEVINL void fl_wait(unsigned fb) { fl_chk<IDX, NEED>(fb, 0u); }

DEVINL int h2f(int x) { return ((x << 1) & 4) | ((x >> 2) & 1); }
DEVINL int pixc(int par, int t, int i) {
    const int k = 14 * t - 6 + i, d = k >= 0 ? 2 * k : 2 * k + 7;
    return par ? 223 - d : d;
}

DEVINL void fold2_slow(const u16* __restrict__ wqkv, const u16* __restrict__ wo, char* __restrict__ dst, unsigned off, int f, int lane) {
    const int rho = lane & 15, g = lane >> 4;
    const int m = f / 12, s = (f % 12) >> 1, d = f & 1;
    const int c0 = 32 * s + rho, c1 = c0 + 16;
    const int cn = 32 * m + 8 * (rho >> 2) + 4 * d + (rho & 3);
    f32x4 a0 = {0.f, 0.f, 0.f, 0.f}, a1 = {0.f, 0.f, 0.f, 0.f};
#pragma unroll 1
    for (int t = 0; t < 6; ++t) {
        u32x4 b;
#pragma unroll
        for (int k = 0; k < 4; ++k)
            b[k] = (unsigned)wo[(size_t)(32 * t + 8 * g + 2 * k) * CH + cn] | ((unsigned)wo[(size_t)(32 * t + 8 * g + 2 * k + 1) * CH + cn] << 16);
        a0 = mfma16(*(const u32x4*)(wqkv + (size_t)c0 * (3 * CH) + 2 * CH + 32 * t + 8 * g), b, a0);
        a1 = mfma16(*(const u32x4*)(wqkv + (size_t)c1 * (3 * CH) + 2 * CH + 32 * t + 8 * g), b, a1);
    }
    gstore16<0>(dst, off, pk8(a0, a1));
}
DEVINL u32x4 fold1_slow(const u16* __restrict__ wqkv, int f, int lane) {
    const int rho = lane & 15, g = lane >> 4, pr = 8 * (rho >> 2) + (rho & 3);
    const int w = f / 18, s = (f % 18) / 3, b = f % 3;
    const int cb = b < 2 ? 3 * w + (w & 1) + b : 3 * w + ((w & 1) ? 0 : 2);
    const u16* pa = wqkv + (size_t)(32 * s + pr) * (3 * CH) + 8 * g;
    const u16* pb = wqkv + (size_t)(32 * (cb >> 1) + 4 * (cb & 1) + pr) * (3 * CH) + CH + 8 * g;
    f32x4 a0 = {0.f, 0.f, 0.f, 0.f}, a1 = {0.f, 0.f, 0.f, 0.f};
#pragma unroll 1
    for (int t = 0; t < 6; ++t) {
        const u32x4 bb = *(const u32x4*)(pb + 32 * t);
        a0 = mfma16(*(const u32x4*)(pa + 32 * t), bb, a0);
        a1 = mfma16(*(const u32x4*)(pa + 4 * 3 * CH + 32 * t), bb, a1);
    }
    return pk8(a0, a1);
}
static constexpr unsigned WS_M1 = 96 * 1024;
static constexpr unsigned WS_FLAG1 = 512 * 1024;
static constexpr unsigned WS_FLAGS = 256 * 1024;
static constexpr unsigned WS_EPOCH = 320 * 1024;
static constexpr unsigned WS_DELEG = 352 * 1024;
static constexpr unsigned WS_ACKS = 384 * 1024;
static constexpr unsigned WS_PRIV = 1024 * 1024;
static constexpr unsigned WS_USED = WS_PRIV + 1024 * 24 * 1024;
static constexpr int NPROD = 72;
DEVINL u64 mix64(u64 z) {
    z += 0x9e3779b97f4a7c15ull;
    z = (z ^ (z >> 30)) * 0xbf58476d1ce4e5b9ull;
    z = (z ^ (z >> 27)) * 0x94d049bb133111ebull;
    return z ^ (z >> 31);
}

struct Att {
    unsigned sT[7];
    unsigned linS, linP, swS, swP;
    unsigned dlin, dsw, zw;
    f32x4 mk[4];
    int t0v;
    unsigned ztab, zflag;
};
DEVINL void lane_terms(int k, int cell, int low, int S0, int L0, bool o, unsigned& lin, unsigned& sw) {
    const int s = S0 + 2 * (cell ^ (cell >> 1)) + (k & 1), e = (k >> 1) & 1;
    lin = (unsigned)(__mul24(s, o ? PIXB : ROWB) + __mul24(L0 + e, o ? ROWB : PIXB));
    sw = (unsigned)(16 * (h2f(s) ^ (2 * (o ? e : (k & 1))) ^ low));
    pin(lin); pin(sw);
}
DEVINL void att_addrS(Att& at, int lane, int S0, int L0, bool o) { lane_terms(lane & 15, (lane >> 2) & 3, lane >> 4, S0, L0, o, at.linS, at.swS); }
DEVINL void att_addrP(Att& at, int lane, int S0, int L0, bool o) {
    lane_terms(lane >> 2, lane >> 4, (lane >> 1) & 1, S0, L0, o, at.linP, at.swP);
    at.linP += 8 * (lane & 1); pin(at.linP);
}
DEVINL void att_tiles(Att& at, int L0, bool o) {
#pragma unroll
    for (int t = 0; t < 7; ++t) at.sT[t] = (unsigned)(2 * t * (o ? ROWB : PIXB) + 16 * h2f(L0 + 2 * t));
}
DEVINL void att_masks(Att& at, int g, int sq, int lq) {
    const int rb = ((sq + 7) & 7) - 2 * (g ^ (g >> 1)), pinv = (lq + 7) & 7;
    at.t0v = lq >> 1;
    const float r0 = rb == 0 ? -1e30f : 0.f, r1 = rb == 1 ? -1e30f : 0.f;
#pragma unroll
    for (int tm = 0; tm < 4; ++tm) {
        const bool c0 = pinv == 2 * tm, c1 = pinv == 2 * tm + 1;
        at.mk[tm] = (f32x4){c0 ? -1e30f : r0, c0 ? -1e30f : r1, c1 ? -1e30f : r0, c1 ? -1e30f : r1};
        pin(at.mk[tm]);
    }
    pin(at.t0v);
}
template <int NT, int NR, bool GATE, bool KEEP, class H>
DEVINL void att_rounds(Att& at, char* smem, unsigned g16, unsigned one, u32x4 (&qq)[NR][6], f32x4 (&accZ)[4][3], u32x4 (&zl)[6], H&& hook) {
    constexpr int NK = (NT + 1) / 2, NS = 6 * NT, NP = 12 * NK, NO = NS + NP, NOPS = NR * NO, BL = 3 * NK;
    u32x4 fr[NB], Pf[3];
    u32x2 Pf3;
    f32x4 accS[7];
    unsigned aS[7], aP[2], zt, gv = 0, gs = 0;
    auto zput = [&](auto kc) LAMBDA_INL {
        constexpr int k = decltype(kc)::value;
        SCHED;
        MEMBAR;
        *(u32x4*)(smem + 128 * (k % 3) + at.zw) = zl[k];
        if constexpr (k == 0) { at.zw ^= 64u; pin(at.zw); }
        SCHED;
    };
    auto load = [&](auto oc) LAMBDA_INL {
        constexpr int O = decltype(oc)::value, i = O / NO, n = O % NO;
        if constexpr (n < NS) {
            constexpr int ch = (n % (3 * NT)) / NT, t = n % NT;
            if constexpr (n == 0 && i > 0) { at.linS += at.dlin; at.swS ^= at.dsw ^ 64u; }
            if constexpr (n == 3 * NT) at.swS ^= 64u;
            if constexpr (ch == 0) aS[t] = xad(at.sT[t], at.swS, at.linS);
            fr[O % NB] = *(const u32x4*)(smem + 128 * ch + aS[t]);
            if constexpr (n == NS - 8) zt = *(const unsigned*)(smem + 128 * i + at.ztab);
        } else {
            constexpr int m = n - NS, cbp = m / BL, tau = (m % BL) / 3, ch = m % 3;
            if constexpr (m == 0 && i > 0) { at.linP += at.dlin; at.swP ^= at.dsw ^ 96u; }
            if constexpr (m > 0 && m % BL == 0) at.swP ^= (cbp == 2 ? 96u : 32u);
            if constexpr (ch == 0) {
                aP[0] = xad(at.sT[2 * tau], at.swP, at.linP);
                if constexpr (2 * tau + 1 < NT) aP[1] = xad(at.sT[2 * tau + 1], at.swP, at.linP);
            }
            if constexpr (2 * tau + 1 < NT) {
                const u32x2 lo = trread(smem + 128 * ch + aP[0]), hi = trread(smem + 128 * ch + aP[1]);
                fr[O % NB] = (u32x4){lo[0], lo[1], hi[0], hi[1]};
            } else if constexpr (ch == 0) {
#pragma unroll
                for (int c = 0; c < 3; ++c) { const u32x2 lo = trread(smem + 128 * c + aP[0]); fr[(O + c) % NB][0] = lo[0]; fr[(O + c) % NB][1] = lo[1]; }
            }
        }
        SCHED;
    };
    static_for<NOPS + PD + (KEEP ? 0 : ZD)>([&](auto sc) LAMBDA_INL {
        constexpr int S = decltype(sc)::value, J = S - PD;
        hook(sc);
        if constexpr (J >= 0 && J < NOPS && J % NO == NS) {
            bool sl[3];
#pragma unroll
            for (int t = 0; t < 3; ++t) sl[t] = t < at.t0v;
            at.zw = ((zt & 0x3fffu) << 4) ^ g16; pin(at.zw); nops<6>();
            SCHED;
            if constexpr (NT == 7) {
#pragma unroll
                for (int t = 0; t < 3; ++t)
#pragma unroll
                    for (int r = 0; r < 4; ++r) accS[t][r] = sl[t] ? accS[t + 4][r] : accS[t][r];
            }
            float ma = max3(accS[3][0], accS[3][1], accS[3][2]), mb = max3(accS[0][0], accS[0][1], accS[0][2]);
            ma = max3(ma, accS[3][3], accS[0][3]);
            mb = max3(mb, accS[1][0], accS[1][1]); ma = max3(ma, accS[1][2], accS[1][3]);
            mb = max3(mb, accS[2][0], accS[2][1]); ma = max3(ma, accS[2][2], accS[2][3]);
            const float mx = xmax(max2(ma, mb));
            const float kappa = 0.10411754112863712f;
            const float mneg = -mx * kappa;
#pragma unroll
            for (int t = 0; t < 4; ++t)
#pragma unroll
                for (int r = 0; r < 4; ++r) accS[t][r] = __builtin_amdgcn_exp2f(__builtin_fmaf(accS[t][r], kappa, mneg));
            const f32x4 s4 = (accS[0] + accS[1]) + (accS[2] + accS[3]);
            const float inv = __builtin_amdgcn_rcpf(xsum((s4[0] + s4[1]) + (s4[2] + s4[3])));
            unsigned pc[4][2];
#pragma unroll
            for (int t = 0; t < 4; ++t) { accS[t] *= inv; pin(accS[t]); }
            SCHED;
#pragma unroll
            for (int t = 0; t < 4; ++t) { pc[t][0] = pk2(accS[t][0], accS[t][1]); pc[t][1] = pk2(accS[t][2], accS[t][3]); }
            if constexpr (NT == 7) {
                Pf[0] = (u32x4){sl[0] ? 0u : pc[0][0], sl[0] ? 0u : pc[0][1], sl[1] ? 0u : pc[1][0], sl[1] ? 0u : pc[1][1]};
                Pf[1] = (u32x4){sl[2] ? 0u : pc[2][0], sl[2] ? 0u : pc[2][1], pc[3][0], pc[3][1]};
                Pf[2] = (u32x4){sl[0] ? pc[0][0] : 0u, sl[0] ? pc[0][1] : 0u, sl[1] ? pc[1][0] : 0u, sl[1] ? pc[1][1] : 0u};
                Pf3 = (u32x2){sl[2] ? pc[2][0] : 0u, sl[2] ? pc[2][1] : 0u};
                pin(Pf[2]); pin(Pf3);
            } else {
                Pf[0] = (u32x4){pc[0][0], pc[0][1], pc[1][0], pc[1][1]};
                Pf[1] = (u32x4){pc[2][0], pc[2][1], pc[3][0], pc[3][1]};
            }
            pin(Pf[0]); pin(Pf[1]);
            SCHED;
        }
        {
            constexpr int i = S / NO, n = S % NO;
            constexpr bool win = i > 0 && i < NR && n < 16;
            if constexpr (win) {
                if constexpr (n < 4) { load(IC(i * NO + 2 * n)); load(IC(i * NO + 2 * n + 1)); }
                if constexpr (n == 4) zput(IC(0));
                if constexpr (n == 7 || n == 8) { load(IC(i * NO + 3 * n - 13)); load(IC(i * NO + 3 * n - 12)); load(IC(i * NO + 3 * n - 11)); }
                if constexpr (n == 9) zput(IC(5));
                if constexpr (n == 11) zput(IC(4));
                if constexpr (n == 13) zput(IC(3));
                if constexpr (n == 15) { fl_set<F_ZF + 2 * i - 2>(at.zflag, one); load(IC(i * NO + 14)); load(IC(i * NO + 15)); }
            } else if constexpr (S < NOPS) load(sc);
            if constexpr (GATE && S == NOPS - 22) gv = fl_ld<F_PF + 2 * NR - 3>(at.zflag);
            if constexpr (GATE && S == NOPS - 17) gs = rfl(gv);
            if constexpr (GATE && S == NOPS - 14) fl_chk<F_PF + 2 * NR - 3, 1>(at.zflag, gs);
            if constexpr (NT == 7 && i < NR && n == NO - 14 && !KEEP) zput(IC(2));
            if constexpr (NT == 7 && i == 0 && n == NO - 2) fl_set<F_PF>(at.zflag, one);
            if constexpr (NT == 7 && i < NR && n == NO - 2 && !KEEP) zput(IC(1));
            if constexpr (NT == 7 && S == NOPS + 1 && !KEEP) zput(IC(0));
            if constexpr (NT == 4 && S == NOPS) zput(IC(2));
            if constexpr (NT == 4 && S == NOPS + 3) zput(IC(1));
            if constexpr (NT == 4 && S == NOPS + 5) zput(IC(0));
        }
        if constexpr (J >= 0 && J < NOPS) {
            constexpr int i = J / NO, n = J % NO;
            if constexpr (n < NS) {
                constexpr int nr = n - n % 3 + 2 - n % 3, Jr = J - n + nr;
                constexpr int sp = nr / (3 * NT), ch = (nr % (3 * NT)) / NT, t = nr % NT;
                if constexpr (nr < NT) mmac(accS[t], fr[Jr % NB], qq[i][0], at.mk[t & 3]);
                else mma(accS[t], fr[Jr % NB], qq[i][2 * ch + sp]);
                if constexpr (n == NT + 4) { use(at.mk[0]); use(at.mk[1]); use(at.mk[2]); use(at.mk[3]); }
            } else {
                constexpr int m = n - NS, cbp = m / BL, tau = (m % BL) / 3, ch = 2 - m % 3, Jr = J - m % 3 + ch;
                if constexpr (2 * tau + 1 < NT) mmf<tau == 0>(accZ[cbp][ch], fr[Jr % NB], Pf[tau]);
                else mmah(accZ[cbp][ch], (u32x2){fr[Jr % NB][0], fr[Jr % NB][1]}, Pf3);
            }
        } else if constexpr (J == NOPS) nops<6>();
        constexpr int Jp = J - ZD;
        if constexpr (Jp >= 0 && Jp < NOPS) {
            constexpr int i = Jp / NO, n = Jp % NO;
            if constexpr (n >= NS) {
                constexpr int m = n - NS, cbp = m / BL, tau = (m % BL) / 3, ch = 2 - m % 3;
                if constexpr ((cbp == 1 || (cbp == 3 && i == NR - 1)) && tau == NK - 1) {
                    zl[3 * (cbp >> 1) + ch] = cvt8(accZ[cbp - 1][ch], accZ[cbp][ch]);
                    if constexpr (cbp == 3) zput(IC(3 + ch));
                    if constexpr (cbp == 3 && ch == 0) fl_set<F_ZF + 2 * NR - 2>(at.zflag, one);
                }
            }
        }
        if constexpr (J >= NO && J < NOPS && J % NO >= 1 && J % NO < 7) {
            constexpr int ch = 2 - (J % NO - 1) / 2, h = (J % NO - 1) & 1;
            zl[3 + ch][2 * h] = cvt2(accZ[2 + h][ch][0], accZ[2 + h][ch][1]); zl[3 + ch][2 * h + 1] = cvt2(accZ[2 + h][ch][2], accZ[2 + h][ch][3]);
        }
        SCHED;
    });
    static_assert(PD == 6, "window schedule");
}

__global__ __launch_bounds__(512, 2) void na_main(const u16* __restrict__ x, const u16* __restrict__ wqkv,
                                                  const u16* __restrict__ wo, char* __restrict__ ws,
                                                  u16* __restrict__ out) {
    __shared__ __attribute__((aligned(1024))) char smem[LDS_TOT];

    u64 tev ;
    {
        const int ln_ = threadIdx.x & 63, wv_ = __builtin_amdgcn_readfirstlane(threadIdx.x >> 6), b_ = blockIdx.x;
        const unsigned lb_ = (unsigned)(size_t)smem;
        unsigned jk_;
        u64 pc_;
        asm volatile("s_getpc_b64 %0" : "=s"(pc_));
        { const unsigned off = min((unsigned)(((b_ >> 3) * 8 + wv_) * 64 + (ln_ >> 4) * 16384), (unsigned)CPF);
          asm volatile("global_load_dword %0, %1, %2" : "=v"(jk_) : "v"(off), "s"(pc_) : "memory"); }
        const int xc_ = b_ & 7, cl_ = xc_ >> 1, tl_ = (xc_ & 1) * 32 + (b_ >> 3);
        const unsigned base_ = (unsigned)(((28 * (tl_ >> 3) + (cl_ >> 1)) * 224 + 28 * (tl_ & 7) + (cl_ & 1)) * 384);
        auto dm_ = [&](const void* sb, unsigned voff, unsigned l) LAMBDA_INL {
            asm volatile("s_mov_b32 m0, %2\n\ts_nop 0\n\tglobal_load_lds_dwordx4 %0, %1" "" :: "v"(voff), "s"(sb), "s"(l) : "memory");
        };
        auto oa_ = [&](int j) LAMBDA_INL -> unsigned {
            int n = min(8 * j + (ln_ >> 3), 195), r = (n * 147) >> 11, c = n - 14 * r;
            return base_ + (unsigned)(r * 2 * 224 * 384 + c * 768 + (ln_ & 7) * 16);
        };
        auto ob_ = [&](int j) LAMBDA_INL -> unsigned {
            int l = min(8 * j + (ln_ >> 3), 587), n = (l * 683) >> 11, t = l - 3 * n, r = (n * 147) >> 11, c = n - 14 * r;
            return base_ + (unsigned)(r * 2 * 224 * 384 + c * 768 + t * 128 + (ln_ & 7) * 16);
        };

        auto pix_ = [&](int n) LAMBDA_INL -> unsigned { n = min(n, 195); const int r = (n * 147) >> 11, c = n - 14 * r; return base_ + (unsigned)(r * 2 * 224 * 384 + c * 768); };
        u32x4 sv_ = {(unsigned)ln_ * 0x9e3779b9u, (unsigned)b_ * 0x85ebca6bu, (unsigned)threadIdx.x * 77777u, 0x12345678u};
        u32x2 sw_ = {sv_[0], sv_[2]};
        if (wv_ < 4) {
for (int pt = 0; pt < 3; ++pt) for (int gq = 0; gq < 3; ++gq) for (int r = 0; r < 2; ++r) { unsigned off = pix_(49 * wv_ + 16 * gq + (ln_ & 14) + r) + (ln_ & 1) * 16 + (ln_ >> 4) * 32 + 128 * pt; asm volatile("global_store_dwordx4 %0, %1, %2 sc1" :: "v"(off), "v"(sv_), "s"(out) : "memory"); }
        }
        waitv<0>(); tev = __builtin_amdgcn_s_memrealtime();
        waitv<0>();
        use(jk_);
        wgbar();
    }
    const int tid = threadIdx.x;
    const int lane = tid & 63;
    const int wave = __builtin_amdgcn_readfirstlane(tid >> 6);
    const int bid = blockIdx.x;
    const unsigned ldsbase = (unsigned)(size_t)smem;
    const int i16 = lane & 15, g = lane >> 4;
    const unsigned lane16 = lane * 16, g16 = (unsigned)g * 16;
    const f32x4 zero4 = {0.f, 0.f, 0.f, 0.f};

    unsigned jkc;
    {
        u64 pc0;
        asm volatile("s_getpc_b64 %0" : "=s"(pc0));
        const unsigned off = min((unsigned)(((bid >> 3) * 8 + wave) * 64 + (lane >> 4) * 16384), (unsigned)CPF);
        asm volatile("global_load_dword %0, %1, %2" : "=v"(jkc) : "v"(off), "s"(pc0) : "memory");
    }
    const u64 cstart = __builtin_amdgcn_s_memtime();
    if (wave == 1 || wave == 2) *(unsigned*)(smem + FLG + 4 * lane + (wave & 1) * 128) = 0u;
    asm volatile("s_waitcnt lgkmcnt(0)" ::: "memory");
    wgbar();
    const bool prod1 = wave == 0 && bid < NPROD, prod2 = wave == 0 && bid >= NPROD && bid < 2 * NPROD;
    asm volatile("" :: "s"(x), "s"(wqkv), "s"(wo), "s"(ws), "s"(out));
    u64 ep = 0;
    if (wave < 4 || wave == 7) asm volatile("global_load_dwordx2 %0, %1, %2 sc1" : "=v"(ep) : "v"(WS_EPOCH), "s"(ws) : "memory");
    const int pr_ = 8 * (i16 >> 2) + (i16 & 3);
    u64 m1tag = 0;
    if (prod1) {
        u32x4 fra[12], fro[6];
        const int w = (bid * 57) >> 10, r = bid - 18 * w, s = (r * 11) >> 5, b = r - 3 * s;
        const int cb = 3 * w + (b < 2 ? (w & 1) + b : (w & 1) ? 0 : 2);
        const unsigned ao = (unsigned)((32 * s + pr_) * (3 * PIXB) + 16 * g), bo = (unsigned)((32 * (cb >> 1) + 4 * (cb & 1) + pr_) * (3 * PIXB) + PIXB + 16 * g);
        static_for<6>([&](auto tc) LAMBDA_INL {
            constexpr int t = decltype(tc)::value;
            fro[t] = gload16<64 * t>((const char*)wqkv, bo);
            fra[2 * t] = gload16<64 * t>((const char*)wqkv, ao);
            fra[2 * t + 1] = gload16<64 * t>((const char*)wqkv + 4 * 3 * PIXB, ao);
        });
        waitv<18>();
        pin(ep);
        const u64 tg1 = mix64(ep);
        SCHED;
        waitv<0>();
#pragma unroll
            for (int i = 0; i < 12; ++i) pin(fra[i]);
#pragma unroll
            for (int i = 0; i < 6; ++i) pin(fro[i]);
            f32x4 pa0 = zero4, pa1 = zero4;
#pragma unroll
            for (int t = 0; t < 6; ++t) { pa0 = mfma16(fra[2 * t], fro[t], pa0); pa1 = mfma16(fra[2 * t + 1], fro[t], pa1); }
            gstore16<0>(ws, WS_M1 + (unsigned)bid * 1024u + lane16, pk8(pa0, pa1));
            m1tag = tg1;
    }
    SCHED;

    const int xcd = bid & 7;
    const int cls = xcd >> 1, tile = (xcd & 1) * 32 + (bid >> 3);
    const int p = cls >> 1, q = cls & 1;
    const int ta = tile >> 3, tb = tile & 7;
    const bool br = ta == 0, bc = tb == 0;
    const int qc4 = i16 >> 2;
    const int Sb = ((qc4 + 1) >> 1) & 1, Lq = 2 * (i16 & 3) + (qc4 & 1);
    const int a = wave & 3;
    const bool o = a & 1, rev = a == 1 || a == 2;
    const int L0 = a >= 2 ? 6 : 0;
    const bool clS = a == 0 ? br : a == 3 ? bc : false, clL = a == 0 ? bc : a == 1 ? br : false;
    const int S00 = rev ? 12 : clS ? 6 : 0, dS = rev ? -2 : clS ? 0 : 2;
    Att at;
    f32x4 accZ[4][3];
    u32x4 zl[6];

    unsigned fb, fbw, one;
    auto set_fb = [&]() LAMBDA_INL { fb = ldsbase + FLG; fbw = fb + (unsigned)a; one = 1; pin(fb); pin(fbw); pin(one); };
    const unsigned ti4 = (unsigned)i16 * 4 + TAB1, lq16 = lane16 + SLOT_B;
    auto finish = [&]() LAMBDA_INL {
        while ((long long)(__builtin_amdgcn_s_memrealtime() - tev) < 4000ll) __builtin_amdgcn_s_sleep(1);
        __builtin_amdgcn_endpgm();
        __builtin_unreachable();
    };

    if (wave < 4) {
        const __amdgpu_buffer_rsrc_t rws = __builtin_amdgcn_make_buffer_rsrc(ws, 0, WS_USED, 0x20000);
        u32x4 fra[12], fro[12];
        if (prod2) {
            const int f = bid - NPROD;
            const unsigned oo = (unsigned)(pr_ * PIXB + 64 * (f / 12) + 16 * g);
            const unsigned ao = (unsigned)((32 * ((f % 12) >> 1) + i16) * (3 * PIXB) + 2 * PIXB + 16 * g);
            static_for<6>([&](auto tc) LAMBDA_INL {
                constexpr int t = decltype(tc)::value;
                fro[2 * t] = gload16<0>((const char*)wo + 32 * t * PIXB, oo);
                fro[2 * t + 1] = gload16<4 * PIXB>((const char*)wo + 32 * t * PIXB, oo);
                fra[2 * t] = gload16<64 * t>((const char*)wqkv, ao);
                fra[2 * t + 1] = gload16<64 * t>((const char*)wqkv + 16 * 3 * PIXB, ao);
            });
        }
        SCHED;
        {
            const bool ctr = g == 3;
            const int sc = (rev ? 12 - 2 * g : 2 * g) + Sb, lc = L0 + Lq;
            int r = o ? lc : sc, c = o ? sc : lc;
            r = ctr ? 6 + Sb : r; c = ctr ? 6 + (Lq & 1) : c;
            const int oi = r < (br ? 6 : 0) ? (r >> 1) + 6 - 3 * (r & 1) : r + 3;
            const int oj = c < (bc ? 6 : 0) ? (c >> 1) + 6 - 3 * (c & 1) : c + 3;
            const unsigned xa = (unsigned)(__mul24(oi, ROWB) + __mul24(oj, PIXB) + 16 * (h2f(oi ^ oj) ^ (2 * (oi & 1))));
            const int zs = sc + (rev ? 6 : 0), zl = 2 * L0 + Lq;
            const int zr = o ? zl : zs, zc = o ? zs : zl;
            const int zmv = (wave == 2 && bc) ? -8 * (ROWB / 16) - 12 * 24 : (wave == 3 && br) ? -12 * (ROWB / 16) + 8 * 24 : 0;
            int za = __mul24(zr, ROWB / 16) + __mul24(zc, 24) + (h2f(zr ^ zc) ^ (2 * (zr & 1))) + (Lq == 0 ? zmv : 0);
            za = ctr ? CQ / 16 + 24 * qc4 + 4 * (qc4 & 1) + (qc4 >> 1) : za;
            const int ur = 14 * ta + r, uc = 14 * tb + c;
            const int dr = max(ur, 2 * ur - 6), dc = max(uc, 2 * uc - 6);
            const int pix = __mul24(dr, p ? -IMW : IMW) + (q ? -dc : dc) + (p ? 223 * IMW : 0) + (q ? 223 : 0);
            const unsigned e = (unsigned)(ctr ? 192 + i16 : 48 * wave + lane) * 4;
            *(unsigned*)(smem + TAB1 + e) = xa;
            *(unsigned*)(smem + TAB2 + e) = (unsigned)za | ((unsigned)pix << 14);
        }
        set_fb();
        const int cbP = 3 * wave + (wave & 1), cbS = 3 * wave + ((wave & 1) ? 0 : 2);
        u32x4 m1[6][3];
        if (prod2) waitv<24>(); else waitv<0>();
        if (prod1 && lane < 8) gstore8(ws, WS_FLAG1 + (unsigned)((lane * NPROD + bid) * 128), m1tag);
        use(jkc);
        pin(ep);
        const u64 tg = mix64(ep);
        const u32x2 tagv = {(unsigned)tg, (unsigned)(tg >> 32)};
        att_addrS(at, lane, S00 + dS, L0, o); att_addrP(at, lane, S00 + dS, L0, o);
        att_masks(at, g, clS ? 0 : Sb, clL ? max(Lq, 6) : Lq);
        fl_set<F_M1>(fbw, one);
        u32x4 xb[2][6];
        f32x4 accQ[2][3];
        unsigned xt, xa0, xa1, fv = 0, fv2 = 0, fv3 = 0, fs = 0, fs2 = 0, fs3 = 0;
        auto ldt1 = [&](auto jc) LAMBDA_INL { xt = *(const unsigned*)(smem + 64 * G1ORD(decltype(jc)::value) + ti4); };
        auto setx = [&]() LAMBDA_INL { xa0 = xt ^ g16; xa1 = xa0 ^ 64u; pin(xa0); pin(xa1); };
        auto ldx = [&](auto jc, auto sc) LAMBDA_INL {
            constexpr int j = decltype(jc)::value, s = decltype(sc)::value;
            xb[j & 1][s] = *(const u32x4*)(smem + 128 * (s >> 1) + ((s & 1) ? xa1 : xa0));
        };
        fl_wait<F_M1, ALL4>(fb);
        ldt1(IC(0)); setx(); ldt1(IC(1));
        if (prod2) {
            waitv<0>();
#pragma unroll
            for (int i = 0; i < 12; ++i) { pin(fra[i]); pin(fro[i]); }
            const int el = 4 * (bid & 1) + (i16 & 3);
            const unsigned on = (g == (i16 >> 2)) ? (0x3f80u << (16 * (el & 1))) : 0u;
            const u32x4 psel = {(el >> 1) == 0 ? on : 0u, (el >> 1) == 1 ? on : 0u, (el >> 1) == 2 ? on : 0u, (el >> 1) == 3 ? on : 0u};
            f32x4 pa0 = zero4, pa1 = zero4;
#pragma unroll
            for (int t = 0; t < 6; ++t) {
                const u32x4 b = pk8(mfma16(fro[2 * t], psel, zero4), mfma16(fro[2 * t + 1], psel, zero4));
                pa0 = mfma16(fra[2 * t], b, pa0);
                pa1 = mfma16(fra[2 * t + 1], b, pa1);
            }
            gstore16<0>(ws, (unsigned)(bid - NPROD) * 1024u + lane16, pk8(pa0, pa1));
        }
        unsigned m1a = WS_M1 + (unsigned)(18 * wave * 1024) + lane16;
        {
            const unsigned f1o = WS_FLAG1 + (unsigned)(((bid & 7) * NPROD + 18 * wave + min(lane, 17)) * 128);
            while (__builtin_amdgcn_s_memtime() - cstart < (u64)T1C) __builtin_amdgcn_s_sleep(1);
            const u64 t0 = __builtin_amdgcn_s_memrealtime();
            bool ok = false;
            for (;;) {
                const u32x2 v = __builtin_amdgcn_raw_buffer_load_b64(rws, f1o, 0, 16);
                if (__all(v[0] == tagv[0] && v[1] == tagv[1])) { ok = true; break; }
                if (__builtin_amdgcn_s_memrealtime() - t0 > 20000ull) break;
            }
            if (!ok) {
                m1a = WS_PRIV + (unsigned)(bid * 4 + wave) * 24576u + lane16;
                for (int f = 0; f < 18; ++f) gstore16<0>(ws, m1a + f * 1024, fold1_slow(wqkv, 18 * wave + f, lane));
                waitv<0>();
            }
            MEMBAR;
        }
        if (prod2) { waitv<0>(); if (lane < 8) gstore8(ws, WS_FLAGS + (unsigned)(bid - NPROD) * 8u + (unsigned)lane * 1024u, tg); }
        static_for<18>([&](auto ic) LAMBDA_INL {
            constexpr int i = decltype(ic)::value;
            m1[i / 3][i % 3] = __builtin_amdgcn_raw_buffer_load_b128(rws, m1a + 1024 * (i & 3), 4096 * (i >> 2), 16);
            if constexpr (i == 14) fv = fl_ld<F_XC>(fb);
            if constexpr (i == 16) fs = rfl(fv);
            SCHED;
        });
        __builtin_amdgcn_s_setprio(2);

        fl_chk<F_XC, ALL4>(fb, fs);
        static_for<6>([&](auto sc) LAMBDA_INL { ldx(IC(0), sc); });
        SCHED;
        unsigned qwP, qwS;
        u32x2 qv[13][3];
        unsigned fgo, m2a, m2b;
        u32x2 mflag;
        u32x4 m2[2][12];
        auto wrq = [&](auto jc, auto bc_) LAMBDA_INL {
            constexpr int j = decltype(jc)::value, bi = decltype(bc_)::value;
            MEMBAR;
            *(u32x2*)(smem + (j == 0 ? 0 : QSLOT(G1ORD(j)) - SLOT_B) + (bi == 1 ? 8 : 0) + (bi < 2 ? qwP : qwS)) = qv[j][bi];
            MEMBAR;
        };
        u32x4 qm[1][6];
        u32x2 hlv;
        unsigned hl0 = 0, hl1 = 0;
        auto pullm = [&](auto gc) LAMBDA_INL {
            constexpr int grp = decltype(gc)::value;
            if (wave == grp / 3) {
                fl_chk<F_QF + grp, ALL4>(fb, fs2);
#pragma unroll
                for (int m = 0; m < 6; ++m) qm[0][m] = *(const u32x4*)(smem + (QSLOT(grp) - SLOT_B) + 1024 * m + lq16);
                fl_set<F_QP + grp>(fb, one);
            }
        };
        auto m2_ready = [&]() LAMBDA_INL {
            m2a = (unsigned)(12 * wave * 1024) + lane16; m2b = (unsigned)(12 * (4 + (wave >> 1)) * 1024) + lane16;
            if (!__all(mflag[0] == tagv[0] && mflag[1] == tagv[1])) {
                const u64 t0 = __builtin_amdgcn_s_memrealtime();
                bool ok = false;
                for (;;) {
                    mflag = __builtin_amdgcn_raw_buffer_load_b64(rws, fgo, 0, 16);
                    if (__all(mflag[0] == tagv[0] && mflag[1] == tagv[1])) { ok = true; break; }
                    if (__builtin_amdgcn_s_memrealtime() - t0 > 20000ull) break;
                    __builtin_amdgcn_s_sleep(4);
                }
                if (!ok) {
                    m2a = WS_PRIV + (unsigned)(bid * 4 + wave) * 24576u + lane16; m2b = m2a + 12288;
                    for (int f = 0; f < 24; ++f) fold2_slow(wqkv, wo, ws, m2a + f * 1024, f < 12 ? 12 * wave + f : 12 * (4 + (wave >> 1)) + f - 12, lane);
                    waitv<0>();
                }
            }
            MEMBAR;
        };
        static_for<14>([&](auto jc) LAMBDA_INL {
            constexpr int j = decltype(jc)::value;
            static_for<18>([&](auto nc) LAMBDA_INL {
                constexpr int n = decltype(nc)::value, s = n / 3, bi = n % 3;
                if constexpr (j == 0) {
                    if constexpr (n == 0) setx();
                    if constexpr (n == 15) {
                        qwP = (unsigned)(16 * (lane >> 2) + (cbP >> 1) * 256 + CQ); qwS = (unsigned)(16 * (lane >> 2) + (cbS >> 1) * 256 + 8 * (cbS & 1) + CQ);
                        pin(qwP); pin(qwS);
                    }
                }
                if constexpr (j == 1 && n == 16) { qwP = lq16 + (unsigned)((cbP >> 1) * 1024); qwS = lq16 + (unsigned)((cbS >> 1) * 1024 + 8 * (cbS & 1)); pin(qwP); pin(qwS); }
                if constexpr (j == 2 && n == 16) {
                    const int lf = min(lane, 23);
                    fgo = WS_FLAGS + (unsigned)((bid & 7) * 1024 + 96 * wave) + (unsigned)(lf * 8 + (lf >= 12 ? 96 * (3 + (wave >> 1) - wave) : 0)); pin(fgo);
                }
                if constexpr (j == 3 && n == 8) fv3 = fl_ld<F_XC + 1>(fb);
                if constexpr (j == 3 && n == 16) fs3 = rfl(fv3);
                if constexpr (j == 4 && n == 0) { fl_chk<F_XC + 1, ALL4>(fb, fs3); }
                if constexpr (j + 1 < 13 && n >= 1 && n < 7) ldx(IC(j + 1), IC(n >= 1 && n < 7 ? n - 1 : 0));
                if constexpr (j + 2 < 13 && n == 7) ldt1(IC(j + 2 < 13 ? j + 2 : 0));
                if constexpr (j + 2 < 13 && n == 12) setx();
                if constexpr (j >= 1) {
                    constexpr int pj = j - 1, grp = G1ORD(pj);
                    if constexpr (n >= 5 && n < 11) qv[pj][(n - 5) >> 1][(n - 5) & 1] = cvt2(accQ[pj & 1][(n - 5) >> 1][2 * ((n - 5) & 1)], accQ[pj & 1][(n - 5) >> 1][2 * ((n - 5) & 1) + 1]);
                    if constexpr (pj != 8 && pj != 10 && pj != 12) {
                        constexpr int need = QPREV(grp) >= 0 ? F_QP + QPREV(grp) : -1;
                        if constexpr (need >= 0 && n == 4) fv = fl_ld<(need >= 0 ? need : 0)>(fb);
                        if constexpr (need >= 0 && n == 9) fs = rfl(fv);
                        if constexpr (need >= 0 && n == 11) fl_chk<(need >= 0 ? need : 0), 1>(fb, fs);
                        if constexpr (n == 11) wrq(IC(pj), IC(0));
                        if constexpr (n == 13) wrq(IC(pj), IC(1));
                        if constexpr (n == 15) wrq(IC(pj), IC(2));
                        if constexpr (n == 17) fl_set<F_QF + grp>(fbw, one);
                    }
                }
                if constexpr (j == 9 || j == 11) {
                    constexpr int grp = G1ORD(j - 2);
                    if constexpr (n == 8) fv2 = fl_ld<F_QF + grp>(fb);
                    if constexpr (n == 11) fs2 = rfl(fv2);
                    if constexpr (n == 14) pullm(IC(grp));
                }
                if constexpr (j == 12 && n == 2) hlv = *(const u32x2*)(smem + FLG + 4 * F_HL);
                if constexpr (j == 12 && n == 12) { hl0 = rfl(hlv[0]); hl1 = rfl(hlv[1]); }
                if constexpr (j == 12 && n == 14) { att_tiles(at, L0, o); at.ztab = ti4 + (unsigned)(832 + 192 * wave + 64); at.zflag = fb + (unsigned)(12 * wave + 4); pin(at.ztab); pin(at.zflag); }
                SCHED;
                if constexpr (j < 13) mmf<s == 0>(accQ[j & 1][bi], m1[s][bi], xb[j & 1][s]);
                else if constexpr (n == 0) nops<8>();
                SCHED;
            });
        });
        __builtin_amdgcn_s_setprio(1);
        const __amdgpu_buffer_rsrc_t rout = __builtin_amdgcn_make_buffer_rsrc(out, 0, XBYTES, 0x20000);
        const unsigned oc = g16 + 64u * (unsigned)wave;
        const int osh = 64 * (4 + (wave >> 1) - wave);
        const unsigned odd3 = (wave & 1) ? 3u : 0u;
        const unsigned tiq[2] = {ti4 + 832 + 64 * (3 - odd3), ti4 + 832 + 64 * odd3};
        const unsigned fbq[2] = {fb + 4 * (3 - odd3), fb + 4 * odd3};
        u32x4 zb[2][6], ov;
        f32x4 accO[2][2], accH[2];
        unsigned t2e, po[3], z0, z1;
        auto ldt2 = [&](auto jc) LAMBDA_INL {
            constexpr int j = decltype(jc)::value;
            if constexpr (j == 13) t2e = *(const unsigned*)(smem + 832 + 64 * 12 + ti4);
            else t2e = *(const unsigned*)(smem + 64 * G2B(j) + tiq[j & 1]);
        };
        auto zfb = [&](auto jc) LAMBDA_INL -> unsigned { constexpr int j = decltype(jc)::value; if constexpr (j == 13) return fb; else return fbq[j & 1]; };
        auto setz = [&](auto jc) LAMBDA_INL {
            constexpr int j = decltype(jc)::value;
            z0 = ((t2e & 0x3fffu) << 4) ^ g16; z1 = z0 ^ 64u; po[j % 3] = __umul24(t2e >> 14, PIXB) + oc;
            pin(z0); pin(z1); pin(po[j % 3]);
        };
        auto ldz = [&](auto jc, auto sc) LAMBDA_INL {
            constexpr int j = decltype(jc)::value, s = decltype(sc)::value;
            zb[j & 1][s] = *(const u32x4*)(smem + 128 * (s >> 1) + ((s & 1) ? z1 : z0));
        };
        auto zout = [&](auto kc) LAMBDA_INL {
            constexpr int k = decltype(kc)::value;
            MEMBAR;
            *(u32x4*)(smem + 128 * (k % 3) + (k >= 3 ? at.zw ^ 64u : at.zw)) = zl[k];
            MEMBAR;
        };
        fs2 = 0;
        pullm(IC(7));
        fl_chk<F_HL, ALL4>(fb, hl0);
        if (wave == 3) fl_chk<F_HL + 1, 0x01010000>(fb, hl1);
        att_rounds<7, 1, false, true>(at, smem, g16, one, qm, accZ, zl, [&](auto sc) LAMBDA_INL {
            constexpr int S = decltype(sc)::value;
            if constexpr (S == 6) {
                if (wave == 3) { fl_wait<F_QP + 7, 1>(fb); wrq(IC(12), IC(0)); wrq(IC(12), IC(1)); wrq(IC(12), IC(2)); fl_set<F_QF + 10>(fbw, one); }
                fs2 = 0;
                pullm(IC(10));
                fv = fl_ld<F_QP + 7>(fb);
            }
            if constexpr (S == 9) fs = rfl(fv);
            if constexpr (S == 11) { if (wave != 3) { fl_chk<F_QP + 7, 1>(fb, fs); wrq(IC(12), IC(0)); wrq(IC(12), IC(1)); wrq(IC(12), IC(2)); fl_set<F_QF + 10>(fbw, one); } }
            if constexpr (S == 24) fv = fl_ld<F_QP + 10>(fb);
            if constexpr (S == 29) fs = rfl(fv);
            if constexpr (S == 31) { fl_chk<F_QP + 10, 1>(fb, fs); wrq(IC(8), IC(0)); }
            if constexpr (S == 33) wrq(IC(8), IC(1));
            if constexpr (S == 35) wrq(IC(8), IC(2));
            if constexpr (S == 37) fl_set<F_QF + 8>(fbw, one);
            if constexpr (S == 16) { mflag = __builtin_amdgcn_raw_buffer_load_b64(rws, fgo, 0, 16); }
            if constexpr (S == 42 + PD) {
                m2_ready();
                static_for<6>([&](auto ic) LAMBDA_INL { constexpr int i = decltype(ic)::value; m2[0][i] = __builtin_amdgcn_raw_buffer_load_b128(rws, m2a + 1024 * (i & 3), 4096 * (i >> 2), 16); });
            }
            if constexpr (S >= 50 && S < 86 && S % 2 == 0) {
                constexpr int i = (S - 50) / 2 + 6, h = i / 12, k = i % 12;
                m2[h][k] = __builtin_amdgcn_raw_buffer_load_b128(rws, (h ? m2b : m2a) + 1024 * (k & 3), 4096 * (k >> 2), 16);
            }
            if constexpr (S == 57) fv = fl_ld<F_QP + 8>(fb);
            if constexpr (S == 61) fs = rfl(fv);
            if constexpr (S == 64) { fl_chk<F_QP + 8, 1>(fb, fs); wrq(IC(10), IC(0)); }
            if constexpr (S == 65) wrq(IC(10), IC(1));
            if constexpr (S == 66) wrq(IC(10), IC(2));
            if constexpr (S == 67) fl_set<F_QF + 11>(fbw, one);
            if constexpr (S == 79) { ldt2(IC(1)); fv = fl_ld<F_ZF + G2B(1)>(fbq[1]); }
            if constexpr (S == 84) { fs = rfl(fv); setz(IC(1)); }
            if constexpr (S == 90) fl_chk<F_ZF + G2B(1), 1>(fbq[1], fs);
            if constexpr (S >= 90 && S < 96) ldz(IC(1), IC(S >= 90 && S < 96 ? S - 90 : 0));
            if constexpr (S == 95) { ldt2(IC(2)); fv = fl_ld<F_ZF + G2B(2)>(fbq[0]); }
            SCHED;
        });
        static_for<13>([&](auto jc0) LAMBDA_INL {
            constexpr int j = decltype(jc0)::value + 1;
            if constexpr (j == G2LOW) __builtin_amdgcn_s_setprio(0);
            constexpr bool sh = (j & 1) && j < 13, more = j < 13;
            constexpr int jn = more ? j + 1 : 1, jn2 = j + 2 <= 13 ? j + 2 : 1, NM = sh ? 24 : 12;
            constexpr int L = sh ? 4 : 0;
            static_for<NM>([&](auto nc) LAMBDA_INL {
                constexpr int n = decltype(nc)::value, k = n % 12;
                if constexpr ((j == 2 || j == 4) && n == 1) setz(IC(jn));
                if constexpr (j == 2 && n == 5) fv = fl_ld<F_ZF + G2B(3)>(zfb(IC(3)));
                if constexpr (j == 2 && n == 11) fs = rfl(fv);
                if constexpr (j == 4 && n == 3) fv2 = fl_ld<F_PF - 1>(at.zflag);
                if constexpr (j == 4 && n == 11) fs2 = rfl(fv2);
                if constexpr (more && j != 2 && j != 4) {
                    if constexpr (n == L) fs = rfl(fv);
                    if constexpr (n == L + 1) setz(IC(jn));
                    if constexpr (n == L + 2) fl_chk<F_ZF + G2B(jn), 1>(zfb(IC(jn)), fs);
                    if constexpr (n >= L + 3 && n < L + 9) ldz(IC(jn), IC(n >= L + 3 && n < L + 9 ? n - L - 3 : 0));
                    if constexpr (j + 2 <= 13 && n == (sh ? 18 : 11)) { ldt2(IC(jn2)); fv = fl_ld<F_ZF + G2B(jn2)>(zfb(IC(jn2))); }
                }
                if constexpr (j == 1 && n >= 7 && n < 13) {
                    constexpr int ch = (n >= 7 ? (n - 7) / 2 : 0), h = (n - 7) & 1;
                    zl[3 + ch][2 * h] = cvt2(accZ[2 + h][ch][0], accZ[2 + h][ch][1]); zl[3 + ch][2 * h + 1] = cvt2(accZ[2 + h][ch][2], accZ[2 + h][ch][3]);
                }
                if constexpr (j == 1 && (n == 13 || n == 16 || n == 21)) { if (wave < 2) { zout(IC(n == 13 ? 0 : n == 16 ? 2 : 4)); zout(IC(n == 13 ? 1 : n == 16 ? 3 : 5)); } }
                if constexpr (j == 2 && n == 0) { if (wave < 2) fl_set<F_ZF>(at.zflag, one); }
                if constexpr (j >= 2) {
                    constexpr bool psh = (j - 1) & 1;
                    if constexpr (n >= 6 && n < 10) {
                        if constexpr (psh) ov[n - 6] = cvt2(accH[(n - 6) >> 1][2 * (n & 1)], accH[(n - 6) >> 1][2 * (n & 1) + 1]);
                        else ov[n - 6] = cvt2(accO[0][(n - 6) >> 1][2 * (n & 1)], accO[0][(n - 6) >> 1][2 * (n & 1) + 1]);
                    }
                    if constexpr (n == 10) __builtin_amdgcn_raw_buffer_store_b128(ov, rout, po[(j - 1) % 3], psh ? osh : 0, 16);
                }
                if constexpr (sh) {
                    if constexpr (n >= 19 && n < 23) ov[n - 19] = cvt2(accO[1][(n - 19) >> 1][2 * ((n - 19) & 1)], accO[1][(n - 19) >> 1][2 * ((n - 19) & 1) + 1]);
                    if constexpr (n == 23) __builtin_amdgcn_raw_buffer_store_b128(ov, rout, po[j % 3], 0, 16);
                }
                SCHED;
                if constexpr (n < 12) mmf<(k < 2)>(accO[j & 1][k & 1], m2[0][k], zb[j & 1][k >> 1]);
                else mmf<(k < 2)>(accH[k & 1], m2[1][k], zb[j & 1][k >> 1]);
                SCHED;
            });
            if constexpr (j == 2 || j == 4) {
                if constexpr (j == 4) {
                    if (wave >= 2) {
                        fl_chk<F_PF - 1, 1>(at.zflag, fs2);
                        static_for<6>([&](auto kc) LAMBDA_INL { zout(kc); });
                        fl_set<F_ZF>(at.zflag, one);
                    }
                    fs = 0;
                }
                fl_chk<F_ZF + G2B(jn), 1>(zfb(IC(jn)), fs);
                static_for<6>([&](auto sc) LAMBDA_INL { ldz(IC(jn), sc); });
                ldt2(IC(jn2)); fv = fl_ld<F_ZF + G2B(jn2)>(zfb(IC(jn2)));
                SCHED;
            }
        });
        if (wave & 1) {
            static_for<12>([&](auto nc) LAMBDA_INL { constexpr int n = decltype(nc)::value; mmf<(n < 2)>(accH[n & 1], m2[1][n], zb[1][n >> 1]); SCHED; });
            __builtin_amdgcn_raw_buffer_store_b128(cvt8(accO[1][0], accO[1][1]), rout, po[13 % 3], 0, 16);
            nops<4>();
            __builtin_amdgcn_raw_buffer_store_b128(cvt8(accH[0], accH[1]), rout, po[13 % 3], osh, 16);
        } else {
            nops<8>();
            __builtin_amdgcn_raw_buffer_store_b128(cvt8(accO[1][0], accO[1][1]), rout, po[13 % 3], 0, 16);
        }
        finish();
    } else {
        const bool w7 = wave == 7;
        const u32x4 rsx = {(unsigned)(size_t)x, (unsigned)((size_t)x >> 32) & 0xffffu, (unsigned)XBYTES, 0x20000u};
        const int half = a & 1, rpar = a >> 1;
        Att atc;
        unsigned cp[4];
        auto setup_cp = [&](auto nc) LAMBDA_INL {
            constexpr int n = decltype(nc)::value;
            const int lam = (n < 3 ? 4 + 24 * half + 8 * n : half ? 0 : 52) + (lane >> 3);
            const int col = (lam * 43) >> 7, chn = lam - 3 * col;
            const int pc = pixc(q, tb, col);
            unsigned v = (unsigned)(__mul24(pc, PIXB) + chn * 128 + 16 * ((lane & 7) ^ h2f(col)));
            cp[n] = (unsigned)pc < 224u ? v : 0x40000000u;
            pin(cp[n]);
        };
        const int rs1 = p ? -2 * IMW * PIXB : 2 * IMW * PIXB;
        const int rs0 = (p ? 223 * IMW * PIXB : 0) + (14 * ta - 6) * rs1;
        int rsw0 = rs0 + (3 + rpar) * rs1;
        const int rfix0 = br ? 7 * (rs1 / 2) : 0, rfix1 = rpar ? 0 : rfix0;
        unsigned hxt = rpar ? HXT(4) : HXT(3);
        const unsigned lown = ldsbase + (unsigned)(512 + 3072 * half), lside = ldsbase + (half ? 0u : 6656u);
        const unsigned lownw = lown + (unsigned)((3 + rpar) * ROWB), lsidew = lside + (unsigned)((3 + rpar) * ROWB);
        auto dma_row = [&](auto kc, auto wc) LAMBDA_INL {
            constexpr int k = decltype(kc)::value, what = decltype(wc)::value;
            unsigned so = (unsigned)(rsw0 + 2 * k * rs1 + (k == 0 ? rfix0 : k == 1 ? rfix1 : 0)), hx = (k == 0 ? hxt << 4 : hxt >> (4 * k - 4)) & 0x70u;
            asm volatile("" : "+s"(so), "+s"(hx));
            auto thr = [&]() LAMBDA_INL { if constexpr (k >= 2) waitv<XQ>(); };
            if constexpr (what & 1) { thr(); dmax<15360 * k>(rsx, cp[0], hx, so, lownw); thr(); dmax<15360 * k + 1024>(rsx, cp[1], hx, so, lownw); thr(); dmax<15360 * k + 2048>(rsx, cp[2], hx, so, lownw); }
            if constexpr (what & 2) { waitv<XQ>(); dmax<15360 * k>(rsx, cp[3], hx, so, lsidew); }
            SCHED;
        };
        auto dma_halo = [&](auto rc) LAMBDA_INL {
            constexpr int r = decltype(rc)::value;
            const unsigned so = (r < 3 && br) ? 0x40000000u : (unsigned)(rs0 + r * rs1), hx = 16 * HRC(r);
            dmax<ROWB * r>(rsx, cp[0], hx, so, lown); dmax<ROWB * r + 1024>(rsx, cp[1], hx, so, lown); dmax<ROWB * r + 2048>(rsx, cp[2], hx, so, lown);
            dmax<ROWB * r>(rsx, cp[3], hx, so, lside);
            SCHED;
        };

        u64 tag = 0;
        u32x4 dlg, ack0, ack1;
        setup_cp(IC(0)); setup_cp(IC(1)); setup_cp(IC(2));
        dma_row(IC(0), IC(1));
        dma_row(IC(1), IC(1));
        setup_cp(IC(3)); set_fb();
        dma_row(IC(2), IC(1));
        att_addrS(at, lane, S00, L0, o);
        dma_row(IC(3), IC(1));
        att_addrP(at, lane, S00, L0, o);
        dma_row(IC(4), IC(1)); dma_row(IC(5), IC(1));
        waitv<6>();
        use(jkc);
        fl_set<F_XC>(fbw, one);
        dma_row(IC(6), IC(1));
        asm volatile("" : "+s"(rsw0), "+s"(hxt));
        static_for<7>([&](auto kc) LAMBDA_INL { dma_row(kc, IC(2)); });
        waitv<7>();
        fl_set<F_XC + 1>(fbw, one);
        u32x4 qf[2][6], qcf[1][6];
        const unsigned fba = fb + 12 * a;
        auto pull = [&](auto ic, unsigned slot) LAMBDA_INL {
            constexpr int i = decltype(ic)::value;
            fl_wait<F_QF + 2 * i, ALL4>(fba);
            const unsigned b = lq16 + slot;
#pragma unroll
            for (int m = 0; m < 6; ++m) qf[i][m] = *(const u32x4*)(smem + 1024 * m + b);
            fl_set<F_QP + 2 * i>(fba, one);
            SCHED;
        };
        if (a < 2) {
            dma_halo(IC(2)); dma_halo(IC(1)); dma_halo(IC(0));
            att_masks(at, g, clS ? 0 : Sb, clL ? max(Lq, 6) : Lq);
            if (a == 0) { att_addrS(atc, lane, 6, 6, false); att_addrP(atc, lane, 6, 6, false); att_masks(atc, g, Sb, Lq & 1); }
            waitv<0>();
            fl_set<F_HL>(fbw, one);
            pull(IC(0), a == 0 ? QSLOT(0) - SLOT_B : QSLOT(3) - SLOT_B);
            pull(IC(1), a == 0 ? QSLOT(2) - SLOT_B : QSLOT(5) - SLOT_B);
        } else {
            dma_halo(IC(17));
            u32x4 xr[8];
            static_for<8>([&](auto ic) LAMBDA_INL { constexpr int i = decltype(ic)::value, r = 18 + i / 4; xr[i] = xload(rsx, cp[i % 4], 16 * HRC(r), (unsigned)(rs0 + r * rs1)); });
            att_masks(at, g, clS ? 0 : Sb, clL ? max(Lq, 6) : Lq);
            waitv<0>();
            fl_set<F_HL>(fbw, one);
            if (w7) {
                pin(ep);
                const u64 tg = mix64(ep);
                tag = ((u64)(unsigned)__builtin_amdgcn_readfirstlane((unsigned)(tg >> 32)) << 32) | (unsigned)__builtin_amdgcn_readfirstlane((unsigned)tg);
                fl_wait<F_M1, ALL4>(fb);
                if (lane == 0) gstore8(ws, WS_ACKS + (unsigned)bid * 8u, tag);
                waitv<0>();
                dlg = gload16s<0>(ws, WS_DELEG);
                waitv<0>();
                pin(dlg);
            }
            const unsigned wo18 = lane16 + (lown - ldsbase) + 18 * ROWB, ws18 = lane16 + (lside - ldsbase) + 18 * ROWB;
            auto put = [&](auto ic) LAMBDA_INL {
                constexpr int i = decltype(ic)::value;
                pin(xr[i]);
                *(u32x4*)(smem + ROWB * (i / 4) + (i % 4 < 3 ? 1024 * (i % 4) : 0) + (i % 4 < 3 ? wo18 : ws18)) = xr[i];
                MEMBAR;
            };
            __builtin_amdgcn_s_setprio(3);
            if (a == 2) pull(IC(0), QSLOT(6) - SLOT_B); else fl_wait<F_QP + 6, 1>(fb);
            static_for<4>([&](auto ic) LAMBDA_INL { put(ic); });
            if (a == 3) pull(IC(0), QSLOT(9) - SLOT_B); else fl_wait<F_QP + 9, 1>(fb);
            static_for<4>([&](auto ic) LAMBDA_INL { put(IC(4 + decltype(ic)::value)); });
            fl_set<F_HL + 1>(fbw, one);
        }
        __builtin_amdgcn_s_setprio(1);
        att_tiles(at, L0, o);
        at.dlin = (unsigned)(2 * dS * (o ? PIXB : ROWB)); at.dsw = dS ? 16u : 0u;
        at.ztab = ti4 + (unsigned)(832 + 192 * a); at.zflag = fba;
        fl_wait<F_HL, ALL4>(fb);
        if (a >= 2) fl_wait<F_HL + 1, 0x01010000>(fb);
        att_rounds<7, 2, true, false>(at, smem, g16, one, qf, accZ, zl, [&](auto sc) LAMBDA_INL {
            constexpr int S = decltype(sc)::value;
            if constexpr (S == 49) { if (w7 && bid == 0 && lane == 0) gstore8(ws, WS_DELEG, tag); }
            if constexpr (S == 139) { if (a < 2) __builtin_amdgcn_s_setprio(3); }
            if constexpr (S == 63) { if (a == 2) pull(IC(1), SLOT_A - SLOT_B); }
            if constexpr (S == 87) { if (a == 3) pull(IC(1), SLOT_A - SLOT_B); }
        });
        if (a == 0) {
#pragma unroll
            for (int m = 0; m < 6; ++m) qcf[0][m] = *(const u32x4*)(smem + CQ + 256 * m + 16 * (lane >> 2));
            att_tiles(atc, 6, false);
            atc.ztab = ti4 + (unsigned)(832 + 64 * 12); atc.zflag = fb + 48;
            att_rounds<4, 1, false, false>(atc, smem, g16, one, qcf, accZ, zl, [&](auto) LAMBDA_INL {});
        }
        if (w7) {
            waitv<0>();
            pin(dlg);
            const unsigned tl = (unsigned)tag, th = (unsigned)(tag >> 32);
            if (bid == 0 || __builtin_amdgcn_readfirstlane(dlg[0] == tl && dlg[1] == th)) {
                ack0 = gload16s<0>(ws, WS_ACKS + lane16); ack1 = gload16s<1024>(ws, WS_ACKS + lane16);
                waitv<0>();
                pin(ack0); pin(ack1);
                const bool good = ack0[0] == tl && ack0[1] == th && ack0[2] == tl && ack0[3] == th &&
                                  ack1[0] == tl && ack1[1] == th && ack1[2] == tl && ack1[3] == th;
                if (__all(good) && lane == 0) gstore8(ws, WS_EPOCH, tag);
            }
        }
        finish();
    }
}

extern "C" void kernel_launch(void* const* d_in, const int* in_sizes, int n_in,
                              void* d_out, int out_size, void* d_ws, size_t ws_size,
                              hipStream_t stream) {
    const u16* x = (const u16*)d_in[0];
    const u16* wqkv = (const u16*)d_in[1];
    const u16* wo = (const u16*)d_in[2];
    na_main<<<256, 512, 0, stream>>>(x, wqkv, wo, (char*)d_ws, (u16*)d_out);
}
```

```cpp
#include <hip/hip_runtime.h>

__device__ const unsigned na_tab[3840] = {
    0x12500b4, 0x2050194, 0x16a, 0x0, 0xf110711, 0x9120112, 0x5131112, 0x10f010f, 0x15f00ee, 0x23f01ce, 0x16a, 0x0,
    0xf110711, 0x9120112, 0x5131112, 0x10f010f, 0x12900b8, 0x2090198, 0x16a, 0x0, 0x10110811, 0xa120212, 0x6131212, 0x20f020f,
    0x16300f2, 0x24301d2, 0x16a, 0x0, 0x10110811, 0xa120212, 0x6131212, 0x20f020f, 0x12c00bd, 0x20c019d, 0x16f, 0x0,
    0x11110911, 0xb120312, 0x7131312, 0x11101110, 0x16600f7, 0x24601d7, 0x16f, 0x0, 0x11110911, 0xb120312, 0x7131312, 0x11101110,
    0x13000c1, 0x21001a1, 0x16f, 0x0, 0x12110a11, 0xc120412, 0x8130012, 0x12101210, 0x16a00fb, 0x24a01db, 0x16f, 0x0,
    0x12110a11, 0xc120412, 0x8130012, 0x12101210, 0x13500c4, 0x21501a4, 0x1a1, 0x0, 0x13110b11, 0xd120512, 0x9130113, 0x13101310,
    0x16f00fe, 0x24f01de, 0x1a1, 0x0, 0x13110b11, 0xd120512, 0x9130113, 0x13101310, 0x13900c8, 0x21901a8, 0x1a1, 0x0,
    0x110c11, 0xe120612, 0xa130213, 0x100010, 0x1730102, 0x25301e2, 0x1a1, 0x0, 0x110c11, 0xe120612, 0xa130213, 0x100010,
    0x13c00cd, 0x21c01ad, 0x1a4, 0x0, 0x1110d11, 0xf120712, 0xb130313, 0x1100110, 0x1760107, 0x25601e7, 0x1a4, 0x0,
    0x1110d11, 0xf120712, 0xb130313, 0x1100110, 0x14000d1, 0x22001b1, 0x1a4, 0x0, 0x2110e11, 0x10120812, 0xc130413, 0x2100210,
    0x17a010b, 0x25a01eb, 0x1a4, 0x0, 0x2110e11, 0x10120812, 0xc130413, 0x2100210, 0x3c000, 0x5557d555, 0x3c000, 0x3c000,
    0x3c000, 0x3c000, 0xaaabeaaa, 0x3c000, 0x0, 0x0, 0x0, 0x0, 0x0, 0x0, 0x0, 0x0,
    0x0, 0x0, 0x0, 0x0, 0x0, 0x0, 0x0, 0x0, 0x0, 0x0, 0x0, 0x0,
    0x0, 0x0, 0x0, 0x0, 0x0, 0x0, 0x0, 0x0, 0x0, 0x0, 0x0, 0x0,
    0x0, 0x0, 0x0, 0x0, 0x0, 0x0, 0x0, 0x0, 0x0, 0x0, 0x0, 0x0,
    0x0, 0x0, 0x0, 0x0, 0x0, 0x0, 0x0, 0x0, 0x0, 0x0, 0x0, 0x0,
    0x12500b4, 0x2050194, 0x16a, 0x0, 0x1000d13, 0x11000900, 0xd010501, 0x10f010f, 0x15f00ee, 0x23f01ce, 0x16a, 0x0,
    0x1000d13, 0x11000900, 0xd010501, 0x10f010f, 0x12900b8, 0x2090198, 0x16a, 0x0, 0x2000e13, 0x12000a00, 0xe010601, 0x20f020f,
    0x16300f2, 0x24301d2, 0x16a, 0x0, 0x2000e13, 0x12000a00, 0xe010601, 0x20f020f, 0x12c00bd, 0x20c019d, 0x16f, 0x0,
    0x3000f13, 0x13000b00, 0xf010701, 0x11101110, 0x16600f7, 0x24601d7, 0x16f, 0x0, 0x3000f13, 0x13000b00, 0xf010701, 0x11101110,
    0x13000c1, 0x21001a1, 0x16f, 0x0, 0x4001013, 0xc00, 0x10010801, 0x12101210, 0x16a00fb, 0x24a01db, 0x16f, 0x0,
    0x4001013, 0xc00, 0x10010801, 0x12101210, 0x13500c4, 0x21501a4, 0x1a1, 0x0, 0x5001113, 0x1010d00, 0x11010901, 0x13101310,
    0x16f00fe, 0x24f01de, 0x1a1, 0x0, 0x5001113, 0x1010d00, 0x11010901, 0x13101310, 0x13900c8, 0x21901a8, 0x1a1, 0x0,
    0x6001213, 0x2010e00, 0x12010a01, 0x100010, 0x1730102, 0x25301e2, 0x1a1, 0x0, 0x6001213, 0x2010e00, 0x12010a01, 0x100010,
    0x13c00cd, 0x21c01ad, 0x1a4, 0x0, 0x7001313, 0x3010f00, 0x13010b01, 0x1100110, 0x1760107, 0x25601e7, 0x1a4, 0x0,
    0x7001313, 0x3010f00, 0x13010b01, 0x1100110, 0x14000d1, 0x22001b1, 0x1a4, 0x0, 0x8000013, 0x4011000, 0x10c01, 0x2100210,
    0x17a010b, 0x25a01eb, 0x1a4, 0x0, 0x8000013, 0x4011000, 0x10c01, 0x2100210, 0x3c000, 0x5557d555, 0x3c000, 0x3c000,
    0x3c000, 0x3c000, 0xaaabeaaa, 0x3c000, 0x0, 0x0, 0x0, 0x0, 0x0, 0x0, 0x0, 0x0,
    0x0, 0x0, 0x0, 0x0, 0x0, 0x0, 0x0, 0x0, 0x0, 0x0, 0x0, 0x0,
    0x0, 0x0, 0x0, 0x0, 0x0, 0x0, 0x0, 0x0, 0x0, 0x0, 0x0, 0x0,
    0x0, 0x0, 0x0, 0x0, 0x0, 0x0, 0x0, 0x0, 0x0, 0x0, 0x0, 0x0,
    0x0, 0x0, 0x0, 0x0, 0x0, 0x0, 0x0, 0x0, 0x0, 0x0, 0x0, 0x0,
    0x12500b4, 0x2050194, 0x16a, 0x0, 0x9020102, 0x1031102, 0x13061105, 0x10f010f, 0x15f00ee, 0x23f01ce, 0x16a, 0x0,
    0x9020102, 0x1031102, 0x13061105, 0x10f010f, 0x12900b8, 0x2090198, 0x16a, 0x0, 0xa020202, 0x2031202, 0x61205, 0x20f020f,
    0x16300f2, 0x24301d2, 0x16a, 0x0, 0xa020202, 0x2031202, 0x61205, 0x20f020f, 0x12c00bd, 0x20c019d, 0x16f, 0x0,
    0xb020302, 0x11041302, 0x1061305, 0x11101110, 0x16600f7, 0x24601d7, 0x16f, 0x0, 0xb020302, 0x11041302, 0x1061305, 0x11101110,
    0x13000c1, 0x21001a1, 0x16f, 0x0, 0xc020402, 0x12040002, 0x2060005, 0x12101210, 0x16a00fb, 0x24a01db, 0x16f, 0x0,
    0xc020402, 0x12040002, 0x2060005, 0x12101210, 0x13500c4, 0x21501a4, 0x1a1, 0x0, 0xd020502, 0x13041103, 0x11070105, 0x13101310,
    0x16f00fe, 0x24f01de, 0x1a1, 0x0, 0xd020502, 0x13041103, 0x11070105, 0x13101310, 0x13900c8, 0x21901a8, 0x1a1, 0x0,
    0xe020602, 0x41203, 0x12070205, 0x100010, 0x1730102, 0x25301e2, 0x1a1, 0x0, 0xe020602, 0x41203, 0x12070205, 0x100010,
    0x13c00cd, 0x21c01ad, 0x1a4, 0x0, 0xf020702, 0x1041303, 0x13071106, 0x1100110, 0x1760107, 0x25601e7, 0x1a4, 0x0,
    0xf020702, 0x1041303, 0x13071106, 0x1100110, 0x14000d1, 0x22001b1, 0x1a4, 0x0, 0x10020802, 0x2040003, 0x71206, 0x2100210,
    0x17a010b, 0x25a01eb, 0x1a4, 0x0, 0x10020802, 0x2040003, 0x71206, 0x2100210, 0x3c000, 0x5557d555, 0x3c000, 0x3c000,
    0x3c000, 0x3c000, 0xaaabeaaa, 0x3c000, 0x0, 0x0, 0x0, 0x0, 0x0, 0x0, 0x0, 0x0,
    0x0, 0x0, 0x0, 0x0, 0x0, 0x0, 0x0, 0x0, 0x0, 0x0, 0x0, 0x0,
    0x0, 0x0, 0x0, 0x0, 0x0, 0x0, 0x0, 0x0, 0x0, 0x0, 0x0, 0x0,
    0x0, 0x0, 0x0, 0x0, 0x0, 0x0, 0x0, 0x0, 0x0, 0x0, 0x0, 0x0,
    0x0, 0x0, 0x0, 0x0, 0x0, 0x0, 0x0, 0x0, 0x0, 0x0, 0x0, 0x0,
    0x12500b4, 0x2050194, 0x16a, 0x0, 0x11090107, 0x10b130a, 0x130e110d, 0x10f010f, 0x15f00ee, 0x23f01ce, 0x16a, 0x0,
    0x11090107, 0x10b130a, 0x130e110d, 0x10f010f, 0x12900b8, 0x2090198, 0x16a, 0x0, 0x12090207, 0x20b000a, 0xe120d, 0x20f020f,
    0x16300f2, 0x24301d2, 0x16a, 0x0, 0x12090207, 0x20b000a, 0xe120d, 0x20f020f, 0x12c00bd, 0x20c019d, 0x16f, 0x0,
    0x13091108, 0x110c010a, 0x10e130d, 0x11101110, 0x16600f7, 0x24601d7, 0x16f, 0x0, 0x13091108, 0x110c010a, 0x10e130d, 0x11101110,
    0x13000c1, 0x21001a1, 0x16f, 0x0, 0x91208, 0x120c020a, 0x20e000d, 0x12101210, 0x16a00fb, 0x24a01db, 0x16f, 0x0,
    0x91208, 0x120c020a, 0x20e000d, 0x12101210, 0x13500c4, 0x21501a4, 0x1a1, 0x0, 0x1091308, 0x130c110b, 0x110f010d, 0x13101310,
    0x16f00fe, 0x24f01de, 0x1a1, 0x0, 0x1091308, 0x130c110b, 0x110f010d, 0x13101310, 0x13900c8, 0x21901a8, 0x1a1, 0x0,
    0x2090008, 0xc120b, 0x120f020d, 0x100010, 0x1730102, 0x25301e2, 0x1a1, 0x0, 0x2090008, 0xc120b, 0x120f020d, 0x100010,
    0x13c00cd, 0x21c01ad, 0x1a4, 0x0, 0x110a0108, 0x10c130b, 0x130f110e, 0x1100110, 0x1760107, 0x25601e7, 0x1a4, 0x0,
    0x110a0108, 0x10c130b, 0x130f110e, 0x1100110, 0x14000d1, 0x22001b1, 0x1a4, 0x0, 0x120a0208, 0x20c000b, 0xf120e, 0x2100210,
    0x17a010b, 0x25a01eb, 0x1a4, 0x0, 0x120a0208, 0x20c000b, 0xf120e, 0x2100210, 0x3c000, 0x5557d555, 0x3c000, 0x3c000,
    0x3c000, 0x3c000, 0xaaabeaaa, 0x3c000, 0x0, 0x0, 0x0, 0x0, 0x0, 0x0, 0x0, 0x0,
    0x0, 0x0, 0x0, 0x0, 0x0, 0x0, 0x0, 0x0, 0x0, 0x0, 0x0, 0x0,
    0x0, 0x0, 0x0, 0x0, 0x0, 0x0, 0x0, 0x0, 0x0, 0x0, 0x0, 0x0,
    0x0, 0x0, 0x0, 0x0, 0x0, 0x0, 0x0, 0x0, 0x0, 0x0, 0x0, 0x0,
    0x0, 0x0, 0x0, 0x0, 0x0, 0x0, 0x0, 0x0, 0x0, 0x0, 0x0, 0x0,
    0xa590a8c, 0xaf80b2d, 0x1290bb4, 0x1980be5, 0x490b54, 0x20b5d, 0xe20073, 0x0, 0xaab0ade, 0x120b3f, 0x1630bce, 0x1d20bff,
    0x830b6e, 0x390b77, 0x11900a8, 0x0, 0xa5c0a40, 0xafd0ae1, 0x12c0bb8, 0x19d0be9, 0x4c0b58, 0x70050, 0xe70076, 0x0,
    0xaae0a92, 0x170b43, 0x1660bd2, 0x1d70c03, 0x860b72, 0x3c008a, 0x11c00ad, 0x0, 0xa600a45, 0xb010ae4, 0x1300bbd, 0x1a10bec,
    0xb80b85, 0xb0b8c, 0xeb007a, 0x0, 0xab20a97, 0x1b0b46, 0x16a0bd7, 0x1db0c06, 0xf20b9f, 0x400ba6, 0x12000b1, 0x0,
    0xa650a49, 0xb040ae8, 0x1350119, 0x1a40188, 0xbd0b89, 0xe00c1, 0xee007f, 0x0, 0xab70a9b, 0x1e0002, 0x16f0153, 0x1de01c2,
    0xf70ba3, 0x4500fb, 0x12500b4, 0x0, 0xa690a4c, 0xb080aed, 0x139011c, 0x1a8018d, 0x59003c, 0x120045, 0xf20083, 0x0,
    0xabb0a9e, 0x220007, 0x1730156, 0x1e201c7, 0x930076, 0x49007f, 0x12900b8, 0x0, 0xa6c0a50, 0xb0d0af1, 0x13c0120, 0x1ad0191,
    0x5c0040, 0x170060, 0xf70086, 0x0, 0xabe0aa2, 0x27000b, 0x176015a, 0x1e701cb, 0x96007a, 0x4c009a, 0x12c00bd, 0x0,
    0xa700a55, 0xb110af4, 0x1400125, 0x1b10194, 0xc800ad, 0x1b00b4, 0xfb008a, 0x0, 0xac20aa7, 0x2b000e, 0x17a015f, 0x1eb01ce,
    0x10200e7, 0x5000ee, 0x13000c1, 0x0, 0x550039, 0xc400a8, 0x550039, 0xc400a8, 0xcd00b1, 0x1e00d1, 0xfe008f, 0x0,
    0x8f0073, 0xfe00e2, 0x8f0073, 0xfe00e2, 0x10700eb, 0x55010b, 0x13500c4, 0x0, 0x3f0003, 0x3fff03ff, 0xff000f, 0xff0fff,
    0x30003, 0x3ff03ff, 0xf000f, 0xfff0fff, 0x3f003f, 0x3fff3fff, 0xaaaaaaaa, 0xaaaaaaaa, 0xaaaaaaaa, 0xaaaa, 0x0, 0x55550000,
    0x55550000, 0x55550000, 0xaaaaaaaa, 0xaaaaaaaa, 0xaaaaaaaa, 0xaaaa, 0x55555555, 0x55555555, 0x55555555, 0x5555, 0x0, 0x0,
    0x0, 0xaaaa0000, 0x0, 0x0, 0x55555555, 0x55555555, 0x55555555, 0x5555, 0xaaaa, 0xaaaa, 0x0, 0x0,
    0x0, 0x0, 0x0, 0x0, 0x0, 0x0, 0x0, 0x0, 0x0, 0x0, 0x0, 0x0,
    0x0, 0x0, 0x0, 0x0, 0x0, 0x0, 0x0, 0x0, 0x0, 0x0, 0x0, 0x0,
    0xf70a5c, 0xfe0a65, 0x1170a7c, 0xb960a85, 0x1070a6c, 0x220b0d, 0x32002b, 0x0, 0xfb0a60, 0x1020a69, 0xb930a80, 0xb9a0a89,
    0x10b0a70, 0x270b11, 0x37002e, 0x0, 0x12c0aae, 0x1350ab7, 0x14c0ace, 0xbad0ad7, 0x13c0abe, 0x590176, 0x690060, 0x0,
    0x1300ab2, 0x1390abb, 0xba80ad2, 0xbb10adb, 0x1400ac2, 0x5c017a, 0x6c0065, 0x0, 0x1660afd, 0x16f0b04, 0x1860b1d, 0xbc70b24,
    0x10e0a75, 0x930b14, 0xa3009a, 0x0, 0x16a0b01, 0x1730b08, 0xbc20b21, 0xbcb0b28, 0x1120a79, 0x960b18, 0xa6009f, 0x0,
    0x19d0017, 0x1a4001e, 0x1bd0037, 0xbdc0b36, 0x1450ac7, 0xc8017f, 0xd800d1, 0x0, 0x1a1001b, 0x1a80022, 0xbd90b33, 0xbe00b3a,
    0x1490acb, 0xcd0183, 0xdd00d4, 0x0, 0x1d7004c, 0x1de0055, 0x1f7006c, 0xbf60b4d, 0x1e7005c, 0x10200cd, 0x112010b, 0x0,
    0x1db0050, 0x1e20059, 0xbf30b48, 0xbfa0b51, 0x1eb0060, 0x10700d1, 0x117010e, 0x0, 0x20c0086, 0x215008f, 0x22c00a6, 0xc0d0b67,
    0x21c0096, 0x1390256, 0x1490140, 0x0, 0x210008a, 0x2190093, 0xc080b62, 0xc110b6b, 0x220009a, 0x13c025a, 0x14c0145, 0x0,
    0x24600bd, 0x24f00c4, 0x26600dd, 0xc270b7c, 0x1ee0065, 0x17300d4, 0x183017a, 0x0, 0x24a00c1, 0x25300c8, 0xc220b79, 0xc2b0b80,
    0x1f20069, 0x17600d8, 0x186017f, 0x0, 0x1ad0027, 0x1b4002e, 0x1ad0027, 0x1b4002e, 0x225009f, 0x1a8025f, 0x1b801b1, 0x0,
    0x1b1002b, 0x1b80032, 0x1b1002b, 0x1b80032, 0x22900a3, 0x1ad0263, 0x1bd01b4, 0x0, 0x3f0003, 0x3fff03ff, 0xff000f, 0xff0fff,
    0x30003, 0x3ff03ff, 0xf000f, 0xfff0fff, 0x3f003f, 0x3fff3fff, 0xaaaaaaaa, 0xaaaaaaaa, 0xaaaaaaaa, 0xaaaa, 0x0, 0x55550000,
    0x55550000, 0x55550000, 0xaaaaaaaa, 0xaaaaaaaa, 0xaaaaaaaa, 0xaaaa, 0x55555555, 0x55555555, 0x55555555, 0x5555, 0x0, 0x0,
    0x0, 0xaaaa0000, 0x0, 0x0, 0x55555555, 0x55555555, 0x55555555, 0x5555, 0xaaaa, 0xaaaa, 0x0, 0x0,
    0x0, 0x0, 0x0, 0x0, 0x0, 0x0, 0x0, 0x0, 0x0, 0x0, 0x0, 0x0,
    0x0, 0x0, 0x0, 0x0, 0x0, 0x0, 0x0, 0x0, 0x0, 0x0, 0x0, 0x0,
    0x1400125, 0x1b10194, 0x30002e5, 0x9d109b4, 0x2200205, 0x1db020c, 0x2bb024a, 0x0, 0x17a015f, 0x1eb01ce, 0x97a031f, 0xa230a06,
    0x25a023f, 0x2100246, 0x2f00281, 0x0, 0x1450129, 0x1b40198, 0x30502e9, 0x9d409b8, 0x2250209, 0x1de0229, 0x2be024f, 0x0,
    0x17f0163, 0x1ee01d2, 0x97f0963, 0xa260a0a, 0x25f0243, 0x2150263, 0x2f50284, 0x0, 0x149012c, 0x1b8019d, 0x30902ec, 0x9d809bd,
    0x2910274, 0x1e2027d, 0x2c20253, 0x0, 0x1830166, 0x1f201d7, 0x9830966, 0xa2a0a0f, 0x2cb02ae, 0x21902b7, 0x2f90288, 0x0,
    0x14c0130, 0x1bd01a1, 0x30c02f0, 0x9dd09c1, 0x2940278, 0x1e70298, 0x2c70256, 0x0, 0x186016a, 0x1f701db, 0x986096a, 0xa2f0a13,
    0x2ce02b2, 0x21c02d2, 0x2fc028d, 0x0, 0xba80135, 0xbd901a4, 0xc6802f5, 0x9e109c4, 0xc080215, 0x1eb021c, 0x2cb025a, 0x0,
    0xbc2016f, 0xbf301de, 0x98a096f, 0xa330a16, 0xc22024f, 0x2200256, 0x3000291, 0x0, 0xbad0139, 0xbdc01a8, 0xc6d02f9, 0x9e409c8,
    0xc0d0219, 0x1ee0c11, 0x2ce025f, 0x0, 0xbc70173, 0xbf601e2, 0x98f0973, 0xa360a1a, 0xc270253, 0x2250c2b, 0x3050294, 0x0,
    0xbb1013c, 0xbe001ad, 0xc7102fc, 0x9e809cd, 0xc390284, 0x1f2028d, 0x2d20263, 0x0, 0xbcb0176, 0xbfa01e7, 0x9930976, 0xa3a0a1f,
    0xc5302be, 0x22902c7, 0x3090298, 0x0, 0x22c0210, 0x29d0281, 0x22c0210, 0x29d0281, 0xc3c0288, 0x1f70c40, 0x2d70266, 0x0,
    0x266024a, 0x2d702bb, 0x266024a, 0x2d702bb, 0xc5602c2, 0x22c0c5a, 0x30c029d, 0x0, 0x3f0003, 0x3fff03ff, 0xff000f, 0xff0fff,
    0x30003, 0x3ff03ff, 0xf000f, 0xfff0fff, 0x3f003f, 0x3fff3fff, 0xaaaaaaaa, 0xaaaaaaaa, 0xaaaaaaaa, 0xaaaa, 0x0, 0x55550000,
    0x55550000, 0x55550000, 0xaaaaaaaa, 0xaaaaaaaa, 0xaaaaaaaa, 0xaaaa, 0x55555555, 0x55555555, 0x55555555, 0x5555, 0x0, 0x0,
    0x0, 0xaaaa0000, 0x0, 0x0, 0x55555555, 0x55555555, 0x55555555, 0x5555, 0xaaaa, 0xaaaa, 0x0, 0x0,
    0x0, 0x0, 0x0, 0x0, 0x0, 0x0, 0x0, 0x0, 0x0, 0x0, 0x0, 0x0,
    0x0, 0x0, 0x0, 0x0, 0x0, 0x0, 0x0, 0x0, 0x0, 0x0, 0x0, 0x0,
    0xc2e0b85, 0xc370b8c, 0x24600bd, 0x24f00c4, 0x23600ad, 0x153011c, 0x163015a, 0x0, 0xc320b89, 0x23300a8, 0x24a00c1, 0x25300c8,
    0x23a00b1, 0x1560120, 0x166015f, 0x0, 0xc450b9f, 0xc4c0ba6, 0x27d00f7, 0x28400fe, 0x26d00e7, 0x18802a7, 0x1980191, 0x0,
    0xc490ba3, 0x26800e2, 0x28100fb, 0x2880102, 0x27100eb, 0x18d02ab, 0x19d0194, 0x0, 0xc5f0bb4, 0xc660bbd, 0x2b7012c, 0x2be0135,
    0x23f00b4, 0x1c20125, 0x1d201cb, 0x0, 0xc630bb8, 0x2a20119, 0x2bb0130, 0x2c20139, 0x24300b8, 0x1c70129, 0x1d701ce, 0x0,
    0xc740bce, 0xc7d0bd7, 0x2ec0166, 0x2f5016f, 0x27400ee, 0x1f902ae, 0x2090200, 0x0, 0xc780bd2, 0x2d90153, 0x2f0016a, 0x2f90173,
    0x27800f2, 0x1fc02b2, 0x20c0205, 0x0, 0x9960be5, 0x99f0bec, 0x966019d, 0x96f01a4, 0x316018d, 0x23301fc, 0x243023a, 0x0,
    0x99a0be9, 0x3130188, 0x96a01a1, 0x97301a8, 0x31a0191, 0x2360200, 0x246023f, 0x0, 0x9ed0bff, 0x9a40c06, 0x9bd01d7, 0x9c401de,
    0x9ad01c7, 0x26809ff, 0x2780271, 0x0, 0x9a10c03, 0x9a801c2, 0x9c101db, 0x9c801e2, 0x9b101cb, 0x26d0a03, 0x27d0274, 0x0,
    0xa3f0c14, 0x9f60c1d, 0xa0f020c, 0xa160215, 0x31f0194, 0x2a20205, 0x2b202ab, 0x0, 0x9f30c18, 0x9fa01f9, 0xa130210, 0xa1a0219,
    0x9630198, 0x2a70209, 0x2b702ae, 0x0, 0x2dc0156, 0x2e5015f, 0x2dc0156, 0x2e5015f, 0x9b401ce, 0x2d90a06, 0x2e902e0, 0x0,
    0x2e0015a, 0x2e90163, 0x2e0015a, 0x2e90163, 0x9b801d2, 0x2dc0a0a, 0x2ec02e5, 0x0, 0x3f0003, 0x3fff03ff, 0xff000f, 0xff0fff,
    0x30003, 0x3ff03ff, 0xf000f, 0xfff0fff, 0x3f003f, 0x3fff3fff, 0xaaaaaaaa, 0xaaaaaaaa, 0xaaaaaaaa, 0xaaaa, 0x0, 0x55550000,
    0x55550000, 0x55550000, 0xaaaaaaaa, 0xaaaaaaaa, 0xaaaaaaaa, 0xaaaa, 0x55555555, 0x55555555, 0x55555555, 0x5555, 0x0, 0x0,
    0x0, 0xaaaa0000, 0x0, 0x0, 0x55555555, 0x55555555, 0x55555555, 0x5555, 0xaaaa, 0xaaaa, 0x0, 0x0,
    0x0, 0x0, 0x0, 0x0, 0x0, 0x0, 0x0, 0x0, 0x0, 0x0, 0x0, 0x0,
    0x0, 0x0, 0x0, 0x0, 0x0, 0x0, 0x0, 0x0, 0x0, 0x0, 0x0, 0x0,
    0xa590a8c, 0xaf80b2d, 0x1290bb4, 0x1980be5, 0x490b54, 0xe0b5d, 0xee007f, 0x0, 0xaab0ade, 0x120b3f, 0x1630bce, 0x1d20bff,
    0x830b6e, 0x450b77, 0x12500b4, 0x0, 0xa5c0a40, 0xafd0ae1, 0x12c0bb8, 0x19d0be9, 0x4c0b58, 0x20050, 0xe20073, 0x0,
    0xaae0a92, 0x170b43, 0x1660bd2, 0x1d70c03, 0x860b72, 0x39008a, 0x11900a8, 0x0, 0xa600a45, 0xb010ae4, 0x1300bbd, 0x1a10bec,
    0xb80b85, 0x120b8c, 0xf20083, 0x0, 0xab20a97, 0x1b0b46, 0x16a0bd7, 0x1db0c06, 0xf20b9f, 0x490ba6, 0x12900b8, 0x0,
    0xa650a49, 0xb040ae8, 0x1350119, 0x1a40188, 0xbd0b89, 0x700c1, 0xe70076, 0x0, 0xab70a9b, 0x1e0002, 0x16f0153, 0x1de01c2,
    0xf70ba3, 0x3c00fb, 0x11c00ad, 0x0, 0xa690a4c, 0xb080aed, 0x139011c, 0x1a8018d, 0x59003c, 0x170045, 0xf70086, 0x0,
    0xabb0a9e, 0x220007, 0x1730156, 0x1e201c7, 0x930076, 0x4c007f, 0x12c00bd, 0x0, 0xa6c0a50, 0xb0d0af1, 0x13c0120, 0x1ad0191,
    0x5c0040, 0xb0060, 0xeb007a, 0x0, 0xabe0aa2, 0x27000b, 0x176015a, 0x1e701cb, 0x96007a, 0x40009a, 0x12000b1, 0x0,
    0xa700a55, 0xb110af4, 0x1400125, 0x1b10194, 0xc800ad, 0x1b00b4, 0xfb008a, 0x0, 0xac20aa7, 0x2b000e, 0x17a015f, 0x1eb01ce,
    0x10200e7, 0x5000ee, 0x13000c1, 0x0, 0x550039, 0xc400a8, 0x550039, 0xc400a8, 0xcd00b1, 0x1e00d1, 0xfe008f, 0x0,
    0x8f0073, 0xfe00e2, 0x8f0073, 0xfe00e2, 0x10700eb, 0x55010b, 0x13500c4, 0x0, 0x0, 0x3fff0000, 0x0, 0x0,
    0x0, 0x0, 0x0, 0x0, 0x0, 0x3fff3fff, 0xaaaaaaaa, 0xaaaaaaaa, 0xaaaaaaaa, 0xaaaa, 0x0, 0x55550000,
    0x55550000, 0x55550000, 0xaaaaaaaa, 0xaaaaaaaa, 0xaaaaaaaa, 0xaaaa, 0x55555555, 0x55555555, 0x55555555, 0x5555, 0x0, 0x0,
    0x0, 0xaaaa0000, 0x0, 0x0, 0x55555555, 0x55555555, 0x55555555, 0x5555, 0xaaaa, 0xaaaa, 0x0, 0x0,
    0x0, 0x0, 0x0, 0x0, 0x0, 0x0, 0x0, 0x0, 0x0, 0x0, 0x0, 0x0,
    0x0, 0x0, 0x0, 0x0, 0x0, 0x0, 0x0, 0x0, 0x0, 0x0, 0x0, 0x0,
    0xf70a5c, 0xfe0a65, 0x1170a7c, 0xb960a85, 0x1070a6c, 0x220b0d, 0x32002b, 0x0, 0xfb0a60, 0x1020a69, 0xb930a80, 0xb9a0a89,
    0x10b0a70, 0x270b11, 0x37002e, 0x0, 0x12c0aae, 0x1350ab7, 0x14c0ace, 0xbad0ad7, 0x13c0abe, 0x590176, 0x690060, 0x0,
    0x1300ab2, 0x1390abb, 0xba80ad2, 0xbb10adb, 0x1400ac2, 0x5c017a, 0x6c0065, 0x0, 0x1660afd, 0x16f0b04, 0x1860b1d, 0xbc70b24,
    0x10e0a75, 0x930b14, 0xa3009a, 0x0, 0x16a0b01, 0x1730b08, 0xbc20b21, 0xbcb0b28, 0x1120a79, 0x960b18, 0xa6009f, 0x0,
    0x19d0017, 0x1a4001e, 0x1bd0037, 0xbdc0b36, 0x1450ac7, 0xc8017f, 0xd800d1, 0x0, 0x1a1001b, 0x1a80022, 0xbd90b33, 0xbe00b3a,
    0x1490acb, 0xcd0183, 0xdd00d4, 0x0, 0x1d7004c, 0x1de0055, 0x1f7006c, 0xbf60b4d, 0x1e7005c, 0x10200cd, 0x112010b, 0x0,
    0x1db0050, 0x1e20059, 0xbf30b48, 0xbfa0b51, 0x1eb0060, 0x10700d1, 0x117010e, 0x0, 0x20c0086, 0x215008f, 0x22c00a6, 0xc0d0b67,
    0x21c0096, 0x1390256, 0x1490140, 0x0, 0x210008a, 0x2190093, 0xc080b62, 0xc110b6b, 0x220009a, 0x13c025a, 0x14c0145, 0x0,
    0x24600bd, 0x24f00c4, 0x26600dd, 0xc270b7c, 0x1ee0065, 0x17300d4, 0x183017a, 0x0, 0x24a00c1, 0x25300c8, 0xc220b79, 0xc2b0b80,
    0x1f20069, 0x17600d8, 0x186017f, 0x0, 0x1ad0027, 0x1b4002e, 0x1ad0027, 0x1b4002e, 0x225009f, 0x1a8025f, 0x1b801b1, 0x0,
    0x1b1002b, 0x1b80032, 0x1b1002b, 0x1b80032, 0x22900a3, 0x1ad0263, 0x1bd01b4, 0x0, 0x3f0003, 0x3fff03ff, 0xff000f, 0xff0fff,
    0x30003, 0x3ff03ff, 0xf000f, 0xfff0fff, 0x3f003f, 0x3fff3fff, 0xaaaaaaaa, 0xaaaaaaaa, 0xaaaaaaaa, 0xaaaa, 0x0, 0x55550000,
    0x55550000, 0x55550000, 0xaaaaaaaa, 0xaaaaaaaa, 0xaaaaaaaa, 0xaaaa, 0x55555555, 0x55555555, 0x55555555, 0x5555, 0x0, 0x0,
    0x0, 0xaaaa0000, 0x0, 0x0, 0x55555555, 0x55555555, 0x55555555, 0x5555, 0xaaaa, 0xaaaa, 0x0, 0x0,
    0x0, 0x0, 0x0, 0x0, 0x0, 0x0, 0x0, 0x0, 0x0, 0x0, 0x0, 0x0,
    0x0, 0x0, 0x0, 0x0, 0x0, 0x0, 0x0, 0x0, 0x0, 0x0, 0x0, 0x0,
    0x1400125, 0x1b10194, 0x30002e5, 0x9d109b4, 0x2200205, 0x1db020c, 0x2bb024a, 0x0, 0x17a015f, 0x1eb01ce, 0x97a031f, 0xa230a06,
    0x25a023f, 0x2100246, 0x2f00281, 0x0, 0x1450129, 0x1b40198, 0x30502e9, 0x9d409b8, 0x2250209, 0x1de0229, 0x2be024f, 0x0,
    0x17f0163, 0x1ee01d2, 0x97f0963, 0xa260a0a, 0x25f0243, 0x2150263, 0x2f50284, 0x0, 0x149012c, 0x1b8019d, 0x30902ec, 0x9d809bd,
    0x2910274, 0x1e2027d, 0x2c20253, 0x0, 0x1830166, 0x1f201d7, 0x9830966, 0xa2a0a0f, 0x2cb02ae, 0x21902b7, 0x2f90288, 0x0,
    0x14c0130, 0x1bd01a1, 0x30c02f0, 0x9dd09c1, 0x2940278, 0x1e70298, 0x2c70256, 0x0, 0x186016a, 0x1f701db, 0x986096a, 0xa2f0a13,
    0x2ce02b2, 0x21c02d2, 0x2fc028d, 0x0, 0xba80135, 0xbd901a4, 0xc6802f5, 0x9e109c4, 0xc080215, 0x1eb021c, 0x2cb025a, 0x0,
    0xbc2016f, 0xbf301de, 0x98a096f, 0xa330a16, 0xc22024f, 0x2200256, 0x3000291, 0x0, 0xbad0139, 0xbdc01a8, 0xc6d02f9, 0x9e409c8,
    0xc0d0219, 0x1ee0c11, 0x2ce025f, 0x0, 0xbc70173, 0xbf601e2, 0x98f0973, 0xa360a1a, 0xc270253, 0x2250c2b, 0x3050294, 0x0,
    0xbb1013c, 0xbe001ad, 0xc7102fc, 0x9e809cd, 0xc390284, 0x1f2028d, 0x2d20263, 0x0, 0xbcb0176, 0xbfa01e7, 0x9930976, 0xa3a0a1f,
    0xc5302be, 0x22902c7, 0x3090298, 0x0, 0x22c0210, 0x29d0281, 0x22c0210, 0x29d0281, 0xc3c0288, 0x1f70c40, 0x2d70266, 0x0,
    0x266024a, 0x2d702bb, 0x266024a, 0x2d702bb, 0xc5602c2, 0x22c0c5a, 0x30c029d, 0x0, 0x3f0003, 0x3fff03ff, 0xff000f, 0xff0fff,
    0x30003, 0x3ff03ff, 0xf000f, 0xfff0fff, 0x3f003f, 0x3fff3fff, 0xaaaaaaaa, 0xaaaaaaaa, 0xaaaaaaaa, 0xaaaa, 0x0, 0x55550000,
    0x55550000, 0x55550000, 0xaaaaaaaa, 0xaaaaaaaa, 0xaaaaaaaa, 0xaaaa, 0x55555555, 0x55555555, 0x55555555, 0x5555, 0x0, 0x0,
    0x0, 0xaaaa0000, 0x0, 0x0, 0x55555555, 0x55555555, 0x55555555, 0x5555, 0xaaaa, 0xaaaa, 0x0, 0x0,
    0x0, 0x0, 0x0, 0x0, 0x0, 0x0, 0x0, 0x0, 0x0, 0x0, 0x0, 0x0,
    0x0, 0x0, 0x0, 0x0, 0x0, 0x0, 0x0, 0x0, 0x0, 0x0, 0x0, 0x0,
    0x25a00d1, 0x23f00b4, 0x25600cd, 0x23f00b4, 0x24600bd, 0x15f012c, 0x1660163, 0x0, 0x25600cd, 0x24300b8, 0x25a00d1, 0x24300b8,
    0x24a00c1, 0x1530130, 0x15a0156, 0x0, 0x291010b, 0x27400ee, 0x28d0107, 0x27400ee, 0x27d00f7, 0x19402b7, 0x19d0198, 0x0,
    0x28d0107, 0x27800f2, 0x291010b, 0x27800f2, 0x28100fb, 0x18802bb, 0x191018d, 0x0, 0x2cb0140, 0x2ae0125, 0x2c7013c, 0x2ae0125,
    0x24f00c4, 0x1ce0135, 0x1d701d2, 0x0, 0x2c7013c, 0x2b20129, 0x2cb0140, 0x2b20129, 0x25300c8, 0x1c20139, 0x1cb01c7, 0x0,
    0x300017a, 0x2e5015f, 0x2fc0176, 0x2e5015f, 0x28400fe, 0x20502be, 0x20c0209, 0x0, 0x2fc0176, 0x2e90163, 0x300017a, 0x2e90163,
    0x2880102, 0x1f902c2, 0x20001fc, 0x0, 0x97a01b1, 0x31f0194, 0x97601ad, 0x31f0194, 0x966019d, 0x23f020c, 0x2460243, 0x0,
    0x97601ad, 0x9630198, 0x97a01b1, 0x9630198, 0x96a01a1, 0x2330210, 0x23a0236, 0x0, 0x9d101eb, 0x9b401ce, 0x9cd01e7, 0x9b401ce,
    0x9bd01d7, 0x2740a0f, 0x27d0278, 0x0, 0x9cd01e7, 0x9b801d2, 0x9d101eb, 0x9b801d2, 0x9c101db, 0x2680a13, 0x271026d, 0x0,
    0xa230220, 0xa060205, 0xa1f021c, 0xa060205, 0x96f01a4, 0x2ae0215, 0x2b702b2, 0x0, 0xa1f021c, 0xa0a0209, 0xa230220, 0xa0a0209,
    0x97301a8, 0x2a20219, 0x2ab02a7, 0x0, 0x2ec0166, 0x2f5016f, 0x2ec0166, 0x2f5016f, 0x9c401de, 0x2e50a16, 0x2ec02e9, 0x0,
    0x2f0016a, 0x2f90173, 0x2f0016a, 0x2f90173, 0x9c801e2, 0x2d90a1a, 0x2e002dc, 0x0, 0x3f0003, 0x3fff03ff, 0xff000f, 0xff0fff,
    0x30003, 0x3ff03ff, 0xf000f, 0xfff0fff, 0x3f003f, 0x3fff3fff, 0xffffffff, 0xffffffff, 0xffffffff, 0xffff, 0x0, 0x0,
    0x0, 0x0, 0x0, 0x0, 0x0, 0x0, 0xffffffff, 0xffffffff, 0xffffffff, 0xffff, 0xffffffff, 0xffffffff,
    0x0, 0x0, 0xffffffff, 0xffff, 0x0, 0x0, 0x0, 0x0, 0x0, 0x0, 0x0, 0x0,
    0x0, 0x0, 0x0, 0x0, 0x0, 0x0, 0x0, 0x0, 0x0, 0x0, 0x0, 0x0,
    0x0, 0x0, 0x0, 0x0, 0x0, 0x0, 0x0, 0x0, 0x0, 0x0, 0x0, 0x0,
    0x2430c2e, 0xb80b85, 0x2090c14, 0xb80b85, 0x1290bb4, 0xa80bbd, 0x11900e2, 0x0, 0x2090c14, 0xf20b9f, 0x2430c2e, 0xf20b9f,
    0x1630bce, 0x20bd7, 0x730039, 0x0, 0x2460c32, 0xbd0b89, 0x20c0c18, 0xbd0b89, 0x12c0bb8, 0xad0130, 0x11c00e7, 0x0,
    0x20c0c18, 0xf70ba3, 0x2460c32, 0xf70ba3, 0x1660bd2, 0x7016a, 0x76003c, 0x0, 0x24a0c37, 0xc10b8c, 0x2100c1d, 0xc10b8c,
    0x1980be5, 0xb10bec, 0x12000eb, 0x0, 0x2100c1d, 0xfb0ba6, 0x24a0c37, 0xfb0ba6, 0x1d20bff, 0xb0c06, 0x7a0040, 0x0,
    0x24f0233, 0xc400a8, 0x21501f9, 0xc400a8, 0x19d0be9, 0xb401a1, 0x12500ee, 0x0, 0x21501f9, 0xfe00e2, 0x24f0233, 0xfe00e2,
    0x1d70c03, 0xe01db, 0x7f0045, 0x0, 0x2530236, 0xc800ad, 0x21901fc, 0xc800ad, 0x139011c, 0xb80125, 0x12900f2, 0x0,
    0x21901fc, 0x10200e7, 0x2530236, 0x10200e7, 0x1730156, 0x12015f, 0x830049, 0x0, 0x256023a, 0xcd00b1, 0x21c0200, 0xcd00b1,
    0x13c0120, 0xbd0140, 0x12c00f7, 0x0, 0x21c0200, 0x10700eb, 0x256023a, 0x10700eb, 0x176015a, 0x17017a, 0x86004c, 0x0,
    0x25a023f, 0xd100b4, 0x2200205, 0xd100b4, 0x1a8018d, 0xc10194, 0x13000fb, 0x0, 0x2200205, 0x10b00ee, 0x25a023f, 0x10b00ee,
    0x1e201c7, 0x1b01ce, 0x8a0050, 0x0, 0x1350119, 0x1a40188, 0x1350119, 0x1a40188, 0x1ad0191, 0xc401b1, 0x13500fe, 0x0,
    0x16f0153, 0x1de01c2, 0x16f0153, 0x1de01c2, 0x1e701cb, 0x1e01eb, 0x8f0055, 0x0, 0x3f0003, 0x3fff03ff, 0xff000f, 0xff0fff,
    0x30003, 0x3ff03ff, 0xf000f, 0xfff0fff, 0x3f003f, 0x3fff3fff, 0xffffffff, 0xffffffff, 0xffffffff, 0xffff, 0x0, 0x0,
    0x0, 0x0, 0x0, 0x0, 0x0, 0x0, 0xffffffff, 0xffffffff, 0xffffffff, 0xffff, 0xffffffff, 0xffffffff,
    0x0, 0x0, 0xffffffff, 0xffff, 0x0, 0x0, 0x0, 0x0, 0x0, 0x0, 0x0, 0x0,
    0x0, 0x0, 0x0, 0x0, 0x0, 0x0, 0x0, 0x0, 0x0, 0x0, 0x0, 0x0,
    0x0, 0x0, 0x0, 0x0, 0x0, 0x0, 0x0, 0x0, 0x0, 0x0, 0x0, 0x0,
    0xf70a5c, 0xfe0a65, 0x1170a7c, 0xb960a85, 0x1070a6c, 0xc80b0d, 0xd800d1, 0x0, 0xfb0a60, 0x1020a69, 0xb930a80, 0xb9a0a89,
    0x10b0a70, 0xcd0b11, 0xdd00d4, 0x0, 0x12c0aae, 0x1350ab7, 0x14c0ace, 0xbad0ad7, 0x13c0abe, 0x220176, 0x32002b, 0x0,
    0x1300ab2, 0x1390abb, 0xba80ad2, 0xbb10adb, 0x1400ac2, 0x27017a, 0x37002e, 0x0, 0x1660afd, 0x16f0b04, 0x1860b1d, 0xbc70b24,
    0x10e0a75, 0x1020b14, 0x112010b, 0x0, 0x16a0b01, 0x1730b08, 0xbc20b21, 0xbcb0b28, 0x1120a79, 0x1070b18, 0x117010e, 0x0,
    0x19d0017, 0x1a4001e, 0x1bd0037, 0xbdc0b36, 0x1450ac7, 0x59017f, 0x690060, 0x0, 0x1a1001b, 0x1a80022, 0xbd90b33, 0xbe00b3a,
    0x1490acb, 0x5c0183, 0x6c0065, 0x0, 0x1d7004c, 0x1de0055, 0x1f7006c, 0xbf60b4d, 0x1e7005c, 0x13900cd, 0x1490140, 0x0,
    0x1db0050, 0x1e20059, 0xbf30b48, 0xbfa0b51, 0x1eb0060, 0x13c00d1, 0x14c0145, 0x0, 0x20c0086, 0x215008f, 0x22c00a6, 0xc0d0b67,
    0x21c0096, 0x930256, 0xa3009a, 0x0, 0x210008a, 0x2190093, 0xc080b62, 0xc110b6b, 0x220009a, 0x96025a, 0xa6009f, 0x0,
    0x24600bd, 0x24f00c4, 0x26600dd, 0xc270b7c, 0x1ee0065, 0x17300d4, 0x183017a, 0x0, 0x24a00c1, 0x25300c8, 0xc220b79, 0xc2b0b80,
    0x1f20069, 0x17600d8, 0x186017f, 0x0, 0x1ad0027, 0x1b4002e, 0x1ad0027, 0x1b4002e, 0x225009f, 0x1a8025f, 0x1b801b1, 0x0,
    0x1b1002b, 0x1b80032, 0x1b1002b, 0x1b80032, 0x22900a3, 0x1ad0263, 0x1bd01b4, 0x0, 0x0, 0x3fff0000, 0x0, 0x0,
    0x0, 0x0, 0x0, 0x0, 0x0, 0x3fff3fff, 0xaaaaaaaa, 0xaaaaaaaa, 0xaaaaaaaa, 0xaaaa, 0x0, 0x55550000,
    0x55550000, 0x55550000, 0xaaaaaaaa, 0xaaaaaaaa, 0xaaaaaaaa, 0xaaaa, 0x55555555, 0x55555555, 0x55555555, 0x5555, 0x0, 0x0,
    0x0, 0xaaaa0000, 0x0, 0x0, 0x55555555, 0x55555555, 0x55555555, 0x5555, 0xaaaa, 0xaaaa, 0x0, 0x0,
    0x0, 0x0, 0x0, 0x0, 0x0, 0x0, 0x0, 0x0, 0x0, 0x0, 0x0, 0x0,
    0x0, 0x0, 0x0, 0x0, 0x0, 0x0, 0x0, 0x0, 0x0, 0x0, 0x0, 0x0,
    0x1400125, 0x1b10194, 0x30002e5, 0x9d109b4, 0x2200205, 0x1db020c, 0x2bb024a, 0x0, 0x17a015f, 0x1eb01ce, 0x97a031f, 0xa230a06,
    0x25a023f, 0x2100246, 0x2f00281, 0x0, 0x1450129, 0x1b40198, 0x30502e9, 0x9d409b8, 0x2250209, 0x1de0229, 0x2be024f, 0x0,
    0x17f0163, 0x1ee01d2, 0x97f0963, 0xa260a0a, 0x25f0243, 0x2150263, 0x2f50284, 0x0, 0x149012c, 0x1b8019d, 0x30902ec, 0x9d809bd,
    0x2910274, 0x1e2027d, 0x2c20253, 0x0, 0x1830166, 0x1f201d7, 0x9830966, 0xa2a0a0f, 0x2cb02ae, 0x21902b7, 0x2f90288, 0x0,
    0x14c0130, 0x1bd01a1, 0x30c02f0, 0x9dd09c1, 0x2940278, 0x1e70298, 0x2c70256, 0x0, 0x186016a, 0x1f701db, 0x986096a, 0xa2f0a13,
    0x2ce02b2, 0x21c02d2, 0x2fc028d, 0x0, 0xba80135, 0xbd901a4, 0xc6802f5, 0x9e109c4, 0xc080215, 0x1eb021c, 0x2cb025a, 0x0,
    0xbc2016f, 0xbf301de, 0x98a096f, 0xa330a16, 0xc22024f, 0x2200256, 0x3000291, 0x0, 0xbad0139, 0xbdc01a8, 0xc6d02f9, 0x9e409c8,
    0xc0d0219, 0x1ee0c11, 0x2ce025f, 0x0, 0xbc70173, 0xbf601e2, 0x98f0973, 0xa360a1a, 0xc270253, 0x2250c2b, 0x3050294, 0x0,
    0xbb1013c, 0xbe001ad, 0xc7102fc, 0x9e809cd, 0xc390284, 0x1f2028d, 0x2d20263, 0x0, 0xbcb0176, 0xbfa01e7, 0x9930976, 0xa3a0a1f,
    0xc5302be, 0x22902c7, 0x3090298, 0x0, 0x22c0210, 0x29d0281, 0x22c0210, 0x29d0281, 0xc3c0288, 0x1f70c40, 0x2d70266, 0x0,
    0x266024a, 0x2d702bb, 0x266024a, 0x2d702bb, 0xc5602c2, 0x22c0c5a, 0x30c029d, 0x0, 0x3f0003, 0x3fff03ff, 0xff000f, 0xff0fff,
    0x30003, 0x3ff03ff, 0xf000f, 0xfff0fff, 0x3f003f, 0x3fff3fff, 0xaaaaaaaa, 0xaaaaaaaa, 0xaaaaaaaa, 0xaaaa, 0x0, 0x55550000,
    0x55550000, 0x55550000, 0xaaaaaaaa, 0xaaaaaaaa, 0xaaaaaaaa, 0xaaaa, 0x55555555, 0x55555555, 0x55555555, 0x5555, 0x0, 0x0,
    0x0, 0xaaaa0000, 0x0, 0x0, 0x55555555, 0x55555555, 0x55555555, 0x5555, 0xaaaa, 0xaaaa, 0x0, 0x0,
    0x0, 0x0, 0x0, 0x0, 0x0, 0x0, 0x0, 0x0, 0x0, 0x0, 0x0, 0x0,
    0x0, 0x0, 0x0, 0x0, 0x0, 0x0, 0x0, 0x0, 0x0, 0x0, 0x0, 0x0,
    0xc2e0b85, 0xc370b8c, 0x24600bd, 0x24f00c4, 0x23600ad, 0x153011c, 0x163015a, 0x0, 0xc320b89, 0x23300a8, 0x24a00c1, 0x25300c8,
    0x23a00b1, 0x1560120, 0x166015f, 0x0, 0xc450b9f, 0xc4c0ba6, 0x27d00f7, 0x28400fe, 0x26d00e7, 0x18802a7, 0x1980191, 0x0,
    0xc490ba3, 0x26800e2, 0x28100fb, 0x2880102, 0x27100eb, 0x18d02ab, 0x19d0194, 0x0, 0xc5f0bb4, 0xc660bbd, 0x2b7012c, 0x2be0135,
    0x23f00b4, 0x1c20125, 0x1d201cb, 0x0, 0xc630bb8, 0x2a20119, 0x2bb0130, 0x2c20139, 0x24300b8, 0x1c70129, 0x1d701ce, 0x0,
    0xc740bce, 0xc7d0bd7, 0x2ec0166, 0x2f5016f, 0x27400ee, 0x1f902ae, 0x2090200, 0x0, 0xc780bd2, 0x2d90153, 0x2f0016a, 0x2f90173,
    0x27800f2, 0x1fc02b2, 0x20c0205, 0x0, 0x9960be5, 0x99f0bec, 0x966019d, 0x96f01a4, 0x316018d, 0x23301fc, 0x243023a, 0x0,
    0x99a0be9, 0x3130188, 0x96a01a1, 0x97301a8, 0x31a0191, 0x2360200, 0x246023f, 0x0, 0x9ed0bff, 0x9a40c06, 0x9bd01d7, 0x9c401de,
    0x9ad01c7, 0x26809ff, 0x2780271, 0x0, 0x9a10c03, 0x9a801c2, 0x9c101db, 0x9c801e2, 0x9b101cb, 0x26d0a03, 0x27d0274, 0x0,
    0xa3f0c14, 0x9f60c1d, 0xa0f020c, 0xa160215, 0x31f0194, 0x2a20205, 0x2b202ab, 0x0, 0x9f30c18, 0x9fa01f9, 0xa130210, 0xa1a0219,
    0x9630198, 0x2a70209, 0x2b702ae, 0x0, 0x2dc0156, 0x2e5015f, 0x2dc0156, 0x2e5015f, 0x9b401ce, 0x2d90a06, 0x2e902e0, 0x0,
    0x2e0015a, 0x2e90163, 0x2e0015a, 0x2e90163, 0x9b801d2, 0x2dc0a0a, 0x2ec02e5, 0x0, 0x3f0003, 0x3fff03ff, 0xff000f, 0xff0fff,
    0x30003, 0x3ff03ff, 0xf000f, 0xfff0fff, 0x3f003f, 0x3fff3fff, 0xaaaaaaaa, 0xaaaaaaaa, 0xaaaaaaaa, 0xaaaa, 0x0, 0x55550000,
    0x55550000, 0x55550000, 0xaaaaaaaa, 0xaaaaaaaa, 0xaaaaaaaa, 0xaaaa, 0x55555555, 0x55555555, 0x55555555, 0x5555, 0x0, 0x0,
    0x0, 0xaaaa0000, 0x0, 0x0, 0x55555555, 0x55555555, 0x55555555, 0x5555, 0xaaaa, 0xaaaa, 0x0, 0x0,
    0x0, 0x0, 0x0, 0x0, 0x0, 0x0, 0x0, 0x0, 0x0, 0x0, 0x0, 0x0,
    0x0, 0x0, 0x0, 0x0, 0x0, 0x0, 0x0, 0x0, 0x0, 0x0, 0x0, 0x0,
    0x2430c2e, 0xb80b85, 0x2090c14, 0xb80b85, 0x1290bb4, 0xb40bbd, 0x12500ee, 0x0, 0x2090c14, 0xf20b9f, 0x2430c2e, 0xf20b9f,
    0x1630bce, 0xe0bd7, 0x7f0045, 0x0, 0x2460c32, 0xbd0b89, 0x20c0c18, 0xbd0b89, 0x12c0bb8, 0xa80130, 0x11900e2, 0x0,
    0x20c0c18, 0xf70ba3, 0x2460c32, 0xf70ba3, 0x1660bd2, 0x2016a, 0x730039, 0x0, 0x24a0c37, 0xc10b8c, 0x2100c1d, 0xc10b8c,
    0x1980be5, 0xb80bec, 0x12900f2, 0x0, 0x2100c1d, 0xfb0ba6, 0x24a0c37, 0xfb0ba6, 0x1d20bff, 0x120c06, 0x830049, 0x0,
    0x24f0233, 0xc400a8, 0x21501f9, 0xc400a8, 0x19d0be9, 0xad01a1, 0x11c00e7, 0x0, 0x21501f9, 0xfe00e2, 0x24f0233, 0xfe00e2,
    0x1d70c03, 0x701db, 0x76003c, 0x0, 0x2530236, 0xc800ad, 0x21901fc, 0xc800ad, 0x139011c, 0xbd0125, 0x12c00f7, 0x0,
    0x21901fc, 0x10200e7, 0x2530236, 0x10200e7, 0x1730156, 0x17015f, 0x86004c, 0x0, 0x256023a, 0xcd00b1, 0x21c0200, 0xcd00b1,
    0x13c0120, 0xb10140, 0x12000eb, 0x0, 0x21c0200, 0x10700eb, 0x256023a, 0x10700eb, 0x176015a, 0xb017a, 0x7a0040, 0x0,
    0x25a023f, 0xd100b4, 0x2200205, 0xd100b4, 0x1a8018d, 0xc10194, 0x13000fb, 0x0, 0x2200205, 0x10b00ee, 0x25a023f, 0x10b00ee,
    0x1e201c7, 0x1b01ce, 0x8a0050, 0x0, 0x1350119, 0x1a40188, 0x1350119, 0x1a40188, 0x1ad0191, 0xc401b1, 0x13500fe, 0x0,
    0x16f0153, 0x1de01c2, 0x16f0153, 0x1de01c2, 0x1e701cb, 0x1e01eb, 0x8f0055, 0x0, 0x0, 0x3fff0000, 0x0, 0x0,
    0x0, 0x0, 0x0, 0x0, 0x0, 0x3fff3fff, 0xffffffff, 0xffffffff, 0xffffffff, 0xffff, 0x0, 0x0,
    0x0, 0x0, 0x0, 0x0, 0x0, 0x0, 0xffffffff, 0xffffffff, 0xffffffff, 0xffff, 0xffffffff, 0xffffffff,
    0x0, 0x0, 0xffffffff, 0xffff, 0x0, 0x0, 0x0, 0x0, 0x0, 0x0, 0x0, 0x0,
    0x0, 0x0, 0x0, 0x0, 0x0, 0x0, 0x0, 0x0, 0x0, 0x0, 0x0, 0x0,
    0x0, 0x0, 0x0, 0x0, 0x0, 0x0, 0x0, 0x0, 0x0, 0x0, 0x0, 0x0,
    0xf70a5c, 0xfe0a65, 0x1170a7c, 0xb960a85, 0x1070a6c, 0xc80b0d, 0xd800d1, 0x0, 0xfb0a60, 0x1020a69, 0xb930a80, 0xb9a0a89,
    0x10b0a70, 0xcd0b11, 0xdd00d4, 0x0, 0x12c0aae, 0x1350ab7, 0x14c0ace, 0xbad0ad7, 0x13c0abe, 0x220176, 0x32002b, 0x0,
    0x1300ab2, 0x1390abb, 0xba80ad2, 0xbb10adb, 0x1400ac2, 0x27017a, 0x37002e, 0x0, 0x1660afd, 0x16f0b04, 0x1860b1d, 0xbc70b24,
    0x10e0a75, 0x1020b14, 0x112010b, 0x0, 0x16a0b01, 0x1730b08, 0xbc20b21, 0xbcb0b28, 0x1120a79, 0x1070b18, 0x117010e, 0x0,
    0x19d0017, 0x1a4001e, 0x1bd0037, 0xbdc0b36, 0x1450ac7, 0x59017f, 0x690060, 0x0, 0x1a1001b, 0x1a80022, 0xbd90b33, 0xbe00b3a,
    0x1490acb, 0x5c0183, 0x6c0065, 0x0, 0x1d7004c, 0x1de0055, 0x1f7006c, 0xbf60b4d, 0x1e7005c, 0x13900cd, 0x1490140, 0x0,
    0x1db0050, 0x1e20059, 0xbf30b48, 0xbfa0b51, 0x1eb0060, 0x13c00d1, 0x14c0145, 0x0, 0x20c0086, 0x215008f, 0x22c00a6, 0xc0d0b67,
    0x21c0096, 0x930256, 0xa3009a, 0x0, 0x210008a, 0x2190093, 0xc080b62, 0xc110b6b, 0x220009a, 0x96025a, 0xa6009f, 0x0,
    0x24600bd, 0x24f00c4, 0x26600dd, 0xc270b7c, 0x1ee0065, 0x17300d4, 0x183017a, 0x0, 0x24a00c1, 0x25300c8, 0xc220b79, 0xc2b0b80,
    0x1f20069, 0x17600d8, 0x186017f, 0x0, 0x1ad0027, 0x1b4002e, 0x1ad0027, 0x1b4002e, 0x225009f, 0x1a8025f, 0x1b801b1, 0x0,
    0x1b1002b, 0x1b80032, 0x1b1002b, 0x1b80032, 0x22900a3, 0x1ad0263, 0x1bd01b4, 0x0, 0x0, 0x3fff0000, 0x0, 0x0,
    0x0, 0x0, 0x0, 0x0, 0x0, 0x3fff3fff, 0xaaaaaaaa, 0xaaaaaaaa, 0xaaaaaaaa, 0xaaaa, 0x0, 0x55550000,
    0x55550000, 0x55550000, 0xaaaaaaaa, 0xaaaaaaaa, 0xaaaaaaaa, 0xaaaa, 0x55555555, 0x55555555, 0x55555555, 0x5555, 0x0, 0x0,
    0x0, 0xaaaa0000, 0x0, 0x0, 0x55555555, 0x55555555, 0x55555555, 0x5555, 0xaaaa, 0xaaaa, 0x0, 0x0,
    0x0, 0x0, 0x0, 0x0, 0x0, 0x0, 0x0, 0x0, 0x0, 0x0, 0x0, 0x0,
    0x0, 0x0, 0x0, 0x0, 0x0, 0x0, 0x0, 0x0, 0x0, 0x0, 0x0, 0x0,
    0x1400125, 0x1b10194, 0x30002e5, 0x9d109b4, 0x2200205, 0x1db020c, 0x2bb024a, 0x0, 0x17a015f, 0x1eb01ce, 0x97a031f, 0xa230a06,
    0x25a023f, 0x2100246, 0x2f00281, 0x0, 0x1450129, 0x1b40198, 0x30502e9, 0x9d409b8, 0x2250209, 0x1de0229, 0x2be024f, 0x0,
    0x17f0163, 0x1ee01d2, 0x97f0963, 0xa260a0a, 0x25f0243, 0x2150263, 0x2f50284, 0x0, 0x149012c, 0x1b8019d, 0x30902ec, 0x9d809bd,
    0x2910274, 0x1e2027d, 0x2c20253, 0x0, 0x1830166, 0x1f201d7, 0x9830966, 0xa2a0a0f, 0x2cb02ae, 0x21902b7, 0x2f90288, 0x0,
    0x14c0130, 0x1bd01a1, 0x30c02f0, 0x9dd09c1, 0x2940278, 0x1e70298, 0x2c70256, 0x0, 0x186016a, 0x1f701db, 0x986096a, 0xa2f0a13,
    0x2ce02b2, 0x21c02d2, 0x2fc028d, 0x0, 0xba80135, 0xbd901a4, 0xc6802f5, 0x9e109c4, 0xc080215, 0x1eb021c, 0x2cb025a, 0x0,
    0xbc2016f, 0xbf301de, 0x98a096f, 0xa330a16, 0xc22024f, 0x2200256, 0x3000291, 0x0, 0xbad0139, 0xbdc01a8, 0xc6d02f9, 0x9e409c8,
    0xc0d0219, 0x1ee0c11, 0x2ce025f, 0x0, 0xbc70173, 0xbf601e2, 0x98f0973, 0xa360a1a, 0xc270253, 0x2250c2b, 0x3050294, 0x0,
    0xbb1013c, 0xbe001ad, 0xc7102fc, 0x9e809cd, 0xc390284, 0x1f2028d, 0x2d20263, 0x0, 0xbcb0176, 0xbfa01e7, 0x9930976, 0xa3a0a1f,
    0xc5302be, 0x22902c7, 0x3090298, 0x0, 0x22c0210, 0x29d0281, 0x22c0210, 0x29d0281, 0xc3c0288, 0x1f70c40, 0x2d70266, 0x0,
    0x266024a, 0x2d702bb, 0x266024a, 0x2d702bb, 0xc5602c2, 0x22c0c5a, 0x30c029d, 0x0, 0x3f0003, 0x3fff03ff, 0xff000f, 0xff0fff,
    0x30003, 0x3ff03ff, 0xf000f, 0xfff0fff, 0x3f003f, 0x3fff3fff, 0xaaaaaaaa, 0xaaaaaaaa, 0xaaaaaaaa, 0xaaaa, 0x0, 0x55550000,
    0x55550000, 0x55550000, 0xaaaaaaaa, 0xaaaaaaaa, 0xaaaaaaaa, 0xaaaa, 0x55555555, 0x55555555, 0x55555555, 0x5555, 0x0, 0x0,
    0x0, 0xaaaa0000, 0x0, 0x0, 0x55555555, 0x55555555, 0x55555555, 0x5555, 0xaaaa, 0xaaaa, 0x0, 0x0,
    0x0, 0x0, 0x0, 0x0, 0x0, 0x0, 0x0, 0x0, 0x0, 0x0, 0x0, 0x0,
    0x0, 0x0, 0x0, 0x0, 0x0, 0x0, 0x0, 0x0, 0x0, 0x0, 0x0, 0x0,
    0x25a00d1, 0x23f00b4, 0x25600cd, 0x23f00b4, 0x24600bd, 0x15f012c, 0x1660163, 0x0, 0x25600cd, 0x24300b8, 0x25a00d1, 0x24300b8,
    0x24a00c1, 0x1530130, 0x15a0156, 0x0, 0x291010b, 0x27400ee, 0x28d0107, 0x27400ee, 0x27d00f7, 0x19402b7, 0x19d0198, 0x0,
    0x28d0107, 0x27800f2, 0x291010b, 0x27800f2, 0x28100fb, 0x18802bb, 0x191018d, 0x0, 0x2cb0140, 0x2ae0125, 0x2c7013c, 0x2ae0125,
    0x24f00c4, 0x1ce0135, 0x1d701d2, 0x0, 0x2c7013c, 0x2b20129, 0x2cb0140, 0x2b20129, 0x25300c8, 0x1c20139, 0x1cb01c7, 0x0,
    0x300017a, 0x2e5015f, 0x2fc0176, 0x2e5015f, 0x28400fe, 0x20502be, 0x20c0209, 0x0, 0x2fc0176, 0x2e90163, 0x300017a, 0x2e90163,
    0x2880102, 0x1f902c2, 0x20001fc, 0x0, 0x97a01b1, 0x31f0194, 0x97601ad, 0x31f0194, 0x966019d, 0x23f020c, 0x2460243, 0x0,
    0x97601ad, 0x9630198, 0x97a01b1, 0x9630198, 0x96a01a1, 0x2330210, 0x23a0236, 0x0, 0x9d101eb, 0x9b401ce, 0x9cd01e7, 0x9b401ce,
    0x9bd01d7, 0x2740a0f, 0x27d0278, 0x0, 0x9cd01e7, 0x9b801d2, 0x9d101eb, 0x9b801d2, 0x9c101db, 0x2680a13, 0x271026d, 0x0,
    0xa230220, 0xa060205, 0xa1f021c, 0xa060205, 0x96f01a4, 0x2ae0215, 0x2b702b2, 0x0, 0xa1f021c, 0xa0a0209, 0xa230220, 0xa0a0209,
    0x97301a8, 0x2a20219, 0x2ab02a7, 0x0, 0x2ec0166, 0x2f5016f, 0x2ec0166, 0x2f5016f, 0x9c401de, 0x2e50a16, 0x2ec02e9, 0x0,
    0x2f0016a, 0x2f90173, 0x2f0016a, 0x2f90173, 0x9c801e2, 0x2d90a1a, 0x2e002dc, 0x0, 0x3f0003, 0x3fff03ff, 0xff000f, 0xff0fff,
    0x30003, 0x3ff03ff, 0xf000f, 0xfff0fff, 0x3f003f, 0x3fff3fff, 0xffffffff, 0xffffffff, 0xffffffff, 0xffff, 0x0, 0x0,
    0x0, 0x0, 0x0, 0x0, 0x0, 0x0, 0xffffffff, 0xffffffff, 0xffffffff, 0xffff, 0xffffffff, 0xffffffff,
    0x0, 0x0, 0xffffffff, 0xffff, 0x0, 0x0, 0x0, 0x0, 0x0, 0x0, 0x0, 0x0,
    0x0, 0x0, 0x0, 0x0, 0x0, 0x0, 0x0, 0x0, 0x0, 0x0, 0x0, 0x0,
    0x0, 0x0, 0x0, 0x0, 0x0, 0x0, 0x0, 0x0, 0x0, 0x0, 0x0, 0x0
};

__global__ __launch_bounds__(512, 2) void na_main(const void* x, const void* wqkv, const void* wo, void* ws, void* out) {
    __shared__ __attribute__((aligned(1024))) char smem[163840];
    asm volatile(
        "s_mov_b32 s18, 0\n"
        "s_mov_b32 s19, 0\n"
        "s_memtime s[8:9]\n"
        "Ltop_%=:\n"
        "s_load_dwordx2 s[24:25], s[0:1], 24\n"
        "s_load_dwordx2 s[32:33], s[0:1], 8\n"
        "s_load_dwordx2 s[20:21], s[0:1], 0\n"
        "s_load_dwordx2 s[36:37], s[0:1], 16\n"
        "s_load_dwordx2 s[28:29], s[0:1], 32\n"
        "s_getpc_b64 s[10:11]\n"
        "v_and_b32 v9, 0x1c0, v0\n"
        "v_bfe_u32 v1, v0, 5, 1\n"
        "s_lshr_b32 s88, s2, 3\n"
        "v_lshl_add_u32 v9, v1, 14, v9\n"
        "s_lshl_b32 s86, s88, 9\n"
        "v_readfirstlane_b32 s3, v0\n"
        "v_add_u32 v9, s86, v9\n"
        "v_min_u32 v9, 28540, v9\n"
        "global_load_dword v21, v9, s[10:11]\n"
        "s_lshr_b32 s3, s3, 6\n"
        "s_xor_b32 s3, s3, 4\n"
        "s_mov_b32 s34, 0x36000\n"
        "s_mov_b32 s35, 0x20000\n"
        "s_mov_b32 s26, 0x3100000\n"
        "s_mov_b32 s27, 0x20000\n"
        "v_and_b32 v8, 63, v0\n"
        "s_waitcnt lgkmcnt(0)\n"
        "s_cmp_lg_u32 s18, 0\n"
        "s_cbranch_scc1 Lnoep_%=\n"
        "s_load_dwordx2 s[6:7], s[24:25], 0x50000\n"
        "Lnoep_%=:\n"
        "s_mov_b32 s40, s2\n"
        "s_mov_b32 s41, 0\n"
        "s_cmp_lg_u32 s19, 0\n"
        "s_cbranch_scc1 Lpriv_%=\n"
        "s_cmp_lg_u32 s3, 4\n"
        "s_cbranch_scc1 Lpdone_%=\n"
        "s_cmp_ge_u32 s2, 72\n"
        "s_cbranch_scc1 Lp2g_%=\n"
        "Lp1_%=:\n"
        "s_mul_i32 s44, s40, 43\n"
        "v_and_b32 v10, 12, v0\n"
        "s_lshr_b32 s42, s44, 8\n"
        "v_and_b32 v11, 15, v0\n"
        "s_lshr_b32 s44, s44, 10\n"
        "s_and_b32 s43, s40, 1\n"
        "s_lshl1_add_u32 s43, s44, s43\n"
        "v_add_u32 v10, v10, v11\n"
        "s_mul_i32 s44, s42, 6\n"
        "s_sub_u32 s44, s40, s44\n"
        "s_and_b32 s44, s44, 6\n"
        "s_and_b32 s42, s42, 3\n"
        "s_lshl1_add_u32 s42, s44, s42\n"
        "v_and_b32 v11, 48, v0\n"
        "s_lshr_b32 s44, s42, 1\n"
        "v_mul_u32_u24 v10, 1152, v10\n"
        "s_mul_i32 s44, s44, 27648\n"
        "v_add_u32 v10, v10, v11\n"
        "s_mul_i32 s42, s42, 4608\n"
        "s_mul_i32 s43, s43, 36864\n"
        "s_add_u32 s42, s42, s44\n"
        "s_add_u32 s44, s43, 4608\n"
        "buffer_load_dwordx4 v[32:35], v10, s[32:35], s42 offen offset:384\n"
        "buffer_load_dwordx4 v[36:39], v10, s[32:35], s43 offen offset:0\n"
        "buffer_load_dwordx4 v[40:43], v10, s[32:35], s44 offen offset:0\n"
        "buffer_load_dwordx4 v[44:47], v10, s[32:35], s42 offen offset:448\n"
        "buffer_load_dwordx4 v[48:51], v10, s[32:35], s43 offen offset:64\n"
        "buffer_load_dwordx4 v[52:55], v10, s[32:35], s44 offen offset:64\n"
        "buffer_load_dwordx4 v[56:59], v10, s[32:35], s42 offen offset:512\n"
        "buffer_load_dwordx4 v[60:63], v10, s[32:35], s43 offen offset:128\n"
        "buffer_load_dwordx4 v[64:67], v10, s[32:35], s44 offen offset:128\n"
        "buffer_load_dwordx4 v[68:71], v10, s[32:35], s42 offen offset:576\n"
        "buffer_load_dwordx4 v[72:75], v10, s[32:35], s43 offen offset:192\n"
        "buffer_load_dwordx4 v[76:79], v10, s[32:35], s44 offen offset:192\n"
        "buffer_load_dwordx4 v[80:83], v10, s[32:35], s42 offen offset:640\n"
        "buffer_load_dwordx4 v[84:87], v10, s[32:35], s43 offen offset:256\n"
        "buffer_load_dwordx4 v[88:91], v10, s[32:35], s44 offen offset:256\n"
        "buffer_load_dwordx4 v[92:95], v10, s[32:35], s42 offen offset:704\n"
        "buffer_load_dwordx4 v[96:99], v10, s[32:35], s43 offen offset:320\n"
        "buffer_load_dwordx4 v[100:103], v10, s[32:35], s44 offen offset:320\n"
        "v_and_b32 v216, 15, v0\n"
        "v_bfe_u32 v217, v0, 2, 4\n"
        "v_lshlrev_b32 v216, 5, v216\n"
        "v_lshlrev_b32 v217, 5, v217\n"
        "global_load_dwordx4 v[200:203], v216, s[4:5]\n"
        "global_load_dwordx4 v[204:207], v216, s[4:5] offset:16\n"
        "global_load_dwordx4 v[208:211], v217, s[4:5]\n"
        "global_load_dwordx4 v[212:215], v217, s[4:5] offset:16\n"
        "s_mov_b32 s89, 0x9e3779b1\n"
        "v_add_u32 v15, 0x51ed27, v8\n"
        "v_mul_lo_u32 v12, v15, s89\n"
        "v_mul_lo_u32 v13, v12, s89\n"
        "v_mul_lo_u32 v14, v13, s89\n"
        "v_mul_lo_u32 v15, v14, s89\n"
        "v_or_b32 v12, 1, v12\n"
        "v_or_b32 v13, 1, v13\n"
        "v_or_b32 v14, 1, v14\n"
        "v_or_b32 v15, 1, v15\n"
        "s_mul_i32 s42, s40, 1152\n"
        "v_lshlrev_b32 v11, 4, v8\n"
        "s_add_u32 s42, s42, s19\n"
        "s_waitcnt lgkmcnt(0)\n"
        "s_bitcmp1_b32 s18, 31\n"
        "s_cbranch_scc1 Ltg1_%=\n"
        "s_bitset1_b32 s18, 31\n"
        "s_mul_i32 s7, s7, 0x4c957f2d\n"
        "s_mul_i32 s86, s6, 0x5851f42d\n"
        "s_add_u32 s7, s7, s86\n"
        "s_mul_hi_u32 s86, s6, 0x4c957f2d\n"
        "s_add_u32 s7, s7, s86\n"
        "s_mul_i32 s6, s6, 0x4c957f2d\n"
        "s_add_u32 s6, s6, 0xf767814f\n"
        "s_addc_u32 s7, s7, 0x14057b7e\n"
        "Ltg1_%=:\n"
        "v_mov_b32 v17, s7\n"
        "s_waitcnt vmcnt(19)\n"
        "v_mfma_f32_16x16x32_bf16 v[24:27], v[36:39], v[32:35], 0\n"
        "v_mfma_f32_16x16x32_bf16 v[28:31], v[40:43], v[32:35], 0\n"
        "s_waitcnt vmcnt(16)\n"
        "v_mfma_f32_16x16x32_bf16 v[24:27], v[48:51], v[44:47], v[24:27]\n"
        "v_mfma_f32_16x16x32_bf16 v[28:31], v[52:55], v[44:47], v[28:31]\n"
        "s_waitcnt vmcnt(13)\n"
        "v_mfma_f32_16x16x32_bf16 v[24:27], v[60:63], v[56:59], v[24:27]\n"
        "v_mfma_f32_16x16x32_bf16 v[28:31], v[64:67], v[56:59], v[28:31]\n"
        "s_waitcnt vmcnt(10)\n"
        "v_mfma_f32_16x16x32_bf16 v[24:27], v[72:75], v[68:71], v[24:27]\n"
        "v_mfma_f32_16x16x32_bf16 v[28:31], v[76:79], v[68:71], v[28:31]\n"
        "s_waitcnt vmcnt(7)\n"
        "v_mfma_f32_16x16x32_bf16 v[24:27], v[84:87], v[80:83], v[24:27]\n"
        "v_mfma_f32_16x16x32_bf16 v[28:31], v[88:91], v[80:83], v[28:31]\n"
        "s_waitcnt vmcnt(4)\n"
        "v_mfma_f32_16x16x32_bf16 v[24:27], v[96:99], v[92:95], v[24:27]\n"
        "v_mfma_f32_16x16x32_bf16 v[28:31], v[100:103], v[92:95], v[28:31]\n"
        "s_nop 6\n"
        "v_mul_f32 v24, 0x3dd53b94, v24\n"
        "v_mul_f32 v25, 0x3dd53b94, v25\n"
        "v_mul_f32 v26, 0x3dd53b94, v26\n"
        "v_mul_f32 v27, 0x3dd53b94, v27\n"
        "v_mul_f32 v28, 0x3dd53b94, v28\n"
        "v_mul_f32 v29, 0x3dd53b94, v29\n"
        "v_mul_f32 v30, 0x3dd53b94, v30\n"
        "v_mul_f32 v31, 0x3dd53b94, v31\n"
        "v_cvt_pk_bf16_f32 v104, v24, v25\n"
        "v_cvt_pk_bf16_f32 v105, v26, v27\n"
        "v_cvt_pk_bf16_f32 v106, v28, v29\n"
        "v_cvt_pk_bf16_f32 v107, v30, v31\n"
        "buffer_store_dwordx4 v[104:107], v11, s[24:27], s42 offen sc1\n"
        "v_mul_lo_u32 v108, v104, v12\n"
        "v_mul_lo_u32 v109, v105, v13\n"
        "v_mul_lo_u32 v110, v106, v14\n"
        "v_mul_lo_u32 v111, v107, v15\n"
        "v_add3_u32 v16, v108, v109, v110\n"
        "v_add_u32 v16, v16, v111\n"
        "s_nop 1\n"
        "v_add_u32_dpp v16, v16, v16 row_shr:1 row_mask:0xf bank_mask:0xf\n"
        "s_nop 1\n"
        "v_add_u32_dpp v16, v16, v16 row_shr:2 row_mask:0xf bank_mask:0xf\n"
        "s_nop 1\n"
        "v_add_u32_dpp v16, v16, v16 row_shr:4 row_mask:0xf bank_mask:0xf\n"
        "s_nop 1\n"
        "v_add_u32_dpp v16, v16, v16 row_shr:8 row_mask:0xf bank_mask:0xf\n"
        "s_nop 1\n"
        "v_add_u32_dpp v16, v16, v16 row_bcast:15 row_mask:0xa bank_mask:0xf\n"
        "s_nop 1\n"
        "v_add_u32_dpp v16, v16, v16 row_bcast:31 row_mask:0xc bank_mask:0xf\n"
        "s_mov_b64 exec, 1\n"
        "s_lshl_b64 exec, exec, 63\n"
        "v_add_u32 v16, s6, v16\n"
        "buffer_store_dwordx2 v[16:17], v11, s[24:27], s42 offen offset:16 sc1\n"
        "s_mov_b64 exec, -1\n"
        "s_add_u32 s40, s40, 8\n"
        "s_cmp_lt_u32 s40, s41\n"
        "s_cbranch_scc1 Lp1_%=\n"
        "s_cmp_lg_u32 s19, 0\n"
        "s_cbranch_scc1 Lp2p_%=\n"
        "s_barrier\n"
        "s_waitcnt vmcnt(2)\n"
        "s_branch Lpdone_%=\n"
        "Lp2g_%=:\n"
        "s_cmp_ge_u32 s2, 144\n"
        "s_cbranch_scc1 Lp2n_%=\n"
        "s_sub_u32 s40, s2, 72\n"
        "s_mov_b32 s38, 0x12000\n"
        "s_mov_b32 s39, 0x20000\n"
        "s_mul_i32 s42, s40, 43\n"
        "v_and_b32 v10, 12, v0\n"
        "s_lshr_b32 s42, s42, 8\n"
        "v_and_b32 v11, 15, v0\n"
        "s_mul_i32 s43, s42, 6\n"
        "v_add_u32 v10, v10, v11\n"
        "s_sub_u32 s43, s40, s43\n"
        "v_and_b32 v12, 48, v0\n"
        "v_mul_u32_u24 v10, 384, v10\n"
        "v_mul_u32_u24 v13, 1152, v11\n"
        "v_add_u32 v10, v10, v12\n"
        "v_add_u32 v13, v13, v12\n"
        "s_lshr_b32 s44, s42, 1\n"
        "s_lshl_b32 s44, s44, 6\n"
        "s_mul_i32 s45, s43, 36864\n"
        "s_add_u32 s46, s45, 18432\n"
        "buffer_load_dwordx4 v[40:43], v10, s[36:39], s44 offen\n"
        "buffer_load_dwordx4 v[44:47], v10, s[36:39], s44 offen offset:1536\n"
        "buffer_load_dwordx4 v[48:51], v13, s[32:35], s45 offen offset:768\n"
        "buffer_load_dwordx4 v[52:55], v13, s[32:35], s46 offen offset:768\n"
        "s_add_u32 s44, s44, 12288\n"
        "buffer_load_dwordx4 v[56:59], v10, s[36:39], s44 offen\n"
        "buffer_load_dwordx4 v[60:63], v10, s[36:39], s44 offen offset:1536\n"
        "buffer_load_dwordx4 v[64:67], v13, s[32:35], s45 offen offset:832\n"
        "buffer_load_dwordx4 v[68:71], v13, s[32:35], s46 offen offset:832\n"
        "s_add_u32 s44, s44, 12288\n"
        "buffer_load_dwordx4 v[72:75], v10, s[36:39], s44 offen\n"
        "buffer_load_dwordx4 v[76:79], v10, s[36:39], s44 offen offset:1536\n"
        "buffer_load_dwordx4 v[80:83], v13, s[32:35], s45 offen offset:896\n"
        "buffer_load_dwordx4 v[84:87], v13, s[32:35], s46 offen offset:896\n"
        "s_add_u32 s44, s44, 12288\n"
        "buffer_load_dwordx4 v[88:91], v10, s[36:39], s44 offen\n"
        "buffer_load_dwordx4 v[92:95], v10, s[36:39], s44 offen offset:1536\n"
        "buffer_load_dwordx4 v[96:99], v13, s[32:35], s45 offen offset:960\n"
        "buffer_load_dwordx4 v[100:103], v13, s[32:35], s46 offen offset:960\n"
        "s_add_u32 s44, s44, 12288\n"
        "buffer_load_dwordx4 v[104:107], v10, s[36:39], s44 offen\n"
        "buffer_load_dwordx4 v[108:111], v10, s[36:39], s44 offen offset:1536\n"
        "buffer_load_dwordx4 v[112:115], v13, s[32:35], s45 offen offset:1024\n"
        "buffer_load_dwordx4 v[116:119], v13, s[32:35], s46 offen offset:1024\n"
        "s_add_u32 s44, s44, 12288\n"
        "buffer_load_dwordx4 v[120:123], v10, s[36:39], s44 offen\n"
        "buffer_load_dwordx4 v[124:127], v10, s[36:39], s44 offen offset:1536\n"
        "buffer_load_dwordx4 v[128:131], v13, s[32:35], s45 offen offset:1088\n"
        "buffer_load_dwordx4 v[132:135], v13, s[32:35], s46 offen offset:1088\n"
        "s_add_u32 s44, s44, 12288\n"
        "s_and_b32 s44, s42, 1\n"
        "s_lshl_b32 s44, s44, 4\n"
        "v_add_u32 v14, s44, v11\n"
        "v_lshrrev_b32 v15, 1, v12\n"
        "v_sub_u32 v14, v14, v15\n"
        "v_and_b32 v15, 1, v14\n"
        "v_lshlrev_b32 v15, 4, v15\n"
        "v_mov_b32 v22, 0x3f80\n"
        "v_lshlrev_b32 v15, v15, v22\n"
        "v_ashrrev_i32 v14, 1, v14\n"
        "v_cmp_eq_u32 vcc, 0, v14\n"
        "s_nop 1\n"
        "v_cndmask_b32 v136, 0, v15, vcc\n"
        "v_cmp_eq_u32 vcc, 1, v14\n"
        "s_nop 1\n"
        "v_cndmask_b32 v137, 0, v15, vcc\n"
        "v_cmp_eq_u32 vcc, 2, v14\n"
        "s_nop 1\n"
        "v_cndmask_b32 v138, 0, v15, vcc\n"
        "v_cmp_eq_u32 vcc, 3, v14\n"
        "s_nop 1\n"
        "v_cndmask_b32 v139, 0, v15, vcc\n"
        "v_and_b32 v14, 3, v0\n"
        "v_mul_u32_u24 v14, 6144, v14\n"
        "v_lshl_add_u32 v14, v12, 4, v14\n"
        "v_and_b32 v15, 12, v0\n"
        "v_lshl_add_u32 v14, v15, 2, v14\n"
        "s_lshr_b32 s44, s42, 2\n"
        "s_mul_i32 s44, s44, 24\n"
        "s_add_u32 s44, s44, s43\n"
        "s_lshl_b32 s44, s44, 10\n"
        "s_and_b32 s45, s42, 3\n"
        "s_lshl_b32 s45, s45, 6\n"
        "s_add_u32 s44, s44, s45\n"
        "s_add_u32 s44, s44, 0x14800\n"
        "v_add_u32 v140, s44, v14\n"
        "v_and_b32 v216, 15, v0\n"
        "v_bfe_u32 v217, v0, 2, 4\n"
        "v_lshlrev_b32 v216, 5, v216\n"
        "v_lshlrev_b32 v217, 5, v217\n"
        "global_load_dwordx4 v[200:203], v216, s[4:5]\n"
        "global_load_dwordx4 v[204:207], v216, s[4:5] offset:16\n"
        "global_load_dwordx4 v[208:211], v217, s[4:5]\n"
        "global_load_dwordx4 v[212:215], v217, s[4:5] offset:16\n"
        "s_waitcnt vmcnt(4)\n"
        "v_mfma_f32_16x16x32_bf16 v[224:227], v[40:43], v[136:139], 0\n"
        "v_mfma_f32_16x16x32_bf16 v[228:231], v[44:47], v[136:139], 0\n"
        "v_mfma_f32_16x16x32_bf16 v[232:235], v[56:59], v[136:139], 0\n"
        "v_mfma_f32_16x16x32_bf16 v[236:239], v[60:63], v[136:139], 0\n"
        "v_mfma_f32_16x16x32_bf16 v[240:243], v[72:75], v[136:139], 0\n"
        "v_mfma_f32_16x16x32_bf16 v[244:247], v[76:79], v[136:139], 0\n"
        "v_mfma_f32_16x16x32_bf16 v[248:251], v[88:91], v[136:139], 0\n"
        "v_mfma_f32_16x16x32_bf16 v[252:255], v[92:95], v[136:139], 0\n"
        "v_mfma_f32_16x16x32_bf16 v[144:147], v[104:107], v[136:139], 0\n"
        "v_mfma_f32_16x16x32_bf16 v[148:151], v[108:111], v[136:139], 0\n"
        "v_mfma_f32_16x16x32_bf16 v[192:195], v[120:123], v[136:139], 0\n"
        "v_mfma_f32_16x16x32_bf16 v[196:199], v[124:127], v[136:139], 0\n"
        "v_cvt_pk_bf16_f32 v40, v224, v225\n"
        "v_cvt_pk_bf16_f32 v41, v226, v227\n"
        "v_cvt_pk_bf16_f32 v42, v228, v229\n"
        "v_cvt_pk_bf16_f32 v43, v230, v231\n"
        "v_cvt_pk_bf16_f32 v56, v232, v233\n"
        "v_cvt_pk_bf16_f32 v57, v234, v235\n"
        "v_cvt_pk_bf16_f32 v58, v236, v237\n"
        "v_cvt_pk_bf16_f32 v59, v238, v239\n"
        "v_cvt_pk_bf16_f32 v72, v240, v241\n"
        "v_cvt_pk_bf16_f32 v73, v242, v243\n"
        "v_cvt_pk_bf16_f32 v74, v244, v245\n"
        "v_cvt_pk_bf16_f32 v75, v246, v247\n"
        "v_cvt_pk_bf16_f32 v88, v248, v249\n"
        "v_cvt_pk_bf16_f32 v89, v250, v251\n"
        "v_cvt_pk_bf16_f32 v90, v252, v253\n"
        "v_cvt_pk_bf16_f32 v91, v254, v255\n"
        "v_cvt_pk_bf16_f32 v104, v144, v145\n"
        "v_cvt_pk_bf16_f32 v105, v146, v147\n"
        "v_cvt_pk_bf16_f32 v106, v148, v149\n"
        "v_cvt_pk_bf16_f32 v107, v150, v151\n"
        "v_cvt_pk_bf16_f32 v120, v192, v193\n"
        "v_cvt_pk_bf16_f32 v121, v194, v195\n"
        "v_cvt_pk_bf16_f32 v122, v196, v197\n"
        "v_cvt_pk_bf16_f32 v123, v198, v199\n"
        "v_mfma_f32_16x16x32_bf16 v[224:227], v[48:51], v[40:43], 0\n"
        "v_mfma_f32_16x16x32_bf16 v[228:231], v[52:55], v[40:43], 0\n"
        "v_mfma_f32_16x16x32_bf16 v[224:227], v[64:67], v[56:59], v[224:227]\n"
        "v_mfma_f32_16x16x32_bf16 v[228:231], v[68:71], v[56:59], v[228:231]\n"
        "v_mfma_f32_16x16x32_bf16 v[224:227], v[80:83], v[72:75], v[224:227]\n"
        "v_mfma_f32_16x16x32_bf16 v[228:231], v[84:87], v[72:75], v[228:231]\n"
        "v_mfma_f32_16x16x32_bf16 v[224:227], v[96:99], v[88:91], v[224:227]\n"
        "v_mfma_f32_16x16x32_bf16 v[228:231], v[100:103], v[88:91], v[228:231]\n"
        "v_mfma_f32_16x16x32_bf16 v[224:227], v[112:115], v[104:107], v[224:227]\n"
        "v_mfma_f32_16x16x32_bf16 v[228:231], v[116:119], v[104:107], v[228:231]\n"
        "v_mfma_f32_16x16x32_bf16 v[224:227], v[128:131], v[120:123], v[224:227]\n"
        "v_mfma_f32_16x16x32_bf16 v[228:231], v[132:135], v[120:123], v[228:231]\n"
        "s_nop 6\n"
        "v_cvt_pk_bf16_f32 v232, v224, v225\n"
        "v_cvt_pk_bf16_f32 v233, v226, v227\n"
        "v_cvt_pk_bf16_f32 v234, v228, v229\n"
        "v_cvt_pk_bf16_f32 v235, v230, v231\n"
        "s_waitcnt vmcnt(0)\n"
        "s_branch Lpdone_%=\n"
        "Lp2n_%=:\n"
        "v_and_b32 v216, 15, v0\n"
        "v_bfe_u32 v217, v0, 2, 4\n"
        "v_lshlrev_b32 v216, 5, v216\n"
        "v_lshlrev_b32 v217, 5, v217\n"
        "global_load_dwordx4 v[200:203], v216, s[4:5]\n"
        "global_load_dwordx4 v[204:207], v216, s[4:5] offset:16\n"
        "global_load_dwordx4 v[208:211], v217, s[4:5]\n"
        "global_load_dwordx4 v[212:215], v217, s[4:5] offset:16\n"
        "s_waitcnt vmcnt(0)\n"
        "Lpdone_%=:\n"
        "s_and_b32 s86, s2, 1\n"
        "s_lshr_b32 s87, s2, 6\n"
        "s_lshl_b32 s86, s86, 2\n"
        "s_add_u32 s46, s86, s87\n"
        "s_bfe_u32 s47, s2, 0x30003\n"
        "s_mul_i32 s12, s46, 28\n"
        "s_mul_i32 s13, s47, 28\n"
        "s_sub_u32 s12, s12, 12\n"
        "s_sub_u32 s13, s13, 12\n"
        "s_bitcmp1_b32 s2, 2\n"
        "s_cselect_b32 s14, -1, 1\n"
        "s_cselect_b32 s16, 0x124b000, 0\n"
        "s_mul_i32 s14, s14, 86016\n"
        "s_bitcmp1_b32 s2, 1\n"
        "s_cselect_b32 s15, -1, 1\n"
        "s_cselect_b32 s17, 0x14e80, 0\n"
        "s_mul_i32 s15, s15, 384\n"
        "s_add_u32 s86, s16, s17\n"
        "s_cmp_gt_u32 s3, 4\n"
        "s_subb_u32 s91, s3, 0\n"
        "v_mov_b32 v9, s86\n"
        "v_and_b32 v7, 7, v0\n"
        "v_lshrrev_b32 v6, 3, v8\n"
        "v_lshl_add_u32 v6, s91, 3, v6\n"
        "s_mov_b32 s22, 0x1260000\n"
        "s_mov_b32 s23, 0x20000\n"
        "s_lshl_b32 s91, s91, 10\n"
        "s_cmp_eq_u32 s46, 0\n"
        "s_cselect_b32 s86, 12, 4\n"
        "s_cmp_eq_u32 s47, 0\n"
        "s_cselect_b32 s87, 4, 0\n"
        "s_add_u32 s86, s86, s87\n"
        "s_cmp_ge_u32 s3, 4\n"
        "s_cselect_b32 s86, -4, s86\n"
        "s_add_u32 s86, s86, s3\n"
        "s_mul_i32 s86, s86, 768\n"
        "s_add_u32 s10, s4, s86\n"
        "s_addc_u32 s11, s5, 0\n"
        "s_cmp_eq_u32 s3, 4\n"
        "s_cbranch_scc1 Lwave4_%=\n"
        "v_add_u32 v10, 42, v6\n"
        "v_mul_u32_u24 v11, 293, v10\n"
        "v_lshrrev_b32 v11, 12, v11\n"
        "v_mad_i32_i24 v10, v11, -14, v10\n"
        "v_add_u32 v10, 3, v10\n"
        "v_lshl_add_u32 v12, v11, 1, s12\n"
        "v_add_u32 v13, 7, v12\n"
        "v_min_u32 v12, v12, v13\n"
        "v_mad_i32_i24 v248, v12, s14, v9\n"
        "v_lshl_add_u32 v12, v10, 1, s13\n"
        "v_add_u32 v13, 7, v12\n"
        "v_min_u32 v12, v12, v13\n"
        "v_mad_i32_i24 v248, v12, s15, v248\n"
        "v_xor_b32 v12, v11, v10\n"
        "v_and_b32 v12, 2, v12\n"
        "v_and_b32 v13, 1, v11\n"
        "v_lshl_or_b32 v12, v13, 2, v12\n"
        "v_xor_b32 v12, v12, v7\n"
        "v_lshl_add_u32 v248, v12, 4, v248\n"
        "s_add_u32 m0, s91, 0x0\n"
        "s_nop 0\n"
        "buffer_load_dwordx4 v248, s[20:23], 0 offen lds\n"
        "v_and_b32 v216, 15, v0\n"
        "v_bfe_u32 v217, v0, 2, 4\n"
        "v_lshlrev_b32 v216, 5, v216\n"
        "v_lshlrev_b32 v217, 5, v217\n"
        "global_load_dwordx4 v[200:203], v216, s[10:11]\n"
        "global_load_dwordx4 v[204:207], v216, s[10:11] offset:16\n"
        "global_load_dwordx4 v[208:211], v217, s[10:11]\n"
        "global_load_dwordx4 v[212:215], v217, s[10:11] offset:16\n"
        "v_add_u32 v10, 98, v6\n"
        "v_mul_u32_u24 v11, 293, v10\n"
        "v_lshrrev_b32 v11, 12, v11\n"
        "v_mad_i32_i24 v10, v11, -14, v10\n"
        "v_add_u32 v10, 3, v10\n"
        "v_lshl_add_u32 v12, v11, 1, s12\n"
        "v_add_u32 v13, 7, v12\n"
        "v_min_u32 v12, v12, v13\n"
        "v_mad_i32_i24 v249, v12, s14, v9\n"
        "v_lshl_add_u32 v12, v10, 1, s13\n"
        "v_add_u32 v13, 7, v12\n"
        "v_min_u32 v12, v12, v13\n"
        "v_mad_i32_i24 v249, v12, s15, v249\n"
        "v_xor_b32 v12, v11, v10\n"
        "v_and_b32 v12, 2, v12\n"
        "v_and_b32 v13, 1, v11\n"
        "v_lshl_or_b32 v12, v13, 2, v12\n"
        "v_xor_b32 v12, v12, v7\n"
        "v_lshl_add_u32 v249, v12, 4, v249\n"
        "s_add_u32 m0, s91, 0x1c00\n"
        "s_nop 0\n"
        "buffer_load_dwordx4 v249, s[20:23], 0 offen lds\n"
        "v_add_u32 v10, 154, v6\n"
        "v_mul_u32_u24 v11, 293, v10\n"
        "v_lshrrev_b32 v11, 12, v11\n"
        "v_mad_i32_i24 v10, v11, -14, v10\n"
        "v_add_u32 v10, 3, v10\n"
        "v_lshl_add_u32 v12, v11, 1, s12\n"
        "v_add_u32 v13, 7, v12\n"
        "v_min_u32 v12, v12, v13\n"
        "v_mad_i32_i24 v250, v12, s14, v9\n"
        "v_lshl_add_u32 v12, v10, 1, s13\n"
        "v_add_u32 v13, 7, v12\n"
        "v_min_u32 v12, v12, v13\n"
        "v_mad_i32_i24 v250, v12, s15, v250\n"
        "v_xor_b32 v12, v11, v10\n"
        "v_and_b32 v12, 2, v12\n"
        "v_and_b32 v13, 1, v11\n"
        "v_lshl_or_b32 v12, v13, 2, v12\n"
        "v_xor_b32 v12, v12, v7\n"
        "v_lshl_add_u32 v250, v12, 4, v250\n"
        "s_add_u32 m0, s91, 0x3800\n"
        "s_nop 0\n"
        "buffer_load_dwordx4 v250, s[20:23], 0 offen lds\n"
        "s_add_u32 m0, s91, 0x6380\n"
        "s_nop 0\n"
        "buffer_load_dwordx4 v248, s[20:23], 0 offen offset:128 lds\n"
        "s_waitcnt lgkmcnt(0)\n"
        "s_bitcmp1_b32 s18, 31\n"
        "s_cbranch_scc1 Ltg2_%=\n"
        "s_bitset1_b32 s18, 31\n"
        "s_mul_i32 s7, s7, 0x4c957f2d\n"
        "s_mul_i32 s86, s6, 0x5851f42d\n"
        "s_add_u32 s7, s7, s86\n"
        "s_mul_hi_u32 s86, s6, 0x4c957f2d\n"
        "s_add_u32 s7, s7, s86\n"
        "s_mul_i32 s6, s6, 0x4c957f2d\n"
        "s_add_u32 s6, s6, 0xf767814f\n"
        "s_addc_u32 s7, s7, 0x14057b7e\n"
        "Ltg2_%=:\n"
        "s_mov_b32 s88, s46\n"
        "s_mov_b32 s89, s47\n"
        "s_load_dwordx16 s[40:55], s[10:11], 512\n"
        "s_load_dwordx16 s[56:71], s[10:11], 576\n"
        "s_load_dwordx4 s[72:75], s[10:11], 640\n"
        "s_load_dwordx2 s[76:77], s[10:11], 656\n"
        "v_mov_b32 v2, 0x27000\n"
        "s_and_b32 s86, s3, 3\n"
        "v_mov_b32 v4, 1\n"
        "v_add_u32 v3, s86, v2\n"
        "s_mov_b32 s30, 0x1260000\n"
        "s_mov_b32 s31, 0x20000\n"
        "s_cmp_lg_u32 s3, 1\n"
        "s_cbranch_scc1 Lnocl_%=\n"
        "v_mov_b32 v10, 0\n"
        "v_lshl_add_u32 v11, v8, 2, v2\n"
        "ds_write_b32 v11, v10\n"
        "Lnocl_%=:\n"
        "s_cmp_lg_u32 s19, 0\n"
        "s_cbranch_scc1 Lnob0_%=\n"
        "s_cmp_ge_u32 s2, 72\n"
        "s_cbranch_scc1 Lnob0_%=\n"
        "s_barrier\n"
        "Lnob0_%=:\n"
        "s_cmp_ge_u32 s3, 4\n"
        "s_cbranch_scc1 Lhelper_%=\n"
        "s_add_u32 m0, s91, 0x7f80\n"
        "s_nop 0\n"
        "buffer_load_dwordx4 v249, s[20:23], 0 offen offset:128 lds\n"
        "v_add_u32 v10, 210, v6\n"
        "v_mul_u32_u24 v11, 293, v10\n"
        "v_lshrrev_b32 v11, 12, v11\n"
        "v_mad_i32_i24 v10, v11, -14, v10\n"
        "v_add_u32 v10, 3, v10\n"
        "v_lshl_add_u32 v12, v11, 1, s12\n"
        "v_add_u32 v13, 7, v12\n"
        "v_min_u32 v12, v12, v13\n"
        "v_mad_i32_i24 v251, v12, s14, v9\n"
        "v_lshl_add_u32 v12, v10, 1, s13\n"
        "v_add_u32 v13, 7, v12\n"
        "v_min_u32 v12, v12, v13\n"
        "v_mad_i32_i24 v251, v12, s15, v251\n"
        "v_xor_b32 v12, v11, v10\n"
        "v_and_b32 v12, 2, v12\n"
        "v_and_b32 v13, 1, v11\n"
        "v_lshl_or_b32 v12, v13, 2, v12\n"
        "v_xor_b32 v12, v12, v7\n"
        "v_lshl_add_u32 v251, v12, 4, v251\n"
        "s_add_u32 m0, s91, 0x5400\n"
        "s_nop 0\n"
        "buffer_load_dwordx4 v251, s[20:23], 0 offen lds\n"
        "s_add_u32 m0, s91, 0x9b80\n"
        "s_nop 0\n"
        "buffer_load_dwordx4 v250, s[20:23], 0 offen offset:128 lds\n"
        "s_add_u32 m0, s91, 0xb780\n"
        "s_nop 0\n"
        "buffer_load_dwordx4 v251, s[20:23], 0 offen offset:128 lds\n"
        "s_bitcmp1_b32 s3, 0\n"
        "s_cselect_b32 s80, s89, s88\n"
        "s_cselect_b32 s81, s88, s89\n"
        "s_cselect_b32 s88, s15, s14\n"
        "s_cselect_b32 s89, s17, s16\n"
        "s_cselect_b32 s82, s14, s15\n"
        "s_cselect_b32 s83, s16, s17\n"
        "s_mul_i32 s81, s81, 14\n"
        "s_cmp_ge_u32 s3, 2\n"
        "s_cselect_b32 s86, 6, 0\n"
        "s_add_u32 s81, s81, s86\n"
        "v_bfe_u32 v10, v0, 4, 2\n"
        "v_lshl_add_u32 v10, v10, 1, s81\n"
        "v_and_b32 v12, 15, v0\n"
        "v_lshl_add_u32 v12, v12, 3, s83\n"
        "v_lshl_add_u32 v11, v10, 1, -6\n"
        "v_max_i32 v11, v11, v10\n"
        "v_mad_i32_i24 v6, v11, s82, v12\n"
        "v_add_u32 v10, 1, v10\n"
        "v_lshl_add_u32 v11, v10, 1, -6\n"
        "v_max_i32 v11, v11, v10\n"
        "v_mad_i32_i24 v7, v11, s82, v12\n"
        "s_mul_i32 s86, s80, 14\n"
        "s_add_u32 s87, s3, 1\n"
        "s_and_b32 s87, s87, 2\n"
        "s_lshl_b32 s87, s87, 2\n"
        "s_add_u32 s86, s86, s87\n"
        "s_lshl_b32 s87, s86, 1\n"
        "s_sub_u32 s87, s87, 6\n"
        "s_max_i32 s87, s87, s86\n"
        "s_mul_i32 s87, s87, s88\n"
        "s_add_u32 s80, s87, s89\n"
        "s_add_u32 s86, s86, 1\n"
        "s_lshl_b32 s87, s86, 1\n"
        "s_sub_u32 s87, s87, 6\n"
        "s_max_i32 s87, s87, s86\n"
        "s_mul_i32 s87, s87, s88\n"
        "s_add_u32 s81, s87, s89\n"
        "s_add_u32 s86, s86, 1\n"
        "s_lshl_b32 s87, s86, 1\n"
        "s_sub_u32 s87, s87, 6\n"
        "s_max_i32 s87, s87, s86\n"
        "s_mul_i32 s87, s87, s88\n"
        "s_add_u32 s82, s87, s89\n"
        "s_add_u32 s86, s86, 1\n"
        "s_lshl_b32 s87, s86, 1\n"
        "s_sub_u32 s87, s87, 6\n"
        "s_max_i32 s87, s87, s86\n"
        "s_mul_i32 s87, s87, s88\n"
        "s_add_u32 s83, s87, s89\n"
        "s_add_u32 s86, s86, 1\n"
        "s_lshl_b32 s87, s86, 1\n"
        "s_sub_u32 s87, s87, 6\n"
        "s_max_i32 s87, s87, s86\n"
        "s_mul_i32 s87, s87, s88\n"
        "s_add_u32 s84, s87, s89\n"
        "s_add_u32 s86, s86, 1\n"
        "s_lshl_b32 s87, s86, 1\n"
        "s_sub_u32 s87, s87, 6\n"
        "s_max_i32 s87, s87, s86\n"
        "s_mul_i32 s87, s87, s88\n"
        "s_add_u32 s85, s87, s89\n"
        "s_add_u32 s86, s86, 1\n"
        "s_waitcnt vmcnt(7)\n"
        "v_mov_b32 v218, 5\n"
        "v_and_b32 v219, 48, v0\n"
        "v_and_b32 v220, 3, v0\n"
        "v_lshlrev_b32 v220, 3, v220\n"
        "v_lshlrev_b32_sdwa v22, v218, v200 dst_sel:DWORD dst_unused:UNUSED_PAD src0_sel:DWORD src1_sel:WORD_0\n"
        "v_xor_b32 v22, v22, v219\n"
        "v_xor_b32 v23, 64, v22\n"
        "v_lshlrev_b32_sdwa v24, v218, v200 dst_sel:DWORD dst_unused:UNUSED_PAD src0_sel:DWORD src1_sel:WORD_1\n"
        "v_xor_b32 v24, v24, v219\n"
        "v_xor_b32 v25, 64, v24\n"
        "v_lshlrev_b32_sdwa v26, v218, v201 dst_sel:DWORD dst_unused:UNUSED_PAD src0_sel:DWORD src1_sel:WORD_0\n"
        "v_xor_b32 v26, v26, v219\n"
        "v_xor_b32 v27, 64, v26\n"
        "v_lshlrev_b32_sdwa v28, v218, v201 dst_sel:DWORD dst_unused:UNUSED_PAD src0_sel:DWORD src1_sel:WORD_1\n"
        "v_xor_b32 v28, v28, v219\n"
        "v_xor_b32 v29, 64, v28\n"
        "v_lshlrev_b32_sdwa v30, v218, v202 dst_sel:DWORD dst_unused:UNUSED_PAD src0_sel:DWORD src1_sel:WORD_0\n"
        "v_xor_b32 v30, v30, v219\n"
        "v_lshlrev_b32_sdwa v32, v218, v202 dst_sel:DWORD dst_unused:UNUSED_PAD src0_sel:DWORD src1_sel:WORD_1\n"
        "v_xor_b32 v32, v32, v219\n"
        "v_lshlrev_b32_sdwa v34, v218, v203 dst_sel:DWORD dst_unused:UNUSED_PAD src0_sel:DWORD src1_sel:WORD_0\n"
        "v_xor_b32 v34, v34, v219\n"
        "v_lshlrev_b32_sdwa v36, v218, v203 dst_sel:DWORD dst_unused:UNUSED_PAD src0_sel:DWORD src1_sel:WORD_1\n"
        "v_xor_b32 v36, v36, v219\n"
        "v_lshlrev_b32_sdwa v38, v218, v204 dst_sel:DWORD dst_unused:UNUSED_PAD src0_sel:DWORD src1_sel:WORD_0\n"
        "v_xor_b32 v38, v38, v219\n"
        "v_xor_b32 v39, 64, v38\n"
        "v_lshlrev_b32_sdwa v40, v218, v204 dst_sel:DWORD dst_unused:UNUSED_PAD src0_sel:DWORD src1_sel:WORD_1\n"
        "v_xor_b32 v40, v40, v219\n"
        "v_lshlrev_b32_sdwa v42, v218, v205 dst_sel:DWORD dst_unused:UNUSED_PAD src0_sel:DWORD src1_sel:WORD_0\n"
        "v_xor_b32 v42, v42, v219\n"
        "v_lshlrev_b32_sdwa v31, v218, v205 dst_sel:DWORD dst_unused:UNUSED_PAD src0_sel:DWORD src1_sel:WORD_1\n"
        "v_xor_b32 v31, v31, v219\n"
        "v_xor_b32 v33, 64, v31\n"
        "v_lshlrev_b32_sdwa v35, v218, v206 dst_sel:DWORD dst_unused:UNUSED_PAD src0_sel:DWORD src1_sel:WORD_0\n"
        "v_xor_b32 v35, v35, v219\n"
        "v_xor_b32 v37, 64, v35\n"
        "v_lshlrev_b32_sdwa v41, v218, v206 dst_sel:DWORD dst_unused:UNUSED_PAD src0_sel:DWORD src1_sel:WORD_1\n"
        "v_xor_b32 v41, v41, v219\n"
        "v_xor_b32 v43, 64, v41\n"
        "v_lshlrev_b32_sdwa v10, v218, v208 dst_sel:DWORD dst_unused:UNUSED_PAD src0_sel:DWORD src1_sel:WORD_0\n"
        "v_or_b32 v10, v10, v220\n"
        "v_lshlrev_b32_sdwa v11, v218, v208 dst_sel:DWORD dst_unused:UNUSED_PAD src0_sel:DWORD src1_sel:WORD_1\n"
        "v_or_b32 v11, v11, v220\n"
        "v_lshlrev_b32_sdwa v12, v218, v209 dst_sel:DWORD dst_unused:UNUSED_PAD src0_sel:DWORD src1_sel:WORD_0\n"
        "v_or_b32 v12, v12, v220\n"
        "v_lshlrev_b32_sdwa v13, v218, v209 dst_sel:DWORD dst_unused:UNUSED_PAD src0_sel:DWORD src1_sel:WORD_1\n"
        "v_or_b32 v13, v13, v220\n"
        "v_lshlrev_b32_sdwa v14, v218, v210 dst_sel:DWORD dst_unused:UNUSED_PAD src0_sel:DWORD src1_sel:WORD_0\n"
        "v_or_b32 v14, v14, v220\n"
        "v_lshlrev_b32_sdwa v15, v218, v210 dst_sel:DWORD dst_unused:UNUSED_PAD src0_sel:DWORD src1_sel:WORD_1\n"
        "v_or_b32 v15, v15, v220\n"
        "v_lshlrev_b32_sdwa v16, v218, v211 dst_sel:DWORD dst_unused:UNUSED_PAD src0_sel:DWORD src1_sel:WORD_0\n"
        "v_or_b32 v16, v16, v220\n"
        "v_lshlrev_b32_sdwa v17, v218, v211 dst_sel:DWORD dst_unused:UNUSED_PAD src0_sel:DWORD src1_sel:WORD_1\n"
        "v_or_b32 v17, v17, v220\n"
        "v_lshlrev_b32_sdwa v18, v218, v212 dst_sel:DWORD dst_unused:UNUSED_PAD src0_sel:DWORD src1_sel:WORD_0\n"
        "v_or_b32 v18, v18, v220\n"
        "v_lshlrev_b32_sdwa v19, v218, v212 dst_sel:DWORD dst_unused:UNUSED_PAD src0_sel:DWORD src1_sel:WORD_1\n"
        "v_or_b32 v19, v19, v220\n"
        "v_lshlrev_b32_sdwa v20, v218, v213 dst_sel:DWORD dst_unused:UNUSED_PAD src0_sel:DWORD src1_sel:WORD_0\n"
        "v_or_b32 v20, v20, v220\n"
        "v_and_b32 v1, 63, v0\n"
        "v_lshlrev_b32 v1, 4, v1\n"
        "v_add_u32 v1, 0x12c00, v1\n"
        "v_add_u32 v9, 0xc800, v1\n"
        "v_mov_b32 v8, 0xf149f2ca\n"
        "Lt13_%=:\n"
        "s_memtime s[86:87]\n"
        "s_waitcnt lgkmcnt(0)\n"
        "s_sub_u32 s86, s86, s8\n"
        "s_cmp_lt_u32 s86, 6300\n"
        "s_cbranch_scc1 Lt13_%=\n"
        "s_add_u32 m0, s91, 0xc700\n"
        "s_nop 0\n"
        "buffer_load_dwordx4 v248, s[20:23], 0 offen offset:256 lds\n"
        "s_add_u32 m0, s91, 0xe300\n"
        "s_nop 0\n"
        "buffer_load_dwordx4 v249, s[20:23], 0 offen offset:256 lds\n"
        "s_add_u32 m0, s91, 0xff00\n"
        "s_nop 0\n"
        "buffer_load_dwordx4 v250, s[20:23], 0 offen offset:256 lds\n"
        "s_add_u32 m0, s91, 0x11b00\n"
        "s_nop 0\n"
        "buffer_load_dwordx4 v251, s[20:23], 0 offen offset:256 lds\n"
        "s_mov_b32 s90, 0x3f803f80\n"
        "s_mov_b32 s91, 0x3f803f80\n"
        "s_setprio 2\n"
        "s_waitcnt vmcnt(4)\n"
        "Lcre_%=:\n"
        "s_waitcnt lgkmcnt(0)\n"
        "s_barrier\n"
        "ds_read_b128 v[232:235], v31 offset:0\n"
        "ds_read_b128 v[236:239], v33 offset:0\n"
        "ds_read_b128 v[200:203], v1 offset:0\n"
        "ds_read_b128 v[204:207], v1 offset:1024\n"
        "ds_read_b128 v[208:211], v1 offset:6144\n"
        "ds_read_b128 v[212:215], v1 offset:7168\n"
        "ds_read_b128 v[240:243], v35 offset:0\n"
        "ds_read_b128 v[244:247], v37 offset:0\n"
        "ds_read_b128 v[188:191], v41 offset:0\n"
        "ds_read_b128 v[192:195], v43 offset:0\n"
        "s_waitcnt lgkmcnt(7)\n"
        "ds_read_b128 v[216:219], v1 offset:12288\n"
        "v_mfma_f32_16x16x32_bf16 v[44:47], v[200:203], v[232:235], 0\n"
        "s_waitcnt lgkmcnt(7)\n"
        "ds_read_b128 v[220:223], v1 offset:13312\n"
        "v_mfma_f32_16x16x32_bf16 v[44:47], v[204:207], v[236:239], v[44:47]\n"
        "s_waitcnt lgkmcnt(7)\n"
        "ds_read_b128 v[224:227], v1 offset:18432\n"
        "v_mfma_f32_16x16x32_bf16 v[116:119], v[208:211], v[232:235], 0\n"
        "s_waitcnt lgkmcnt(7)\n"
        "ds_read_b128 v[228:231], v1 offset:19456\n"
        "v_mfma_f32_16x16x32_bf16 v[116:119], v[212:215], v[236:239], v[116:119]\n"
        "s_waitcnt lgkmcnt(7)\n"
        "ds_read_b128 v[196:199], v1 offset:2048\n"
        "v_mfma_f32_16x16x32_bf16 v[68:71], v[200:203], v[240:243], 0\n"
        "s_waitcnt lgkmcnt(7)\n"
        "v_mfma_f32_16x16x32_bf16 v[68:71], v[204:207], v[244:247], v[68:71]\n"
        "v_mfma_f32_16x16x32_bf16 v[120:123], v[208:211], v[240:243], 0\n"
        "v_mfma_f32_16x16x32_bf16 v[120:123], v[212:215], v[244:247], v[120:123]\n"
        "s_waitcnt lgkmcnt(6)\n"
        "ds_read_b128 v[248:251], v31 offset:25600\n"
        "v_mfma_f32_16x16x32_bf16 v[92:95], v[200:203], v[188:191], 0\n"
        "s_waitcnt lgkmcnt(6)\n"
        "ds_read_b128 v[200:203], v1 offset:3072\n"
        "v_mfma_f32_16x16x32_bf16 v[92:95], v[204:207], v[192:195], v[92:95]\n"
        "ds_read_b128 v[204:207], v1 offset:8192\n"
        "v_mfma_f32_16x16x32_bf16 v[124:127], v[208:211], v[188:191], 0\n"
        "ds_read_b128 v[208:211], v1 offset:9216\n"
        "v_mfma_f32_16x16x32_bf16 v[124:127], v[212:215], v[192:195], v[124:127]\n"
        "s_waitcnt lgkmcnt(8)\n"
        "ds_read_b128 v[212:215], v1 offset:14336\n"
        "v_mfma_f32_16x16x32_bf16 v[48:51], v[216:219], v[232:235], 0\n"
        "s_waitcnt lgkmcnt(8)\n"
        "ds_read_b128 v[252:255], v33 offset:25600\n"
        "v_mfma_f32_16x16x32_bf16 v[48:51], v[220:223], v[236:239], v[48:51]\n"
        "v_mfma_f32_16x16x32_bf16 v[72:75], v[216:219], v[240:243], 0\n"
        "v_mfma_f32_16x16x32_bf16 v[72:75], v[220:223], v[244:247], v[72:75]\n"
        "v_mfma_f32_16x16x32_bf16 v[96:99], v[216:219], v[188:191], 0\n"
        "ds_read_b128 v[216:219], v1 offset:15360\n"
        "v_mfma_f32_16x16x32_bf16 v[96:99], v[220:223], v[192:195], v[96:99]\n"
        "s_waitcnt lgkmcnt(9)\n"
        "ds_read_b128 v[220:223], v1 offset:20480\n"
        "v_mfma_f32_16x16x32_bf16 v[144:147], v[224:227], v[232:235], 0\n"
        "s_waitcnt lgkmcnt(9)\n"
        "v_mfma_f32_16x16x32_bf16 v[144:147], v[228:231], v[236:239], v[144:147]\n"
        "v_mfma_f32_16x16x32_bf16 v[128:131], v[224:227], v[240:243], 0\n"
        "v_mfma_f32_16x16x32_bf16 v[128:131], v[228:231], v[244:247], v[128:131]\n"
        "v_mfma_f32_16x16x32_bf16 v[148:151], v[224:227], v[188:191], 0\n"
        "ds_read_b128 v[224:227], v1 offset:21504\n"
        "v_mfma_f32_16x16x32_bf16 v[148:151], v[228:231], v[192:195], v[148:151]\n"
        "s_waitcnt lgkmcnt(9)\n"
        "ds_read_b128 v[228:231], v1 offset:4096\n"
        "v_mfma_f32_16x16x32_bf16 v[52:55], v[196:199], v[232:235], 0\n"
        "s_waitcnt lgkmcnt(8)\n"
        "v_mfma_f32_16x16x32_bf16 v[52:55], v[200:203], v[236:239], v[52:55]\n"
        "v_mfma_f32_16x16x32_bf16 v[76:79], v[196:199], v[240:243], 0\n"
        "v_mfma_f32_16x16x32_bf16 v[76:79], v[200:203], v[244:247], v[76:79]\n"
        "v_mfma_f32_16x16x32_bf16 v[100:103], v[196:199], v[188:191], 0\n"
        "ds_read_b128 v[196:199], v1 offset:5120\n"
        "v_mfma_f32_16x16x32_bf16 v[100:103], v[200:203], v[192:195], v[100:103]\n"
        "s_waitcnt lgkmcnt(8)\n"
        "ds_read_b128 v[200:203], v1 offset:10240\n"
        "v_mfma_f32_16x16x32_bf16 v[132:135], v[204:207], v[232:235], 0\n"
        "s_waitcnt lgkmcnt(8)\n"
        "v_mfma_f32_16x16x32_bf16 v[132:135], v[208:211], v[236:239], v[132:135]\n"
        "v_mfma_f32_16x16x32_bf16 v[160:163], v[204:207], v[240:243], 0\n"
        "v_mfma_f32_16x16x32_bf16 v[160:163], v[208:211], v[244:247], v[160:163]\n"
        "v_mfma_f32_16x16x32_bf16 v[136:139], v[204:207], v[188:191], 0\n"
        "ds_read_b128 v[204:207], v1 offset:11264\n"
        "v_mfma_f32_16x16x32_bf16 v[136:139], v[208:211], v[192:195], v[136:139]\n"
        "s_waitcnt lgkmcnt(8)\n"
        "ds_read_b128 v[208:211], v1 offset:16384\n"
        "v_mfma_f32_16x16x32_bf16 v[56:59], v[212:215], v[232:235], 0\n"
        "s_waitcnt lgkmcnt(7)\n"
        "v_mfma_f32_16x16x32_bf16 v[56:59], v[216:219], v[236:239], v[56:59]\n"
        "v_mfma_f32_16x16x32_bf16 v[80:83], v[212:215], v[240:243], 0\n"
        "v_mfma_f32_16x16x32_bf16 v[80:83], v[216:219], v[244:247], v[80:83]\n"
        "v_mfma_f32_16x16x32_bf16 v[104:107], v[212:215], v[188:191], 0\n"
        "ds_read_b128 v[212:215], v1 offset:17408\n"
        "v_mfma_f32_16x16x32_bf16 v[104:107], v[216:219], v[192:195], v[104:107]\n"
        "s_waitcnt lgkmcnt(7)\n"
        "ds_read_b128 v[216:219], v1 offset:22528\n"
        "v_mfma_f32_16x16x32_bf16 v[164:167], v[220:223], v[232:235], 0\n"
        "s_waitcnt lgkmcnt(7)\n"
        "v_mfma_f32_16x16x32_bf16 v[164:167], v[224:227], v[236:239], v[164:167]\n"
        "v_mfma_f32_16x16x32_bf16 v[140:143], v[220:223], v[240:243], 0\n"
        "ds_read_b32 v5, v2 offset:0\n"
        "v_mfma_f32_16x16x32_bf16 v[140:143], v[224:227], v[244:247], v[140:143]\n"
        "v_mfma_f32_16x16x32_bf16 v[168:171], v[220:223], v[188:191], 0\n"
        "ds_read_b128 v[220:223], v1 offset:23552\n"
        "v_mfma_f32_16x16x32_bf16 v[168:171], v[224:227], v[192:195], v[168:171]\n"
        "s_waitcnt lgkmcnt(8)\n"
        "ds_write_b8 v3, v4 offset:8\n"
        "v_mfma_f32_16x16x32_bf16 v[60:63], v[228:231], v[232:235], 0\n"
        "s_waitcnt lgkmcnt(8)\n"
        "v_mfma_f32_16x16x32_bf16 v[60:63], v[196:199], v[236:239], v[60:63]\n"
        "v_mfma_f32_16x16x32_bf16 v[84:87], v[228:231], v[240:243], 0\n"
        "s_waitcnt lgkmcnt(2)\n"
        "v_readfirstlane_b32 s86, v5\n"
        "v_mfma_f32_16x16x32_bf16 v[84:87], v[196:199], v[244:247], v[84:87]\n"
        "v_mfma_f32_16x16x32_bf16 v[108:111], v[228:231], v[188:191], 0\n"
        "v_mfma_f32_16x16x32_bf16 v[108:111], v[196:199], v[192:195], v[108:111]\n"
        "s_cmp_eq_u32 s86, 0x1010101\n"
        "v_mfma_f32_16x16x32_bf16 v[152:155], v[200:203], v[232:235], 0\n"
        "s_cbranch_scc0 Lsl4_%=\n"
        "Lrt5_%=:\n"
        "v_mfma_f32_16x16x32_bf16 v[152:155], v[204:207], v[236:239], v[152:155]\n"
        "ds_read_b128 v[224:227], v1 offset:25600\n"
        "v_mfma_f32_16x16x32_bf16 v[156:159], v[200:203], v[240:243], 0\n"
        "ds_read_b128 v[228:231], v1 offset:26624\n"
        "v_mfma_f32_16x16x32_bf16 v[156:159], v[204:207], v[244:247], v[156:159]\n"
        "ds_read_b128 v[196:199], v1 offset:31744\n"
        "v_mfma_f32_16x16x32_bf16 v[172:175], v[200:203], v[188:191], 0\n"
        "ds_read_b128 v[200:203], v1 offset:32768\n"
        "v_mfma_f32_16x16x32_bf16 v[172:175], v[204:207], v[192:195], v[172:175]\n"
        "ds_read_b128 v[204:207], v1 offset:37888\n"
        "v_mfma_f32_16x16x32_bf16 v[64:67], v[208:211], v[232:235], 0\n"
        "v_mfma_f32_16x16x32_bf16 v[64:67], v[212:215], v[236:239], v[64:67]\n"
        "v_mfma_f32_16x16x32_bf16 v[176:179], v[216:219], v[232:235], 0\n"
        "s_waitcnt lgkmcnt(6)\n"
        "v_mfma_f32_16x16x32_bf16 v[176:179], v[220:223], v[236:239], v[176:179]\n"
        "ds_read_b128 v[232:235], v35 offset:25600\n"
        "v_mfma_f32_16x16x32_bf16 v[88:91], v[208:211], v[240:243], 0\n"
        "ds_read_b128 v[236:239], v37 offset:25600\n"
        "v_mfma_f32_16x16x32_bf16 v[88:91], v[212:215], v[244:247], v[88:91]\n"
        "v_mfma_f32_16x16x32_bf16 v[180:183], v[216:219], v[240:243], 0\n"
        "v_mfma_f32_16x16x32_bf16 v[180:183], v[220:223], v[244:247], v[180:183]\n"
        "ds_read_b128 v[240:243], v41 offset:25600\n"
        "v_mfma_f32_16x16x32_bf16 v[112:115], v[208:211], v[188:191], 0\n"
        "ds_read_b128 v[244:247], v43 offset:25600\n"
        "v_mfma_f32_16x16x32_bf16 v[112:115], v[212:215], v[192:195], v[112:115]\n"
        "ds_read_b128 v[208:211], v1 offset:38912\n"
        "v_mfma_f32_16x16x32_bf16 v[184:187], v[216:219], v[188:191], 0\n"
        "ds_read_b128 v[212:215], v1 offset:44032\n"
        "v_mfma_f32_16x16x32_bf16 v[184:187], v[220:223], v[192:195], v[184:187]\n"
        "s_waitcnt lgkmcnt(10)\n"
        "ds_read_b128 v[216:219], v1 offset:45056\n"
        "v_mfma_f32_16x16x32_bf16 v[44:47], v[224:227], v[248:251], v[44:47]\n"
        "s_waitcnt lgkmcnt(10)\n"
        "ds_read_b128 v[220:223], v1 offset:27648\n"
        "v_mfma_f32_16x16x32_bf16 v[44:47], v[228:231], v[252:255], v[44:47]\n"
        "s_waitcnt lgkmcnt(10)\n"
        "v_mfma_f32_16x16x32_bf16 v[116:119], v[196:199], v[248:251], v[116:119]\n"
        "s_waitcnt lgkmcnt(9)\n"
        "v_mfma_f32_16x16x32_bf16 v[116:119], v[200:203], v[252:255], v[116:119]\n"
        "s_waitcnt lgkmcnt(7)\n"
        "v_mfma_f32_16x16x32_bf16 v[68:71], v[224:227], v[232:235], v[68:71]\n"
        "s_waitcnt lgkmcnt(6)\n"
        "v_mfma_f32_16x16x32_bf16 v[68:71], v[228:231], v[236:239], v[68:71]\n"
        "v_mfma_f32_16x16x32_bf16 v[120:123], v[196:199], v[232:235], v[120:123]\n"
        "v_mfma_f32_16x16x32_bf16 v[120:123], v[200:203], v[236:239], v[120:123]\n"
        "s_waitcnt lgkmcnt(5)\n"
        "v_mfma_f32_16x16x32_bf16 v[92:95], v[224:227], v[240:243], v[92:95]\n"
        "s_waitcnt lgkmcnt(4)\n"
        "ds_read_b128 v[224:227], v1 offset:28672\n"
        "v_mfma_f32_16x16x32_bf16 v[92:95], v[228:231], v[244:247], v[92:95]\n"
        "ds_read_b128 v[228:231], v1 offset:33792\n"
        "v_mfma_f32_16x16x32_bf16 v[124:127], v[196:199], v[240:243], v[124:127]\n"
        "ds_read_b128 v[196:199], v1 offset:34816\n"
        "v_mfma_f32_16x16x32_bf16 v[124:127], v[200:203], v[244:247], v[124:127]\n"
        "ds_read_b128 v[200:203], v1 offset:39936\n"
        "v_mfma_f32_16x16x32_bf16 v[48:51], v[204:207], v[248:251], v[48:51]\n"
        "s_waitcnt lgkmcnt(7)\n"
        "v_mfma_f32_16x16x32_bf16 v[48:51], v[208:211], v[252:255], v[48:51]\n"
        "v_mfma_f32_16x16x32_bf16 v[72:75], v[204:207], v[232:235], v[72:75]\n"
        "v_mfma_f32_16x16x32_bf16 v[72:75], v[208:211], v[236:239], v[72:75]\n"
        "v_mfma_f32_16x16x32_bf16 v[96:99], v[204:207], v[240:243], v[96:99]\n"
        "ds_read_b128 v[204:207], v1 offset:40960\n"
        "v_mfma_f32_16x16x32_bf16 v[96:99], v[208:211], v[244:247], v[96:99]\n"
        "s_waitcnt lgkmcnt(7)\n"
        "ds_read_b128 v[208:211], v1 offset:46080\n"
        "v_mfma_f32_16x16x32_bf16 v[144:147], v[212:215], v[248:251], v[144:147]\n"
        "s_waitcnt lgkmcnt(7)\n"
        "v_mfma_f32_16x16x32_bf16 v[144:147], v[216:219], v[252:255], v[144:147]\n"
        "v_mfma_f32_16x16x32_bf16 v[128:131], v[212:215], v[232:235], v[128:131]\n"
        "v_mfma_f32_16x16x32_bf16 v[128:131], v[216:219], v[236:239], v[128:131]\n"
        "v_mfma_f32_16x16x32_bf16 v[148:151], v[212:215], v[240:243], v[148:151]\n"
        "ds_read_b128 v[212:215], v1 offset:47104\n"
        "v_mfma_f32_16x16x32_bf16 v[148:151], v[216:219], v[244:247], v[148:151]\n"
        "s_waitcnt lgkmcnt(7)\n"
        "s_waitcnt vmcnt(0)\n"
        "ds_read_b128 v[216:219], v1 offset:29696\n"
        "v_mfma_f32_16x16x32_bf16 v[52:55], v[220:223], v[248:251], v[52:55]\n"
        "s_waitcnt lgkmcnt(7)\n"
        "v_mfma_f32_16x16x32_bf16 v[52:55], v[224:227], v[252:255], v[52:55]\n"
        "v_mfma_f32_16x16x32_bf16 v[76:79], v[220:223], v[232:235], v[76:79]\n"
        "ds_write_b8 v3, v4 offset:68\n"
        "v_mfma_f32_16x16x32_bf16 v[76:79], v[224:227], v[236:239], v[76:79]\n"
        "v_mfma_f32_16x16x32_bf16 v[100:103], v[220:223], v[240:243], v[100:103]\n"
        "ds_read_b128 v[220:223], v1 offset:30720\n"
        "v_mfma_f32_16x16x32_bf16 v[100:103], v[224:227], v[244:247], v[100:103]\n"
        "s_waitcnt lgkmcnt(8)\n"
        "ds_read_b128 v[224:227], v1 offset:35840\n"
        "v_mfma_f32_16x16x32_bf16 v[132:135], v[228:231], v[248:251], v[132:135]\n"
        "s_waitcnt lgkmcnt(8)\n"
        "v_mfma_f32_16x16x32_bf16 v[132:135], v[196:199], v[252:255], v[132:135]\n"
        "v_mfma_f32_16x16x32_bf16 v[160:163], v[228:231], v[232:235], v[160:163]\n"
        "ds_read_b32 v5, v2 offset:68\n"
        "v_mfma_f32_16x16x32_bf16 v[160:163], v[196:199], v[236:239], v[160:163]\n"
        "v_mfma_f32_16x16x32_bf16 v[136:139], v[228:231], v[240:243], v[136:139]\n"
        "ds_read_b128 v[228:231], v1 offset:36864\n"
        "v_mfma_f32_16x16x32_bf16 v[136:139], v[196:199], v[244:247], v[136:139]\n"
        "s_waitcnt lgkmcnt(9)\n"
        "ds_read_b128 v[196:199], v1 offset:41984\n"
        "v_mfma_f32_16x16x32_bf16 v[56:59], v[200:203], v[248:251], v[56:59]\n"
        "s_waitcnt lgkmcnt(9)\n"
        "v_mfma_f32_16x16x32_bf16 v[56:59], v[204:207], v[252:255], v[56:59]\n"
        "v_mfma_f32_16x16x32_bf16 v[80:83], v[200:203], v[232:235], v[80:83]\n"
        "s_waitcnt lgkmcnt(2)\n"
        "v_readfirstlane_b32 s86, v5\n"
        "v_mfma_f32_16x16x32_bf16 v[80:83], v[204:207], v[236:239], v[80:83]\n"
        "v_mfma_f32_16x16x32_bf16 v[104:107], v[200:203], v[240:243], v[104:107]\n"
        "ds_read_b128 v[200:203], v1 offset:43008\n"
        "v_mfma_f32_16x16x32_bf16 v[104:107], v[204:207], v[244:247], v[104:107]\n"
        "s_cmp_eq_u32 s86, 0x1010101\n"
        "ds_read_b128 v[204:207], v1 offset:48128\n"
        "v_mfma_f32_16x16x32_bf16 v[164:167], v[208:211], v[248:251], v[164:167]\n"
        "s_cbranch_scc0 Lsl7_%=\n"
        "Lrt8_%=:\n"
        "v_mfma_f32_16x16x32_bf16 v[164:167], v[212:215], v[252:255], v[164:167]\n"
        "v_mfma_f32_16x16x32_bf16 v[140:143], v[208:211], v[232:235], v[140:143]\n"
        "ds_read_b32 v5, v2 offset:4\n"
        "v_mfma_f32_16x16x32_bf16 v[140:143], v[212:215], v[236:239], v[140:143]\n"
        "v_mfma_f32_16x16x32_bf16 v[168:171], v[208:211], v[240:243], v[168:171]\n"
        "ds_read_b128 v[208:211], v1 offset:49152\n"
        "v_mfma_f32_16x16x32_bf16 v[168:171], v[212:215], v[244:247], v[168:171]\n"
        "ds_write_b8 v3, v4 offset:12\n"
        "v_mfma_f32_16x16x32_bf16 v[60:63], v[216:219], v[248:251], v[60:63]\n"
        "v_mfma_f32_16x16x32_bf16 v[60:63], v[220:223], v[252:255], v[60:63]\n"
        "v_mfma_f32_16x16x32_bf16 v[84:87], v[216:219], v[232:235], v[84:87]\n"
        "s_waitcnt lgkmcnt(2)\n"
        "v_readfirstlane_b32 s86, v5\n"
        "v_mfma_f32_16x16x32_bf16 v[84:87], v[220:223], v[236:239], v[84:87]\n"
        "v_mfma_f32_16x16x32_bf16 v[108:111], v[216:219], v[240:243], v[108:111]\n"
        "v_mfma_f32_16x16x32_bf16 v[108:111], v[220:223], v[244:247], v[108:111]\n"
        "s_cmp_eq_u32 s86, 0x1010101\n"
        "v_mfma_f32_16x16x32_bf16 v[152:155], v[224:227], v[248:251], v[152:155]\n"
        "s_cbranch_scc0 Lsl10_%=\n"
        "Lrt11_%=:\n"
        "v_mfma_f32_16x16x32_bf16 v[152:155], v[228:231], v[252:255], v[152:155]\n"
        "ds_read_b128 v[212:215], v9 offset:0\n"
        "v_mfma_f32_16x16x32_bf16 v[156:159], v[224:227], v[232:235], v[156:159]\n"
        "ds_read_b128 v[216:219], v9 offset:1024\n"
        "v_mfma_f32_16x16x32_bf16 v[156:159], v[228:231], v[236:239], v[156:159]\n"
        "ds_read_b128 v[188:191], v31 offset:51200\n"
        "v_mfma_f32_16x16x32_bf16 v[172:175], v[224:227], v[240:243], v[172:175]\n"
        "ds_read_b128 v[192:195], v33 offset:51200\n"
        "v_xor_b32 v31, 64, v30\n"
        "v_mfma_f32_16x16x32_bf16 v[172:175], v[228:231], v[244:247], v[172:175]\n"
        "ds_read_b128 v[220:223], v9 offset:6144\n"
        "v_xor_b32 v33, 64, v32\n"
        "v_mfma_f32_16x16x32_bf16 v[64:67], v[196:199], v[248:251], v[64:67]\n"
        "ds_read_b128 v[224:227], v9 offset:7168\n"
        "v_mfma_f32_16x16x32_bf16 v[64:67], v[200:203], v[252:255], v[64:67]\n"
        "ds_read_b128 v[228:231], v9 offset:12288\n"
        "v_mfma_f32_16x16x32_bf16 v[176:179], v[204:207], v[248:251], v[176:179]\n"
        "s_waitcnt lgkmcnt(8)\n"
        "v_mfma_f32_16x16x32_bf16 v[176:179], v[208:211], v[252:255], v[176:179]\n"
        "ds_read_b128 v[248:251], v35 offset:51200\n"
        "v_mfma_f32_16x16x32_bf16 v[88:91], v[196:199], v[232:235], v[88:91]\n"
        "ds_read_b128 v[252:255], v37 offset:51200\n"
        "v_xor_b32 v35, 64, v34\n"
        "v_mfma_f32_16x16x32_bf16 v[88:91], v[200:203], v[236:239], v[88:91]\n"
        "v_xor_b32 v37, 64, v36\n"
        "v_mfma_f32_16x16x32_bf16 v[180:183], v[204:207], v[232:235], v[180:183]\n"
        "v_mfma_f32_16x16x32_bf16 v[180:183], v[208:211], v[236:239], v[180:183]\n"
        "ds_read_b128 v[232:235], v41 offset:51200\n"
        "v_mfma_f32_16x16x32_bf16 v[112:115], v[196:199], v[240:243], v[112:115]\n"
        "ds_read_b128 v[236:239], v43 offset:51200\n"
        "v_xor_b32 v41, 64, v40\n"
        "v_mfma_f32_16x16x32_bf16 v[112:115], v[200:203], v[244:247], v[112:115]\n"
        "ds_read_b128 v[196:199], v9 offset:13312\n"
        "v_xor_b32 v43, 64, v42\n"
        "v_mfma_f32_16x16x32_bf16 v[184:187], v[204:207], v[240:243], v[184:187]\n"
        "ds_read_b128 v[200:203], v9 offset:18432\n"
        "v_mfma_f32_16x16x32_bf16 v[184:187], v[208:211], v[244:247], v[184:187]\n"
        "s_waitcnt lgkmcnt(10)\n"
        "ds_read_b128 v[204:207], v9 offset:19456\n"
        "v_mfma_f32_16x16x32_bf16 v[44:47], v[212:215], v[188:191], v[44:47]\n"
        "s_waitcnt lgkmcnt(10)\n"
        "ds_read_b128 v[208:211], v9 offset:2048\n"
        "v_mfma_f32_16x16x32_bf16 v[44:47], v[216:219], v[192:195], v[44:47]\n"
        "s_waitcnt lgkmcnt(10)\n"
        "ds_read_b128 v[240:243], v9 offset:3072\n"
        "v_mfma_f32_16x16x32_bf16 v[116:119], v[220:223], v[188:191], v[116:119]\n"
        "s_waitcnt lgkmcnt(10)\n"
        "ds_read_b128 v[244:247], v9 offset:8192\n"
        "v_mfma_f32_16x16x32_bf16 v[116:119], v[224:227], v[192:195], v[116:119]\n"
        "s_waitcnt lgkmcnt(9)\n"
        "v_mfma_f32_16x16x32_bf16 v[68:71], v[212:215], v[248:251], v[68:71]\n"
        "s_waitcnt lgkmcnt(8)\n"
        "v_cvt_pk_bf16_f32 v44, v44, v45\n"
        "v_cvt_pk_bf16_f32 v45, v46, v47\n"
        "v_mfma_f32_16x16x32_bf16 v[68:71], v[216:219], v[252:255], v[68:71]\n"
        "v_mfma_f32_16x16x32_bf16 v[120:123], v[220:223], v[248:251], v[120:123]\n"
        "v_mfma_f32_16x16x32_bf16 v[120:123], v[224:227], v[252:255], v[120:123]\n"
        "s_waitcnt lgkmcnt(7)\n"
        "v_cvt_pk_bf16_f32 v46, v116, v117\n"
        "v_cvt_pk_bf16_f32 v47, v118, v119\n"
        "v_mfma_f32_16x16x32_bf16 v[92:95], v[212:215], v[232:235], v[92:95]\n"
        "s_waitcnt lgkmcnt(6)\n"
        "ds_read_b128 v[212:215], v9 offset:9216\n"
        "v_mfma_f32_16x16x32_bf16 v[92:95], v[216:219], v[236:239], v[92:95]\n"
        "v_cvt_pk_bf16_f32 v68, v68, v69\n"
        "v_cvt_pk_bf16_f32 v69, v70, v71\n"
        "v_mfma_f32_16x16x32_bf16 v[124:127], v[220:223], v[232:235], v[124:127]\n"
        "v_cvt_pk_bf16_f32 v70, v120, v121\n"
        "v_cvt_pk_bf16_f32 v71, v122, v123\n"
        "v_mfma_f32_16x16x32_bf16 v[124:127], v[224:227], v[236:239], v[124:127]\n"
        "ds_read_b128 v[216:219], v9 offset:14336\n"
        "v_mfma_f32_16x16x32_bf16 v[48:51], v[228:231], v[188:191], v[48:51]\n"
        "s_waitcnt lgkmcnt(7)\n"
        "v_cvt_pk_bf16_f32 v92, v92, v93\n"
        "v_cvt_pk_bf16_f32 v93, v94, v95\n"
        "v_mfma_f32_16x16x32_bf16 v[48:51], v[196:199], v[192:195], v[48:51]\n"
        "ds_read_b128 v[220:223], v9 offset:15360\n"
        "v_mfma_f32_16x16x32_bf16 v[72:75], v[228:231], v[248:251], v[72:75]\n"
        "v_cvt_pk_bf16_f32 v94, v124, v125\n"
        "v_cvt_pk_bf16_f32 v95, v126, v127\n"
        "v_mfma_f32_16x16x32_bf16 v[72:75], v[196:199], v[252:255], v[72:75]\n"
        "ds_read_b128 v[224:227], v9 offset:20480\n"
        "v_mfma_f32_16x16x32_bf16 v[96:99], v[228:231], v[232:235], v[96:99]\n"
        "ds_read_b128 v[116:119], v9 offset:21504\n"
        "v_mfma_f32_16x16x32_bf16 v[96:99], v[196:199], v[236:239], v[96:99]\n"
        "s_waitcnt lgkmcnt(9)\n"
        "v_cvt_pk_bf16_f32 v48, v48, v49\n"
        "v_cvt_pk_bf16_f32 v49, v50, v51\n"
        "v_mfma_f32_16x16x32_bf16 v[144:147], v[200:203], v[188:191], v[144:147]\n"
        "s_waitcnt lgkmcnt(8)\n"
        "v_cvt_pk_bf16_f32 v72, v72, v73\n"
        "v_cvt_pk_bf16_f32 v73, v74, v75\n"
        "v_mfma_f32_16x16x32_bf16 v[144:147], v[204:207], v[192:195], v[144:147]\n"
        "v_cvt_pk_bf16_f32 v96, v96, v97\n"
        "v_cvt_pk_bf16_f32 v97, v98, v99\n"
        "v_mfma_f32_16x16x32_bf16 v[128:131], v[200:203], v[248:251], v[128:131]\n"
        "ds_read_b128 v[228:231], v9 offset:4096\n"
        "v_mfma_f32_16x16x32_bf16 v[128:131], v[204:207], v[252:255], v[128:131]\n"
        "ds_read_b128 v[196:199], v9 offset:5120\n"
        "v_mfma_f32_16x16x32_bf16 v[148:151], v[200:203], v[232:235], v[148:151]\n"
        "ds_read_b128 v[120:123], v9 offset:10240\n"
        "v_mfma_f32_16x16x32_bf16 v[148:151], v[204:207], v[236:239], v[148:151]\n"
        "s_waitcnt lgkmcnt(10)\n"
        "v_cvt_pk_bf16_f32 v50, v144, v145\n"
        "v_cvt_pk_bf16_f32 v51, v146, v147\n"
        "v_mfma_f32_16x16x32_bf16 v[52:55], v[208:211], v[188:191], v[52:55]\n"
        "s_waitcnt lgkmcnt(9)\n"
        "v_cvt_pk_bf16_f32 v74, v128, v129\n"
        "v_cvt_pk_bf16_f32 v75, v130, v131\n"
        "v_mfma_f32_16x16x32_bf16 v[52:55], v[240:243], v[192:195], v[52:55]\n"
        "v_cvt_pk_bf16_f32 v98, v148, v149\n"
        "v_cvt_pk_bf16_f32 v99, v150, v151\n"
        "v_mfma_f32_16x16x32_bf16 v[76:79], v[208:211], v[248:251], v[76:79]\n"
        "ds_read_b128 v[200:203], v9 offset:11264\n"
        "v_mfma_f32_16x16x32_bf16 v[76:79], v[240:243], v[252:255], v[76:79]\n"
        "ds_read_b128 v[204:207], v9 offset:16384\n"
        "v_mfma_f32_16x16x32_bf16 v[100:103], v[208:211], v[232:235], v[100:103]\n"
        "ds_read_b128 v[124:127], v9 offset:17408\n"
        "v_mfma_f32_16x16x32_bf16 v[100:103], v[240:243], v[236:239], v[100:103]\n"
        "s_waitcnt lgkmcnt(11)\n"
        "v_cvt_pk_bf16_f32 v52, v52, v53\n"
        "v_cvt_pk_bf16_f32 v53, v54, v55\n"
        "v_mfma_f32_16x16x32_bf16 v[132:135], v[244:247], v[188:191], v[132:135]\n"
        "s_waitcnt lgkmcnt(10)\n"
        "v_cvt_pk_bf16_f32 v76, v76, v77\n"
        "v_cvt_pk_bf16_f32 v77, v78, v79\n"
        "v_mfma_f32_16x16x32_bf16 v[132:135], v[212:215], v[192:195], v[132:135]\n"
        "v_cvt_pk_bf16_f32 v100, v100, v101\n"
        "v_cvt_pk_bf16_f32 v101, v102, v103\n"
        "v_mfma_f32_16x16x32_bf16 v[160:163], v[244:247], v[248:251], v[160:163]\n"
        "ds_read_b128 v[208:211], v9 offset:22528\n"
        "v_mfma_f32_16x16x32_bf16 v[160:163], v[212:215], v[252:255], v[160:163]\n"
        "ds_read_b128 v[240:243], v9 offset:23552\n"
        "v_mfma_f32_16x16x32_bf16 v[136:139], v[244:247], v[232:235], v[136:139]\n"
        "ds_write_b8 v3, v4 offset:16\n"
        "v_mfma_f32_16x16x32_bf16 v[136:139], v[212:215], v[236:239], v[136:139]\n"
        "s_waitcnt lgkmcnt(12)\n"
        "v_cvt_pk_bf16_f32 v54, v132, v133\n"
        "v_cvt_pk_bf16_f32 v55, v134, v135\n"
        "v_mfma_f32_16x16x32_bf16 v[56:59], v[216:219], v[188:191], v[56:59]\n"
        "s_waitcnt lgkmcnt(11)\n"
        "v_cvt_pk_bf16_f32 v78, v160, v161\n"
        "v_cvt_pk_bf16_f32 v79, v162, v163\n"
        "v_mfma_f32_16x16x32_bf16 v[56:59], v[220:223], v[192:195], v[56:59]\n"
        "v_cvt_pk_bf16_f32 v102, v136, v137\n"
        "v_cvt_pk_bf16_f32 v103, v138, v139\n"
        "v_mfma_f32_16x16x32_bf16 v[80:83], v[216:219], v[248:251], v[80:83]\n"
        "v_mfma_f32_16x16x32_bf16 v[80:83], v[220:223], v[252:255], v[80:83]\n"
        "v_mfma_f32_16x16x32_bf16 v[104:107], v[216:219], v[232:235], v[104:107]\n"
        "v_mfma_f32_16x16x32_bf16 v[104:107], v[220:223], v[236:239], v[104:107]\n"
        "s_waitcnt lgkmcnt(10)\n"
        "v_mfma_f32_16x16x32_bf16 v[164:167], v[224:227], v[188:191], v[164:167]\n"
        "s_waitcnt lgkmcnt(9)\n"
        "v_cvt_pk_bf16_f32 v56, v56, v57\n"
        "v_cvt_pk_bf16_f32 v57, v58, v59\n"
        "v_mfma_f32_16x16x32_bf16 v[164:167], v[116:119], v[192:195], v[164:167]\n"
        "v_cvt_pk_bf16_f32 v80, v80, v81\n"
        "v_cvt_pk_bf16_f32 v81, v82, v83\n"
        "v_mfma_f32_16x16x32_bf16 v[140:143], v[224:227], v[248:251], v[140:143]\n"
        "v_cvt_pk_bf16_f32 v104, v104, v105\n"
        "v_cvt_pk_bf16_f32 v105, v106, v107\n"
        "v_mfma_f32_16x16x32_bf16 v[140:143], v[116:119], v[252:255], v[140:143]\n"
        "v_mfma_f32_16x16x32_bf16 v[168:171], v[224:227], v[232:235], v[168:171]\n"
        "v_mfma_f32_16x16x32_bf16 v[168:171], v[116:119], v[236:239], v[168:171]\n"
        "s_waitcnt lgkmcnt(8)\n"
        "v_cvt_pk_bf16_f32 v58, v164, v165\n"
        "v_cvt_pk_bf16_f32 v59, v166, v167\n"
        "v_mfma_f32_16x16x32_bf16 v[60:63], v[228:231], v[188:191], v[60:63]\n"
        "s_waitcnt lgkmcnt(7)\n"
        "v_mfma_f32_16x16x32_bf16 v[60:63], v[196:199], v[192:195], v[60:63]\n"
        "v_cvt_pk_bf16_f32 v82, v140, v141\n"
        "v_cvt_pk_bf16_f32 v83, v142, v143\n"
        "v_mfma_f32_16x16x32_bf16 v[84:87], v[228:231], v[248:251], v[84:87]\n"
        "ds_read_b32 v5, v2 offset:20\n"
        "v_cvt_pk_bf16_f32 v106, v168, v169\n"
        "v_mfma_f32_16x16x32_bf16 v[84:87], v[196:199], v[252:255], v[84:87]\n"
        "v_cvt_pk_bf16_f32 v107, v170, v171\n"
        "v_mfma_f32_16x16x32_bf16 v[108:111], v[228:231], v[232:235], v[108:111]\n"
        "v_cvt_pk_bf16_f32 v60, v60, v61\n"
        "v_cvt_pk_bf16_f32 v61, v62, v63\n"
        "v_mfma_f32_16x16x32_bf16 v[108:111], v[196:199], v[236:239], v[108:111]\n"
        "s_waitcnt lgkmcnt(7)\n"
        "v_mfma_f32_16x16x32_bf16 v[152:155], v[120:123], v[188:191], v[152:155]\n"
        "s_waitcnt lgkmcnt(6)\n"
        "v_mfma_f32_16x16x32_bf16 v[152:155], v[200:203], v[192:195], v[152:155]\n"
        "v_cvt_pk_bf16_f32 v84, v84, v85\n"
        "v_cvt_pk_bf16_f32 v85, v86, v87\n"
        "v_mfma_f32_16x16x32_bf16 v[156:159], v[120:123], v[248:251], v[156:159]\n"
        "s_waitcnt lgkmcnt(0)\n"
        "v_readfirstlane_b32 s86, v5\n"
        "v_mfma_f32_16x16x32_bf16 v[156:159], v[200:203], v[252:255], v[156:159]\n"
        "v_cvt_pk_bf16_f32 v108, v108, v109\n"
        "v_cvt_pk_bf16_f32 v109, v110, v111\n"
        "v_mfma_f32_16x16x32_bf16 v[172:175], v[120:123], v[232:235], v[172:175]\n"
        "v_cvt_pk_bf16_f32 v62, v152, v153\n"
        "v_cvt_pk_bf16_f32 v63, v154, v155\n"
        "v_mfma_f32_16x16x32_bf16 v[172:175], v[200:203], v[236:239], v[172:175]\n"
        "s_cmp_eq_u32 s86, 0x1010101\n"
        "v_mfma_f32_16x16x32_bf16 v[64:67], v[204:207], v[188:191], v[64:67]\n"
        "s_cbranch_scc0 Lsl13_%=\n"
        "Lrt14_%=:\n"
        "v_mfma_f32_16x16x32_bf16 v[64:67], v[124:127], v[192:195], v[64:67]\n"
        "v_cvt_pk_bf16_f32 v86, v156, v157\n"
        "v_cvt_pk_bf16_f32 v87, v158, v159\n"
        "v_mfma_f32_16x16x32_bf16 v[176:179], v[208:211], v[188:191], v[176:179]\n"
        "v_mfma_f32_16x16x32_bf16 v[176:179], v[240:243], v[192:195], v[176:179]\n"
        "ds_read_b128 v[216:219], v22 offset:0\n"
        "v_cvt_pk_bf16_f32 v110, v172, v173\n"
        "v_mfma_f32_16x16x32_bf16 v[88:91], v[204:207], v[248:251], v[88:91]\n"
        "ds_read_b128 v[220:223], v23 offset:0\n"
        "v_cvt_pk_bf16_f32 v111, v174, v175\n"
        "v_mfma_f32_16x16x32_bf16 v[88:91], v[124:127], v[252:255], v[88:91]\n"
        "ds_read_b128 v[224:227], v24 offset:0\n"
        "v_cvt_pk_bf16_f32 v64, v64, v65\n"
        "v_mfma_f32_16x16x32_bf16 v[180:183], v[208:211], v[248:251], v[180:183]\n"
        "ds_read_b128 v[228:231], v25 offset:0\n"
        "v_cvt_pk_bf16_f32 v65, v66, v67\n"
        "v_mfma_f32_16x16x32_bf16 v[180:183], v[240:243], v[252:255], v[180:183]\n"
        "v_cvt_pk_bf16_f32 v66, v176, v177\n"
        "v_cvt_pk_bf16_f32 v67, v178, v179\n"
        "v_mfma_f32_16x16x32_bf16 v[112:115], v[204:207], v[232:235], v[112:115]\n"
        "ds_read_b128 v[200:203], v26 offset:0\n"
        "v_cvt_pk_bf16_f32 v88, v88, v89\n"
        "v_mfma_f32_16x16x32_bf16 v[112:115], v[124:127], v[236:239], v[112:115]\n"
        "ds_read_b128 v[204:207], v27 offset:0\n"
        "v_cvt_pk_bf16_f32 v89, v90, v91\n"
        "v_mfma_f32_16x16x32_bf16 v[184:187], v[208:211], v[232:235], v[184:187]\n"
        "v_cvt_pk_bf16_f32 v90, v180, v181\n"
        "v_cvt_pk_bf16_f32 v91, v182, v183\n"
        "v_mfma_f32_16x16x32_bf16 v[184:187], v[240:243], v[236:239], v[184:187]\n"
        "s_waitcnt lgkmcnt(5)\n"
        "ds_read_b128 v[208:211], v28 offset:0\n"
        "v_mfma_f32_16x16x32_bf16 v[116:119], v[216:219], v[44:47], 0\n"
        "s_waitcnt lgkmcnt(5)\n"
        "ds_read_b128 v[212:215], v29 offset:0\n"
        "v_cvt_pk_bf16_f32 v112, v112, v113\n"
        "v_mfma_f32_16x16x32_bf16 v[116:119], v[220:223], v[48:51], v[116:119]\n"
        "s_waitcnt lgkmcnt(5)\n"
        "v_cvt_pk_bf16_f32 v113, v114, v115\n"
        "v_cvt_pk_bf16_f32 v114, v184, v185\n"
        "v_mfma_f32_16x16x32_bf16 v[120:123], v[224:227], v[44:47], 0\n"
        "s_waitcnt lgkmcnt(4)\n"
        "v_cvt_pk_bf16_f32 v115, v186, v187\n"
        "v_mfma_f32_16x16x32_bf16 v[120:123], v[228:231], v[48:51], v[120:123]\n"
        "s_waitcnt lgkmcnt(3)\n"
        "ds_read_b128 v[232:235], v38 offset:0\n"
        "v_cndmask_b32 v116, v116, v8, s[50:51]\n"
        "v_mfma_f32_16x16x32_bf16 v[124:127], v[200:203], v[44:47], 0\n"
        "s_waitcnt lgkmcnt(3)\n"
        "ds_read_b128 v[236:239], v39 offset:0\n"
        "v_cndmask_b32 v118, v118, v8, s[52:53]\n"
        "v_mfma_f32_16x16x32_bf16 v[124:127], v[204:207], v[48:51], v[124:127]\n"
        "v_cndmask_b32 v120, v120, v8, s[50:51]\n"
        "v_cndmask_b32 v122, v122, v8, s[52:53]\n"
        "v_mfma_f32_16x16x32_bf16 v[144:147], v[200:203], v[68:71], 0\n"
        "v_mfma_f32_16x16x32_bf16 v[144:147], v[204:207], v[72:75], v[144:147]\n"
        "s_waitcnt lgkmcnt(3)\n"
        "v_mfma_f32_16x16x32_bf16 v[128:131], v[208:211], v[44:47], 0\n"
        "s_waitcnt lgkmcnt(2)\n"
        "ds_read_b32 v5, v2 offset:24\n"
        "v_mfma_f32_16x16x32_bf16 v[128:131], v[212:215], v[48:51], v[128:131]\n"
        "ds_read_b128 v[240:243], v40 offset:0\n"
        "v_cndmask_b32 v127, v127, v8, s[54:55]\n"
        "v_mfma_f32_16x16x32_bf16 v[148:151], v[208:211], v[68:71], 0\n"
        "ds_read_b128 v[244:247], v41 offset:0\n"
        "v_cndmask_b32 v144, v144, v8, s[58:59]\n"
        "v_mfma_f32_16x16x32_bf16 v[148:151], v[212:215], v[72:75], v[148:151]\n"
        "s_waitcnt lgkmcnt(4)\n"
        "v_cndmask_b32 v146, v146, v8, s[60:61]\n"
        "v_mfma_f32_16x16x32_bf16 v[132:135], v[232:235], v[44:47], 0\n"
        "s_waitcnt lgkmcnt(3)\n"
        "v_cndmask_b32 v131, v131, v8, s[54:55]\n"
        "v_mfma_f32_16x16x32_bf16 v[132:135], v[236:239], v[48:51], v[132:135]\n"
        "v_mfma_f32_16x16x32_bf16 v[160:163], v[232:235], v[68:71], 0\n"
        "s_waitcnt lgkmcnt(2)\n"
        "v_readfirstlane_b32 s86, v5\n"
        "v_cndmask_b32 v148, v148, v8, s[58:59]\n"
        "v_mfma_f32_16x16x32_bf16 v[160:163], v[236:239], v[72:75], v[160:163]\n"
        "ds_read_b128 v[216:219], v42 offset:0\n"
        "v_cndmask_b32 v150, v150, v8, s[60:61]\n"
        "v_mfma_f32_16x16x32_bf16 v[188:191], v[232:235], v[92:95], 0\n"
        "ds_read_b128 v[220:223], v43 offset:0\n"
        "v_cndmask_b32 v133, v133, v8, s[56:57]\n"
        "v_mfma_f32_16x16x32_bf16 v[188:191], v[236:239], v[96:99], v[188:191]\n"
        "s_waitcnt lgkmcnt(3)\n"
        "s_cmp_eq_u32 s86, 0x1010101\n"
        "v_cndmask_b32 v135, v135, v8, s[56:57]\n"
        "v_mfma_f32_16x16x32_bf16 v[136:139], v[240:243], v[44:47], 0\n"
        "s_waitcnt lgkmcnt(2)\n"
        "v_mfma_f32_16x16x32_bf16 v[136:139], v[244:247], v[48:51], v[136:139]\n"
        "s_cbranch_scc0 Lsl16_%=\n"
        "Lrt17_%=:\n"
        "v_mfma_f32_16x16x32_bf16 v[164:167], v[240:243], v[68:71], 0\n"
        "ds_read_b128 v[224:227], v22 offset:25600\n"
        "v_cndmask_b32 v188, v188, v8, s[76:77]\n"
        "v_mfma_f32_16x16x32_bf16 v[164:167], v[244:247], v[72:75], v[164:167]\n"
        "ds_read_b128 v[228:231], v23 offset:25600\n"
        "v_cndmask_b32 v190, v190, v8, s[76:77]\n"
        "v_mfma_f32_16x16x32_bf16 v[192:195], v[240:243], v[92:95], 0\n"
        "ds_read_b128 v[200:203], v24 offset:25600\n"
        "v_cndmask_b32 v137, v137, v8, s[56:57]\n"
        "v_mfma_f32_16x16x32_bf16 v[192:195], v[244:247], v[96:99], v[192:195]\n"
        "s_waitcnt lgkmcnt(4)\n"
        "ds_read_b128 v[204:207], v25 offset:25600\n"
        "v_cndmask_b32 v139, v139, v8, s[56:57]\n"
        "v_mfma_f32_16x16x32_bf16 v[140:143], v[216:219], v[44:47], 0\n"
        "s_waitcnt lgkmcnt(4)\n"
        "ds_read_b128 v[208:211], v26 offset:25600\n"
        "v_mfma_f32_16x16x32_bf16 v[140:143], v[220:223], v[48:51], v[140:143]\n"
        "ds_read_b128 v[212:215], v27 offset:25600\n"
        "v_cndmask_b32 v192, v192, v8, s[76:77]\n"
        "v_mfma_f32_16x16x32_bf16 v[168:171], v[216:219], v[68:71], 0\n"
        "v_cndmask_b32 v194, v194, v8, s[76:77]\n"
        "v_mfma_f32_16x16x32_bf16 v[168:171], v[220:223], v[72:75], v[168:171]\n"
        "v_mfma_f32_16x16x32_bf16 v[196:199], v[216:219], v[92:95], 0\n"
        "ds_read_b128 v[232:235], v28 offset:25600\n"
        "v_mfma_f32_16x16x32_bf16 v[196:199], v[220:223], v[96:99], v[196:199]\n"
        "s_waitcnt lgkmcnt(6)\n"
        "ds_read_b128 v[236:239], v29 offset:25600\n"
        "v_cndmask_b32 v141, v141, v8, s[56:57]\n"
        "v_mfma_f32_16x16x32_bf16 v[116:119], v[224:227], v[52:55], v[116:119]\n"
        "s_waitcnt lgkmcnt(6)\n"
        "v_cndmask_b32 v143, v143, v8, s[56:57]\n"
        "v_mov_b32 v152, 0\n"
        "v_mfma_f32_16x16x32_bf16 v[116:119], v[228:231], v[56:59], v[116:119]\n"
        "s_waitcnt lgkmcnt(5)\n"
        "v_cndmask_b32 v196, v196, v8, s[76:77]\n"
        "v_cndmask_b32 v198, v198, v8, s[76:77]\n"
        "v_mfma_f32_16x16x32_bf16 v[120:123], v[200:203], v[52:55], v[120:123]\n"
        "s_waitcnt lgkmcnt(4)\n"
        "ds_read_b128 v[240:243], v38 offset:25600\n"
        "v_cndmask_b32 v153, 0, v8, s[62:63]\n"
        "v_mfma_f32_16x16x32_bf16 v[120:123], v[204:207], v[56:59], v[120:123]\n"
        "s_waitcnt lgkmcnt(4)\n"
        "ds_read_b128 v[244:247], v39 offset:25600\n"
        "v_mov_b32 v154, 0\n"
        "v_mfma_f32_16x16x32_bf16 v[124:127], v[208:211], v[52:55], v[124:127]\n"
        "s_waitcnt lgkmcnt(4)\n"
        "v_cndmask_b32 v155, 0, v8, s[64:65]\n"
        "v_mov_b32 v156, 0\n"
        "v_mfma_f32_16x16x32_bf16 v[124:127], v[212:215], v[56:59], v[124:127]\n"
        "v_cndmask_b32 v157, 0, v8, s[62:63]\n"
        "v_mov_b32 v158, 0\n"
        "v_mfma_f32_16x16x32_bf16 v[144:147], v[208:211], v[76:79], v[144:147]\n"
        "v_cndmask_b32 v159, 0, v8, s[64:65]\n"
        "v_mov_b32 v172, 0\n"
        "v_mfma_f32_16x16x32_bf16 v[144:147], v[212:215], v[80:83], v[144:147]\n"
        "s_waitcnt lgkmcnt(3)\n"
        "v_cndmask_b32 v173, 0, v8, s[66:67]\n"
        "v_cndmask_b32 v174, 0, v8, s[68:69]\n"
        "v_mfma_f32_16x16x32_bf16 v[128:131], v[232:235], v[52:55], v[128:131]\n"
        "s_waitcnt lgkmcnt(2)\n"
        "ds_read_b32 v5, v2 offset:28\n"
        "v_cndmask_b32 v175, 0, v8, s[70:71]\n"
        "v_mfma_f32_16x16x32_bf16 v[128:131], v[236:239], v[56:59], v[128:131]\n"
        "ds_read_b128 v[216:219], v40 offset:25600\n"
        "v_mov_b32 v176, 0\n"
        "v_mfma_f32_16x16x32_bf16 v[148:151], v[232:235], v[76:79], v[148:151]\n"
        "ds_read_b128 v[220:223], v41 offset:25600\n"
        "v_cndmask_b32 v177, 0, v8, s[66:67]\n"
        "v_mfma_f32_16x16x32_bf16 v[148:151], v[236:239], v[80:83], v[148:151]\n"
        "s_waitcnt lgkmcnt(4)\n"
        "v_cndmask_b32 v178, 0, v8, s[68:69]\n"
        "v_cndmask_b32 v179, 0, v8, s[70:71]\n"
        "v_mfma_f32_16x16x32_bf16 v[132:135], v[240:243], v[52:55], v[132:135]\n"
        "s_waitcnt lgkmcnt(3)\n"
        "v_mov_b32 v180, 0\n"
        "v_cndmask_b32 v181, 0, v8, s[72:73]\n"
        "v_mfma_f32_16x16x32_bf16 v[132:135], v[244:247], v[56:59], v[132:135]\n"
        "v_mov_b32 v182, 0\n"
        "v_cndmask_b32 v183, 0, v8, s[74:75]\n"
        "v_mfma_f32_16x16x32_bf16 v[160:163], v[240:243], v[76:79], v[160:163]\n"
        "s_waitcnt lgkmcnt(2)\n"
        "ds_read_b128 v[224:227], v42 offset:25600\n"
        "v_readfirstlane_b32 s86, v5\n"
        "v_mfma_f32_16x16x32_bf16 v[160:163], v[244:247], v[80:83], v[160:163]\n"
        "ds_read_b128 v[228:231], v43 offset:25600\n"
        "v_mov_b32 v184, 0\n"
        "v_mfma_f32_16x16x32_bf16 v[188:191], v[240:243], v[100:103], v[188:191]\n"
        "v_cndmask_b32 v185, 0, v8, s[72:73]\n"
        "v_mov_b32 v186, 0\n"
        "v_mfma_f32_16x16x32_bf16 v[188:191], v[244:247], v[104:107], v[188:191]\n"
        "s_waitcnt lgkmcnt(3)\n"
        "s_cmp_eq_u32 s86, 0x1010101\n"
        "v_cndmask_b32 v187, 0, v8, s[74:75]\n"
        "v_mfma_f32_16x16x32_bf16 v[136:139], v[216:219], v[52:55], v[136:139]\n"
        "s_waitcnt lgkmcnt(2)\n"
        "v_xor_b32 v44, 32, v10\n"
        "v_xor_b32 v45, 32, v11\n"
        "v_mfma_f32_16x16x32_bf16 v[136:139], v[220:223], v[56:59], v[136:139]\n"
        "s_cbranch_scc0 Lsl19_%=\n"
        "Lrt20_%=:\n"
        "v_mfma_f32_16x16x32_bf16 v[164:167], v[216:219], v[76:79], v[164:167]\n"
        "ds_read_b128 v[200:203], v22 offset:51200\n"
        "v_xor_b32 v46, 32, v12\n"
        "v_mfma_f32_16x16x32_bf16 v[164:167], v[220:223], v[80:83], v[164:167]\n"
        "ds_read_b128 v[204:207], v23 offset:51200\n"
        "v_xor_b32 v47, 32, v13\n"
        "v_mfma_f32_16x16x32_bf16 v[192:195], v[216:219], v[100:103], v[192:195]\n"
        "ds_read_b128 v[208:211], v24 offset:51200\n"
        "v_xor_b32 v48, 32, v14\n"
        "v_mfma_f32_16x16x32_bf16 v[192:195], v[220:223], v[104:107], v[192:195]\n"
        "s_waitcnt lgkmcnt(4)\n"
        "ds_read_b128 v[212:215], v25 offset:51200\n"
        "v_xor_b32 v49, 32, v15\n"
        "v_mfma_f32_16x16x32_bf16 v[140:143], v[224:227], v[52:55], v[140:143]\n"
        "s_waitcnt lgkmcnt(4)\n"
        "ds_read_b128 v[232:235], v26 offset:51200\n"
        "v_xor_b32 v50, 32, v16\n"
        "v_mfma_f32_16x16x32_bf16 v[140:143], v[228:231], v[56:59], v[140:143]\n"
        "ds_read_b128 v[236:239], v27 offset:51200\n"
        "v_xor_b32 v51, 32, v17\n"
        "v_mfma_f32_16x16x32_bf16 v[168:171], v[224:227], v[76:79], v[168:171]\n"
        "ds_read_b128 v[240:243], v28 offset:51200\n"
        "v_xor_b32 v21, 32, v18\n"
        "v_mfma_f32_16x16x32_bf16 v[168:171], v[228:231], v[80:83], v[168:171]\n"
        "ds_read_b128 v[244:247], v29 offset:51200\n"
        "v_xor_b32 v252, 32, v19\n"
        "v_mfma_f32_16x16x32_bf16 v[196:199], v[224:227], v[100:103], v[196:199]\n"
        "ds_read_b128 v[216:219], v38 offset:51200\n"
        "v_xor_b32 v253, 32, v20\n"
        "v_mfma_f32_16x16x32_bf16 v[196:199], v[228:231], v[104:107], v[196:199]\n"
        "s_waitcnt lgkmcnt(8)\n"
        "ds_read_b128 v[220:223], v39 offset:51200\n"
        "v_mfma_f32_16x16x32_bf16 v[116:119], v[200:203], v[60:63], v[116:119]\n"
        "s_waitcnt lgkmcnt(8)\n"
        "ds_read_b128 v[224:227], v40 offset:51200\n"
        "v_mfma_f32_16x16x32_bf16 v[116:119], v[204:207], v[64:67], v[116:119]\n"
        "s_waitcnt lgkmcnt(8)\n"
        "ds_read_b128 v[228:231], v41 offset:51200\n"
        "v_mfma_f32_16x16x32_bf16 v[120:123], v[208:211], v[60:63], v[120:123]\n"
        "s_waitcnt lgkmcnt(8)\n"
        "ds_read_b128 v[200:203], v42 offset:51200\n"
        "v_mfma_f32_16x16x32_bf16 v[120:123], v[212:215], v[64:67], v[120:123]\n"
        "s_waitcnt lgkmcnt(8)\n"
        "ds_read_b128 v[204:207], v43 offset:51200\n"
        "v_mfma_f32_16x16x32_bf16 v[124:127], v[232:235], v[60:63], v[124:127]\n"
        "s_waitcnt lgkmcnt(8)\n"
        "v_mfma_f32_16x16x32_bf16 v[124:127], v[236:239], v[64:67], v[124:127]\n"
        "s_waitcnt lgkmcnt(7)\n"
        "v_mfma_f32_16x16x32_bf16 v[128:131], v[240:243], v[60:63], v[128:131]\n"
        "s_waitcnt lgkmcnt(6)\n"
        "v_mfma_f32_16x16x32_bf16 v[128:131], v[244:247], v[64:67], v[128:131]\n"
        "s_waitcnt lgkmcnt(5)\n"
        "v_cndmask_b32 v116, v120, v116, s[40:41]\n"
        "v_cndmask_b32 v117, v121, v117, s[40:41]\n"
        "v_mfma_f32_16x16x32_bf16 v[132:135], v[216:219], v[60:63], v[132:135]\n"
        "s_waitcnt lgkmcnt(4)\n"
        "v_cndmask_b32 v118, v122, v118, s[42:43]\n"
        "v_cndmask_b32 v119, v123, v119, s[42:43]\n"
        "v_mfma_f32_16x16x32_bf16 v[132:135], v[220:223], v[64:67], v[132:135]\n"
        "s_waitcnt lgkmcnt(3)\n"
        "v_cndmask_b32 v124, v128, v124, s[40:41]\n"
        "v_cndmask_b32 v125, v129, v125, s[40:41]\n"
        "v_mfma_f32_16x16x32_bf16 v[136:139], v[224:227], v[60:63], v[136:139]\n"
        "s_waitcnt lgkmcnt(2)\n"
        "v_cndmask_b32 v126, v130, v126, s[42:43]\n"
        "v_cndmask_b32 v127, v131, v127, s[42:43]\n"
        "v_mfma_f32_16x16x32_bf16 v[136:139], v[228:231], v[64:67], v[136:139]\n"
        "s_waitcnt lgkmcnt(1)\n"
        "v_max3_f32 v120, v116, v117, v118\n"
        "v_max3_f32 v120, v120, v119, v124\n"
        "v_mfma_f32_16x16x32_bf16 v[140:143], v[200:203], v[60:63], v[140:143]\n"
        "s_waitcnt lgkmcnt(0)\n"
        "v_max3_f32 v120, v120, v125, v126\n"
        "v_mfma_f32_16x16x32_bf16 v[140:143], v[204:207], v[64:67], v[140:143]\n"
        "v_mov_b64 v[66:67], 0\n"
        "v_mfma_f32_16x16x32_bf16 v[144:147], v[232:235], v[84:87], v[144:147]\n"
        "v_cndmask_b32 v132, v136, v132, s[44:45]\n"
        "v_cndmask_b32 v133, v137, v133, s[44:45]\n"
        "v_mfma_f32_16x16x32_bf16 v[144:147], v[236:239], v[88:91], v[144:147]\n"
        "v_cndmask_b32 v134, v138, v134, s[46:47]\n"
        "v_cndmask_b32 v135, v139, v135, s[46:47]\n"
        "v_mfma_f32_16x16x32_bf16 v[148:151], v[240:243], v[84:87], v[148:151]\n"
        "v_cndmask_b32 v140, v142, v140, s[48:49]\n"
        "v_cndmask_b32 v141, v143, v141, s[48:49]\n"
        "v_mfma_f32_16x16x32_bf16 v[148:151], v[244:247], v[88:91], v[148:151]\n"
        "v_max3_f32 v120, v120, v127, v132\n"
        "v_max3_f32 v120, v120, v133, v134\n"
        "v_mfma_f32_16x16x32_bf16 v[160:163], v[216:219], v[84:87], v[160:163]\n"
        "v_max3_f32 v120, v120, v135, v140\n"
        "v_max_f32 v120, v120, v141\n"
        "v_mfma_f32_16x16x32_bf16 v[160:163], v[220:223], v[88:91], v[160:163]\n"
        "v_mov_b32 v121, v120\n"
        "v_mov_b64 v[136:137], s[90:91]\n"
        "v_mfma_f32_16x16x32_bf16 v[188:191], v[216:219], v[108:111], v[188:191]\n"
        "ds_read_b128 v[208:211], v30 offset:0\n"
        "v_mov_b64 v[138:139], s[90:91]\n"
        "v_mfma_f32_16x16x32_bf16 v[188:191], v[220:223], v[112:115], v[188:191]\n"
        "ds_read_b128 v[212:215], v31 offset:0\n"
        "v_mfma_f32_16x16x32_bf16 v[164:167], v[224:227], v[84:87], v[164:167]\n"
        "v_permlane16_swap_b32 v120, v121\n"
        "v_mfma_f32_16x16x32_bf16 v[164:167], v[228:231], v[88:91], v[164:167]\n"
        "v_max_f32 v120, v120, v121\n"
        "v_mov_b32 v121, v120\n"
        "v_mfma_f32_16x16x32_bf16 v[192:195], v[224:227], v[108:111], v[192:195]\n"
        "ds_read_b128 v[232:235], v32 offset:0\n"
        "v_mfma_f32_16x16x32_bf16 v[192:195], v[228:231], v[112:115], v[192:195]\n"
        "ds_read_b128 v[236:239], v33 offset:0\n"
        "v_mfma_f32_16x16x32_bf16 v[168:171], v[200:203], v[84:87], v[168:171]\n"
        "v_permlane32_swap_b32 v120, v121\n"
        "v_mfma_f32_16x16x32_bf16 v[168:171], v[204:207], v[88:91], v[168:171]\n"
        "v_max_f32 v120, v120, v121\n"
        "v_sub_f32 v116, v116, v120\n"
        "v_mfma_f32_16x16x32_bf16 v[196:199], v[200:203], v[108:111], v[196:199]\n"
        "ds_read_b128 v[240:243], v34 offset:0\n"
        "v_sub_f32 v117, v117, v120\n"
        "v_mfma_f32_16x16x32_bf16 v[196:199], v[204:207], v[112:115], v[196:199]\n"
        "s_waitcnt lgkmcnt(4)\n"
        "ds_read_b128 v[244:247], v35 offset:0\n"
        "v_sub_f32 v118, v118, v120\n"
        "v_mfma_f32_16x16x32_bf16 v[152:155], v[208:211], v[68:71], v[152:155]\n"
        "s_waitcnt lgkmcnt(4)\n"
        "ds_read_b128 v[216:219], v36 offset:0\n"
        "v_sub_f32 v119, v119, v120\n"
        "v_mfma_f32_16x16x32_bf16 v[152:155], v[212:215], v[72:75], v[152:155]\n"
        "ds_read_b128 v[220:223], v37 offset:0\n"
        "v_sub_f32 v124, v124, v120\n"
        "v_mfma_f32_16x16x32_bf16 v[172:175], v[208:211], v[92:95], v[172:175]\n"
        "ds_read_b128 v[224:227], v30 offset:25600\n"
        "v_sub_f32 v125, v125, v120\n"
        "v_mfma_f32_16x16x32_bf16 v[172:175], v[212:215], v[96:99], v[172:175]\n"
        "s_waitcnt lgkmcnt(6)\n"
        "ds_read_b128 v[228:231], v31 offset:25600\n"
        "v_sub_f32 v126, v126, v120\n"
        "v_mfma_f32_16x16x32_bf16 v[156:159], v[232:235], v[68:71], v[156:159]\n"
        "s_waitcnt lgkmcnt(6)\n"
        "v_exp_f32 v116, v116\n"
        "v_mfma_f32_16x16x32_bf16 v[156:159], v[236:239], v[72:75], v[156:159]\n"
        "v_exp_f32 v117, v117\n"
        "v_mfma_f32_16x16x32_bf16 v[176:179], v[232:235], v[92:95], v[176:179]\n"
        "ds_read_b128 v[200:203], v32 offset:25600\n"
        "v_sub_f32 v127, v127, v120\n"
        "v_mfma_f32_16x16x32_bf16 v[176:179], v[236:239], v[96:99], v[176:179]\n"
        "s_waitcnt lgkmcnt(6)\n"
        "ds_read_b128 v[204:207], v33 offset:25600\n"
        "v_sub_f32 v132, v132, v120\n"
        "v_mfma_f32_16x16x32_bf16 v[180:183], v[240:243], v[92:95], v[180:183]\n"
        "s_waitcnt lgkmcnt(6)\n"
        "v_exp_f32 v118, v118\n"
        "v_mfma_f32_16x16x32_bf16 v[180:183], v[244:247], v[96:99], v[180:183]\n"
        "s_waitcnt lgkmcnt(5)\n"
        "v_exp_f32 v119, v119\n"
        "v_mfma_f32_16x16x32_bf16 v[184:187], v[216:219], v[92:95], v[184:187]\n"
        "s_waitcnt lgkmcnt(4)\n"
        "ds_read_b128 v[208:211], v34 offset:25600\n"
        "v_sub_f32 v133, v133, v120\n"
        "v_mfma_f32_16x16x32_bf16 v[184:187], v[220:223], v[96:99], v[184:187]\n"
        "s_waitcnt lgkmcnt(4)\n"
        "ds_read_b128 v[212:215], v35 offset:25600\n"
        "v_sub_f32 v134, v134, v120\n"
        "v_mfma_f32_16x16x32_bf16 v[152:155], v[224:227], v[76:79], v[152:155]\n"
        "s_waitcnt lgkmcnt(4)\n"
        "ds_read_b128 v[232:235], v36 offset:25600\n"
        "v_sub_f32 v135, v135, v120\n"
        "v_mfma_f32_16x16x32_bf16 v[152:155], v[228:231], v[80:83], v[152:155]\n"
        "ds_read_b128 v[236:239], v37 offset:25600\n"
        "v_sub_f32 v140, v140, v120\n"
        "v_mfma_f32_16x16x32_bf16 v[172:175], v[224:227], v[100:103], v[172:175]\n"
        "ds_read_b128 v[240:243], v30 offset:51200\n"
        "v_sub_f32 v141, v141, v120\n"
        "v_mfma_f32_16x16x32_bf16 v[172:175], v[228:231], v[104:107], v[172:175]\n"
        "s_waitcnt lgkmcnt(6)\n"
        "ds_read_b128 v[244:247], v31 offset:51200\n"
        "v_cvt_pk_bf16_f32 v54, v116, v117\n"
        "v_mfma_f32_16x16x32_bf16 v[156:159], v[200:203], v[76:79], v[156:159]\n"
        "s_waitcnt lgkmcnt(6)\n"
        "ds_read_b128 v[216:219], v32 offset:51200\n"
        "v_cndmask_b32 v52, 0, v54, s[40:41]\n"
        "v_mfma_f32_16x16x32_bf16 v[156:159], v[204:207], v[80:83], v[156:159]\n"
        "ds_read_b128 v[220:223], v33 offset:51200\n"
        "v_xor_b32 v54, v54, v52\n"
        "v_mfma_f32_16x16x32_bf16 v[176:179], v[200:203], v[100:103], v[176:179]\n"
        "v_exp_f32 v124, v124\n"
        "v_mfma_f32_16x16x32_bf16 v[176:179], v[204:207], v[104:107], v[176:179]\n"
        "s_waitcnt lgkmcnt(7)\n"
        "v_exp_f32 v125, v125\n"
        "v_mfma_f32_16x16x32_bf16 v[180:183], v[208:211], v[100:103], v[180:183]\n"
        "s_waitcnt lgkmcnt(6)\n"
        "v_exp_f32 v126, v126\n"
        "v_mfma_f32_16x16x32_bf16 v[180:183], v[212:215], v[104:107], v[180:183]\n"
        "s_waitcnt lgkmcnt(5)\n"
        "v_exp_f32 v127, v127\n"
        "v_mfma_f32_16x16x32_bf16 v[184:187], v[232:235], v[100:103], v[184:187]\n"
        "s_waitcnt lgkmcnt(4)\n"
        "ds_read_b128 v[224:227], v34 offset:51200\n"
        "v_cvt_pk_bf16_f32 v55, v118, v119\n"
        "v_mfma_f32_16x16x32_bf16 v[184:187], v[236:239], v[104:107], v[184:187]\n"
        "s_waitcnt lgkmcnt(4)\n"
        "ds_read_b128 v[228:231], v35 offset:51200\n"
        "v_cndmask_b32 v53, 0, v55, s[42:43]\n"
        "v_mfma_f32_16x16x32_bf16 v[152:155], v[240:243], v[84:87], v[152:155]\n"
        "s_waitcnt lgkmcnt(4)\n"
        "ds_read_b128 v[200:203], v36 offset:51200\n"
        "v_xor_b32 v55, v55, v53\n"
        "v_mfma_f32_16x16x32_bf16 v[152:155], v[244:247], v[88:91], v[152:155]\n"
        "s_waitcnt lgkmcnt(4)\n"
        "ds_read_b128 v[204:207], v37 offset:51200\n"
        "v_cvt_pk_bf16_f32 v58, v124, v125\n"
        "v_mfma_f32_16x16x32_bf16 v[156:159], v[216:219], v[84:87], v[156:159]\n"
        "s_waitcnt lgkmcnt(4)\n"
        "v_exp_f32 v132, v132\n"
        "v_mfma_f32_16x16x32_bf16 v[156:159], v[220:223], v[88:91], v[156:159]\n"
        "v_exp_f32 v133, v133\n"
        "v_mfma_f32_16x16x32_bf16 v[172:175], v[240:243], v[108:111], v[172:175]\n"
        "v_exp_f32 v134, v134\n"
        "v_mfma_f32_16x16x32_bf16 v[172:175], v[244:247], v[112:115], v[172:175]\n"
        "v_exp_f32 v135, v135\n"
        "v_mfma_f32_16x16x32_bf16 v[176:179], v[216:219], v[108:111], v[176:179]\n"
        "v_exp_f32 v140, v140\n"
        "v_mfma_f32_16x16x32_bf16 v[176:179], v[220:223], v[112:115], v[176:179]\n"
        "s_waitcnt lgkmcnt(3)\n"
        "v_exp_f32 v141, v141\n"
        "v_mfma_f32_16x16x32_bf16 v[180:183], v[224:227], v[108:111], v[180:183]\n"
        "s_waitcnt lgkmcnt(2)\n"
        "v_cndmask_b32 v56, 0, v58, s[40:41]\n"
        "v_xor_b32 v58, v58, v56\n"
        "v_mfma_f32_16x16x32_bf16 v[180:183], v[228:231], v[112:115], v[180:183]\n"
        "s_waitcnt lgkmcnt(1)\n"
        "v_cvt_pk_bf16_f32 v59, v126, v127\n"
        "v_cndmask_b32 v57, 0, v59, s[42:43]\n"
        "v_mfma_f32_16x16x32_bf16 v[184:187], v[200:203], v[108:111], v[184:187]\n"
        "s_waitcnt lgkmcnt(0)\n"
        "v_xor_b32 v59, v59, v57\n"
        "v_cvt_pk_bf16_f32 v62, v132, v133\n"
        "v_mfma_f32_16x16x32_bf16 v[184:187], v[204:207], v[112:115], v[184:187]\n"
        "ds_read_b64_tr_b16 v[200:201], v10 offset:0\n"
        "ds_read_b64_tr_b16 v[202:203], v11 offset:0\n"
        "ds_read_b64_tr_b16 v[204:205], v12 offset:0\n"
        "ds_read_b64_tr_b16 v[206:207], v13 offset:0\n"
        "ds_read_b64_tr_b16 v[216:217], v18 offset:0\n"
        "ds_read_b64_tr_b16 v[218:219], v19 offset:0\n"
        "ds_read_b64_tr_b16 v[220:221], v20 offset:0\n"
        "ds_read_b64_tr_b16 v[208:209], v14 offset:0\n"
        "ds_read_b64_tr_b16 v[210:211], v15 offset:0\n"
        "ds_read_b64_tr_b16 v[212:213], v16 offset:0\n"
        "ds_read_b64_tr_b16 v[214:215], v17 offset:0\n"
        "v_cndmask_b32 v60, 0, v62, s[44:45]\n"
        "v_xor_b32 v62, v62, v60\n"
        "v_cvt_pk_bf16_f32 v63, v134, v135\n"
        "v_cndmask_b32 v61, 0, v63, s[46:47]\n"
        "v_xor_b32 v63, v63, v61\n"
        "v_cvt_pk_bf16_f32 v65, v140, v141\n"
        "v_cndmask_b32 v64, 0, v65, s[48:49]\n"
        "v_xor_b32 v65, v65, v64\n"
        "s_not_b64 exec, s[44:45]\n"
        "v_mov_b64 v[160:161], v[164:165]\n"
        "v_mov_b64 v[188:189], v[192:193]\n"
        "s_not_b64 exec, s[46:47]\n"
        "v_mov_b64 v[162:163], v[166:167]\n"
        "v_mov_b64 v[190:191], v[194:195]\n"
        "s_not_b64 exec, s[48:49]\n"
        "v_mov_b64 v[168:169], v[170:171]\n"
        "v_mov_b64 v[196:197], v[198:199]\n"
        "s_not_b64 exec, s[40:41]\n"
        "v_mov_b64 v[144:145], v[148:149]\n"
        "v_mov_b64 v[152:153], v[156:157]\n"
        "v_mov_b64 v[172:173], v[176:177]\n"
        "v_mov_b64 v[180:181], v[184:185]\n"
        "s_not_b64 exec, s[42:43]\n"
        "v_mov_b64 v[146:147], v[150:151]\n"
        "v_mov_b64 v[154:155], v[158:159]\n"
        "v_mov_b64 v[174:175], v[178:179]\n"
        "v_mov_b64 v[182:183], v[186:187]\n"
        "s_mov_b64 exec, -1\n"
        "v_max3_f32 v148, v144, v145, v146\n"
        "v_max3_f32 v176, v172, v173, v174\n"
        "v_max3_f32 v148, v148, v147, v152\n"
        "v_max3_f32 v176, v176, v175, v180\n"
        "v_max3_f32 v148, v148, v153, v154\n"
        "v_max3_f32 v176, v176, v181, v182\n"
        "v_max3_f32 v148, v148, v155, v160\n"
        "v_max3_f32 v176, v176, v183, v188\n"
        "v_max3_f32 v148, v148, v161, v162\n"
        "v_max3_f32 v176, v176, v189, v190\n"
        "v_max3_f32 v148, v148, v163, v168\n"
        "v_max3_f32 v176, v176, v191, v196\n"
        "v_max_f32 v148, v148, v169\n"
        "v_max_f32 v176, v176, v197\n"
        "v_mov_b32 v149, v148\n"
        "v_mov_b32 v177, v176\n"
        "v_mov_b64 v[82:83], 0\n"
        "v_permlane16_swap_b32 v148, v149\n"
        "v_permlane16_swap_b32 v176, v177\n"
        "v_max_f32 v148, v148, v149\n"
        "v_max_f32 v176, v176, v177\n"
        "v_mov_b32 v149, v148\n"
        "v_mov_b32 v177, v176\n"
        "v_mov_b64 v[106:107], 0\n"
        "v_permlane32_swap_b32 v148, v149\n"
        "v_permlane32_swap_b32 v176, v177\n"
        "v_max_f32 v148, v148, v149\n"
        "v_max_f32 v176, v176, v177\n"
        "v_pk_add_f32 v[144:145], v[144:145], v[148:149] op_sel_hi:[1,0] neg_lo:[0,1] neg_hi:[0,1]\n"
        "v_pk_add_f32 v[146:147], v[146:147], v[148:149] op_sel_hi:[1,0] neg_lo:[0,1] neg_hi:[0,1]\n"
        "v_pk_add_f32 v[152:153], v[152:153], v[148:149] op_sel_hi:[1,0] neg_lo:[0,1] neg_hi:[0,1]\n"
        "v_pk_add_f32 v[154:155], v[154:155], v[148:149] op_sel_hi:[1,0] neg_lo:[0,1] neg_hi:[0,1]\n"
        "v_pk_add_f32 v[160:161], v[160:161], v[148:149] op_sel_hi:[1,0] neg_lo:[0,1] neg_hi:[0,1]\n"
        "v_pk_add_f32 v[162:163], v[162:163], v[148:149] op_sel_hi:[1,0] neg_lo:[0,1] neg_hi:[0,1]\n"
        "v_pk_add_f32 v[168:169], v[168:169], v[148:149] op_sel_hi:[1,0] neg_lo:[0,1] neg_hi:[0,1]\n"
        "v_pk_add_f32 v[172:173], v[172:173], v[176:177] op_sel_hi:[1,0] neg_lo:[0,1] neg_hi:[0,1]\n"
        "v_pk_add_f32 v[174:175], v[174:175], v[176:177] op_sel_hi:[1,0] neg_lo:[0,1] neg_hi:[0,1]\n"
        "v_pk_add_f32 v[180:181], v[180:181], v[176:177] op_sel_hi:[1,0] neg_lo:[0,1] neg_hi:[0,1]\n"
        "v_pk_add_f32 v[182:183], v[182:183], v[176:177] op_sel_hi:[1,0] neg_lo:[0,1] neg_hi:[0,1]\n"
        "v_pk_add_f32 v[188:189], v[188:189], v[176:177] op_sel_hi:[1,0] neg_lo:[0,1] neg_hi:[0,1]\n"
        "v_pk_add_f32 v[190:191], v[190:191], v[176:177] op_sel_hi:[1,0] neg_lo:[0,1] neg_hi:[0,1]\n"
        "v_pk_add_f32 v[196:197], v[196:197], v[176:177] op_sel_hi:[1,0] neg_lo:[0,1] neg_hi:[0,1]\n"
        "v_mfma_f32_16x16x32_bf16 v[108:111], v[52:55], v[136:139], 0\n"
        "ds_read_b64_tr_b16 v[224:225], v44 offset:0\n"
        "ds_read_b64_tr_b16 v[226:227], v45 offset:0\n"
        "ds_read_b64_tr_b16 v[228:229], v46 offset:0\n"
        "v_exp_f32 v144, v144\n"
        "v_mfma_f32_16x16x32_bf16 v[108:111], v[56:59], v[136:139], v[108:111]\n"
        "s_waitcnt lgkmcnt(12)\n"
        "ds_read_b64_tr_b16 v[230:231], v47 offset:0\n"
        "ds_read_b64_tr_b16 v[240:241], v21 offset:0\n"
        "ds_read_b64_tr_b16 v[242:243], v252 offset:0\n"
        "v_exp_f32 v145, v145\n"
        "v_mfma_f32_16x16x32_bf16 v[108:111], v[60:63], v[136:139], v[108:111]\n"
        "s_waitcnt lgkmcnt(12)\n"
        "ds_read_b64_tr_b16 v[244:245], v253 offset:0\n"
        "ds_read_b64_tr_b16 v[232:233], v48 offset:0\n"
        "ds_read_b64_tr_b16 v[234:235], v49 offset:0\n"
        "v_exp_f32 v146, v146\n"
        "v_mfma_f32_16x16x32_bf16 v[108:111], v[64:67], v[136:139], v[108:111]\n"
        "s_waitcnt lgkmcnt(13)\n"
        "ds_read_b64_tr_b16 v[236:237], v50 offset:0\n"
        "ds_read_b64_tr_b16 v[238:239], v51 offset:0\n"
        "v_exp_f32 v147, v147\n"
        "v_exp_f32 v152, v152\n"
        "v_mfma_f32_16x16x32_bf16 v[124:127], v[200:203], v[52:55], 0\n"
        "v_exp_f32 v153, v153\n"
        "v_exp_f32 v154, v154\n"
        "v_mfma_f32_16x16x32_bf16 v[124:127], v[204:207], v[56:59], v[124:127]\n"
        "v_exp_f32 v155, v155\n"
        "v_exp_f32 v160, v160\n"
        "v_exp_f32 v161, v161\n"
        "v_mfma_f32_16x16x32_bf16 v[124:127], v[216:219], v[60:63], v[124:127]\n"
        "v_exp_f32 v162, v162\n"
        "v_exp_f32 v163, v163\n"
        "v_mfma_f32_16x16x32_bf16 v[124:127], v[220:223], v[64:67], v[124:127]\n"
        "s_waitcnt lgkmcnt(9)\n"
        "v_exp_f32 v168, v168\n"
        "v_exp_f32 v169, v169\n"
        "v_cvt_pk_bf16_f32 v70, v144, v145\n"
        "v_mfma_f32_16x16x32_bf16 v[128:131], v[224:227], v[52:55], 0\n"
        "s_waitcnt lgkmcnt(7)\n"
        "v_cvt_pk_bf16_f32 v71, v146, v147\n"
        "v_cvt_pk_bf16_f32 v74, v152, v153\n"
        "v_cvt_pk_bf16_f32 v75, v154, v155\n"
        "v_cvt_pk_bf16_f32 v78, v160, v161\n"
        "v_cvt_pk_bf16_f32 v79, v162, v163\n"
        "v_mfma_f32_16x16x32_bf16 v[128:131], v[228:231], v[56:59], v[128:131]\n"
        "s_waitcnt lgkmcnt(5)\n"
        "v_cvt_pk_bf16_f32 v81, v168, v169\n"
        "v_cndmask_b32 v68, 0, v70, s[40:41]\n"
        "v_cndmask_b32 v69, 0, v71, s[42:43]\n"
        "v_cndmask_b32 v72, 0, v74, s[40:41]\n"
        "v_cndmask_b32 v73, 0, v75, s[42:43]\n"
        "v_mfma_f32_16x16x32_bf16 v[128:131], v[240:243], v[60:63], v[128:131]\n"
        "s_waitcnt lgkmcnt(4)\n"
        "v_cndmask_b32 v76, 0, v78, s[44:45]\n"
        "v_cndmask_b32 v77, 0, v79, s[46:47]\n"
        "v_cndmask_b32 v80, 0, v81, s[48:49]\n"
        "v_xor_b32 v70, v70, v68\n"
        "v_xor_b32 v71, v71, v69\n"
        "v_mfma_f32_16x16x32_bf16 v[128:131], v[244:247], v[64:67], v[128:131]\n"
        "v_xor_b32 v74, v74, v72\n"
        "v_xor_b32 v75, v75, v73\n"
        "v_xor_b32 v78, v78, v76\n"
        "v_xor_b32 v79, v79, v77\n"
        "v_xor_b32 v81, v81, v80\n"
        "v_mfma_f32_16x16x32_bf16 v[132:135], v[204:207], v[68:71], 0\n"
        "v_exp_f32 v172, v172\n"
        "v_exp_f32 v173, v173\n"
        "v_exp_f32 v174, v174\n"
        "v_mfma_f32_16x16x32_bf16 v[132:135], v[208:211], v[72:75], v[132:135]\n"
        "v_exp_f32 v175, v175\n"
        "v_exp_f32 v180, v180\n"
        "v_mfma_f32_16x16x32_bf16 v[132:135], v[216:219], v[76:79], v[132:135]\n"
        "v_exp_f32 v181, v181\n"
        "v_exp_f32 v182, v182\n"
        "v_mfma_f32_16x16x32_bf16 v[132:135], v[220:223], v[80:83], v[132:135]\n"
        "v_cvt_pk_bf16_f32 v116, v124, v125\n"
        "v_cvt_pk_bf16_f32 v117, v126, v127\n"
        "v_exp_f32 v183, v183\n"
        "v_exp_f32 v188, v188\n"
        "v_mfma_f32_16x16x32_bf16 v[124:127], v[228:231], v[68:71], 0\n"
        "s_waitcnt lgkmcnt(2)\n"
        "v_exp_f32 v189, v189\n"
        "v_exp_f32 v190, v190\n"
        "v_mfma_f32_16x16x32_bf16 v[124:127], v[232:235], v[72:75], v[124:127]\n"
        "v_exp_f32 v191, v191\n"
        "v_exp_f32 v196, v196\n"
        "v_mfma_f32_16x16x32_bf16 v[124:127], v[240:243], v[76:79], v[124:127]\n"
        "v_exp_f32 v197, v197\n"
        "v_cvt_pk_bf16_f32 v94, v172, v173\n"
        "v_mfma_f32_16x16x32_bf16 v[124:127], v[244:247], v[80:83], v[124:127]\n"
        "v_cvt_pk_bf16_f32 v118, v128, v129\n"
        "v_cvt_pk_bf16_f32 v119, v130, v131\n"
        "v_cvt_pk_bf16_f32 v95, v174, v175\n"
        "v_cvt_pk_bf16_f32 v98, v180, v181\n"
        "v_cvt_pk_bf16_f32 v99, v182, v183\n"
        "v_cvt_pk_bf16_f32 v102, v188, v189\n"
        "v_mfma_f32_16x16x32_bf16 v[112:115], v[68:71], v[136:139], 0\n"
        "v_cvt_pk_bf16_f32 v103, v190, v191\n"
        "v_cvt_pk_bf16_f32 v105, v196, v197\n"
        "v_cndmask_b32 v92, 0, v94, s[40:41]\n"
        "v_cndmask_b32 v93, 0, v95, s[42:43]\n"
        "v_mfma_f32_16x16x32_bf16 v[112:115], v[72:75], v[136:139], v[112:115]\n"
        "v_cndmask_b32 v96, 0, v98, s[40:41]\n"
        "v_cndmask_b32 v97, 0, v99, s[42:43]\n"
        "v_cndmask_b32 v100, 0, v102, s[44:45]\n"
        "v_cndmask_b32 v101, 0, v103, s[46:47]\n"
        "v_mfma_f32_16x16x32_bf16 v[112:115], v[76:79], v[136:139], v[112:115]\n"
        "v_cndmask_b32 v104, 0, v105, s[48:49]\n"
        "v_xor_b32 v94, v94, v92\n"
        "v_xor_b32 v95, v95, v93\n"
        "v_xor_b32 v98, v98, v96\n"
        "v_mfma_f32_16x16x32_bf16 v[112:115], v[80:83], v[136:139], v[112:115]\n"
        "v_cvt_pk_bf16_f32 v140, v132, v133\n"
        "v_cvt_pk_bf16_f32 v141, v134, v135\n"
        "v_xor_b32 v99, v99, v97\n"
        "v_xor_b32 v102, v102, v100\n"
        "v_xor_b32 v103, v103, v101\n"
        "v_xor_b32 v105, v105, v104\n"
        "v_mfma_f32_16x16x32_bf16 v[128:131], v[208:211], v[92:95], 0\n"
        "v_cvt_pk_bf16_f32 v142, v124, v125\n"
        "v_cvt_pk_bf16_f32 v143, v126, v127\n"
        "v_mfma_f32_16x16x32_bf16 v[128:131], v[212:215], v[96:99], v[128:131]\n"
        "v_xor_b32 v22, 64, v10\n"
        "v_xor_b32 v23, 64, v11\n"
        "v_mfma_f32_16x16x32_bf16 v[128:131], v[216:219], v[100:103], v[128:131]\n"
        "v_xor_b32 v24, 64, v12\n"
        "v_xor_b32 v25, 64, v13\n"
        "v_mfma_f32_16x16x32_bf16 v[128:131], v[220:223], v[104:107], v[128:131]\n"
        "v_xor_b32 v38, 64, v18\n"
        "v_xor_b32 v39, 64, v19\n"
        "v_mfma_f32_16x16x32_bf16 v[132:135], v[232:235], v[92:95], 0\n"
        "s_waitcnt lgkmcnt(0)\n"
        "ds_read_b64_tr_b16 v[200:201], v22 offset:0\n"
        "v_xor_b32 v40, 64, v20\n"
        "v_mfma_f32_16x16x32_bf16 v[132:135], v[236:239], v[96:99], v[132:135]\n"
        "ds_read_b64_tr_b16 v[202:203], v23 offset:0\n"
        "v_xor_b32 v26, 64, v14\n"
        "v_mfma_f32_16x16x32_bf16 v[132:135], v[240:243], v[100:103], v[132:135]\n"
        "ds_read_b64_tr_b16 v[204:205], v24 offset:0\n"
        "v_xor_b32 v27, 64, v15\n"
        "v_mfma_f32_16x16x32_bf16 v[132:135], v[244:247], v[104:107], v[132:135]\n"
        "ds_read_b64_tr_b16 v[206:207], v25 offset:0\n"
        "v_cvt_pk_bf16_f32 v164, v128, v129\n"
        "v_mfma_f32_16x16x32_bf16 v[248:251], v[92:95], v[136:139], 0\n"
        "ds_read_b64_tr_b16 v[216:217], v38 offset:0\n"
        "ds_read_b64_tr_b16 v[218:219], v39 offset:0\n"
        "v_mfma_f32_16x16x32_bf16 v[248:251], v[96:99], v[136:139], v[248:251]\n"
        "ds_read_b64_tr_b16 v[220:221], v40 offset:0\n"
        "v_cvt_pk_bf16_f32 v165, v130, v131\n"
        "v_mfma_f32_16x16x32_bf16 v[248:251], v[100:103], v[136:139], v[248:251]\n"
        "ds_read_b64_tr_b16 v[208:209], v26 offset:0\n"
        "v_xor_b32 v28, 64, v16\n"
        "v_mfma_f32_16x16x32_bf16 v[248:251], v[104:107], v[136:139], v[248:251]\n"
        "s_waitcnt lgkmcnt(6)\n"
        "ds_read_b64_tr_b16 v[210:211], v27 offset:0\n"
        "v_xor_b32 v29, 64, v17\n"
        "v_mfma_f32_16x16x32_bf16 v[188:191], v[200:203], v[52:55], 0\n"
        "s_waitcnt lgkmcnt(5)\n"
        "v_cvt_pk_bf16_f32 v166, v132, v133\n"
        "v_cvt_pk_bf16_f32 v167, v134, v135\n"
        "v_mfma_f32_16x16x32_bf16 v[188:191], v[204:207], v[56:59], v[188:191]\n"
        "s_waitcnt lgkmcnt(3)\n"
        "ds_read_b64_tr_b16 v[212:213], v28 offset:0\n"
        "v_xor_b32 v41, 96, v10\n"
        "v_mfma_f32_16x16x32_bf16 v[188:191], v[216:219], v[60:63], v[188:191]\n"
        "s_waitcnt lgkmcnt(3)\n"
        "ds_read_b64_tr_b16 v[214:215], v29 offset:0\n"
        "v_xor_b32 v42, 96, v11\n"
        "v_mfma_f32_16x16x32_bf16 v[188:191], v[220:223], v[64:67], v[188:191]\n"
        "v_xor_b32 v43, 96, v12\n"
        "v_xor_b32 v30, 96, v13\n"
        "v_mfma_f32_16x16x32_bf16 v[192:195], v[204:207], v[68:71], 0\n"
        "s_waitcnt lgkmcnt(2)\n"
        "v_rcp_f32 v108, v108\n"
        "v_mfma_f32_16x16x32_bf16 v[192:195], v[208:211], v[72:75], v[192:195]\n"
        "v_rcp_f32 v109, v109\n"
        "v_mfma_f32_16x16x32_bf16 v[192:195], v[216:219], v[76:79], v[192:195]\n"
        "ds_read_b64_tr_b16 v[224:225], v41 offset:0\n"
        "ds_read_b64_tr_b16 v[226:227], v42 offset:0\n"
        "v_xor_b32 v35, 96, v18\n"
        "v_xor_b32 v36, 96, v19\n"
        "v_mfma_f32_16x16x32_bf16 v[192:195], v[220:223], v[80:83], v[192:195]\n"
        "ds_read_b64_tr_b16 v[228:229], v43 offset:0\n"
        "ds_read_b64_tr_b16 v[230:231], v30 offset:0\n"
        "v_xor_b32 v37, 96, v20\n"
        "v_mfma_f32_16x16x32_bf16 v[196:199], v[208:211], v[92:95], 0\n"
        "s_waitcnt lgkmcnt(4)\n"
        "ds_read_b64_tr_b16 v[240:241], v35 offset:0\n"
        "ds_read_b64_tr_b16 v[242:243], v36 offset:0\n"
        "v_mfma_f32_16x16x32_bf16 v[196:199], v[212:215], v[96:99], v[196:199]\n"
        "ds_read_b64_tr_b16 v[244:245], v37 offset:0\n"
        "v_xor_b32 v31, 96, v14\n"
        "v_xor_b32 v32, 96, v15\n"
        "v_mfma_f32_16x16x32_bf16 v[196:199], v[216:219], v[100:103], v[196:199]\n"
        "v_rcp_f32 v110, v110\n"
        "v_mfma_f32_16x16x32_bf16 v[196:199], v[220:223], v[104:107], v[196:199]\n"
        "s_waitcnt lgkmcnt(5)\n"
        "ds_read_b64_tr_b16 v[232:233], v31 offset:0\n"
        "ds_read_b64_tr_b16 v[234:235], v32 offset:0\n"
        "v_cvt_pk_bf16_f32 v120, v188, v189\n"
        "v_cvt_pk_bf16_f32 v121, v190, v191\n"
        "v_mfma_f32_16x16x32_bf16 v[188:191], v[224:227], v[52:55], 0\n"
        "s_waitcnt lgkmcnt(5)\n"
        "v_rcp_f32 v111, v111\n"
        "v_mfma_f32_16x16x32_bf16 v[188:191], v[228:231], v[56:59], v[188:191]\n"
        "s_waitcnt lgkmcnt(3)\n"
        "v_xor_b32 v33, 96, v16\n"
        "v_xor_b32 v34, 96, v17\n"
        "v_mfma_f32_16x16x32_bf16 v[188:191], v[240:243], v[60:63], v[188:191]\n"
        "s_waitcnt lgkmcnt(2)\n"
        "v_cvt_pk_bf16_f32 v144, v192, v193\n"
        "v_cvt_pk_bf16_f32 v145, v194, v195\n"
        "v_mfma_f32_16x16x32_bf16 v[188:191], v[244:247], v[64:67], v[188:191]\n"
        "ds_read_b64_tr_b16 v[236:237], v33 offset:0\n"
        "ds_read_b64_tr_b16 v[238:239], v34 offset:0\n"
        "v_mfma_f32_16x16x32_bf16 v[192:195], v[228:231], v[68:71], 0\n"
        "s_waitcnt lgkmcnt(2)\n"
        "ds_write_b8 v3, v4 offset:32\n"
        "v_mfma_f32_16x16x32_bf16 v[192:195], v[232:235], v[72:75], v[192:195]\n"
        "ds_read_b64_tr_b16 v[200:201], v10 offset:25600\n"
        "v_cvt_pk_bf16_f32 v168, v196, v197\n"
        "v_mfma_f32_16x16x32_bf16 v[192:195], v[240:243], v[76:79], v[192:195]\n"
        "ds_read_b64_tr_b16 v[202:203], v11 offset:25600\n"
        "v_cvt_pk_bf16_f32 v169, v198, v199\n"
        "v_mfma_f32_16x16x32_bf16 v[192:195], v[244:247], v[80:83], v[192:195]\n"
        "ds_read_b64_tr_b16 v[204:205], v12 offset:25600\n"
        "ds_read_b64_tr_b16 v[206:207], v13 offset:25600\n"
        "v_mfma_f32_16x16x32_bf16 v[196:199], v[232:235], v[92:95], 0\n"
        "s_waitcnt lgkmcnt(5)\n"
        "ds_read_b64_tr_b16 v[216:217], v18 offset:25600\n"
        "ds_read_b64_tr_b16 v[218:219], v19 offset:25600\n"
        "v_mfma_f32_16x16x32_bf16 v[196:199], v[236:239], v[96:99], v[196:199]\n"
        "ds_read_b64_tr_b16 v[220:221], v20 offset:25600\n"
        "v_cvt_pk_bf16_f32 v122, v188, v189\n"
        "v_mfma_f32_16x16x32_bf16 v[196:199], v[240:243], v[100:103], v[196:199]\n"
        "ds_read_b64_tr_b16 v[208:209], v14 offset:25600\n"
        "v_cvt_pk_bf16_f32 v123, v190, v191\n"
        "v_mfma_f32_16x16x32_bf16 v[196:199], v[244:247], v[104:107], v[196:199]\n"
        "s_waitcnt lgkmcnt(6)\n"
        "ds_read_b64_tr_b16 v[210:211], v15 offset:25600\n"
        "v_cvt_pk_bf16_f32 v146, v192, v193\n"
        "v_mfma_f32_16x16x32_bf16 v[188:191], v[200:203], v[52:55], 0\n"
        "s_waitcnt lgkmcnt(5)\n"
        "v_rcp_f32 v112, v112\n"
        "v_mfma_f32_16x16x32_bf16 v[188:191], v[204:207], v[56:59], v[188:191]\n"
        "s_waitcnt lgkmcnt(3)\n"
        "v_rcp_f32 v113, v113\n"
        "v_mfma_f32_16x16x32_bf16 v[188:191], v[216:219], v[60:63], v[188:191]\n"
        "s_waitcnt lgkmcnt(2)\n"
        "v_rcp_f32 v114, v114\n"
        "v_mfma_f32_16x16x32_bf16 v[188:191], v[220:223], v[64:67], v[188:191]\n"
        "ds_read_b64_tr_b16 v[212:213], v16 offset:25600\n"
        "ds_read_b64_tr_b16 v[214:215], v17 offset:25600\n"
        "v_cvt_pk_bf16_f32 v147, v194, v195\n"
        "v_mfma_f32_16x16x32_bf16 v[192:195], v[204:207], v[68:71], 0\n"
        "s_waitcnt lgkmcnt(2)\n"
        "v_rcp_f32 v115, v115\n"
        "v_mfma_f32_16x16x32_bf16 v[192:195], v[208:211], v[72:75], v[192:195]\n"
        "ds_read_b64_tr_b16 v[224:225], v44 offset:25600\n"
        "v_cvt_pk_bf16_f32 v170, v196, v197\n"
        "v_mfma_f32_16x16x32_bf16 v[192:195], v[216:219], v[76:79], v[192:195]\n"
        "ds_read_b64_tr_b16 v[226:227], v45 offset:25600\n"
        "v_cvt_pk_bf16_f32 v171, v198, v199\n"
        "v_mfma_f32_16x16x32_bf16 v[192:195], v[220:223], v[80:83], v[192:195]\n"
        "ds_read_b64_tr_b16 v[228:229], v46 offset:25600\n"
        "ds_read_b64_tr_b16 v[230:231], v47 offset:25600\n"
        "v_mfma_f32_16x16x32_bf16 v[196:199], v[208:211], v[92:95], 0\n"
        "s_waitcnt lgkmcnt(4)\n"
        "ds_read_b64_tr_b16 v[240:241], v21 offset:25600\n"
        "ds_read_b64_tr_b16 v[242:243], v252 offset:25600\n"
        "v_mfma_f32_16x16x32_bf16 v[196:199], v[212:215], v[96:99], v[196:199]\n"
        "ds_read_b64_tr_b16 v[244:245], v253 offset:25600\n"
        "v_cvt_pk_bf16_f32 v124, v188, v189\n"
        "v_mfma_f32_16x16x32_bf16 v[196:199], v[216:219], v[100:103], v[196:199]\n"
        "ds_read_b64_tr_b16 v[232:233], v48 offset:25600\n"
        "v_cvt_pk_bf16_f32 v125, v190, v191\n"
        "v_mfma_f32_16x16x32_bf16 v[196:199], v[220:223], v[104:107], v[196:199]\n"
        "s_waitcnt lgkmcnt(6)\n"
        "ds_read_b64_tr_b16 v[234:235], v49 offset:25600\n"
        "v_cvt_pk_bf16_f32 v148, v192, v193\n"
        "v_mfma_f32_16x16x32_bf16 v[188:191], v[224:227], v[52:55], 0\n"
        "s_waitcnt lgkmcnt(5)\n"
        "v_rcp_f32 v248, v248\n"
        "v_mfma_f32_16x16x32_bf16 v[188:191], v[228:231], v[56:59], v[188:191]\n"
        "s_waitcnt lgkmcnt(3)\n"
        "v_rcp_f32 v249, v249\n"
        "v_mfma_f32_16x16x32_bf16 v[188:191], v[240:243], v[60:63], v[188:191]\n"
        "s_waitcnt lgkmcnt(2)\n"
        "v_rcp_f32 v250, v250\n"
        "v_mfma_f32_16x16x32_bf16 v[188:191], v[244:247], v[64:67], v[188:191]\n"
        "ds_read_b64_tr_b16 v[236:237], v50 offset:25600\n"
        "ds_read_b64_tr_b16 v[238:239], v51 offset:25600\n"
        "v_cvt_pk_bf16_f32 v149, v194, v195\n"
        "v_mfma_f32_16x16x32_bf16 v[192:195], v[228:231], v[68:71], 0\n"
        "s_waitcnt lgkmcnt(2)\n"
        "v_rcp_f32 v251, v251\n"
        "v_mfma_f32_16x16x32_bf16 v[192:195], v[232:235], v[72:75], v[192:195]\n"
        "ds_read_b64_tr_b16 v[200:201], v22 offset:25600\n"
        "v_cvt_pk_bf16_f32 v172, v196, v197\n"
        "v_mfma_f32_16x16x32_bf16 v[192:195], v[240:243], v[76:79], v[192:195]\n"
        "ds_read_b64_tr_b16 v[202:203], v23 offset:25600\n"
        "v_cvt_pk_bf16_f32 v173, v198, v199\n"
        "v_mfma_f32_16x16x32_bf16 v[192:195], v[244:247], v[80:83], v[192:195]\n"
        "ds_read_b64_tr_b16 v[204:205], v24 offset:25600\n"
        "ds_read_b64_tr_b16 v[206:207], v25 offset:25600\n"
        "v_mfma_f32_16x16x32_bf16 v[196:199], v[232:235], v[92:95], 0\n"
        "s_waitcnt lgkmcnt(4)\n"
        "ds_read_b64_tr_b16 v[216:217], v38 offset:25600\n"
        "ds_read_b64_tr_b16 v[218:219], v39 offset:25600\n"
        "v_mfma_f32_16x16x32_bf16 v[196:199], v[236:239], v[96:99], v[196:199]\n"
        "ds_read_b64_tr_b16 v[220:221], v40 offset:25600\n"
        "v_cvt_pk_bf16_f32 v126, v188, v189\n"
        "v_mfma_f32_16x16x32_bf16 v[196:199], v[240:243], v[100:103], v[196:199]\n"
        "ds_read_b64_tr_b16 v[208:209], v26 offset:25600\n"
        "v_cvt_pk_bf16_f32 v127, v190, v191\n"
        "v_mfma_f32_16x16x32_bf16 v[196:199], v[244:247], v[104:107], v[196:199]\n"
        "s_waitcnt lgkmcnt(6)\n"
        "ds_read_b64_tr_b16 v[210:211], v27 offset:25600\n"
        "v_cvt_pk_bf16_f32 v150, v192, v193\n"
        "v_mfma_f32_16x16x32_bf16 v[188:191], v[200:203], v[52:55], 0\n"
        "s_waitcnt lgkmcnt(5)\n"
        "ds_read_b64_tr_b16 v[212:213], v28 offset:25600\n"
        "v_cvt_pk_bf16_f32 v151, v194, v195\n"
        "v_mfma_f32_16x16x32_bf16 v[188:191], v[204:207], v[56:59], v[188:191]\n"
        "s_waitcnt lgkmcnt(4)\n"
        "ds_read_b64_tr_b16 v[214:215], v29 offset:25600\n"
        "v_cvt_pk_bf16_f32 v174, v196, v197\n"
        "v_mfma_f32_16x16x32_bf16 v[188:191], v[216:219], v[60:63], v[188:191]\n"
        "s_waitcnt lgkmcnt(4)\n"
        "ds_read_b64_tr_b16 v[224:225], v41 offset:25600\n"
        "v_cvt_pk_bf16_f32 v175, v198, v199\n"
        "v_mfma_f32_16x16x32_bf16 v[188:191], v[220:223], v[64:67], v[188:191]\n"
        "ds_read_b64_tr_b16 v[226:227], v42 offset:25600\n"
        "v_mfma_f32_16x16x32_bf16 v[192:195], v[204:207], v[68:71], 0\n"
        "s_waitcnt lgkmcnt(4)\n"
        "ds_read_b64_tr_b16 v[228:229], v43 offset:25600\n"
        "v_mfma_f32_16x16x32_bf16 v[192:195], v[208:211], v[72:75], v[192:195]\n"
        "ds_read_b64_tr_b16 v[230:231], v30 offset:25600\n"
        "v_mfma_f32_16x16x32_bf16 v[192:195], v[216:219], v[76:79], v[192:195]\n"
        "ds_read_b64_tr_b16 v[240:241], v35 offset:25600\n"
        "v_cvt_pk_bf16_f32 v128, v188, v189\n"
        "v_mfma_f32_16x16x32_bf16 v[192:195], v[220:223], v[80:83], v[192:195]\n"
        "ds_read_b64_tr_b16 v[242:243], v36 offset:25600\n"
        "v_cvt_pk_bf16_f32 v129, v190, v191\n"
        "v_mfma_f32_16x16x32_bf16 v[196:199], v[208:211], v[92:95], 0\n"
        "s_waitcnt lgkmcnt(6)\n"
        "ds_read_b64_tr_b16 v[244:245], v37 offset:25600\n"
        "v_mfma_f32_16x16x32_bf16 v[196:199], v[212:215], v[96:99], v[196:199]\n"
        "ds_read_b64_tr_b16 v[232:233], v31 offset:25600\n"
        "v_mfma_f32_16x16x32_bf16 v[196:199], v[216:219], v[100:103], v[196:199]\n"
        "ds_read_b64_tr_b16 v[234:235], v32 offset:25600\n"
        "v_cvt_pk_bf16_f32 v152, v192, v193\n"
        "v_mfma_f32_16x16x32_bf16 v[196:199], v[220:223], v[104:107], v[196:199]\n"
        "s_waitcnt lgkmcnt(7)\n"
        "ds_read_b64_tr_b16 v[236:237], v33 offset:25600\n"
        "v_cvt_pk_bf16_f32 v153, v194, v195\n"
        "v_mfma_f32_16x16x32_bf16 v[188:191], v[224:227], v[52:55], 0\n"
        "s_waitcnt lgkmcnt(6)\n"
        "ds_read_b64_tr_b16 v[238:239], v34 offset:25600\n"
        "v_mfma_f32_16x16x32_bf16 v[188:191], v[228:231], v[56:59], v[188:191]\n"
        "s_waitcnt lgkmcnt(5)\n"
        "ds_write_b8 v3, v4 offset:36\n"
        "v_mfma_f32_16x16x32_bf16 v[188:191], v[240:243], v[60:63], v[188:191]\n"
        "s_waitcnt lgkmcnt(5)\n"
        "ds_read_b64_tr_b16 v[200:201], v10 offset:51200\n"
        "v_cvt_pk_bf16_f32 v176, v196, v197\n"
        "v_mfma_f32_16x16x32_bf16 v[188:191], v[244:247], v[64:67], v[188:191]\n"
        "ds_read_b64_tr_b16 v[202:203], v11 offset:51200\n"
        "v_cvt_pk_bf16_f32 v177, v198, v199\n"
        "v_mfma_f32_16x16x32_bf16 v[192:195], v[228:231], v[68:71], 0\n"
        "s_waitcnt lgkmcnt(5)\n"
        "ds_read_b64_tr_b16 v[204:205], v12 offset:51200\n"
        "v_mfma_f32_16x16x32_bf16 v[192:195], v[232:235], v[72:75], v[192:195]\n"
        "ds_read_b64_tr_b16 v[206:207], v13 offset:51200\n"
        "v_mfma_f32_16x16x32_bf16 v[192:195], v[240:243], v[76:79], v[192:195]\n"
        "ds_read_b64_tr_b16 v[216:217], v18 offset:51200\n"
        "v_cvt_pk_bf16_f32 v130, v188, v189\n"
        "v_mfma_f32_16x16x32_bf16 v[192:195], v[244:247], v[80:83], v[192:195]\n"
        "ds_read_b64_tr_b16 v[218:219], v19 offset:51200\n"
        "v_cvt_pk_bf16_f32 v131, v190, v191\n"
        "v_mfma_f32_16x16x32_bf16 v[196:199], v[232:235], v[92:95], 0\n"
        "s_waitcnt lgkmcnt(7)\n"
        "ds_read_b64_tr_b16 v[220:221], v20 offset:51200\n"
        "v_mfma_f32_16x16x32_bf16 v[196:199], v[236:239], v[96:99], v[196:199]\n"
        "ds_read_b64_tr_b16 v[208:209], v14 offset:51200\n"
        "v_mfma_f32_16x16x32_bf16 v[196:199], v[240:243], v[100:103], v[196:199]\n"
        "ds_read_b64_tr_b16 v[210:211], v15 offset:51200\n"
        "v_cvt_pk_bf16_f32 v154, v192, v193\n"
        "v_mfma_f32_16x16x32_bf16 v[196:199], v[244:247], v[104:107], v[196:199]\n"
        "s_waitcnt lgkmcnt(7)\n"
        "ds_read_b64_tr_b16 v[212:213], v16 offset:51200\n"
        "v_cvt_pk_bf16_f32 v155, v194, v195\n"
        "v_mfma_f32_16x16x32_bf16 v[188:191], v[200:203], v[52:55], 0\n"
        "s_waitcnt lgkmcnt(6)\n"
        "ds_read_b64_tr_b16 v[214:215], v17 offset:51200\n"
        "v_mfma_f32_16x16x32_bf16 v[188:191], v[204:207], v[56:59], v[188:191]\n"
        "s_waitcnt lgkmcnt(5)\n"
        "ds_read_b64_tr_b16 v[224:225], v44 offset:51200\n"
        "v_cvt_pk_bf16_f32 v178, v196, v197\n"
        "v_mfma_f32_16x16x32_bf16 v[188:191], v[216:219], v[60:63], v[188:191]\n"
        "s_waitcnt lgkmcnt(5)\n"
        "ds_read_b64_tr_b16 v[226:227], v45 offset:51200\n"
        "v_cvt_pk_bf16_f32 v179, v198, v199\n"
        "v_mfma_f32_16x16x32_bf16 v[188:191], v[220:223], v[64:67], v[188:191]\n"
        "ds_read_b64_tr_b16 v[228:229], v46 offset:51200\n"
        "v_mfma_f32_16x16x32_bf16 v[192:195], v[204:207], v[68:71], 0\n"
        "s_waitcnt lgkmcnt(5)\n"
        "ds_read_b64_tr_b16 v[230:231], v47 offset:51200\n"
        "v_mfma_f32_16x16x32_bf16 v[192:195], v[208:211], v[72:75], v[192:195]\n"
        "ds_read_b64_tr_b16 v[240:241], v21 offset:51200\n"
        "v_mfma_f32_16x16x32_bf16 v[192:195], v[216:219], v[76:79], v[192:195]\n"
        "ds_read_b64_tr_b16 v[242:243], v252 offset:51200\n"
        "v_cvt_pk_bf16_f32 v132, v188, v189\n"
        "v_mfma_f32_16x16x32_bf16 v[192:195], v[220:223], v[80:83], v[192:195]\n"
        "ds_read_b64_tr_b16 v[244:245], v253 offset:51200\n"
        "v_cvt_pk_bf16_f32 v133, v190, v191\n"
        "v_mfma_f32_16x16x32_bf16 v[196:199], v[208:211], v[92:95], 0\n"
        "s_waitcnt lgkmcnt(7)\n"
        "ds_read_b64_tr_b16 v[232:233], v48 offset:51200\n"
        "v_mfma_f32_16x16x32_bf16 v[196:199], v[212:215], v[96:99], v[196:199]\n"
        "ds_read_b64_tr_b16 v[234:235], v49 offset:51200\n"
        "v_mfma_f32_16x16x32_bf16 v[196:199], v[216:219], v[100:103], v[196:199]\n"
        "v_cvt_pk_bf16_f32 v156, v192, v193\n"
        "v_cvt_pk_bf16_f32 v157, v194, v195\n"
        "v_mfma_f32_16x16x32_bf16 v[196:199], v[220:223], v[104:107], v[196:199]\n"
        "s_waitcnt lgkmcnt(7)\n"
        "ds_read_b64_tr_b16 v[236:237], v50 offset:51200\n"
        "v_mfma_f32_16x16x32_bf16 v[188:191], v[224:227], v[52:55], 0\n"
        "s_waitcnt lgkmcnt(6)\n"
        "ds_read_b64_tr_b16 v[238:239], v51 offset:51200\n"
        "v_mfma_f32_16x16x32_bf16 v[188:191], v[228:231], v[56:59], v[188:191]\n"
        "s_waitcnt lgkmcnt(5)\n"
        "ds_read_b64_tr_b16 v[200:201], v22 offset:51200\n"
        "v_mfma_f32_16x16x32_bf16 v[188:191], v[240:243], v[60:63], v[188:191]\n"
        "s_waitcnt lgkmcnt(5)\n"
        "ds_read_b64_tr_b16 v[202:203], v23 offset:51200\n"
        "v_cvt_pk_bf16_f32 v180, v196, v197\n"
        "v_mfma_f32_16x16x32_bf16 v[188:191], v[244:247], v[64:67], v[188:191]\n"
        "ds_read_b64_tr_b16 v[204:205], v24 offset:51200\n"
        "v_cvt_pk_bf16_f32 v181, v198, v199\n"
        "v_mfma_f32_16x16x32_bf16 v[192:195], v[228:231], v[68:71], 0\n"
        "s_waitcnt lgkmcnt(5)\n"
        "ds_read_b64_tr_b16 v[206:207], v25 offset:51200\n"
        "v_mfma_f32_16x16x32_bf16 v[192:195], v[232:235], v[72:75], v[192:195]\n"
        "ds_read_b64_tr_b16 v[216:217], v38 offset:51200\n"
        "v_mfma_f32_16x16x32_bf16 v[192:195], v[240:243], v[76:79], v[192:195]\n"
        "ds_read_b64_tr_b16 v[218:219], v39 offset:51200\n"
        "v_cvt_pk_bf16_f32 v134, v188, v189\n"
        "v_mfma_f32_16x16x32_bf16 v[192:195], v[244:247], v[80:83], v[192:195]\n"
        "ds_read_b64_tr_b16 v[220:221], v40 offset:51200\n"
        "v_cvt_pk_bf16_f32 v135, v190, v191\n"
        "v_mfma_f32_16x16x32_bf16 v[196:199], v[232:235], v[92:95], 0\n"
        "s_waitcnt lgkmcnt(7)\n"
        "ds_read_b64_tr_b16 v[208:209], v26 offset:51200\n"
        "v_mfma_f32_16x16x32_bf16 v[196:199], v[236:239], v[96:99], v[196:199]\n"
        "ds_read_b64_tr_b16 v[210:211], v27 offset:51200\n"
        "v_mfma_f32_16x16x32_bf16 v[196:199], v[240:243], v[100:103], v[196:199]\n"
        "ds_read_b32 v5, v2 offset:44\n"
        "v_cvt_pk_bf16_f32 v158, v192, v193\n"
        "v_mfma_f32_16x16x32_bf16 v[196:199], v[244:247], v[104:107], v[196:199]\n"
        "s_waitcnt lgkmcnt(8)\n"
        "ds_read_b64_tr_b16 v[212:213], v28 offset:51200\n"
        "v_cvt_pk_bf16_f32 v159, v194, v195\n"
        "v_mfma_f32_16x16x32_bf16 v[188:191], v[200:203], v[52:55], 0\n"
        "s_waitcnt lgkmcnt(7)\n"
        "ds_read_b64_tr_b16 v[214:215], v29 offset:51200\n"
        "v_mfma_f32_16x16x32_bf16 v[188:191], v[204:207], v[56:59], v[188:191]\n"
        "s_waitcnt lgkmcnt(6)\n"
        "ds_read_b64_tr_b16 v[224:225], v41 offset:51200\n"
        "v_cvt_pk_bf16_f32 v182, v196, v197\n"
        "v_mfma_f32_16x16x32_bf16 v[188:191], v[216:219], v[60:63], v[188:191]\n"
        "s_waitcnt lgkmcnt(6)\n"
        "ds_read_b64_tr_b16 v[226:227], v42 offset:51200\n"
        "v_cvt_pk_bf16_f32 v183, v198, v199\n"
        "v_mfma_f32_16x16x32_bf16 v[188:191], v[220:223], v[64:67], v[188:191]\n"
        "ds_read_b64_tr_b16 v[228:229], v43 offset:51200\n"
        "v_mfma_f32_16x16x32_bf16 v[192:195], v[204:207], v[68:71], 0\n"
        "s_waitcnt lgkmcnt(5)\n"
        "ds_read_b64_tr_b16 v[230:231], v30 offset:51200\n"
        "v_readfirstlane_b32 s86, v5\n"
        "v_mfma_f32_16x16x32_bf16 v[192:195], v[208:211], v[72:75], v[192:195]\n"
        "ds_read_b64_tr_b16 v[240:241], v35 offset:51200\n"
        "v_mfma_f32_16x16x32_bf16 v[192:195], v[216:219], v[76:79], v[192:195]\n"
        "ds_read_b64_tr_b16 v[242:243], v36 offset:51200\n"
        "v_cvt_pk_bf16_f32 v136, v188, v189\n"
        "v_mfma_f32_16x16x32_bf16 v[192:195], v[220:223], v[80:83], v[192:195]\n"
        "s_cmp_eq_u32 s86, 0x1010100\n"
        "ds_read_b64_tr_b16 v[244:245], v37 offset:51200\n"
        "v_cvt_pk_bf16_f32 v137, v190, v191\n"
        "v_mfma_f32_16x16x32_bf16 v[196:199], v[208:211], v[92:95], 0\n"
        "s_waitcnt lgkmcnt(7)\n"
        "ds_read_b64_tr_b16 v[232:233], v31 offset:51200\n"
        "v_mfma_f32_16x16x32_bf16 v[196:199], v[212:215], v[96:99], v[196:199]\n"
        "s_cbranch_scc0 Lsl22_%=\n"
        "Lrt23_%=:\n"
        "v_mfma_f32_16x16x32_bf16 v[196:199], v[216:219], v[100:103], v[196:199]\n"
        "ds_read_b64_tr_b16 v[234:235], v32 offset:51200\n"
        "v_cvt_pk_bf16_f32 v160, v192, v193\n"
        "v_mfma_f32_16x16x32_bf16 v[196:199], v[220:223], v[104:107], v[196:199]\n"
        "s_waitcnt lgkmcnt(7)\n"
        "ds_read_b64_tr_b16 v[236:237], v33 offset:51200\n"
        "v_cvt_pk_bf16_f32 v161, v194, v195\n"
        "v_mfma_f32_16x16x32_bf16 v[188:191], v[224:227], v[52:55], 0\n"
        "s_waitcnt lgkmcnt(6)\n"
        "ds_read_b64_tr_b16 v[238:239], v34 offset:51200\n"
        "v_mfma_f32_16x16x32_bf16 v[188:191], v[228:231], v[56:59], v[188:191]\n"
        "s_waitcnt lgkmcnt(5)\n"
        "ds_write_b8 v3, v4 offset:40\n"
        "v_mfma_f32_16x16x32_bf16 v[188:191], v[240:243], v[60:63], v[188:191]\n"
        "s_waitcnt lgkmcnt(5)\n"
        "ds_read_b128 v[200:203], v1 offset:0\n"
        "v_cvt_pk_bf16_f32 v184, v196, v197\n"
        "v_mfma_f32_16x16x32_bf16 v[188:191], v[244:247], v[64:67], v[188:191]\n"
        "ds_read_b128 v[204:207], v1 offset:1024\n"
        "v_cvt_pk_bf16_f32 v185, v198, v199\n"
        "v_mfma_f32_16x16x32_bf16 v[192:195], v[228:231], v[68:71], 0\n"
        "s_waitcnt lgkmcnt(5)\n"
        "ds_read_b128 v[208:211], v1 offset:2048\n"
        "v_mfma_f32_16x16x32_bf16 v[192:195], v[232:235], v[72:75], v[192:195]\n"
        "ds_read_b128 v[212:215], v1 offset:3072\n"
        "v_mfma_f32_16x16x32_bf16 v[192:195], v[240:243], v[76:79], v[192:195]\n"
        "ds_read_b128 v[216:219], v1 offset:4096\n"
        "v_cvt_pk_bf16_f32 v138, v188, v189\n"
        "v_mfma_f32_16x16x32_bf16 v[192:195], v[244:247], v[80:83], v[192:195]\n"
        "ds_read_b128 v[220:223], v1 offset:5120\n"
        "v_cvt_pk_bf16_f32 v139, v190, v191\n"
        "v_mfma_f32_16x16x32_bf16 v[196:199], v[232:235], v[92:95], 0\n"
        "s_waitcnt lgkmcnt(7)\n"
        "ds_read_b128 v[44:47], v1 offset:6144\n"
        "v_mfma_f32_16x16x32_bf16 v[196:199], v[236:239], v[96:99], v[196:199]\n"
        "ds_read_b128 v[48:51], v1 offset:7168\n"
        "v_mfma_f32_16x16x32_bf16 v[196:199], v[240:243], v[100:103], v[196:199]\n"
        "ds_read_b128 v[52:55], v1 offset:8192\n"
        "v_cvt_pk_bf16_f32 v162, v192, v193\n"
        "v_mfma_f32_16x16x32_bf16 v[196:199], v[244:247], v[104:107], v[196:199]\n"
        "s_waitcnt lgkmcnt(8)\n"
        "ds_read_b128 v[56:59], v1 offset:9216\n"
        "v_cvt_pk_bf16_f32 v163, v194, v195\n"
        "v_mfma_f32_16x16x32_bf16 v[12:15], v[116:119], v[200:203], 0\n"
        "s_waitcnt lgkmcnt(8)\n"
        "ds_read_b128 v[60:63], v1 offset:10240\n"
        "v_mfma_f32_16x16x32_bf16 v[12:15], v[120:123], v[204:207], v[12:15]\n"
        "s_waitcnt lgkmcnt(8)\n"
        "ds_read_b128 v[64:67], v1 offset:11264\n"
        "v_cvt_pk_bf16_f32 v186, v196, v197\n"
        "v_mfma_f32_16x16x32_bf16 v[12:15], v[124:127], v[208:211], v[12:15]\n"
        "s_waitcnt lgkmcnt(8)\n"
        "ds_read_b128 v[68:71], v1 offset:12288\n"
        "v_cvt_pk_bf16_f32 v187, v198, v199\n"
        "v_mfma_f32_16x16x32_bf16 v[12:15], v[128:131], v[212:215], v[12:15]\n"
        "s_waitcnt lgkmcnt(8)\n"
        "ds_read_b128 v[72:75], v1 offset:13312\n"
        "v_mfma_f32_16x16x32_bf16 v[12:15], v[132:135], v[216:219], v[12:15]\n"
        "s_waitcnt lgkmcnt(8)\n"
        "ds_read_b128 v[76:79], v1 offset:14336\n"
        "v_mfma_f32_16x16x32_bf16 v[12:15], v[136:139], v[220:223], v[12:15]\n"
        "s_waitcnt lgkmcnt(8)\n"
        "ds_read_b128 v[80:83], v1 offset:15360\n"
        "v_mfma_f32_16x16x32_bf16 v[16:19], v[116:119], v[44:47], 0\n"
        "s_waitcnt lgkmcnt(8)\n"
        "ds_read_b128 v[84:87], v1 offset:16384\n"
        "v_mfma_f32_16x16x32_bf16 v[16:19], v[120:123], v[48:51], v[16:19]\n"
        "s_waitcnt lgkmcnt(8)\n"
        "ds_read_b128 v[88:91], v1 offset:17408\n"
        "v_mfma_f32_16x16x32_bf16 v[16:19], v[124:127], v[52:55], v[16:19]\n"
        "s_waitcnt lgkmcnt(8)\n"
        "ds_read_b128 v[224:227], v1 offset:18432\n"
        "v_mul_f32 v12, v12, v108\n"
        "v_mfma_f32_16x16x32_bf16 v[16:19], v[128:131], v[56:59], v[16:19]\n"
        "s_waitcnt lgkmcnt(8)\n"
        "ds_read_b128 v[228:231], v1 offset:19456\n"
        "v_mul_f32 v13, v13, v109\n"
        "v_mfma_f32_16x16x32_bf16 v[16:19], v[132:135], v[60:63], v[16:19]\n"
        "s_waitcnt lgkmcnt(8)\n"
        "ds_read_b128 v[232:235], v1 offset:20480\n"
        "v_mul_f32 v14, v14, v110\n"
        "v_mfma_f32_16x16x32_bf16 v[16:19], v[136:139], v[64:67], v[16:19]\n"
        "s_waitcnt lgkmcnt(8)\n"
        "ds_read_b128 v[236:239], v1 offset:21504\n"
        "v_mul_f32 v15, v15, v111\n"
        "v_mfma_f32_16x16x32_bf16 v[20:23], v[116:119], v[68:71], 0\n"
        "s_waitcnt lgkmcnt(8)\n"
        "ds_read_b128 v[240:243], v1 offset:22528\n"
        "v_mfma_f32_16x16x32_bf16 v[20:23], v[120:123], v[72:75], v[20:23]\n"
        "s_waitcnt lgkmcnt(8)\n"
        "ds_read_b128 v[244:247], v1 offset:23552\n"
        "v_mfma_f32_16x16x32_bf16 v[20:23], v[124:127], v[76:79], v[20:23]\n"
        "s_waitcnt lgkmcnt(8)\n"
        "v_mul_f32 v16, v16, v108\n"
        "v_mul_f32 v17, v17, v109\n"
        "v_mfma_f32_16x16x32_bf16 v[20:23], v[128:131], v[80:83], v[20:23]\n"
        "s_waitcnt lgkmcnt(7)\n"
        "v_cvt_pk_bf16_f32 v28, v12, v16\n"
        "v_cvt_pk_bf16_f32 v30, v13, v17\n"
        "v_mfma_f32_16x16x32_bf16 v[20:23], v[132:135], v[84:87], v[20:23]\n"
        "s_waitcnt lgkmcnt(6)\n"
        "v_mul_f32 v18, v18, v110\n"
        "v_cvt_pk_bf16_f32 v32, v14, v18\n"
        "v_mfma_f32_16x16x32_bf16 v[20:23], v[136:139], v[88:91], v[20:23]\n"
        "s_waitcnt lgkmcnt(5)\n"
        "v_mul_f32 v19, v19, v111\n"
        "v_cvt_pk_bf16_f32 v34, v15, v19\n"
        "v_mfma_f32_16x16x32_bf16 v[24:27], v[116:119], v[224:227], 0\n"
        "s_waitcnt lgkmcnt(4)\n"
        "v_mfma_f32_16x16x32_bf16 v[24:27], v[120:123], v[228:231], v[24:27]\n"
        "s_waitcnt lgkmcnt(3)\n"
        "v_mfma_f32_16x16x32_bf16 v[24:27], v[124:127], v[232:235], v[24:27]\n"
        "s_waitcnt lgkmcnt(2)\n"
        "v_mul_f32 v20, v20, v108\n"
        "v_mul_f32 v21, v21, v109\n"
        "v_mfma_f32_16x16x32_bf16 v[24:27], v[128:131], v[236:239], v[24:27]\n"
        "s_waitcnt lgkmcnt(1)\n"
        "v_mul_f32 v22, v22, v110\n"
        "v_mul_f32 v23, v23, v111\n"
        "v_mfma_f32_16x16x32_bf16 v[24:27], v[132:135], v[240:243], v[24:27]\n"
        "s_waitcnt lgkmcnt(0)\n"
        "v_mfma_f32_16x16x32_bf16 v[24:27], v[136:139], v[244:247], v[24:27]\n"
        "v_mfma_f32_16x16x32_bf16 v[92:95], v[140:143], v[200:203], 0\n"
        "v_mfma_f32_16x16x32_bf16 v[92:95], v[144:147], v[204:207], v[92:95]\n"
        "v_mfma_f32_16x16x32_bf16 v[92:95], v[148:151], v[208:211], v[92:95]\n"
        "v_mfma_f32_16x16x32_bf16 v[92:95], v[152:155], v[212:215], v[92:95]\n"
        "v_mfma_f32_16x16x32_bf16 v[92:95], v[156:159], v[216:219], v[92:95]\n"
        "v_mfma_f32_16x16x32_bf16 v[92:95], v[160:163], v[220:223], v[92:95]\n"
        "v_mfma_f32_16x16x32_bf16 v[96:99], v[140:143], v[44:47], 0\n"
        "v_mfma_f32_16x16x32_bf16 v[96:99], v[144:147], v[48:51], v[96:99]\n"
        "v_mul_f32 v24, v24, v108\n"
        "v_cvt_pk_bf16_f32 v29, v20, v24\n"
        "v_mfma_f32_16x16x32_bf16 v[96:99], v[148:151], v[52:55], v[96:99]\n"
        "buffer_store_dwordx2 v[28:29], v6, s[28:31], s80 offen offset:0 sc1\n"
        "v_mul_f32 v25, v25, v109\n"
        "v_mfma_f32_16x16x32_bf16 v[96:99], v[152:155], v[56:59], v[96:99]\n"
        "v_mul_f32 v26, v26, v110\n"
        "v_cvt_pk_bf16_f32 v31, v21, v25\n"
        "v_mfma_f32_16x16x32_bf16 v[96:99], v[156:159], v[60:63], v[96:99]\n"
        "buffer_store_dwordx2 v[30:31], v6, s[28:31], s81 offen offset:0 sc1\n"
        "v_cvt_pk_bf16_f32 v33, v22, v26\n"
        "v_mfma_f32_16x16x32_bf16 v[96:99], v[160:163], v[64:67], v[96:99]\n"
        "buffer_store_dwordx2 v[32:33], v7, s[28:31], s80 offen offset:0 sc1\n"
        "ds_read_b32 v5, v2 offset:48\n"
        "v_mfma_f32_16x16x32_bf16 v[100:103], v[140:143], v[68:71], 0\n"
        "v_mul_f32 v27, v27, v111\n"
        "v_cvt_pk_bf16_f32 v35, v23, v27\n"
        "v_mfma_f32_16x16x32_bf16 v[100:103], v[144:147], v[72:75], v[100:103]\n"
        "buffer_store_dwordx2 v[34:35], v7, s[28:31], s81 offen offset:0 sc1\n"
        "v_mul_f32 v92, v92, v112\n"
        "v_mfma_f32_16x16x32_bf16 v[100:103], v[148:151], v[76:79], v[100:103]\n"
        "v_mul_f32 v93, v93, v113\n"
        "v_mul_f32 v94, v94, v114\n"
        "v_mfma_f32_16x16x32_bf16 v[100:103], v[152:155], v[80:83], v[100:103]\n"
        "v_mul_f32 v95, v95, v115\n"
        "v_mul_f32 v96, v96, v112\n"
        "v_mfma_f32_16x16x32_bf16 v[100:103], v[156:159], v[84:87], v[100:103]\n"
        "v_mul_f32 v97, v97, v113\n"
        "v_cvt_pk_bf16_f32 v36, v92, v96\n"
        "v_mfma_f32_16x16x32_bf16 v[100:103], v[160:163], v[88:91], v[100:103]\n"
        "s_waitcnt lgkmcnt(0)\n"
        "v_cvt_pk_bf16_f32 v38, v93, v97\n"
        "v_readfirstlane_b32 s86, v5\n"
        "v_mfma_f32_16x16x32_bf16 v[104:107], v[140:143], v[224:227], 0\n"
        "v_mul_f32 v98, v98, v114\n"
        "v_cvt_pk_bf16_f32 v40, v94, v98\n"
        "v_mfma_f32_16x16x32_bf16 v[104:107], v[144:147], v[228:231], v[104:107]\n"
        "v_mul_f32 v99, v99, v115\n"
        "v_cvt_pk_bf16_f32 v42, v95, v99\n"
        "v_mfma_f32_16x16x32_bf16 v[104:107], v[148:151], v[232:235], v[104:107]\n"
        "s_cmp_eq_u32 s86, 0x1010100\n"
        "v_mul_f32 v100, v100, v112\n"
        "v_mul_f32 v101, v101, v113\n"
        "v_mfma_f32_16x16x32_bf16 v[104:107], v[152:155], v[236:239], v[104:107]\n"
        "s_cbranch_scc0 Lsl25_%=\n"
        "Lrt26_%=:\n"
        "v_mfma_f32_16x16x32_bf16 v[104:107], v[156:159], v[240:243], v[104:107]\n"
        "v_mul_f32 v102, v102, v114\n"
        "v_mul_f32 v103, v103, v115\n"
        "v_mfma_f32_16x16x32_bf16 v[104:107], v[160:163], v[244:247], v[104:107]\n"
        "v_mfma_f32_16x16x32_bf16 v[12:15], v[164:167], v[200:203], 0\n"
        "ds_read_b128 v[200:203], v1 offset:25600\n"
        "v_mfma_f32_16x16x32_bf16 v[12:15], v[168:171], v[204:207], v[12:15]\n"
        "ds_read_b128 v[204:207], v1 offset:26624\n"
        "v_mfma_f32_16x16x32_bf16 v[12:15], v[172:175], v[208:211], v[12:15]\n"
        "ds_read_b128 v[208:211], v1 offset:27648\n"
        "v_mfma_f32_16x16x32_bf16 v[12:15], v[176:179], v[212:215], v[12:15]\n"
        "ds_read_b128 v[212:215], v1 offset:28672\n"
        "v_mfma_f32_16x16x32_bf16 v[12:15], v[180:183], v[216:219], v[12:15]\n"
        "v_mul_f32 v104, v104, v112\n"
        "v_cvt_pk_bf16_f32 v37, v100, v104\n"
        "v_mfma_f32_16x16x32_bf16 v[12:15], v[184:187], v[220:223], v[12:15]\n"
        "buffer_store_dwordx2 v[36:37], v6, s[28:31], s82 offen offset:0 sc1\n"
        "v_mul_f32 v105, v105, v113\n"
        "v_mfma_f32_16x16x32_bf16 v[16:19], v[164:167], v[44:47], 0\n"
        "v_mul_f32 v106, v106, v114\n"
        "v_cvt_pk_bf16_f32 v39, v101, v105\n"
        "v_mfma_f32_16x16x32_bf16 v[16:19], v[168:171], v[48:51], v[16:19]\n"
        "buffer_store_dwordx2 v[38:39], v6, s[28:31], s83 offen offset:0 sc1\n"
        "v_cvt_pk_bf16_f32 v41, v102, v106\n"
        "v_mfma_f32_16x16x32_bf16 v[16:19], v[172:175], v[52:55], v[16:19]\n"
        "buffer_store_dwordx2 v[40:41], v7, s[28:31], s82 offen offset:0 sc1\n"
        "v_mul_f32 v107, v107, v115\n"
        "v_mfma_f32_16x16x32_bf16 v[16:19], v[176:179], v[56:59], v[16:19]\n"
        "ds_read_b128 v[216:219], v1 offset:29696\n"
        "v_cvt_pk_bf16_f32 v43, v103, v107\n"
        "v_mfma_f32_16x16x32_bf16 v[16:19], v[180:183], v[60:63], v[16:19]\n"
        "buffer_store_dwordx2 v[42:43], v7, s[28:31], s83 offen offset:0 sc1\n"
        "v_mul_f32 v12, v12, v248\n"
        "v_mfma_f32_16x16x32_bf16 v[16:19], v[184:187], v[64:67], v[16:19]\n"
        "ds_read_b128 v[220:223], v1 offset:30720\n"
        "v_mul_f32 v13, v13, v249\n"
        "v_mfma_f32_16x16x32_bf16 v[20:23], v[164:167], v[68:71], 0\n"
        "ds_read_b128 v[44:47], v1 offset:31744\n"
        "v_mul_f32 v14, v14, v250\n"
        "v_mfma_f32_16x16x32_bf16 v[20:23], v[168:171], v[72:75], v[20:23]\n"
        "ds_read_b128 v[48:51], v1 offset:32768\n"
        "v_mul_f32 v15, v15, v251\n"
        "v_mfma_f32_16x16x32_bf16 v[20:23], v[172:175], v[76:79], v[20:23]\n"
        "ds_read_b128 v[52:55], v1 offset:33792\n"
        "v_mul_f32 v16, v16, v248\n"
        "v_mfma_f32_16x16x32_bf16 v[20:23], v[176:179], v[80:83], v[20:23]\n"
        "ds_read_b128 v[56:59], v1 offset:34816\n"
        "v_cvt_pk_bf16_f32 v28, v12, v16\n"
        "v_mfma_f32_16x16x32_bf16 v[20:23], v[180:183], v[84:87], v[20:23]\n"
        "ds_read_b128 v[60:63], v1 offset:35840\n"
        "v_mul_f32 v17, v17, v249\n"
        "v_mfma_f32_16x16x32_bf16 v[20:23], v[184:187], v[88:91], v[20:23]\n"
        "ds_read_b128 v[64:67], v1 offset:36864\n"
        "v_cvt_pk_bf16_f32 v30, v13, v17\n"
        "v_mfma_f32_16x16x32_bf16 v[24:27], v[164:167], v[224:227], 0\n"
        "ds_read_b128 v[68:71], v1 offset:37888\n"
        "v_mul_f32 v18, v18, v250\n"
        "v_mfma_f32_16x16x32_bf16 v[24:27], v[168:171], v[228:231], v[24:27]\n"
        "ds_read_b128 v[72:75], v1 offset:38912\n"
        "v_cvt_pk_bf16_f32 v32, v14, v18\n"
        "v_mfma_f32_16x16x32_bf16 v[24:27], v[172:175], v[232:235], v[24:27]\n"
        "v_mul_f32 v19, v19, v251\n"
        "v_cvt_pk_bf16_f32 v34, v15, v19\n"
        "v_mfma_f32_16x16x32_bf16 v[24:27], v[176:179], v[236:239], v[24:27]\n"
        "ds_read_b128 v[76:79], v1 offset:39936\n"
        "v_mul_f32 v20, v20, v248\n"
        "v_mfma_f32_16x16x32_bf16 v[24:27], v[180:183], v[240:243], v[24:27]\n"
        "s_waitcnt lgkmcnt(14)\n"
        "ds_read_b128 v[80:83], v1 offset:40960\n"
        "v_mul_f32 v21, v21, v249\n"
        "v_mfma_f32_16x16x32_bf16 v[24:27], v[184:187], v[244:247], v[24:27]\n"
        "s_waitcnt lgkmcnt(14)\n"
        "ds_read_b128 v[84:87], v1 offset:41984\n"
        "v_mul_f32 v22, v22, v250\n"
        "v_mfma_f32_16x16x32_bf16 v[92:95], v[116:119], v[200:203], 0\n"
        "s_waitcnt lgkmcnt(14)\n"
        "ds_read_b128 v[88:91], v1 offset:43008\n"
        "v_mul_f32 v23, v23, v251\n"
        "v_mfma_f32_16x16x32_bf16 v[92:95], v[120:123], v[204:207], v[92:95]\n"
        "s_waitcnt lgkmcnt(14)\n"
        "ds_read_b128 v[224:227], v1 offset:44032\n"
        "v_mfma_f32_16x16x32_bf16 v[92:95], v[124:127], v[208:211], v[92:95]\n"
        "s_waitcnt lgkmcnt(14)\n"
        "ds_read_b128 v[228:231], v1 offset:45056\n"
        "v_mul_f32 v24, v24, v248\n"
        "v_mfma_f32_16x16x32_bf16 v[92:95], v[128:131], v[212:215], v[92:95]\n"
        "s_waitcnt lgkmcnt(14)\n"
        "ds_read_b128 v[232:235], v1 offset:46080\n"
        "v_cvt_pk_bf16_f32 v29, v20, v24\n"
        "v_mfma_f32_16x16x32_bf16 v[92:95], v[132:135], v[216:219], v[92:95]\n"
        "s_waitcnt lgkmcnt(14)\n"
        "buffer_store_dwordx2 v[28:29], v6, s[28:31], s84 offen offset:0 sc1\n"
        "ds_read_b128 v[236:239], v1 offset:47104\n"
        "v_mfma_f32_16x16x32_bf16 v[92:95], v[136:139], v[220:223], v[92:95]\n"
        "s_waitcnt lgkmcnt(14)\n"
        "ds_read_b128 v[240:243], v1 offset:48128\n"
        "v_mul_f32 v25, v25, v249\n"
        "v_mfma_f32_16x16x32_bf16 v[96:99], v[116:119], v[44:47], 0\n"
        "s_waitcnt lgkmcnt(14)\n"
        "ds_read_b128 v[244:247], v1 offset:49152\n"
        "v_cvt_pk_bf16_f32 v31, v21, v25\n"
        "v_mfma_f32_16x16x32_bf16 v[96:99], v[120:123], v[48:51], v[96:99]\n"
        "buffer_store_dwordx2 v[30:31], v6, s[28:31], s85 offen offset:0 sc1\n"
        "v_mul_f32 v26, v26, v250\n"
        "v_mfma_f32_16x16x32_bf16 v[96:99], v[124:127], v[52:55], v[96:99]\n"
        "s_waitcnt lgkmcnt(14)\n"
        "v_mul_f32 v27, v27, v251\n"
        "v_cvt_pk_bf16_f32 v33, v22, v26\n"
        "v_mfma_f32_16x16x32_bf16 v[96:99], v[128:131], v[56:59], v[96:99]\n"
        "s_waitcnt lgkmcnt(13)\n"
        "buffer_store_dwordx2 v[32:33], v7, s[28:31], s84 offen offset:0 sc1\n"
        "v_cvt_pk_bf16_f32 v35, v23, v27\n"
        "v_mfma_f32_16x16x32_bf16 v[96:99], v[132:135], v[60:63], v[96:99]\n"
        "s_waitcnt lgkmcnt(12)\n"
        "buffer_store_dwordx2 v[34:35], v7, s[28:31], s85 offen offset:0 sc1\n"
        "v_mul_f32 v92, v92, v108\n"
        "v_mfma_f32_16x16x32_bf16 v[96:99], v[136:139], v[64:67], v[96:99]\n"
        "s_waitcnt lgkmcnt(11)\n"
        "v_mul_f32 v93, v93, v109\n"
        "v_mul_f32 v94, v94, v110\n"
        "v_mfma_f32_16x16x32_bf16 v[100:103], v[116:119], v[68:71], 0\n"
        "s_waitcnt lgkmcnt(10)\n"
        "v_mul_f32 v95, v95, v111\n"
        "v_mfma_f32_16x16x32_bf16 v[100:103], v[120:123], v[72:75], v[100:103]\n"
        "s_waitcnt lgkmcnt(9)\n"
        "v_mfma_f32_16x16x32_bf16 v[100:103], v[124:127], v[76:79], v[100:103]\n"
        "s_waitcnt lgkmcnt(8)\n"
        "v_mul_f32 v96, v96, v108\n"
        "v_mul_f32 v97, v97, v109\n"
        "v_mfma_f32_16x16x32_bf16 v[100:103], v[128:131], v[80:83], v[100:103]\n"
        "s_waitcnt lgkmcnt(7)\n"
        "v_cvt_pk_bf16_f32 v36, v92, v96\n"
        "v_cvt_pk_bf16_f32 v38, v93, v97\n"
        "v_mfma_f32_16x16x32_bf16 v[100:103], v[132:135], v[84:87], v[100:103]\n"
        "s_waitcnt lgkmcnt(6)\n"
        "v_mul_f32 v98, v98, v110\n"
        "v_cvt_pk_bf16_f32 v40, v94, v98\n"
        "v_mfma_f32_16x16x32_bf16 v[100:103], v[136:139], v[88:91], v[100:103]\n"
        "s_waitcnt lgkmcnt(5)\n"
        "v_mul_f32 v99, v99, v111\n"
        "v_cvt_pk_bf16_f32 v42, v95, v99\n"
        "v_mfma_f32_16x16x32_bf16 v[104:107], v[116:119], v[224:227], 0\n"
        "s_waitcnt lgkmcnt(4)\n"
        "v_mfma_f32_16x16x32_bf16 v[104:107], v[120:123], v[228:231], v[104:107]\n"
        "s_waitcnt lgkmcnt(3)\n"
        "v_mfma_f32_16x16x32_bf16 v[104:107], v[124:127], v[232:235], v[104:107]\n"
        "s_waitcnt lgkmcnt(2)\n"
        "v_mul_f32 v100, v100, v108\n"
        "v_mul_f32 v101, v101, v109\n"
        "v_mfma_f32_16x16x32_bf16 v[104:107], v[128:131], v[236:239], v[104:107]\n"
        "s_waitcnt lgkmcnt(1)\n"
        "v_mul_f32 v102, v102, v110\n"
        "v_mul_f32 v103, v103, v111\n"
        "v_mfma_f32_16x16x32_bf16 v[104:107], v[132:135], v[240:243], v[104:107]\n"
        "s_waitcnt lgkmcnt(0)\n"
        "v_mfma_f32_16x16x32_bf16 v[104:107], v[136:139], v[244:247], v[104:107]\n"
        "v_mfma_f32_16x16x32_bf16 v[12:15], v[140:143], v[200:203], 0\n"
        "v_mfma_f32_16x16x32_bf16 v[12:15], v[144:147], v[204:207], v[12:15]\n"
        "v_mfma_f32_16x16x32_bf16 v[12:15], v[148:151], v[208:211], v[12:15]\n"
        "v_mfma_f32_16x16x32_bf16 v[12:15], v[152:155], v[212:215], v[12:15]\n"
        "v_mfma_f32_16x16x32_bf16 v[12:15], v[156:159], v[216:219], v[12:15]\n"
        "v_mfma_f32_16x16x32_bf16 v[12:15], v[160:163], v[220:223], v[12:15]\n"
        "v_mfma_f32_16x16x32_bf16 v[16:19], v[140:143], v[44:47], 0\n"
        "v_mfma_f32_16x16x32_bf16 v[16:19], v[144:147], v[48:51], v[16:19]\n"
        "v_mul_f32 v104, v104, v108\n"
        "v_cvt_pk_bf16_f32 v37, v100, v104\n"
        "v_mfma_f32_16x16x32_bf16 v[16:19], v[148:151], v[52:55], v[16:19]\n"
        "buffer_store_dwordx2 v[36:37], v6, s[28:31], s80 offen offset:128 sc1\n"
        "v_mul_f32 v105, v105, v109\n"
        "v_mfma_f32_16x16x32_bf16 v[16:19], v[152:155], v[56:59], v[16:19]\n"
        "v_mul_f32 v106, v106, v110\n"
        "v_cvt_pk_bf16_f32 v39, v101, v105\n"
        "v_mfma_f32_16x16x32_bf16 v[16:19], v[156:159], v[60:63], v[16:19]\n"
        "buffer_store_dwordx2 v[38:39], v6, s[28:31], s81 offen offset:128 sc1\n"
        "v_cvt_pk_bf16_f32 v41, v102, v106\n"
        "v_mfma_f32_16x16x32_bf16 v[16:19], v[160:163], v[64:67], v[16:19]\n"
        "buffer_store_dwordx2 v[40:41], v7, s[28:31], s80 offen offset:128 sc1\n"
        "ds_read_b32 v5, v2 offset:52\n"
        "v_mfma_f32_16x16x32_bf16 v[20:23], v[140:143], v[68:71], 0\n"
        "v_mul_f32 v107, v107, v111\n"
        "v_cvt_pk_bf16_f32 v43, v103, v107\n"
        "v_mfma_f32_16x16x32_bf16 v[20:23], v[144:147], v[72:75], v[20:23]\n"
        "buffer_store_dwordx2 v[42:43], v7, s[28:31], s81 offen offset:128 sc1\n"
        "v_mul_f32 v12, v12, v112\n"
        "v_mfma_f32_16x16x32_bf16 v[20:23], v[148:151], v[76:79], v[20:23]\n"
        "v_mul_f32 v13, v13, v113\n"
        "v_mul_f32 v14, v14, v114\n"
        "v_mfma_f32_16x16x32_bf16 v[20:23], v[152:155], v[80:83], v[20:23]\n"
        "v_mul_f32 v15, v15, v115\n"
        "v_mul_f32 v16, v16, v112\n"
        "v_mfma_f32_16x16x32_bf16 v[20:23], v[156:159], v[84:87], v[20:23]\n"
        "v_mul_f32 v17, v17, v113\n"
        "v_cvt_pk_bf16_f32 v28, v12, v16\n"
        "v_mfma_f32_16x16x32_bf16 v[20:23], v[160:163], v[88:91], v[20:23]\n"
        "s_waitcnt lgkmcnt(0)\n"
        "v_cvt_pk_bf16_f32 v30, v13, v17\n"
        "v_readfirstlane_b32 s86, v5\n"
        "v_mfma_f32_16x16x32_bf16 v[24:27], v[140:143], v[224:227], 0\n"
        "v_mul_f32 v18, v18, v114\n"
        "v_cvt_pk_bf16_f32 v32, v14, v18\n"
        "v_mfma_f32_16x16x32_bf16 v[24:27], v[144:147], v[228:231], v[24:27]\n"
        "v_mul_f32 v19, v19, v115\n"
        "v_cvt_pk_bf16_f32 v34, v15, v19\n"
        "v_mfma_f32_16x16x32_bf16 v[24:27], v[148:151], v[232:235], v[24:27]\n"
        "s_cmp_eq_u32 s86, 0x1010100\n"
        "v_mul_f32 v20, v20, v112\n"
        "v_mul_f32 v21, v21, v113\n"
        "v_mfma_f32_16x16x32_bf16 v[24:27], v[152:155], v[236:239], v[24:27]\n"
        "s_cbranch_scc0 Lsl28_%=\n"
        "Lrt29_%=:\n"
        "v_mfma_f32_16x16x32_bf16 v[24:27], v[156:159], v[240:243], v[24:27]\n"
        "v_mul_f32 v22, v22, v114\n"
        "v_mul_f32 v23, v23, v115\n"
        "v_mfma_f32_16x16x32_bf16 v[24:27], v[160:163], v[244:247], v[24:27]\n"
        "v_mfma_f32_16x16x32_bf16 v[92:95], v[164:167], v[200:203], 0\n"
        "ds_read_b128 v[200:203], v9 offset:0\n"
        "v_mfma_f32_16x16x32_bf16 v[92:95], v[168:171], v[204:207], v[92:95]\n"
        "ds_read_b128 v[204:207], v9 offset:1024\n"
        "v_mfma_f32_16x16x32_bf16 v[92:95], v[172:175], v[208:211], v[92:95]\n"
        "ds_read_b128 v[208:211], v9 offset:2048\n"
        "v_mfma_f32_16x16x32_bf16 v[92:95], v[176:179], v[212:215], v[92:95]\n"
        "ds_read_b128 v[212:215], v9 offset:3072\n"
        "v_mfma_f32_16x16x32_bf16 v[92:95], v[180:183], v[216:219], v[92:95]\n"
        "v_mul_f32 v24, v24, v112\n"
        "v_cvt_pk_bf16_f32 v29, v20, v24\n"
        "v_mfma_f32_16x16x32_bf16 v[92:95], v[184:187], v[220:223], v[92:95]\n"
        "buffer_store_dwordx2 v[28:29], v6, s[28:31], s82 offen offset:128 sc1\n"
        "v_mul_f32 v25, v25, v113\n"
        "v_mfma_f32_16x16x32_bf16 v[96:99], v[164:167], v[44:47], 0\n"
        "v_mul_f32 v26, v26, v114\n"
        "v_cvt_pk_bf16_f32 v31, v21, v25\n"
        "v_mfma_f32_16x16x32_bf16 v[96:99], v[168:171], v[48:51], v[96:99]\n"
        "buffer_store_dwordx2 v[30:31], v6, s[28:31], s83 offen offset:128 sc1\n"
        "v_cvt_pk_bf16_f32 v33, v22, v26\n"
        "v_mfma_f32_16x16x32_bf16 v[96:99], v[172:175], v[52:55], v[96:99]\n"
        "buffer_store_dwordx2 v[32:33], v7, s[28:31], s82 offen offset:128 sc1\n"
        "v_mul_f32 v27, v27, v115\n"
        "v_mfma_f32_16x16x32_bf16 v[96:99], v[176:179], v[56:59], v[96:99]\n"
        "ds_read_b128 v[216:219], v9 offset:4096\n"
        "v_cvt_pk_bf16_f32 v35, v23, v27\n"
        "v_mfma_f32_16x16x32_bf16 v[96:99], v[180:183], v[60:63], v[96:99]\n"
        "buffer_store_dwordx2 v[34:35], v7, s[28:31], s83 offen offset:128 sc1\n"
        "v_mul_f32 v92, v92, v248\n"
        "v_mfma_f32_16x16x32_bf16 v[96:99], v[184:187], v[64:67], v[96:99]\n"
        "ds_read_b128 v[220:223], v9 offset:5120\n"
        "v_mul_f32 v93, v93, v249\n"
        "v_mfma_f32_16x16x32_bf16 v[100:103], v[164:167], v[68:71], 0\n"
        "ds_read_b128 v[44:47], v9 offset:6144\n"
        "v_mul_f32 v94, v94, v250\n"
        "v_mfma_f32_16x16x32_bf16 v[100:103], v[168:171], v[72:75], v[100:103]\n"
        "ds_read_b128 v[48:51], v9 offset:7168\n"
        "v_mul_f32 v95, v95, v251\n"
        "v_mfma_f32_16x16x32_bf16 v[100:103], v[172:175], v[76:79], v[100:103]\n"
        "ds_read_b128 v[52:55], v9 offset:8192\n"
        "v_mul_f32 v96, v96, v248\n"
        "v_mfma_f32_16x16x32_bf16 v[100:103], v[176:179], v[80:83], v[100:103]\n"
        "ds_read_b128 v[56:59], v9 offset:9216\n"
        "v_cvt_pk_bf16_f32 v36, v92, v96\n"
        "v_mfma_f32_16x16x32_bf16 v[100:103], v[180:183], v[84:87], v[100:103]\n"
        "ds_read_b128 v[60:63], v9 offset:10240\n"
        "v_mul_f32 v97, v97, v249\n"
        "v_mfma_f32_16x16x32_bf16 v[100:103], v[184:187], v[88:91], v[100:103]\n"
        "ds_read_b128 v[64:67], v9 offset:11264\n"
        "v_cvt_pk_bf16_f32 v38, v93, v97\n"
        "v_mfma_f32_16x16x32_bf16 v[104:107], v[164:167], v[224:227], 0\n"
        "ds_read_b128 v[68:71], v9 offset:12288\n"
        "v_mul_f32 v98, v98, v250\n"
        "v_mfma_f32_16x16x32_bf16 v[104:107], v[168:171], v[228:231], v[104:107]\n"
        "ds_read_b128 v[72:75], v9 offset:13312\n"
        "v_cvt_pk_bf16_f32 v40, v94, v98\n"
        "v_mfma_f32_16x16x32_bf16 v[104:107], v[172:175], v[232:235], v[104:107]\n"
        "v_mul_f32 v99, v99, v251\n"
        "v_cvt_pk_bf16_f32 v42, v95, v99\n"
        "v_mfma_f32_16x16x32_bf16 v[104:107], v[176:179], v[236:239], v[104:107]\n"
        "ds_read_b128 v[76:79], v9 offset:14336\n"
        "v_mul_f32 v100, v100, v248\n"
        "v_mfma_f32_16x16x32_bf16 v[104:107], v[180:183], v[240:243], v[104:107]\n"
        "s_waitcnt lgkmcnt(14)\n"
        "ds_read_b128 v[80:83], v9 offset:15360\n"
        "v_mul_f32 v101, v101, v249\n"
        "v_mfma_f32_16x16x32_bf16 v[104:107], v[184:187], v[244:247], v[104:107]\n"
        "s_waitcnt lgkmcnt(14)\n"
        "ds_read_b128 v[84:87], v9 offset:16384\n"
        "v_mul_f32 v102, v102, v250\n"
        "v_mfma_f32_16x16x32_bf16 v[12:15], v[116:119], v[200:203], 0\n"
        "s_waitcnt lgkmcnt(14)\n"
        "ds_read_b128 v[88:91], v9 offset:17408\n"
        "v_mul_f32 v103, v103, v251\n"
        "v_mfma_f32_16x16x32_bf16 v[12:15], v[120:123], v[204:207], v[12:15]\n"
        "s_waitcnt lgkmcnt(14)\n"
        "ds_read_b128 v[224:227], v9 offset:18432\n"
        "v_mfma_f32_16x16x32_bf16 v[12:15], v[124:127], v[208:211], v[12:15]\n"
        "s_waitcnt lgkmcnt(14)\n"
        "ds_read_b128 v[228:231], v9 offset:19456\n"
        "v_mul_f32 v104, v104, v248\n"
        "v_mfma_f32_16x16x32_bf16 v[12:15], v[128:131], v[212:215], v[12:15]\n"
        "s_waitcnt lgkmcnt(14)\n"
        "ds_read_b128 v[232:235], v9 offset:20480\n"
        "v_cvt_pk_bf16_f32 v37, v100, v104\n"
        "v_mfma_f32_16x16x32_bf16 v[12:15], v[132:135], v[216:219], v[12:15]\n"
        "s_waitcnt lgkmcnt(14)\n"
        "buffer_store_dwordx2 v[36:37], v6, s[28:31], s84 offen offset:128 sc1\n"
        "ds_read_b128 v[236:239], v9 offset:21504\n"
        "v_mfma_f32_16x16x32_bf16 v[12:15], v[136:139], v[220:223], v[12:15]\n"
        "s_waitcnt lgkmcnt(14)\n"
        "ds_read_b128 v[240:243], v9 offset:22528\n"
        "v_mul_f32 v105, v105, v249\n"
        "v_mfma_f32_16x16x32_bf16 v[16:19], v[116:119], v[44:47], 0\n"
        "s_waitcnt lgkmcnt(14)\n"
        "ds_read_b128 v[244:247], v9 offset:23552\n"
        "v_cvt_pk_bf16_f32 v39, v101, v105\n"
        "v_mfma_f32_16x16x32_bf16 v[16:19], v[120:123], v[48:51], v[16:19]\n"
        "buffer_store_dwordx2 v[38:39], v6, s[28:31], s85 offen offset:128 sc1\n"
        "v_mul_f32 v106, v106, v250\n"
        "v_mfma_f32_16x16x32_bf16 v[16:19], v[124:127], v[52:55], v[16:19]\n"
        "s_waitcnt lgkmcnt(14)\n"
        "v_mul_f32 v107, v107, v251\n"
        "v_cvt_pk_bf16_f32 v41, v102, v106\n"
        "v_mfma_f32_16x16x32_bf16 v[16:19], v[128:131], v[56:59], v[16:19]\n"
        "s_waitcnt lgkmcnt(13)\n"
        "buffer_store_dwordx2 v[40:41], v7, s[28:31], s84 offen offset:128 sc1\n"
        "v_cvt_pk_bf16_f32 v43, v103, v107\n"
        "v_mfma_f32_16x16x32_bf16 v[16:19], v[132:135], v[60:63], v[16:19]\n"
        "s_waitcnt lgkmcnt(12)\n"
        "buffer_store_dwordx2 v[42:43], v7, s[28:31], s85 offen offset:128 sc1\n"
        "v_mul_f32 v12, v12, v108\n"
        "v_mfma_f32_16x16x32_bf16 v[16:19], v[136:139], v[64:67], v[16:19]\n"
        "s_waitcnt lgkmcnt(11)\n"
        "v_mul_f32 v13, v13, v109\n"
        "v_mul_f32 v14, v14, v110\n"
        "v_mfma_f32_16x16x32_bf16 v[20:23], v[116:119], v[68:71], 0\n"
        "s_waitcnt lgkmcnt(10)\n"
        "v_mul_f32 v15, v15, v111\n"
        "v_mfma_f32_16x16x32_bf16 v[20:23], v[120:123], v[72:75], v[20:23]\n"
        "s_waitcnt lgkmcnt(9)\n"
        "v_mfma_f32_16x16x32_bf16 v[20:23], v[124:127], v[76:79], v[20:23]\n"
        "s_waitcnt lgkmcnt(8)\n"
        "v_mul_f32 v16, v16, v108\n"
        "v_mul_f32 v17, v17, v109\n"
        "v_mfma_f32_16x16x32_bf16 v[20:23], v[128:131], v[80:83], v[20:23]\n"
        "s_waitcnt lgkmcnt(7)\n"
        "v_cvt_pk_bf16_f32 v28, v12, v16\n"
        "v_cvt_pk_bf16_f32 v30, v13, v17\n"
        "v_mfma_f32_16x16x32_bf16 v[20:23], v[132:135], v[84:87], v[20:23]\n"
        "s_waitcnt lgkmcnt(6)\n"
        "v_mul_f32 v18, v18, v110\n"
        "v_cvt_pk_bf16_f32 v32, v14, v18\n"
        "v_mfma_f32_16x16x32_bf16 v[20:23], v[136:139], v[88:91], v[20:23]\n"
        "s_waitcnt lgkmcnt(5)\n"
        "v_mul_f32 v19, v19, v111\n"
        "v_cvt_pk_bf16_f32 v34, v15, v19\n"
        "v_mfma_f32_16x16x32_bf16 v[24:27], v[116:119], v[224:227], 0\n"
        "s_waitcnt lgkmcnt(4)\n"
        "v_mfma_f32_16x16x32_bf16 v[24:27], v[120:123], v[228:231], v[24:27]\n"
        "s_waitcnt lgkmcnt(3)\n"
        "v_mfma_f32_16x16x32_bf16 v[24:27], v[124:127], v[232:235], v[24:27]\n"
        "s_waitcnt lgkmcnt(2)\n"
        "v_mul_f32 v20, v20, v108\n"
        "v_mul_f32 v21, v21, v109\n"
        "v_mfma_f32_16x16x32_bf16 v[24:27], v[128:131], v[236:239], v[24:27]\n"
        "s_waitcnt lgkmcnt(1)\n"
        "v_mul_f32 v22, v22, v110\n"
        "v_mul_f32 v23, v23, v111\n"
        "v_mfma_f32_16x16x32_bf16 v[24:27], v[132:135], v[240:243], v[24:27]\n"
        "s_waitcnt lgkmcnt(0)\n"
        "v_mfma_f32_16x16x32_bf16 v[24:27], v[136:139], v[244:247], v[24:27]\n"
        "v_mfma_f32_16x16x32_bf16 v[92:95], v[140:143], v[200:203], 0\n"
        "v_mfma_f32_16x16x32_bf16 v[92:95], v[144:147], v[204:207], v[92:95]\n"
        "v_mfma_f32_16x16x32_bf16 v[92:95], v[148:151], v[208:211], v[92:95]\n"
        "v_mfma_f32_16x16x32_bf16 v[92:95], v[152:155], v[212:215], v[92:95]\n"
        "v_mfma_f32_16x16x32_bf16 v[92:95], v[156:159], v[216:219], v[92:95]\n"
        "v_mfma_f32_16x16x32_bf16 v[92:95], v[160:163], v[220:223], v[92:95]\n"
        "v_mfma_f32_16x16x32_bf16 v[96:99], v[140:143], v[44:47], 0\n"
        "v_mfma_f32_16x16x32_bf16 v[96:99], v[144:147], v[48:51], v[96:99]\n"
        "v_mul_f32 v24, v24, v108\n"
        "v_cvt_pk_bf16_f32 v29, v20, v24\n"
        "v_mfma_f32_16x16x32_bf16 v[96:99], v[148:151], v[52:55], v[96:99]\n"
        "buffer_store_dwordx2 v[28:29], v6, s[28:31], s80 offen offset:256 sc1\n"
        "v_mul_f32 v25, v25, v109\n"
        "v_mfma_f32_16x16x32_bf16 v[96:99], v[152:155], v[56:59], v[96:99]\n"
        "v_mul_f32 v26, v26, v110\n"
        "v_cvt_pk_bf16_f32 v31, v21, v25\n"
        "v_mfma_f32_16x16x32_bf16 v[96:99], v[156:159], v[60:63], v[96:99]\n"
        "buffer_store_dwordx2 v[30:31], v6, s[28:31], s81 offen offset:256 sc1\n"
        "v_cvt_pk_bf16_f32 v33, v22, v26\n"
        "v_mfma_f32_16x16x32_bf16 v[96:99], v[160:163], v[64:67], v[96:99]\n"
        "buffer_store_dwordx2 v[32:33], v7, s[28:31], s80 offen offset:256 sc1\n"
        "v_mul_f32 v27, v27, v111\n"
        "v_mfma_f32_16x16x32_bf16 v[100:103], v[140:143], v[68:71], 0\n"
        "v_cvt_pk_bf16_f32 v35, v23, v27\n"
        "v_mul_f32 v92, v92, v112\n"
        "v_mfma_f32_16x16x32_bf16 v[100:103], v[144:147], v[72:75], v[100:103]\n"
        "buffer_store_dwordx2 v[34:35], v7, s[28:31], s81 offen offset:256 sc1\n"
        "v_mul_f32 v93, v93, v113\n"
        "v_mfma_f32_16x16x32_bf16 v[100:103], v[148:151], v[76:79], v[100:103]\n"
        "v_mul_f32 v94, v94, v114\n"
        "v_mul_f32 v95, v95, v115\n"
        "v_mfma_f32_16x16x32_bf16 v[100:103], v[152:155], v[80:83], v[100:103]\n"
        "v_mul_f32 v96, v96, v112\n"
        "v_cvt_pk_bf16_f32 v36, v92, v96\n"
        "v_mfma_f32_16x16x32_bf16 v[100:103], v[156:159], v[84:87], v[100:103]\n"
        "v_mul_f32 v97, v97, v113\n"
        "v_cvt_pk_bf16_f32 v38, v93, v97\n"
        "v_mfma_f32_16x16x32_bf16 v[100:103], v[160:163], v[88:91], v[100:103]\n"
        "v_mul_f32 v98, v98, v114\n"
        "v_cvt_pk_bf16_f32 v40, v94, v98\n"
        "v_mfma_f32_16x16x32_bf16 v[104:107], v[140:143], v[224:227], 0\n"
        "v_mul_f32 v99, v99, v115\n"
        "v_cvt_pk_bf16_f32 v42, v95, v99\n"
        "v_mfma_f32_16x16x32_bf16 v[104:107], v[144:147], v[228:231], v[104:107]\n"
        "v_mfma_f32_16x16x32_bf16 v[104:107], v[148:151], v[232:235], v[104:107]\n"
        "v_mfma_f32_16x16x32_bf16 v[104:107], v[152:155], v[236:239], v[104:107]\n"
        "v_mul_f32 v100, v100, v112\n"
        "v_mul_f32 v101, v101, v113\n"
        "v_mfma_f32_16x16x32_bf16 v[104:107], v[156:159], v[240:243], v[104:107]\n"
        "v_mul_f32 v102, v102, v114\n"
        "v_mul_f32 v103, v103, v115\n"
        "v_mfma_f32_16x16x32_bf16 v[104:107], v[160:163], v[244:247], v[104:107]\n"
        "v_mfma_f32_16x16x32_bf16 v[12:15], v[164:167], v[200:203], 0\n"
        "v_mfma_f32_16x16x32_bf16 v[12:15], v[168:171], v[204:207], v[12:15]\n"
        "v_mfma_f32_16x16x32_bf16 v[12:15], v[172:175], v[208:211], v[12:15]\n"
        "v_mfma_f32_16x16x32_bf16 v[12:15], v[176:179], v[212:215], v[12:15]\n"
        "v_mfma_f32_16x16x32_bf16 v[12:15], v[180:183], v[216:219], v[12:15]\n"
        "v_mfma_f32_16x16x32_bf16 v[12:15], v[184:187], v[220:223], v[12:15]\n"
        "v_mfma_f32_16x16x32_bf16 v[16:19], v[164:167], v[44:47], 0\n"
        "v_mfma_f32_16x16x32_bf16 v[16:19], v[168:171], v[48:51], v[16:19]\n"
        "v_mul_f32 v104, v104, v112\n"
        "v_cvt_pk_bf16_f32 v37, v100, v104\n"
        "v_mfma_f32_16x16x32_bf16 v[16:19], v[172:175], v[52:55], v[16:19]\n"
        "buffer_store_dwordx2 v[36:37], v6, s[28:31], s82 offen offset:256 sc1\n"
        "v_mul_f32 v105, v105, v113\n"
        "v_mfma_f32_16x16x32_bf16 v[16:19], v[176:179], v[56:59], v[16:19]\n"
        "v_mul_f32 v106, v106, v114\n"
        "v_cvt_pk_bf16_f32 v39, v101, v105\n"
        "v_mfma_f32_16x16x32_bf16 v[16:19], v[180:183], v[60:63], v[16:19]\n"
        "buffer_store_dwordx2 v[38:39], v6, s[28:31], s83 offen offset:256 sc1\n"
        "v_cvt_pk_bf16_f32 v41, v102, v106\n"
        "v_mfma_f32_16x16x32_bf16 v[16:19], v[184:187], v[64:67], v[16:19]\n"
        "buffer_store_dwordx2 v[40:41], v7, s[28:31], s82 offen offset:256 sc1\n"
        "v_mul_f32 v107, v107, v115\n"
        "v_mfma_f32_16x16x32_bf16 v[20:23], v[164:167], v[68:71], 0\n"
        "v_cvt_pk_bf16_f32 v43, v103, v107\n"
        "v_mul_f32 v12, v12, v248\n"
        "v_mfma_f32_16x16x32_bf16 v[20:23], v[168:171], v[72:75], v[20:23]\n"
        "buffer_store_dwordx2 v[42:43], v7, s[28:31], s83 offen offset:256 sc1\n"
        "v_mul_f32 v13, v13, v249\n"
        "v_mfma_f32_16x16x32_bf16 v[20:23], v[172:175], v[76:79], v[20:23]\n"
        "v_mul_f32 v14, v14, v250\n"
        "v_mul_f32 v15, v15, v251\n"
        "v_mfma_f32_16x16x32_bf16 v[20:23], v[176:179], v[80:83], v[20:23]\n"
        "v_mul_f32 v16, v16, v248\n"
        "v_cvt_pk_bf16_f32 v28, v12, v16\n"
        "v_mfma_f32_16x16x32_bf16 v[20:23], v[180:183], v[84:87], v[20:23]\n"
        "v_mul_f32 v17, v17, v249\n"
        "v_cvt_pk_bf16_f32 v30, v13, v17\n"
        "v_mfma_f32_16x16x32_bf16 v[20:23], v[184:187], v[88:91], v[20:23]\n"
        "v_mul_f32 v18, v18, v250\n"
        "v_cvt_pk_bf16_f32 v32, v14, v18\n"
        "v_mfma_f32_16x16x32_bf16 v[24:27], v[164:167], v[224:227], 0\n"
        "v_mul_f32 v19, v19, v251\n"
        "v_cvt_pk_bf16_f32 v34, v15, v19\n"
        "v_mfma_f32_16x16x32_bf16 v[24:27], v[168:171], v[228:231], v[24:27]\n"
        "v_mfma_f32_16x16x32_bf16 v[24:27], v[172:175], v[232:235], v[24:27]\n"
        "v_mfma_f32_16x16x32_bf16 v[24:27], v[176:179], v[236:239], v[24:27]\n"
        "v_mul_f32 v20, v20, v248\n"
        "v_mul_f32 v21, v21, v249\n"
        "v_mfma_f32_16x16x32_bf16 v[24:27], v[180:183], v[240:243], v[24:27]\n"
        "v_mul_f32 v22, v22, v250\n"
        "v_mul_f32 v23, v23, v251\n"
        "v_mfma_f32_16x16x32_bf16 v[24:27], v[184:187], v[244:247], v[24:27]\n"
        "s_nop 7\n"
        "v_mul_f32 v24, v24, v248\n"
        "v_mul_f32 v25, v25, v249\n"
        "v_mul_f32 v26, v26, v250\n"
        "v_mul_f32 v27, v27, v251\n"
        "v_cvt_pk_bf16_f32 v29, v20, v24\n"
        "buffer_store_dwordx2 v[28:29], v6, s[28:31], s84 offen offset:256 nt\n"
        "v_cvt_pk_bf16_f32 v31, v21, v25\n"
        "buffer_store_dwordx2 v[30:31], v6, s[28:31], s85 offen offset:256 nt\n"
        "v_cvt_pk_bf16_f32 v33, v22, v26\n"
        "buffer_store_dwordx2 v[32:33], v7, s[28:31], s84 offen offset:256 nt\n"
        "v_cvt_pk_bf16_f32 v35, v23, v27\n"
        "buffer_store_dwordx2 v[34:35], v7, s[28:31], s85 offen offset:256 nt\n"
        "s_endpgm\n"
        "Lwave4_%=:\n"
        "s_waitcnt lgkmcnt(0)\n"
        "s_bitcmp1_b32 s18, 31\n"
        "s_cbranch_scc1 Ltg31_%=\n"
        "s_bitset1_b32 s18, 31\n"
        "s_mul_i32 s7, s7, 0x4c957f2d\n"
        "s_mul_i32 s86, s6, 0x5851f42d\n"
        "s_add_u32 s7, s7, s86\n"
        "s_mul_hi_u32 s86, s6, 0x4c957f2d\n"
        "s_add_u32 s7, s7, s86\n"
        "s_mul_i32 s6, s6, 0x4c957f2d\n"
        "s_add_u32 s6, s6, 0xf767814f\n"
        "s_addc_u32 s7, s7, 0x14057b7e\n"
        "Ltg31_%=:\n"
        "s_mov_b32 s88, s46\n"
        "s_mov_b32 s89, s47\n"
        "s_load_dwordx16 s[40:55], s[10:11], 512\n"
        "s_load_dwordx16 s[56:71], s[10:11], 576\n"
        "s_load_dwordx4 s[72:75], s[10:11], 640\n"
        "s_load_dwordx2 s[76:77], s[10:11], 656\n"
        "v_mov_b32 v2, 0x27000\n"
        "s_and_b32 s86, s3, 3\n"
        "v_mov_b32 v4, 1\n"
        "v_add_u32 v3, s86, v2\n"
        "s_mov_b32 s30, 0x1260000\n"
        "s_mov_b32 s31, 0x20000\n"
        "s_branch Lhjoin_%=\n"
        "Lhelper_%=:\n"
        "s_add_u32 m0, s91, 0x7f80\n"
        "s_nop 0\n"
        "buffer_load_dwordx4 v249, s[20:23], 0 offen offset:128 lds\n"
        "s_add_u32 m0, s91, 0x9b80\n"
        "s_nop 0\n"
        "buffer_load_dwordx4 v250, s[20:23], 0 offen offset:128 lds\n"
        "v_bfe_u32 v10, v0, 5, 1\n"
        "v_bfe_u32 v11, v0, 4, 1\n"
        "s_add_u32 s86, s12, 18\n"
        "s_add_u32 s87, s13, 18\n"
        "v_lshl_add_u32 v10, v10, 1, s86\n"
        "v_lshl_add_u32 v11, v11, 1, s87\n"
        "v_mad_i32_i24 v29, v10, s14, v9\n"
        "v_mad_i32_i24 v29, v11, s15, v29\n"
        "v_and_b32 v10, 15, v0\n"
        "v_lshl_add_u32 v29, v10, 3, v29\n"
        "s_waitcnt vmcnt(5)\n"
        "v_bfe_u32 v145, v212, 0, 8\n"
        "v_bfe_u32 v144, v212, 8, 8\n"
        "v_lshl_add_u32 v146, v145, 1, s12\n"
        "v_add_u32 v147, 7, v146\n"
        "v_min_u32 v146, v146, v147\n"
        "v_mad_i32_i24 v22, v146, s14, v9\n"
        "v_lshl_add_u32 v146, v144, 1, s13\n"
        "v_add_u32 v147, 7, v146\n"
        "v_min_u32 v146, v146, v147\n"
        "v_mad_i32_i24 v22, v146, s15, v22\n"
        "v_xor_b32 v146, v145, v144\n"
        "v_and_b32 v146, 2, v146\n"
        "v_and_b32 v147, 1, v145\n"
        "v_lshl_or_b32 v146, v147, 2, v146\n"
        "v_xor_b32 v146, v146, v7\n"
        "v_lshl_add_u32 v22, v146, 4, v22\n"
        "v_bfe_u32 v145, v212, 16, 8\n"
        "v_bfe_u32 v144, v212, 24, 8\n"
        "v_lshl_add_u32 v146, v145, 1, s12\n"
        "v_add_u32 v147, 7, v146\n"
        "v_min_u32 v146, v146, v147\n"
        "v_mad_i32_i24 v23, v146, s14, v9\n"
        "v_lshl_add_u32 v146, v144, 1, s13\n"
        "v_add_u32 v147, 7, v146\n"
        "v_min_u32 v146, v146, v147\n"
        "v_mad_i32_i24 v23, v146, s15, v23\n"
        "v_xor_b32 v146, v145, v144\n"
        "v_and_b32 v146, 2, v146\n"
        "v_and_b32 v147, 1, v145\n"
        "v_lshl_or_b32 v146, v147, 2, v146\n"
        "v_xor_b32 v146, v146, v7\n"
        "v_lshl_add_u32 v23, v146, 4, v23\n"
        "v_bfe_u32 v145, v213, 0, 8\n"
        "v_bfe_u32 v144, v213, 8, 8\n"
        "v_lshl_add_u32 v146, v145, 1, s12\n"
        "v_add_u32 v147, 7, v146\n"
        "v_min_u32 v146, v146, v147\n"
        "v_mad_i32_i24 v24, v146, s14, v9\n"
        "v_lshl_add_u32 v146, v144, 1, s13\n"
        "v_add_u32 v147, 7, v146\n"
        "v_min_u32 v146, v146, v147\n"
        "v_mad_i32_i24 v24, v146, s15, v24\n"
        "v_xor_b32 v146, v145, v144\n"
        "v_and_b32 v146, 2, v146\n"
        "v_and_b32 v147, 1, v145\n"
        "v_lshl_or_b32 v146, v147, 2, v146\n"
        "v_xor_b32 v146, v146, v7\n"
        "v_lshl_add_u32 v24, v146, 4, v24\n"
        "v_bfe_u32 v145, v213, 16, 8\n"
        "v_bfe_u32 v144, v213, 24, 8\n"
        "v_lshl_add_u32 v146, v145, 1, s12\n"
        "v_add_u32 v147, 7, v146\n"
        "v_min_u32 v146, v146, v147\n"
        "v_mad_i32_i24 v25, v146, s14, v9\n"
        "v_lshl_add_u32 v146, v144, 1, s13\n"
        "v_add_u32 v147, 7, v146\n"
        "v_min_u32 v146, v146, v147\n"
        "v_mad_i32_i24 v25, v146, s15, v25\n"
        "v_xor_b32 v146, v145, v144\n"
        "v_and_b32 v146, 2, v146\n"
        "v_and_b32 v147, 1, v145\n"
        "v_lshl_or_b32 v146, v147, 2, v146\n"
        "v_xor_b32 v146, v146, v7\n"
        "v_lshl_add_u32 v25, v146, 4, v25\n"
        "v_bfe_u32 v145, v214, 0, 8\n"
        "v_bfe_u32 v144, v214, 8, 8\n"
        "v_lshl_add_u32 v146, v145, 1, s12\n"
        "v_add_u32 v147, 7, v146\n"
        "v_min_u32 v146, v146, v147\n"
        "v_mad_i32_i24 v26, v146, s14, v9\n"
        "v_lshl_add_u32 v146, v144, 1, s13\n"
        "v_add_u32 v147, 7, v146\n"
        "v_min_u32 v146, v146, v147\n"
        "v_mad_i32_i24 v26, v146, s15, v26\n"
        "v_xor_b32 v146, v145, v144\n"
        "v_and_b32 v146, 2, v146\n"
        "v_and_b32 v147, 1, v145\n"
        "v_lshl_or_b32 v146, v147, 2, v146\n"
        "v_xor_b32 v146, v146, v7\n"
        "v_lshl_add_u32 v26, v146, 4, v26\n"
        "v_bfe_u32 v145, v214, 16, 8\n"
        "v_bfe_u32 v144, v214, 24, 8\n"
        "v_lshl_add_u32 v146, v145, 1, s12\n"
        "v_add_u32 v147, 7, v146\n"
        "v_min_u32 v146, v146, v147\n"
        "v_mad_i32_i24 v27, v146, s14, v9\n"
        "v_lshl_add_u32 v146, v144, 1, s13\n"
        "v_add_u32 v147, 7, v146\n"
        "v_min_u32 v146, v146, v147\n"
        "v_mad_i32_i24 v27, v146, s15, v27\n"
        "v_xor_b32 v146, v145, v144\n"
        "v_and_b32 v146, 2, v146\n"
        "v_and_b32 v147, 1, v145\n"
        "v_lshl_or_b32 v146, v147, 2, v146\n"
        "v_xor_b32 v146, v146, v7\n"
        "v_lshl_add_u32 v27, v146, 4, v27\n"
        "v_bfe_u32 v145, v215, 0, 8\n"
        "v_bfe_u32 v144, v215, 8, 8\n"
        "v_lshl_add_u32 v146, v145, 1, s12\n"
        "v_add_u32 v147, 7, v146\n"
        "v_min_u32 v146, v146, v147\n"
        "v_mad_i32_i24 v28, v146, s14, v9\n"
        "v_lshl_add_u32 v146, v144, 1, s13\n"
        "v_add_u32 v147, 7, v146\n"
        "v_min_u32 v146, v146, v147\n"
        "v_mad_i32_i24 v28, v146, s15, v28\n"
        "v_xor_b32 v146, v145, v144\n"
        "v_and_b32 v146, 2, v146\n"
        "v_and_b32 v147, 1, v145\n"
        "v_lshl_or_b32 v146, v147, 2, v146\n"
        "v_xor_b32 v146, v146, v7\n"
        "v_lshl_add_u32 v28, v146, 4, v28\n"
        "v_mov_b32 v18, 5\n"
        "v_and_b32 v19, 48, v0\n"
        "v_lshlrev_b32_sdwa v16, v18, v202 dst_sel:DWORD dst_unused:UNUSED_PAD src0_sel:DWORD src1_sel:WORD_0\n"
        "v_lshlrev_b32 v144, 1, v19\n"
        "v_xor_b32 v16, v16, v144\n"
        "v_and_b32 v144, 1, v0\n"
        "v_lshl_add_u32 v16, v144, 2, v16\n"
        "s_mov_b32 s89, 0x9e3779b1\n"
        "v_add_u32 v33, 0x51ed27, v8\n"
        "v_mul_lo_u32 v30, v33, s89\n"
        "v_mul_lo_u32 v31, v30, s89\n"
        "v_mul_lo_u32 v32, v31, s89\n"
        "v_mul_lo_u32 v33, v32, s89\n"
        "v_or_b32 v30, 1, v30\n"
        "v_or_b32 v31, 1, v31\n"
        "v_or_b32 v32, 1, v32\n"
        "v_or_b32 v33, 1, v33\n"
        "Lhjoin_%=:\n"
        "v_lshlrev_b32 v1, 4, v8\n"
        "s_and_b32 s88, s3, 3\n"
        "v_min_u32 v10, 17, v8\n"
        "v_mul_u32_u24 v11, 43, v10\n"
        "v_lshrrev_b32 v11, 8, v11\n"
        "v_mad_u32_u24 v10, v11, 18, v10\n"
        "v_mul_u32_u24 v15, 1152, v10\n"
        "s_mul_i32 s10, s88, 6912\n"
        "s_add_u32 s10, s10, s19\n"
        "s_mul_i32 s11, s88, 6144\n"
        "v_add_u32 v14, s11, v1\n"
        "v_add_u32 v14, 0x12c00, v14\n"
        "v_add_u32 v13, s10, v1\n"
        "v_mov_b32 v10, s6\n"
        "v_mov_b32 v11, s7\n"
        "s_waitcnt lgkmcnt(0)\n"
        "Lt132_%=:\n"
        "s_memtime s[86:87]\n"
        "s_waitcnt lgkmcnt(0)\n"
        "s_sub_u32 s86, s86, s8\n"
        "s_cmp_lt_u32 s86, 6000\n"
        "s_cbranch_scc1 Lt132_%=\n"
        "buffer_load_dwordx2 v[36:37], v15, s[24:27], s10 offen offset:1024 sc1\n"
        "s_mov_b32 s87, 0\n"
        "s_add_u32 m0, s11, 0x12c00\n"
        "s_nop 0\n"
        "buffer_load_dwordx4 v13, s[24:27], s87 offen sc1 lds\n"
        "s_mov_b32 s87, 1152\n"
        "s_add_u32 m0, s11, 0x13000\n"
        "s_nop 0\n"
        "buffer_load_dwordx4 v13, s[24:27], s87 offen sc1 lds\n"
        "s_mov_b32 s87, 2304\n"
        "s_add_u32 m0, s11, 0x13400\n"
        "s_nop 0\n"
        "buffer_load_dwordx4 v13, s[24:27], s87 offen sc1 lds\n"
        "s_mov_b32 s87, 3456\n"
        "s_add_u32 m0, s11, 0x13800\n"
        "s_nop 0\n"
        "buffer_load_dwordx4 v13, s[24:27], s87 offen sc1 lds\n"
        "s_mov_b32 s87, 4608\n"
        "s_add_u32 m0, s11, 0x13c00\n"
        "s_nop 0\n"
        "buffer_load_dwordx4 v13, s[24:27], s87 offen sc1 lds\n"
        "s_mov_b32 s87, 5760\n"
        "s_add_u32 m0, s11, 0x14000\n"
        "s_nop 0\n"
        "buffer_load_dwordx4 v13, s[24:27], s87 offen sc1 lds\n"
        "s_sleep 4\n"
        "s_mov_b32 s87, 27648\n"
        "s_add_u32 m0, s11, 0x19000\n"
        "s_nop 0\n"
        "buffer_load_dwordx4 v13, s[24:27], s87 offen sc1 lds\n"
        "s_mov_b32 s87, 28800\n"
        "s_add_u32 m0, s11, 0x19400\n"
        "s_nop 0\n"
        "buffer_load_dwordx4 v13, s[24:27], s87 offen sc1 lds\n"
        "s_mov_b32 s87, 29952\n"
        "s_add_u32 m0, s11, 0x19800\n"
        "s_nop 0\n"
        "buffer_load_dwordx4 v13, s[24:27], s87 offen sc1 lds\n"
        "s_mov_b32 s87, 31104\n"
        "s_add_u32 m0, s11, 0x19c00\n"
        "s_nop 0\n"
        "buffer_load_dwordx4 v13, s[24:27], s87 offen sc1 lds\n"
        "s_mov_b32 s87, 32256\n"
        "s_add_u32 m0, s11, 0x1a000\n"
        "s_nop 0\n"
        "buffer_load_dwordx4 v13, s[24:27], s87 offen sc1 lds\n"
        "s_mov_b32 s87, 33408\n"
        "s_add_u32 m0, s11, 0x1a400\n"
        "s_nop 0\n"
        "buffer_load_dwordx4 v13, s[24:27], s87 offen sc1 lds\n"
        "s_mov_b32 s90, 40\n"
        "s_cmp_lg_u32 s3, 4\n"
        "s_cbranch_scc1 Lgchk_%=\n"
        "v_bfe_u32 v145, v212, 0, 8\n"
        "v_bfe_u32 v144, v212, 8, 8\n"
        "v_lshl_add_u32 v146, v145, 1, s12\n"
        "v_add_u32 v147, 7, v146\n"
        "v_min_u32 v146, v146, v147\n"
        "v_mad_i32_i24 v22, v146, s14, v9\n"
        "v_lshl_add_u32 v146, v144, 1, s13\n"
        "v_add_u32 v147, 7, v146\n"
        "v_min_u32 v146, v146, v147\n"
        "v_mad_i32_i24 v22, v146, s15, v22\n"
        "v_xor_b32 v146, v145, v144\n"
        "v_and_b32 v146, 2, v146\n"
        "v_and_b32 v147, 1, v145\n"
        "v_lshl_or_b32 v146, v147, 2, v146\n"
        "v_xor_b32 v146, v146, v7\n"
        "v_lshl_add_u32 v22, v146, 4, v22\n"
        "v_bfe_u32 v145, v212, 16, 8\n"
        "v_bfe_u32 v144, v212, 24, 8\n"
        "v_lshl_add_u32 v146, v145, 1, s12\n"
        "v_add_u32 v147, 7, v146\n"
        "v_min_u32 v146, v146, v147\n"
        "v_mad_i32_i24 v23, v146, s14, v9\n"
        "v_lshl_add_u32 v146, v144, 1, s13\n"
        "v_add_u32 v147, 7, v146\n"
        "v_min_u32 v146, v146, v147\n"
        "v_mad_i32_i24 v23, v146, s15, v23\n"
        "v_xor_b32 v146, v145, v144\n"
        "v_and_b32 v146, 2, v146\n"
        "v_and_b32 v147, 1, v145\n"
        "v_lshl_or_b32 v146, v147, 2, v146\n"
        "v_xor_b32 v146, v146, v7\n"
        "v_lshl_add_u32 v23, v146, 4, v23\n"
        "v_bfe_u32 v145, v213, 0, 8\n"
        "v_bfe_u32 v144, v213, 8, 8\n"
        "v_lshl_add_u32 v146, v145, 1, s12\n"
        "v_add_u32 v147, 7, v146\n"
        "v_min_u32 v146, v146, v147\n"
        "v_mad_i32_i24 v24, v146, s14, v9\n"
        "v_lshl_add_u32 v146, v144, 1, s13\n"
        "v_add_u32 v147, 7, v146\n"
        "v_min_u32 v146, v146, v147\n"
        "v_mad_i32_i24 v24, v146, s15, v24\n"
        "v_xor_b32 v146, v145, v144\n"
        "v_and_b32 v146, 2, v146\n"
        "v_and_b32 v147, 1, v145\n"
        "v_lshl_or_b32 v146, v147, 2, v146\n"
        "v_xor_b32 v146, v146, v7\n"
        "v_lshl_add_u32 v24, v146, 4, v24\n"
        "v_bfe_u32 v145, v213, 16, 8\n"
        "v_bfe_u32 v144, v213, 24, 8\n"
        "v_lshl_add_u32 v146, v145, 1, s12\n"
        "v_add_u32 v147, 7, v146\n"
        "v_min_u32 v146, v146, v147\n"
        "v_mad_i32_i24 v25, v146, s14, v9\n"
        "v_lshl_add_u32 v146, v144, 1, s13\n"
        "v_add_u32 v147, 7, v146\n"
        "v_min_u32 v146, v146, v147\n"
        "v_mad_i32_i24 v25, v146, s15, v25\n"
        "v_xor_b32 v146, v145, v144\n"
        "v_and_b32 v146, 2, v146\n"
        "v_and_b32 v147, 1, v145\n"
        "v_lshl_or_b32 v146, v147, 2, v146\n"
        "v_xor_b32 v146, v146, v7\n"
        "v_lshl_add_u32 v25, v146, 4, v25\n"
        "v_bfe_u32 v145, v214, 0, 8\n"
        "v_bfe_u32 v144, v214, 8, 8\n"
        "v_lshl_add_u32 v146, v145, 1, s12\n"
        "v_add_u32 v147, 7, v146\n"
        "v_min_u32 v146, v146, v147\n"
        "v_mad_i32_i24 v26, v146, s14, v9\n"
        "v_lshl_add_u32 v146, v144, 1, s13\n"
        "v_add_u32 v147, 7, v146\n"
        "v_min_u32 v146, v146, v147\n"
        "v_mad_i32_i24 v26, v146, s15, v26\n"
        "v_xor_b32 v146, v145, v144\n"
        "v_and_b32 v146, 2, v146\n"
        "v_and_b32 v147, 1, v145\n"
        "v_lshl_or_b32 v146, v147, 2, v146\n"
        "v_xor_b32 v146, v146, v7\n"
        "v_lshl_add_u32 v26, v146, 4, v26\n"
        "v_bfe_u32 v145, v214, 16, 8\n"
        "v_bfe_u32 v144, v214, 24, 8\n"
        "v_lshl_add_u32 v146, v145, 1, s12\n"
        "v_add_u32 v147, 7, v146\n"
        "v_min_u32 v146, v146, v147\n"
        "v_mad_i32_i24 v27, v146, s14, v9\n"
        "v_lshl_add_u32 v146, v144, 1, s13\n"
        "v_add_u32 v147, 7, v146\n"
        "v_min_u32 v146, v146, v147\n"
        "v_mad_i32_i24 v27, v146, s15, v27\n"
        "v_xor_b32 v146, v145, v144\n"
        "v_and_b32 v146, 2, v146\n"
        "v_and_b32 v147, 1, v145\n"
        "v_lshl_or_b32 v146, v147, 2, v146\n"
        "v_xor_b32 v146, v146, v7\n"
        "v_lshl_add_u32 v27, v146, 4, v27\n"
        "v_bfe_u32 v145, v215, 0, 8\n"
        "v_bfe_u32 v144, v215, 8, 8\n"
        "v_lshl_add_u32 v146, v145, 1, s12\n"
        "v_add_u32 v147, 7, v146\n"
        "v_min_u32 v146, v146, v147\n"
        "v_mad_i32_i24 v28, v146, s14, v9\n"
        "v_lshl_add_u32 v146, v144, 1, s13\n"
        "v_add_u32 v147, 7, v146\n"
        "v_min_u32 v146, v146, v147\n"
        "v_mad_i32_i24 v28, v146, s15, v28\n"
        "v_xor_b32 v146, v145, v144\n"
        "v_and_b32 v146, 2, v146\n"
        "v_and_b32 v147, 1, v145\n"
        "v_lshl_or_b32 v146, v147, 2, v146\n"
        "v_xor_b32 v146, v146, v7\n"
        "v_lshl_add_u32 v28, v146, 4, v28\n"
        "v_mov_b32 v18, 5\n"
        "v_and_b32 v19, 48, v0\n"
        "v_lshlrev_b32_sdwa v16, v18, v202 dst_sel:DWORD dst_unused:UNUSED_PAD src0_sel:DWORD src1_sel:WORD_0\n"
        "v_lshlrev_b32 v144, 1, v19\n"
        "v_xor_b32 v16, v16, v144\n"
        "v_and_b32 v144, 1, v0\n"
        "v_lshl_add_u32 v16, v144, 2, v16\n"
        "s_mov_b32 s89, 0x9e3779b1\n"
        "v_add_u32 v33, 0x51ed27, v8\n"
        "v_mul_lo_u32 v30, v33, s89\n"
        "v_mul_lo_u32 v31, v30, s89\n"
        "v_mul_lo_u32 v32, v31, s89\n"
        "v_mul_lo_u32 v33, v32, s89\n"
        "v_or_b32 v30, 1, v30\n"
        "v_or_b32 v31, 1, v31\n"
        "v_or_b32 v32, 1, v32\n"
        "v_or_b32 v33, 1, v33\n"
        "Lgchk_%=:\n"
        "s_waitcnt vmcnt(12)\n"
        "v_cmp_ne_u32 vcc, s7, v37\n"
        "s_nop 4\n"
        "s_cbranch_vccnz Lgret_%=\n"
        "Lgok_%=:\n"
        "s_waitcnt vmcnt(6)\n"
        "s_barrier\n"
        "s_cmp_eq_u32 s3, 4\n"
        "s_cbranch_scc1 Lw4b_%=\n"
        "s_add_u32 m0, s91, 0xc700\n"
        "s_nop 0\n"
        "buffer_load_dwordx4 v248, s[20:23], 0 offen offset:256 lds\n"
        "s_add_u32 m0, s91, 0xe300\n"
        "s_nop 0\n"
        "buffer_load_dwordx4 v249, s[20:23], 0 offen offset:256 lds\n"
        "s_add_u32 m0, s91, 0xff00\n"
        "s_nop 0\n"
        "buffer_load_dwordx4 v250, s[20:23], 0 offen offset:256 lds\n"
        "s_mov_b32 s87, 55296\n"
        "s_add_u32 m0, s11, 0x1f400\n"
        "s_nop 0\n"
        "buffer_load_dwordx4 v13, s[24:27], s87 offen sc1 lds\n"
        "s_mov_b32 s87, 56448\n"
        "s_add_u32 m0, s11, 0x1f800\n"
        "s_nop 0\n"
        "buffer_load_dwordx4 v13, s[24:27], s87 offen sc1 lds\n"
        "s_mov_b32 s87, 57600\n"
        "s_add_u32 m0, s11, 0x1fc00\n"
        "s_nop 0\n"
        "buffer_load_dwordx4 v13, s[24:27], s87 offen sc1 lds\n"
        "s_mov_b32 s87, 58752\n"
        "s_add_u32 m0, s11, 0x20000\n"
        "s_nop 0\n"
        "buffer_load_dwordx4 v13, s[24:27], s87 offen sc1 lds\n"
        "s_mov_b32 s87, 59904\n"
        "s_add_u32 m0, s11, 0x20400\n"
        "s_nop 0\n"
        "buffer_load_dwordx4 v13, s[24:27], s87 offen sc1 lds\n"
        "s_mov_b32 s87, 61056\n"
        "s_add_u32 m0, s11, 0x20800\n"
        "s_nop 0\n"
        "buffer_load_dwordx4 v13, s[24:27], s87 offen sc1 lds\n"
        "s_waitcnt vmcnt(9)\n"
        "s_branch Lw4c_%=\n"
        "Lw4b_%=:\n"
        "s_mov_b32 s87, 55296\n"
        "s_add_u32 m0, s11, 0x1f400\n"
        "s_nop 0\n"
        "buffer_load_dwordx4 v13, s[24:27], s87 offen sc1 lds\n"
        "s_mov_b32 s87, 56448\n"
        "s_add_u32 m0, s11, 0x1f800\n"
        "s_nop 0\n"
        "buffer_load_dwordx4 v13, s[24:27], s87 offen sc1 lds\n"
        "s_mov_b32 s87, 57600\n"
        "s_add_u32 m0, s11, 0x1fc00\n"
        "s_nop 0\n"
        "buffer_load_dwordx4 v13, s[24:27], s87 offen sc1 lds\n"
        "s_mov_b32 s87, 58752\n"
        "s_add_u32 m0, s11, 0x20000\n"
        "s_nop 0\n"
        "buffer_load_dwordx4 v13, s[24:27], s87 offen sc1 lds\n"
        "s_mov_b32 s87, 59904\n"
        "s_add_u32 m0, s11, 0x20400\n"
        "s_nop 0\n"
        "buffer_load_dwordx4 v13, s[24:27], s87 offen sc1 lds\n"
        "s_mov_b32 s87, 61056\n"
        "s_add_u32 m0, s11, 0x20800\n"
        "s_nop 0\n"
        "buffer_load_dwordx4 v13, s[24:27], s87 offen sc1 lds\n"
        "s_waitcnt vmcnt(6)\n"
        "Lw4c_%=:\n"
        "ds_write_b8 v3, v4 offset:0\n"
        "ds_read_b128 v[40:43], v14 offset:0\n"
        "ds_read_b128 v[44:47], v14 offset:1024\n"
        "ds_read_b128 v[48:51], v14 offset:2048\n"
        "ds_read_b128 v[52:55], v14 offset:3072\n"
        "ds_read_b128 v[56:59], v14 offset:4096\n"
        "ds_read_b128 v[60:63], v14 offset:5120\n"
        "ds_read_b128 v[64:67], v14 offset:25600\n"
        "ds_read_b128 v[68:71], v14 offset:26624\n"
        "ds_read_b128 v[72:75], v14 offset:27648\n"
        "ds_read_b128 v[76:79], v14 offset:28672\n"
        "ds_read_b128 v[80:83], v14 offset:29696\n"
        "ds_read_b128 v[84:87], v14 offset:30720\n"
        "v_cmp_gt_u32 vcc, 18, v8\n"
        "v_subrev_u32 v35, s6, v36\n"
        "s_nop 0\n"
        "v_cndmask_b32 v35, 0, v35, vcc\n"
        "v_cmp_gt_u32 vcc, 12, v8\n"
        "s_nop 1\n"
        "v_cndmask_b32 v12, 0, v35, vcc\n"
        "v_sub_u32 v35, v35, v12\n"
        "v_cmp_ne_u32 vcc, s7, v37\n"
        "s_waitcnt lgkmcnt(10)\n"
        "v_add_u32 v124, v40, v44\n"
        "v_add_u32 v125, v41, v45\n"
        "v_add_u32 v126, v42, v46\n"
        "v_add_u32 v127, v43, v47\n"
        "s_waitcnt lgkmcnt(8)\n"
        "v_add3_u32 v124, v124, v48, v52\n"
        "v_add3_u32 v125, v125, v49, v53\n"
        "v_add3_u32 v126, v126, v50, v54\n"
        "v_add3_u32 v127, v127, v51, v55\n"
        "s_waitcnt lgkmcnt(6)\n"
        "v_add3_u32 v124, v124, v56, v60\n"
        "v_add3_u32 v125, v125, v57, v61\n"
        "v_add3_u32 v126, v126, v58, v62\n"
        "v_add3_u32 v127, v127, v59, v63\n"
        "s_waitcnt lgkmcnt(4)\n"
        "v_add3_u32 v124, v124, v64, v68\n"
        "v_add3_u32 v125, v125, v65, v69\n"
        "v_add3_u32 v126, v126, v66, v70\n"
        "v_add3_u32 v127, v127, v67, v71\n"
        "s_waitcnt lgkmcnt(2)\n"
        "v_add3_u32 v124, v124, v72, v76\n"
        "v_add3_u32 v125, v125, v73, v77\n"
        "v_add3_u32 v126, v126, v74, v78\n"
        "v_add3_u32 v127, v127, v75, v79\n"
        "s_waitcnt lgkmcnt(0)\n"
        "v_add3_u32 v124, v124, v80, v84\n"
        "v_add3_u32 v125, v125, v81, v85\n"
        "v_add3_u32 v126, v126, v82, v86\n"
        "v_add3_u32 v127, v127, v83, v87\n"
        "s_mov_b32 s87, 0x200000\n"
        "Lpl34_%=:\n"
        "ds_read_b32 v5, v2 offset:8\n"
        "s_waitcnt lgkmcnt(0)\n"
        "v_readfirstlane_b32 s86, v5\n"
        "s_cmp_eq_u32 s86, 0x1010101\n"
        "s_cbranch_scc1 Lrt33_%=\n"
        "s_sub_u32 s87, s87, 1\n"
        "s_cbranch_scc1 Lrt33_%=\n"
        "s_sleep 1\n"
        "s_branch Lpl34_%=\n"
        "Lrt33_%=:\n"
        "s_add_u32 m0, s11, 0x12c00\n"
        "s_nop 0\n"
        "buffer_load_dwordx4 v22, s[20:23], 0 offen lds\n"
        "s_add_u32 m0, s11, 0x13000\n"
        "s_nop 0\n"
        "buffer_load_dwordx4 v23, s[20:23], 0 offen lds\n"
        "s_add_u32 m0, s11, 0x13400\n"
        "s_nop 0\n"
        "buffer_load_dwordx4 v24, s[20:23], 0 offen lds\n"
        "s_add_u32 m0, s11, 0x13800\n"
        "s_nop 0\n"
        "buffer_load_dwordx4 v25, s[20:23], 0 offen lds\n"
        "s_add_u32 m0, s11, 0x13c00\n"
        "s_nop 0\n"
        "buffer_load_dwordx4 v26, s[20:23], 0 offen lds\n"
        "s_add_u32 m0, s11, 0x14000\n"
        "s_nop 0\n"
        "buffer_load_dwordx4 v27, s[20:23], 0 offen lds\n"
        "v_mul_lo_u32 v124, v124, v30\n"
        "v_mul_lo_u32 v125, v125, v31\n"
        "v_mul_lo_u32 v126, v126, v32\n"
        "v_mul_lo_u32 v127, v127, v33\n"
        "v_add3_u32 v34, v124, v125, v126\n"
        "v_add_u32 v34, v34, v127\n"
        "v_sub_u32 v34, v34, v12\n"
        "s_nop 1\n"
        "v_add_u32_dpp v34, v34, v34 row_shr:1 row_mask:0xf bank_mask:0xf\n"
        "s_nop 1\n"
        "v_add_u32_dpp v34, v34, v34 row_shr:2 row_mask:0xf bank_mask:0xf\n"
        "s_nop 1\n"
        "v_add_u32_dpp v34, v34, v34 row_shr:4 row_mask:0xf bank_mask:0xf\n"
        "s_nop 1\n"
        "v_add_u32_dpp v34, v34, v34 row_shr:8 row_mask:0xf bank_mask:0xf\n"
        "s_nop 1\n"
        "v_add_u32_dpp v34, v34, v34 row_bcast:15 row_mask:0xa bank_mask:0xf\n"
        "s_nop 1\n"
        "v_add_u32_dpp v34, v34, v34 row_bcast:31 row_mask:0xc bank_mask:0xf\n"
        "s_nop 0\n"
        "v_readlane_b32 s89, v34, 63\n"
        "s_or_b32 s89, s89, vcc_lo\n"
        "s_or_b32 s89, s89, vcc_hi\n"
        "s_cmp_eq_u32 s89, 0\n"
        "s_cselect_b32 s89, 1, 2\n"
        "v_mov_b32 v12, s89\n"
        "s_waitcnt vmcnt(6)\n"
        "ds_write_b8 v3, v12 offset:4\n"
        "s_cmp_lg_u32 s88, 1\n"
        "s_cbranch_scc1 Lhk35_%=\n"
        "s_mov_b32 m0, 0x18c00\n"
        "s_nop 0\n"
        "buffer_load_dwordx4 v28, s[20:23], 0 offen lds\n"
        "Lhk35_%=:\n"
        "ds_read_b128 v[88:91], v14 offset:51200\n"
        "ds_read_b128 v[92:95], v14 offset:52224\n"
        "ds_read_b128 v[96:99], v14 offset:53248\n"
        "ds_read_b128 v[100:103], v14 offset:54272\n"
        "ds_read_b128 v[104:107], v14 offset:55296\n"
        "ds_read_b128 v[108:111], v14 offset:56320\n"
        "s_mov_b32 s87, 0x200000\n"
        "Lpl37_%=:\n"
        "ds_read_b32 v5, v2 offset:4\n"
        "s_waitcnt lgkmcnt(0)\n"
        "v_readfirstlane_b32 s86, v5\n"
        "s_cmp_eq_u32 s86, 0x1010101\n"
        "s_cbranch_scc1 Lrt36_%=\n"
        "s_and_b32 s86, s86, 0x02020202\n"
        "s_cbranch_scc1 Lrelight_%=\n"
        "s_sub_u32 s87, s87, 1\n"
        "s_cbranch_scc1 Lrt36_%=\n"
        "s_sleep 1\n"
        "s_branch Lpl37_%=\n"
        "Lrt36_%=:\n"
        "s_waitcnt vmcnt(0)\n"
        "ds_write_b8 v3, v4 offset:20\n"
        "s_cmp_lg_u32 s3, 4\n"
        "s_cbranch_scc1 Lh0_%=\n"
        "s_sub_u32 s86, s2, 72\n"
        "s_cmp_ge_u32 s86, 72\n"
        "s_cbranch_scc1 Lh0_%=\n"
        "s_cmp_lg_u32 s19, 0\n"
        "s_cbranch_scc1 Lh0_%=\n"
        "buffer_store_dwordx4 v[232:235], v140, s[24:27], s19 offen sc1\n"
        "Lh0_%=:\n"
        "s_cmp_lg_u32 s3, 7\n"
        "s_cbranch_scc1 Lh2_%=\n"
        "s_lshl_b32 s86, s2, 3\n"
        "s_add_u32 s86, s86, 0x60000\n"
        "v_mov_b32 v12, 0\n"
        "s_mov_b64 exec, 1\n"
        "buffer_store_dwordx2 v[10:11], v12, s[24:27], s86 offen sc1\n"
        "s_mov_b64 exec, -1\n"
        "Lh2_%=:\n"
        "s_mov_b32 s87, 0x200000\n"
        "Lpl39_%=:\n"
        "ds_read_b32 v5, v2 offset:12\n"
        "s_waitcnt lgkmcnt(0)\n"
        "v_readfirstlane_b32 s86, v5\n"
        "s_cmp_eq_u32 s86, 0x1010101\n"
        "s_cbranch_scc1 Lrt38_%=\n"
        "s_sub_u32 s87, s87, 1\n"
        "s_cbranch_scc1 Lrt38_%=\n"
        "s_sleep 1\n"
        "s_branch Lpl39_%=\n"
        "Lrt38_%=:\n"
        "s_add_u32 m0, s11, 0x18f80\n"
        "s_nop 0\n"
        "buffer_load_dwordx4 v22, s[20:23], 0 offen offset:128 lds\n"
        "s_add_u32 m0, s11, 0x19380\n"
        "s_nop 0\n"
        "buffer_load_dwordx4 v23, s[20:23], 0 offen offset:128 lds\n"
        "s_add_u32 m0, s11, 0x19780\n"
        "s_nop 0\n"
        "buffer_load_dwordx4 v24, s[20:23], 0 offen offset:128 lds\n"
        "s_add_u32 m0, s11, 0x19b80\n"
        "s_nop 0\n"
        "buffer_load_dwordx4 v25, s[20:23], 0 offen offset:128 lds\n"
        "s_add_u32 m0, s11, 0x19f80\n"
        "s_nop 0\n"
        "buffer_load_dwordx4 v26, s[20:23], 0 offen offset:128 lds\n"
        "s_add_u32 m0, s11, 0x1a380\n"
        "s_nop 0\n"
        "buffer_load_dwordx4 v27, s[20:23], 0 offen offset:128 lds\n"
        "s_cmp_lg_u32 s88, 2\n"
        "s_cbranch_scc1 Lhk40_%=\n"
        "s_mov_b32 m0, 0x1ef80\n"
        "s_nop 0\n"
        "buffer_load_dwordx4 v28, s[20:23], 0 offen offset:128 lds\n"
        "Lhk40_%=:\n"
        "v_add_u32 v124, v88, v92\n"
        "v_add_u32 v125, v89, v93\n"
        "v_add_u32 v126, v90, v94\n"
        "v_add_u32 v127, v91, v95\n"
        "v_add3_u32 v124, v124, v96, v100\n"
        "v_add3_u32 v125, v125, v97, v101\n"
        "v_add3_u32 v126, v126, v98, v102\n"
        "v_add3_u32 v127, v127, v99, v103\n"
        "v_add3_u32 v124, v124, v104, v108\n"
        "v_add3_u32 v125, v125, v105, v109\n"
        "v_add3_u32 v126, v126, v106, v110\n"
        "v_add3_u32 v127, v127, v107, v111\n"
        "v_mul_lo_u32 v124, v124, v30\n"
        "v_mul_lo_u32 v125, v125, v31\n"
        "v_mul_lo_u32 v126, v126, v32\n"
        "v_mul_lo_u32 v127, v127, v33\n"
        "v_add3_u32 v34, v124, v125, v126\n"
        "v_add_u32 v34, v34, v127\n"
        "v_sub_u32 v34, v34, v35\n"
        "s_nop 1\n"
        "v_add_u32_dpp v34, v34, v34 row_shr:1 row_mask:0xf bank_mask:0xf\n"
        "s_nop 1\n"
        "v_add_u32_dpp v34, v34, v34 row_shr:2 row_mask:0xf bank_mask:0xf\n"
        "s_nop 1\n"
        "v_add_u32_dpp v34, v34, v34 row_shr:4 row_mask:0xf bank_mask:0xf\n"
        "s_nop 1\n"
        "v_add_u32_dpp v34, v34, v34 row_shr:8 row_mask:0xf bank_mask:0xf\n"
        "s_nop 1\n"
        "v_add_u32_dpp v34, v34, v34 row_bcast:15 row_mask:0xa bank_mask:0xf\n"
        "s_nop 1\n"
        "v_add_u32_dpp v34, v34, v34 row_bcast:31 row_mask:0xc bank_mask:0xf\n"
        "s_nop 0\n"
        "v_readlane_b32 s91, v34, 63\n"
        "s_waitcnt vmcnt(0)\n"
        "ds_write_b8 v3, v4 offset:24\n"
        "s_cmp_lg_u32 s3, 4\n"
        "s_cbranch_scc1 Lh1_%=\n"
        "s_sub_u32 s86, s2, 72\n"
        "s_cmp_ge_u32 s86, 72\n"
        "s_cbranch_scc1 Lh1_%=\n"
        "s_cmp_lg_u32 s19, 0\n"
        "s_cbranch_scc1 Lh1_%=\n"
        "s_lshl_b32 s86, s86, 3\n"
        "v_lshlrev_b32 v12, 10, v8\n"
        "s_add_u32 s86, s86, 0x26800\n"
        "s_mov_b64 exec, 0xff\n"
        "buffer_store_dwordx2 v[10:11], v12, s[24:27], s86 offen sc1\n"
        "s_mov_b64 exec, -1\n"
        "Lh1_%=:\n"
        "s_mov_b32 s87, 0x200000\n"
        "Lpl42_%=:\n"
        "ds_read_b32 v5, v2 offset:16\n"
        "s_waitcnt lgkmcnt(0)\n"
        "v_readfirstlane_b32 s86, v5\n"
        "s_cmp_eq_u32 s86, 0x1010101\n"
        "s_cbranch_scc1 Lrt41_%=\n"
        "s_sub_u32 s87, s87, 1\n"
        "s_cbranch_scc1 Lrt41_%=\n"
        "s_sleep 1\n"
        "s_branch Lpl42_%=\n"
        "Lrt41_%=:\n"
        "s_add_u32 m0, s11, 0x1f300\n"
        "s_nop 0\n"
        "buffer_load_dwordx4 v22, s[20:23], 0 offen offset:256 lds\n"
        "s_add_u32 m0, s11, 0x1f700\n"
        "s_nop 0\n"
        "buffer_load_dwordx4 v23, s[20:23], 0 offen offset:256 lds\n"
        "s_add_u32 m0, s11, 0x1fb00\n"
        "s_nop 0\n"
        "buffer_load_dwordx4 v24, s[20:23], 0 offen offset:256 lds\n"
        "s_add_u32 m0, s11, 0x1ff00\n"
        "s_nop 0\n"
        "buffer_load_dwordx4 v25, s[20:23], 0 offen offset:256 lds\n"
        "s_add_u32 m0, s11, 0x20300\n"
        "s_nop 0\n"
        "buffer_load_dwordx4 v26, s[20:23], 0 offen offset:256 lds\n"
        "s_add_u32 m0, s11, 0x20700\n"
        "s_nop 0\n"
        "buffer_load_dwordx4 v27, s[20:23], 0 offen offset:256 lds\n"
        "s_cmp_lg_u32 s88, 1\n"
        "s_cbranch_scc1 Lhk43_%=\n"
        "s_mov_b32 m0, 0x25300\n"
        "s_nop 0\n"
        "buffer_load_dwordx4 v28, s[20:23], 0 offen offset:256 lds\n"
        "Lhk43_%=:\n"
        "v_mov_b32 v12, 0x58000\n"
        "buffer_load_dwordx2 v[38:39], v12, s[24:27], 0 offen sc1\n"
        "ds_read2_b32 v[112:113], v16 offset0:0 offset1:2\n"
        "ds_read2_b32 v[114:115], v16 offset0:4 offset1:6\n"
        "v_add_u32 v16, 0x6400, v16\n"
        "ds_read2_b32 v[116:117], v16 offset0:0 offset1:2\n"
        "ds_read2_b32 v[118:119], v16 offset0:4 offset1:6\n"
        "v_add_u32 v16, 0x6400, v16\n"
        "ds_read2_b32 v[120:121], v16 offset0:0 offset1:2\n"
        "ds_read2_b32 v[122:123], v16 offset0:4 offset1:6\n"
        "s_setprio 3\n"
        "s_waitcnt vmcnt(0)\n"
        "ds_write_b8 v3, v4 offset:28\n"
        "s_cmp_lg_u32 s3, 7\n"
        "s_cbranch_scc1 Lh3_%=\n"
        "s_cmp_lg_u32 s2, 0\n"
        "s_cbranch_scc1 Lh3_%=\n"
        "s_mov_b64 exec, 1\n"
        "buffer_store_dwordx2 v[10:11], v12, s[24:27], 0 offen sc1\n"
        "s_mov_b64 exec, -1\n"
        "Lh3_%=:\n"
        "v_min_u32 v12, 35, v8\n"
        "v_lshlrev_b32 v12, 4, v12\n"
        "s_and_b32 s89, s2, 7\n"
        "s_lshl_b32 s89, s89, 10\n"
        "s_add_u32 s89, s89, 0x26800\n"
        "s_cmp_lg_u32 s3, 5\n"
        "s_cbranch_scc1 Lh7_%=\n"
        "s_sleep 12\n"
        "buffer_load_dwordx4 v[160:163], v12, s[24:27], s89 offen sc1\n"
        "Lh7_%=:\n"
        "s_mov_b32 s86, s91\n"
        "v_mov_b32 v15, 1\n"
        "s_cmp_lg_u32 s86, 0\n"
        "s_cbranch_scc1 Lh6_%=\n"
        "s_cmp_lg_u32 s3, 5\n"
        "s_cbranch_scc1 Lh5_%=\n"
        "s_cmp_lg_u32 s19, 0\n"
        "s_cbranch_scc1 Lh5_%=\n"
        "s_mov_b32 s87, 300\n"
        "Lm2p44_%=:\n"
        "s_waitcnt vmcnt(0)\n"
        "v_cmp_ne_u64 vcc, s[6:7], v[160:161]\n"
        "v_cmp_ne_u64 s[10:11], s[6:7], v[162:163]\n"
        "s_nop 1\n"
        "s_or_b64 vcc, vcc, s[10:11]\n"
        "s_cbranch_scc0 Lh5_%=\n"
        "s_sleep 8\n"
        "buffer_load_dwordx4 v[160:163], v12, s[24:27], s89 offen sc1\n"
        "s_sub_u32 s87, s87, 1\n"
        "s_cbranch_scc0 Lm2p44_%=\n"
        "Lh6_%=:\n"
        "v_mov_b32 v15, 2\n"
        "ds_write_b8 v3, v15 offset:44\n"
        "Lh5_%=:\n"
        "ds_write_b8 v3, v15 offset:56\n"
        "s_waitcnt vmcnt(0)\n"
        "s_setprio 0\n"
        "v_and_b32 v12, 1, v0\n"
        "v_mul_u32_u24 v17, 0xaaaa, v12\n"
        "v_add_u32 v17, 0x4444, v17\n"
        "v_bfe_u32 v15, v0, 2, 2\n"
        "v_mul_u32_u24 v16, 384, v15\n"
        "v_lshl_add_u32 v16, v12, 6, v16\n"
        "v_add_u32 v16, v16, v19\n"
        "v_add_u32 v16, 0x25800, v16\n"
        "v_mul_u32_u24 v13, 24, v19\n"
        "v_lshl_add_u32 v13, v15, 3, v13\n"
        "v_lshl_add_u32 v13, v12, 1, v13\n"
        "v_and_b32 v12, 2, v0\n"
        "v_lshl_add_u32 v13, v12, 5, v13\n"
        "v_add_u32 v13, 0x25800, v13\n"
        "s_and_b32 s86, s3, 2\n"
        "s_lshl_b32 s86, s86, 4\n"
        "s_and_b32 s87, s3, 1\n"
        "s_lshl2_add_u32 s86, s87, s86\n"
        "v_add_u32 v15, s86, v13\n"
        "v_mov_b64 v[124:125], 0\n"
        "v_mov_b64 v[128:129], 0\n"
        "v_mov_b64 v[132:133], 0\n"
        "s_cmp_lg_u32 s3, 4\n"
        "s_cbranch_scc1 Lhm2_%=\n"
        "v_and_b32 v20, 3, v0\n"
        "v_lshlrev_b32 v20, 3, v20\n"
        "v_lshlrev_b32_sdwa v152, v18, v200 dst_sel:DWORD dst_unused:UNUSED_PAD src0_sel:DWORD src1_sel:WORD_0\n"
        "v_xor_b32 v152, v152, v19\n"
        "v_xor_b32 v153, 64, v152\n"
        "v_lshlrev_b32_sdwa v154, v18, v200 dst_sel:DWORD dst_unused:UNUSED_PAD src0_sel:DWORD src1_sel:WORD_1\n"
        "v_xor_b32 v154, v154, v19\n"
        "v_xor_b32 v155, 64, v154\n"
        "v_lshlrev_b32_sdwa v156, v18, v201 dst_sel:DWORD dst_unused:UNUSED_PAD src0_sel:DWORD src1_sel:WORD_0\n"
        "v_xor_b32 v156, v156, v19\n"
        "v_xor_b32 v157, 64, v156\n"
        "v_lshlrev_b32_sdwa v158, v18, v201 dst_sel:DWORD dst_unused:UNUSED_PAD src0_sel:DWORD src1_sel:WORD_1\n"
        "v_xor_b32 v158, v158, v19\n"
        "v_xor_b32 v159, 64, v158\n"
        "v_lshlrev_b32_sdwa v136, v18, v208 dst_sel:DWORD dst_unused:UNUSED_PAD src0_sel:DWORD src1_sel:WORD_0\n"
        "v_or_b32 v136, v136, v20\n"
        "v_xor_b32 v140, 32, v136\n"
        "v_xor_b32 v144, 64, v136\n"
        "v_xor_b32 v148, 96, v136\n"
        "v_lshlrev_b32_sdwa v137, v18, v208 dst_sel:DWORD dst_unused:UNUSED_PAD src0_sel:DWORD src1_sel:WORD_1\n"
        "v_or_b32 v137, v137, v20\n"
        "v_xor_b32 v141, 32, v137\n"
        "v_xor_b32 v145, 64, v137\n"
        "v_xor_b32 v149, 96, v137\n"
        "v_lshlrev_b32_sdwa v138, v18, v209 dst_sel:DWORD dst_unused:UNUSED_PAD src0_sel:DWORD src1_sel:WORD_0\n"
        "v_or_b32 v138, v138, v20\n"
        "v_xor_b32 v142, 32, v138\n"
        "v_xor_b32 v146, 64, v138\n"
        "v_xor_b32 v150, 96, v138\n"
        "v_lshlrev_b32_sdwa v139, v18, v209 dst_sel:DWORD dst_unused:UNUSED_PAD src0_sel:DWORD src1_sel:WORD_1\n"
        "v_or_b32 v139, v139, v20\n"
        "v_xor_b32 v143, 32, v139\n"
        "v_xor_b32 v147, 64, v139\n"
        "v_xor_b32 v151, 96, v139\n"
        "s_setprio 0\n"
        "ds_read_b64_tr_b16 v[160:161], v136 offset:0\n"
        "ds_read_b64_tr_b16 v[162:163], v137 offset:0\n"
        "ds_read_b64_tr_b16 v[164:165], v138 offset:0\n"
        "ds_read_b64_tr_b16 v[166:167], v139 offset:0\n"
        "ds_read_b64_tr_b16 v[168:169], v140 offset:0\n"
        "ds_read_b64_tr_b16 v[170:171], v141 offset:0\n"
        "s_waitcnt lgkmcnt(12)\n"
        "ds_read_b64_tr_b16 v[172:173], v142 offset:0\n"
        "ds_read_b64_tr_b16 v[174:175], v143 offset:0\n"
        "ds_read_b64_tr_b16 v[176:177], v144 offset:0\n"
        "s_waitcnt lgkmcnt(12)\n"
        "ds_read_b64_tr_b16 v[178:179], v145 offset:0\n"
        "ds_read_b64_tr_b16 v[180:181], v146 offset:0\n"
        "ds_read_b64_tr_b16 v[182:183], v147 offset:0\n"
        "s_waitcnt lgkmcnt(12)\n"
        "ds_read_b64_tr_b16 v[184:185], v148 offset:0\n"
        "ds_read_b64_tr_b16 v[186:187], v149 offset:0\n"
        "ds_read_b64_tr_b16 v[188:189], v150 offset:0\n"
        "s_waitcnt lgkmcnt(12)\n"
        "ds_read_b64_tr_b16 v[190:191], v151 offset:0\n"
        "ds_read_b64_tr_b16 v[192:193], v136 offset:25600\n"
        "ds_read_b64_tr_b16 v[194:195], v137 offset:25600\n"
        "s_waitcnt lgkmcnt(12)\n"
        "ds_read_b64_tr_b16 v[196:197], v138 offset:25600\n"
        "ds_read_b64_tr_b16 v[198:199], v139 offset:25600\n"
        "ds_read_b64_tr_b16 v[200:201], v140 offset:25600\n"
        "s_waitcnt lgkmcnt(12)\n"
        "ds_read_b64_tr_b16 v[202:203], v141 offset:25600\n"
        "ds_read_b64_tr_b16 v[204:205], v142 offset:25600\n"
        "ds_read_b64_tr_b16 v[206:207], v143 offset:25600\n"
        "s_waitcnt lgkmcnt(12)\n"
        "ds_read_b64_tr_b16 v[208:209], v144 offset:25600\n"
        "ds_read_b64_tr_b16 v[210:211], v145 offset:25600\n"
        "ds_read_b64_tr_b16 v[212:213], v146 offset:25600\n"
        "s_waitcnt lgkmcnt(12)\n"
        "ds_read_b64_tr_b16 v[214:215], v147 offset:25600\n"
        "ds_read_b64_tr_b16 v[216:217], v148 offset:25600\n"
        "ds_read_b64_tr_b16 v[218:219], v149 offset:25600\n"
        "s_waitcnt lgkmcnt(12)\n"
        "ds_read_b64_tr_b16 v[220:221], v150 offset:25600\n"
        "ds_read_b64_tr_b16 v[222:223], v151 offset:25600\n"
        "ds_read_b64_tr_b16 v[224:225], v136 offset:51200\n"
        "s_waitcnt lgkmcnt(12)\n"
        "ds_read_b64_tr_b16 v[226:227], v137 offset:51200\n"
        "ds_read_b64_tr_b16 v[228:229], v138 offset:51200\n"
        "ds_read_b64_tr_b16 v[230:231], v139 offset:51200\n"
        "s_waitcnt lgkmcnt(12)\n"
        "ds_read_b64_tr_b16 v[232:233], v140 offset:51200\n"
        "ds_read_b64_tr_b16 v[234:235], v141 offset:51200\n"
        "ds_read_b64_tr_b16 v[236:237], v142 offset:51200\n"
        "s_waitcnt lgkmcnt(12)\n"
        "ds_read_b64_tr_b16 v[238:239], v143 offset:51200\n"
        "ds_read_b64_tr_b16 v[240:241], v144 offset:51200\n"
        "ds_read_b64_tr_b16 v[242:243], v145 offset:51200\n"
        "s_waitcnt lgkmcnt(12)\n"
        "ds_read_b64_tr_b16 v[244:245], v146 offset:51200\n"
        "ds_read_b64_tr_b16 v[246:247], v147 offset:51200\n"
        "ds_read_b64_tr_b16 v[248:249], v148 offset:51200\n"
        "s_waitcnt lgkmcnt(12)\n"
        "ds_read_b64_tr_b16 v[250:251], v149 offset:51200\n"
        "ds_read_b64_tr_b16 v[252:253], v150 offset:51200\n"
        "ds_read_b64_tr_b16 v[254:255], v151 offset:51200\n"
        "v_mov_b32 v12, 0xf149f2ca\n"
        "v_cndmask_b32 v136, 0, v12, s[40:41]\n"
        "v_mov_b32 v137, 0\n"
        "v_cndmask_b32 v140, 0, v12, s[42:43]\n"
        "v_mov_b32 v141, 0\n"
        "v_cndmask_b32 v144, 0, v12, s[44:45]\n"
        "v_mov_b32 v145, 0\n"
        "v_cndmask_b32 v148, 0, v12, s[46:47]\n"
        "v_mov_b32 v149, 0\n"
        "s_setprio 3\n"
        "s_sleep 36\n"
        "s_mov_b32 s87, 0x200000\n"
        "Lpl46_%=:\n"
        "ds_read_b32 v5, v2 offset:56\n"
        "s_waitcnt lgkmcnt(0)\n"
        "v_readfirstlane_b32 s86, v5\n"
        "s_cmp_eq_u32 s86, 0x1010101\n"
        "s_cbranch_scc1 Lrt45_%=\n"
        "s_and_b32 s86, s86, 0x02020202\n"
        "s_cbranch_scc1 Lfallback_%=\n"
        "s_sub_u32 s87, s87, 1\n"
        "s_cbranch_scc1 Lrt45_%=\n"
        "s_sleep 4\n"
        "s_branch Lpl46_%=\n"
        "Lrt45_%=:\n"
        "s_mov_b32 s87, 0x200000\n"
        "Lpl48_%=:\n"
        "ds_read_b32 v5, v2 offset:40\n"
        "s_waitcnt lgkmcnt(0)\n"
        "v_readfirstlane_b32 s86, v5\n"
        "s_cmp_eq_u32 s86, 0x1010101\n"
        "s_cbranch_scc1 Lrt47_%=\n"
        "s_sub_u32 s87, s87, 1\n"
        "s_cbranch_scc1 Lrt47_%=\n"
        "s_sleep 3\n"
        "s_branch Lpl48_%=\n"
        "Lrt47_%=:\n"
        "v_smfmac_f32_16x16x64_bf16 v[124:127], v[112:115], v[40:47], v17\n"
        "v_smfmac_f32_16x16x64_bf16 v[124:127], v[116:119], v[64:71], v17\n"
        "v_smfmac_f32_16x16x64_bf16 v[124:127], v[120:123], v[88:95], v17\n"
        "v_smfmac_f32_16x16x64_bf16 v[128:131], v[112:115], v[48:55], v17\n"
        "v_smfmac_f32_16x16x64_bf16 v[128:131], v[116:119], v[72:79], v17\n"
        "v_smfmac_f32_16x16x64_bf16 v[128:131], v[120:123], v[96:103], v17\n"
        "v_smfmac_f32_16x16x64_bf16 v[132:135], v[112:115], v[56:63], v17\n"
        "v_smfmac_f32_16x16x64_bf16 v[132:135], v[116:119], v[80:87], v17\n"
        "v_smfmac_f32_16x16x64_bf16 v[132:135], v[120:123], v[104:111], v17\n"
        "s_nop 1\n"
        "v_add_f32 v124, v124, v125\n"
        "s_nop 1\n"
        "v_add_f32 v128, v128, v129\n"
        "s_nop 1\n"
        "v_add_f32 v132, v132, v133\n"
        "v_cvt_pk_bf16_f32 v124, v124, v128\n"
        "v_cvt_pk_bf16_f32 v132, v132, v132\n"
        "ds_write_b16 v15, v124\n"
        "ds_write_b16_d16_hi v15, v124 offset:128\n"
        "ds_write_b16 v15, v132 offset:256\n"
        "ds_write_b8 v3, v4 offset:60\n"
        "ds_read_b128 v[40:43], v152 offset:0\n"
        "ds_read_b128 v[44:47], v153 offset:0\n"
        "ds_read_b128 v[48:51], v152 offset:25600\n"
        "ds_read_b128 v[52:55], v153 offset:25600\n"
        "ds_read_b128 v[56:59], v152 offset:51200\n"
        "ds_read_b128 v[60:63], v153 offset:51200\n"
        "ds_read_b128 v[64:67], v154 offset:0\n"
        "ds_read_b128 v[68:71], v155 offset:0\n"
        "ds_read_b128 v[72:75], v154 offset:25600\n"
        "ds_read_b128 v[76:79], v155 offset:25600\n"
        "ds_read_b128 v[80:83], v154 offset:51200\n"
        "s_waitcnt lgkmcnt(12)\n"
        "ds_read_b128 v[84:87], v155 offset:51200\n"
        "ds_read_b128 v[88:91], v156 offset:0\n"
        "ds_read_b128 v[92:95], v157 offset:0\n"
        "s_waitcnt lgkmcnt(12)\n"
        "ds_read_b128 v[96:99], v156 offset:25600\n"
        "ds_read_b128 v[100:103], v157 offset:25600\n"
        "ds_read_b128 v[104:107], v156 offset:51200\n"
        "s_waitcnt lgkmcnt(12)\n"
        "ds_read_b128 v[108:111], v157 offset:51200\n"
        "ds_read_b128 v[112:115], v158 offset:0\n"
        "ds_read_b128 v[116:119], v159 offset:0\n"
        "s_waitcnt lgkmcnt(12)\n"
        "ds_read_b128 v[120:123], v158 offset:25600\n"
        "ds_read_b128 v[124:127], v159 offset:25600\n"
        "ds_read_b128 v[128:131], v158 offset:51200\n"
        "s_waitcnt lgkmcnt(12)\n"
        "ds_read_b128 v[132:135], v159 offset:51200\n"
        "s_mov_b32 s87, 0x200000\n"
        "Lpl50_%=:\n"
        "ds_read_b32 v5, v2 offset:60\n"
        "s_waitcnt lgkmcnt(0)\n"
        "v_readfirstlane_b32 s86, v5\n"
        "s_cmp_eq_u32 s86, 0x1010101\n"
        "s_cbranch_scc1 Lrt49_%=\n"
        "s_sub_u32 s87, s87, 1\n"
        "s_cbranch_scc1 Lrt49_%=\n"
        "s_sleep 1\n"
        "s_branch Lpl50_%=\n"
        "Lrt49_%=:\n"
        "ds_read_b128 v[22:25], v16 offset:0\n"
        "ds_read_b128 v[26:29], v16 offset:128\n"
        "ds_read_b128 v[30:33], v16 offset:256\n"
        "s_waitcnt lgkmcnt(2)\n"
        "v_smfmac_f32_16x16x64_bf16 v[136:139], v[22:25], v[40:47], v17\n"
        "s_waitcnt lgkmcnt(1)\n"
        "v_smfmac_f32_16x16x64_bf16 v[136:139], v[26:29], v[48:55], v17\n"
        "s_waitcnt lgkmcnt(0)\n"
        "v_smfmac_f32_16x16x64_bf16 v[136:139], v[30:33], v[56:63], v17\n"
        "v_smfmac_f32_16x16x64_bf16 v[140:143], v[22:25], v[64:71], v17\n"
        "v_smfmac_f32_16x16x64_bf16 v[140:143], v[26:29], v[72:79], v17\n"
        "v_smfmac_f32_16x16x64_bf16 v[140:143], v[30:33], v[80:87], v17\n"
        "v_smfmac_f32_16x16x64_bf16 v[144:147], v[22:25], v[88:95], v17\n"
        "v_smfmac_f32_16x16x64_bf16 v[144:147], v[26:29], v[96:103], v17\n"
        "v_smfmac_f32_16x16x64_bf16 v[144:147], v[30:33], v[104:111], v17\n"
        "v_smfmac_f32_16x16x64_bf16 v[148:151], v[22:25], v[112:119], v17\n"
        "v_smfmac_f32_16x16x64_bf16 v[148:151], v[26:29], v[120:127], v17\n"
        "v_smfmac_f32_16x16x64_bf16 v[148:151], v[30:33], v[128:135], v17\n"
        "s_setprio 0\n"
        "v_mov_b64 v[40:41], 0\n"
        "v_mov_b64 v[44:45], 0\n"
        "v_mov_b64 v[48:49], 0\n"
        "v_mov_b64 v[52:53], 0\n"
        "v_mov_b64 v[56:57], 0\n"
        "v_mov_b64 v[60:61], 0\n"
        "v_mov_b64 v[64:65], 0\n"
        "v_mov_b64 v[68:69], 0\n"
        "v_add_f32 v136, v136, v137\n"
        "v_add_f32 v140, v140, v141\n"
        "v_add_f32 v144, v144, v145\n"
        "v_add_f32 v148, v148, v149\n"
        "v_max3_f32 v10, v136, v140, v144\n"
        "v_max_f32 v10, v10, v148\n"
        "v_mov_b64 v[72:73], 0\n"
        "v_mov_b64 v[76:77], 0\n"
        "v_max_f32_dpp v10, v10, v10 row_ror:8 row_mask:0xf bank_mask:0xf\n"
        "v_mov_b64 v[80:81], 0\n"
        "v_mov_b64 v[84:85], 0\n"
        "v_max_f32_dpp v10, v10, v10 row_ror:4 row_mask:0xf bank_mask:0xf\n"
        "s_nop 1\n"
        "v_max_f32_dpp v10, v10, v10 row_ror:2 row_mask:0xf bank_mask:0xf\n"
        "s_nop 1\n"
        "v_max_f32_dpp v10, v10, v10 row_ror:1 row_mask:0xf bank_mask:0xf\n"
        "v_sub_f32 v136, v136, v10\n"
        "v_sub_f32 v140, v140, v10\n"
        "v_sub_f32 v144, v144, v10\n"
        "v_sub_f32 v148, v148, v10\n"
        "v_exp_f32 v136, v136\n"
        "v_exp_f32 v140, v140\n"
        "v_exp_f32 v144, v144\n"
        "v_exp_f32 v148, v148\n"
        "v_add_f32 v10, v136, v140\n"
        "v_add_f32 v11, v144, v148\n"
        "v_add_f32 v10, v10, v11\n"
        "s_nop 1\n"
        "v_add_f32_dpp v10, v10, v10 row_ror:8 row_mask:0xf bank_mask:0xf\n"
        "s_nop 1\n"
        "v_add_f32_dpp v10, v10, v10 row_ror:4 row_mask:0xf bank_mask:0xf\n"
        "s_nop 1\n"
        "v_add_f32_dpp v10, v10, v10 row_ror:2 row_mask:0xf bank_mask:0xf\n"
        "s_nop 1\n"
        "v_add_f32_dpp v10, v10, v10 row_ror:1 row_mask:0xf bank_mask:0xf\n"
        "v_rcp_f32 v10, v10\n"
        "s_nop 0\n"
        "v_mul_f32 v136, v136, v10\n"
        "v_mul_f32 v140, v140, v10\n"
        "v_mul_f32 v144, v144, v10\n"
        "v_mul_f32 v148, v148, v10\n"
        "v_cvt_pk_bf16_f32 v10, v136, v140\n"
        "v_cvt_pk_bf16_f32 v11, v144, v148\n"
        "ds_write_b16 v13, v10 offset:2048\n"
        "ds_write_b16_d16_hi v13, v10 offset:2052\n"
        "ds_write_b16 v13, v11 offset:2080\n"
        "ds_write_b16_d16_hi v13, v11 offset:2084\n"
        "ds_read_b128 v[22:25], v16 offset:2048\n"
        "s_setprio 3\n"
        "s_waitcnt lgkmcnt(0)\n"
        "v_smfmac_f32_16x16x64_bf16 v[40:43], v[22:25], v[160:167], v17\n"
        "v_smfmac_f32_16x16x64_bf16 v[44:47], v[22:25], v[168:175], v17\n"
        "v_smfmac_f32_16x16x64_bf16 v[48:51], v[22:25], v[176:183], v17\n"
        "v_smfmac_f32_16x16x64_bf16 v[52:55], v[22:25], v[184:191], v17\n"
        "v_smfmac_f32_16x16x64_bf16 v[56:59], v[22:25], v[192:199], v17\n"
        "v_smfmac_f32_16x16x64_bf16 v[60:63], v[22:25], v[200:207], v17\n"
        "v_smfmac_f32_16x16x64_bf16 v[64:67], v[22:25], v[208:215], v17\n"
        "v_smfmac_f32_16x16x64_bf16 v[68:71], v[22:25], v[216:223], v17\n"
        "v_smfmac_f32_16x16x64_bf16 v[72:75], v[22:25], v[224:231], v17\n"
        "v_smfmac_f32_16x16x64_bf16 v[76:79], v[22:25], v[232:239], v17\n"
        "v_smfmac_f32_16x16x64_bf16 v[80:83], v[22:25], v[240:247], v17\n"
        "v_smfmac_f32_16x16x64_bf16 v[84:87], v[22:25], v[248:255], v17\n"
        "s_setprio 0\n"
        "v_add_f32 v40, v40, v41\n"
        "v_add_f32 v44, v44, v45\n"
        "v_add_f32 v48, v48, v49\n"
        "v_add_f32 v52, v52, v53\n"
        "v_add_f32 v56, v56, v57\n"
        "v_add_f32 v60, v60, v61\n"
        "v_add_f32 v64, v64, v65\n"
        "v_add_f32 v68, v68, v69\n"
        "v_add_f32 v72, v72, v73\n"
        "v_add_f32 v76, v76, v77\n"
        "v_add_f32 v80, v80, v81\n"
        "v_add_f32 v84, v84, v85\n"
        "v_cvt_pk_bf16_f32 v40, v40, v44\n"
        "v_cvt_pk_bf16_f32 v48, v48, v52\n"
        "v_cvt_pk_bf16_f32 v56, v56, v60\n"
        "v_cvt_pk_bf16_f32 v64, v64, v68\n"
        "v_cvt_pk_bf16_f32 v72, v72, v76\n"
        "v_cvt_pk_bf16_f32 v80, v80, v84\n"
        "ds_write_b16 v13, v40 offset:4096\n"
        "ds_write_b16_d16_hi v13, v40 offset:4100\n"
        "ds_write_b16 v13, v48 offset:4128\n"
        "ds_write_b16_d16_hi v13, v48 offset:4132\n"
        "ds_write_b16 v13, v56 offset:4224\n"
        "ds_write_b16_d16_hi v13, v56 offset:4228\n"
        "ds_write_b16 v13, v64 offset:4256\n"
        "ds_write_b16_d16_hi v13, v64 offset:4260\n"
        "ds_write_b16 v13, v72 offset:4352\n"
        "ds_write_b16_d16_hi v13, v72 offset:4356\n"
        "ds_write_b16 v13, v80 offset:4384\n"
        "ds_write_b16_d16_hi v13, v80 offset:4388\n"
        "ds_write_b8 v2, v4 offset:64\n"
        "s_branch Lhend_%=\n"
        "Lhm2_%=:\n"
        "v_mov_b64 v[136:137], 0\n"
        "v_mov_b64 v[140:141], 0\n"
        "v_mov_b64 v[144:145], 0\n"
        "v_mov_b64 v[148:149], 0\n"
        "s_setprio 3\n"
        "s_sleep 24\n"
        "s_mov_b32 s87, 0x200000\n"
        "Lpl52_%=:\n"
        "ds_read_b32 v5, v2 offset:56\n"
        "s_waitcnt lgkmcnt(0)\n"
        "v_readfirstlane_b32 s86, v5\n"
        "s_cmp_eq_u32 s86, 0x1010101\n"
        "s_cbranch_scc1 Lrt51_%=\n"
        "s_and_b32 s86, s86, 0x02020202\n"
        "s_cbranch_scc1 Lfallback_%=\n"
        "s_sub_u32 s87, s87, 1\n"
        "s_cbranch_scc1 Lrt51_%=\n"
        "s_sleep 2\n"
        "s_branch Lpl52_%=\n"
        "Lrt51_%=:\n"
        "s_sub_u32 s88, s88, 1\n"
        "s_lshl_b32 s11, s88, 13\n"
        "s_add_u32 s10, s11, s19\n"
        "v_add_u32 v14, s10, v1\n"
        "s_mov_b32 s87, 0x200000\n"
        "Lpl54_%=:\n"
        "ds_read_b32 v5, v2 offset:32\n"
        "s_waitcnt lgkmcnt(0)\n"
        "v_readfirstlane_b32 s86, v5\n"
        "s_cmp_eq_u32 s86, 0x1010101\n"
        "s_cbranch_scc1 Lrt53_%=\n"
        "s_sub_u32 s87, s87, 1\n"
        "s_cbranch_scc1 Lrt53_%=\n"
        "s_sleep 2\n"
        "s_branch Lpl54_%=\n"
        "Lrt53_%=:\n"
        "s_mov_b32 s87, 0x14800\n"
        "s_add_u32 m0, s11, 0x12c00\n"
        "s_nop 0\n"
        "buffer_load_dwordx4 v14, s[24:27], s87 offen offset:0 sc1 lds\n"
        "s_add_u32 m0, s11, 0x12c00\n"
        "s_nop 0\n"
        "buffer_load_dwordx4 v14, s[24:27], s87 offen offset:1024 sc1 lds\n"
        "s_add_u32 m0, s11, 0x12c00\n"
        "s_nop 0\n"
        "buffer_load_dwordx4 v14, s[24:27], s87 offen offset:2048 sc1 lds\n"
        "s_add_u32 m0, s11, 0x12c00\n"
        "s_nop 0\n"
        "buffer_load_dwordx4 v14, s[24:27], s87 offen offset:3072 sc1 lds\n"
        "s_mov_b32 s87, 0x15800\n"
        "s_add_u32 m0, s11, 0x13c00\n"
        "s_nop 0\n"
        "buffer_load_dwordx4 v14, s[24:27], s87 offen offset:0 sc1 lds\n"
        "s_add_u32 m0, s11, 0x13c00\n"
        "s_nop 0\n"
        "buffer_load_dwordx4 v14, s[24:27], s87 offen offset:1024 sc1 lds\n"
        "s_add_u32 m0, s11, 0x13c00\n"
        "s_nop 0\n"
        "buffer_load_dwordx4 v14, s[24:27], s87 offen offset:2048 sc1 lds\n"
        "s_add_u32 m0, s11, 0x13c00\n"
        "s_nop 0\n"
        "buffer_load_dwordx4 v14, s[24:27], s87 offen offset:3072 sc1 lds\n"
        "s_waitcnt vmcnt(0)\n"
        "ds_write_b8 v3, v4 offset:44\n"
        "s_mov_b32 s87, 0x200000\n"
        "Lpl56_%=:\n"
        "ds_read_b32 v5, v2 offset:36\n"
        "s_waitcnt lgkmcnt(0)\n"
        "v_readfirstlane_b32 s86, v5\n"
        "s_cmp_eq_u32 s86, 0x1010101\n"
        "s_cbranch_scc1 Lrt55_%=\n"
        "s_sub_u32 s87, s87, 1\n"
        "s_cbranch_scc1 Lrt55_%=\n"
        "s_sleep 2\n"
        "s_branch Lpl56_%=\n"
        "Lrt55_%=:\n"
        "s_mov_b32 s87, 0x1a800\n"
        "s_add_u32 m0, s11, 0x19000\n"
        "s_nop 0\n"
        "buffer_load_dwordx4 v14, s[24:27], s87 offen offset:0 sc1 lds\n"
        "s_add_u32 m0, s11, 0x19000\n"
        "s_nop 0\n"
        "buffer_load_dwordx4 v14, s[24:27], s87 offen offset:1024 sc1 lds\n"
        "s_add_u32 m0, s11, 0x19000\n"
        "s_nop 0\n"
        "buffer_load_dwordx4 v14, s[24:27], s87 offen offset:2048 sc1 lds\n"
        "s_add_u32 m0, s11, 0x19000\n"
        "s_nop 0\n"
        "buffer_load_dwordx4 v14, s[24:27], s87 offen offset:3072 sc1 lds\n"
        "s_mov_b32 s87, 0x1b800\n"
        "s_add_u32 m0, s11, 0x1a000\n"
        "s_nop 0\n"
        "buffer_load_dwordx4 v14, s[24:27], s87 offen offset:0 sc1 lds\n"
        "s_add_u32 m0, s11, 0x1a000\n"
        "s_nop 0\n"
        "buffer_load_dwordx4 v14, s[24:27], s87 offen offset:1024 sc1 lds\n"
        "s_add_u32 m0, s11, 0x1a000\n"
        "s_nop 0\n"
        "buffer_load_dwordx4 v14, s[24:27], s87 offen offset:2048 sc1 lds\n"
        "s_add_u32 m0, s11, 0x1a000\n"
        "s_nop 0\n"
        "buffer_load_dwordx4 v14, s[24:27], s87 offen offset:3072 sc1 lds\n"
        "s_waitcnt vmcnt(0)\n"
        "ds_write_b8 v3, v4 offset:48\n"
        "s_mov_b32 s87, 0x200000\n"
        "Lpl58_%=:\n"
        "ds_read_b32 v5, v2 offset:40\n"
        "s_waitcnt lgkmcnt(0)\n"
        "v_readfirstlane_b32 s86, v5\n"
        "s_cmp_eq_u32 s86, 0x1010101\n"
        "s_cbranch_scc1 Lrt57_%=\n"
        "s_sub_u32 s87, s87, 1\n"
        "s_cbranch_scc1 Lrt57_%=\n"
        "s_sleep 2\n"
        "s_branch Lpl58_%=\n"
        "Lrt57_%=:\n"
        "s_mov_b32 s87, 0x20800\n"
        "s_add_u32 m0, s11, 0x1f400\n"
        "s_nop 0\n"
        "buffer_load_dwordx4 v14, s[24:27], s87 offen offset:0 sc1 lds\n"
        "s_add_u32 m0, s11, 0x1f400\n"
        "s_nop 0\n"
        "buffer_load_dwordx4 v14, s[24:27], s87 offen offset:1024 sc1 lds\n"
        "s_add_u32 m0, s11, 0x1f400\n"
        "s_nop 0\n"
        "buffer_load_dwordx4 v14, s[24:27], s87 offen offset:2048 sc1 lds\n"
        "s_add_u32 m0, s11, 0x1f400\n"
        "s_nop 0\n"
        "buffer_load_dwordx4 v14, s[24:27], s87 offen offset:3072 sc1 lds\n"
        "s_mov_b32 s87, 0x21800\n"
        "s_add_u32 m0, s11, 0x20400\n"
        "s_nop 0\n"
        "buffer_load_dwordx4 v14, s[24:27], s87 offen offset:0 sc1 lds\n"
        "s_add_u32 m0, s11, 0x20400\n"
        "s_nop 0\n"
        "buffer_load_dwordx4 v14, s[24:27], s87 offen offset:1024 sc1 lds\n"
        "s_add_u32 m0, s11, 0x20400\n"
        "s_nop 0\n"
        "buffer_load_dwordx4 v14, s[24:27], s87 offen offset:2048 sc1 lds\n"
        "s_add_u32 m0, s11, 0x20400\n"
        "s_nop 0\n"
        "buffer_load_dwordx4 v14, s[24:27], s87 offen offset:3072 sc1 lds\n"
        "v_smfmac_f32_16x16x64_bf16 v[124:127], v[112:115], v[40:47], v17\n"
        "v_smfmac_f32_16x16x64_bf16 v[124:127], v[116:119], v[64:71], v17\n"
        "v_smfmac_f32_16x16x64_bf16 v[124:127], v[120:123], v[88:95], v17\n"
        "v_smfmac_f32_16x16x64_bf16 v[128:131], v[112:115], v[48:55], v17\n"
        "v_smfmac_f32_16x16x64_bf16 v[128:131], v[116:119], v[72:79], v17\n"
        "v_smfmac_f32_16x16x64_bf16 v[128:131], v[120:123], v[96:103], v17\n"
        "v_smfmac_f32_16x16x64_bf16 v[132:135], v[112:115], v[56:63], v17\n"
        "v_smfmac_f32_16x16x64_bf16 v[132:135], v[116:119], v[80:87], v17\n"
        "v_smfmac_f32_16x16x64_bf16 v[132:135], v[120:123], v[104:111], v17\n"
        "s_nop 1\n"
        "v_add_f32 v124, v124, v125\n"
        "s_nop 1\n"
        "v_add_f32 v128, v128, v129\n"
        "s_nop 1\n"
        "v_add_f32 v132, v132, v133\n"
        "v_cvt_pk_bf16_f32 v124, v124, v128\n"
        "v_cvt_pk_bf16_f32 v132, v132, v132\n"
        "ds_write_b16 v15, v124\n"
        "ds_write_b16_d16_hi v15, v124 offset:128\n"
        "ds_write_b16 v15, v132 offset:256\n"
        "ds_write_b8 v3, v4 offset:60\n"
        "s_waitcnt vmcnt(0)\n"
        "ds_write_b8 v3, v4 offset:52\n"
        "s_cmp_lg_u32 s3, 7\n"
        "s_cbranch_scc1 Lhcx_%=\n"
        "v_cmp_eq_u64 vcc, s[6:7], v[38:39]\n"
        "s_cmp_eq_u32 s2, 0\n"
        "s_cbranch_scc1 Lhcl_%=\n"
        "s_nop 4\n"
        "s_cbranch_vccz Lhcx_%=\n"
        "Lhcl_%=:\n"
        "s_mov_b32 s86, 0x60000\n"
        "buffer_load_dwordx4 v[160:163], v1, s[24:27], s86 offen sc1\n"
        "buffer_load_dwordx4 v[164:167], v1, s[24:27], s86 offen offset:1024 sc1\n"
        "s_waitcnt vmcnt(0)\n"
        "v_cmp_ne_u64 vcc, s[6:7], v[160:161]\n"
        "v_cmp_ne_u64 s[10:11], s[6:7], v[162:163]\n"
        "v_cmp_ne_u64 s[40:41], s[6:7], v[164:165]\n"
        "v_cmp_ne_u64 s[42:43], s[6:7], v[166:167]\n"
        "s_nop 1\n"
        "s_or_b64 vcc, vcc, s[10:11]\n"
        "s_or_b64 vcc, vcc, s[40:41]\n"
        "s_or_b64 vcc, vcc, s[42:43]\n"
        "s_cbranch_scc1 Lhcx_%=\n"
        "v_mov_b32 v12, 0x50000\n"
        "s_mov_b64 exec, 1\n"
        "buffer_store_dwordx2 v[10:11], v12, s[24:27], 0 offen sc1\n"
        "s_mov_b64 exec, -1\n"
        "Lhcx_%=:\n"
        "s_mul_i32 s89, s88, 25600\n"
        "v_add_u32 v14, s89, v1\n"
        "v_add_u32 v14, 0x12c00, v14\n"
        "s_lshl_b32 s89, s88, 7\n"
        "s_setprio 0\n"
        "s_mov_b32 s87, 0x200000\n"
        "Lpl60_%=:\n"
        "ds_read_b32 v5, v2 offset:52\n"
        "s_waitcnt lgkmcnt(0)\n"
        "v_readfirstlane_b32 s86, v5\n"
        "s_cmp_eq_u32 s86, 0x1010100\n"
        "s_cbranch_scc1 Lrt59_%=\n"
        "s_sub_u32 s87, s87, 1\n"
        "s_cbranch_scc1 Lrt59_%=\n"
        "s_sleep 1\n"
        "s_branch Lpl60_%=\n"
        "Lrt59_%=:\n"
        "ds_read_b128 v[40:43], v14 offset:0\n"
        "ds_read_b128 v[44:47], v14 offset:1024\n"
        "ds_read_b128 v[48:51], v14 offset:2048\n"
        "ds_read_b128 v[52:55], v14 offset:3072\n"
        "ds_read_b128 v[56:59], v14 offset:4096\n"
        "ds_read_b128 v[60:63], v14 offset:5120\n"
        "ds_read_b128 v[64:67], v14 offset:6144\n"
        "ds_read_b128 v[68:71], v14 offset:7168\n"
        "ds_read_b128 v[72:75], v14 offset:8192\n"
        "ds_read_b128 v[76:79], v14 offset:9216\n"
        "ds_read_b128 v[80:83], v14 offset:10240\n"
        "ds_read_b128 v[84:87], v14 offset:11264\n"
        "ds_read_b128 v[88:91], v14 offset:12288\n"
        "ds_read_b128 v[92:95], v14 offset:13312\n"
        "ds_read_b128 v[96:99], v14 offset:14336\n"
        "s_waitcnt lgkmcnt(12)\n"
        "ds_read_b128 v[100:103], v14 offset:15360\n"
        "ds_read_b128 v[104:107], v14 offset:16384\n"
        "ds_read_b128 v[108:111], v14 offset:17408\n"
        "s_waitcnt lgkmcnt(12)\n"
        "ds_read_b128 v[112:115], v14 offset:18432\n"
        "ds_read_b128 v[116:119], v14 offset:19456\n"
        "ds_read_b128 v[120:123], v14 offset:20480\n"
        "s_waitcnt lgkmcnt(12)\n"
        "ds_read_b128 v[124:127], v14 offset:21504\n"
        "ds_read_b128 v[128:131], v14 offset:22528\n"
        "ds_read_b128 v[132:135], v14 offset:23552\n"
        "s_mov_b32 s87, 0x200000\n"
        "Lpl62_%=:\n"
        "ds_read_b32 v5, v2 offset:64\n"
        "s_waitcnt lgkmcnt(0)\n"
        "v_readfirstlane_b32 s86, v5\n"
        "s_cmp_eq_u32 s86, 0x1\n"
        "s_cbranch_scc1 Lrt61_%=\n"
        "s_sub_u32 s87, s87, 1\n"
        "s_cbranch_scc1 Lrt61_%=\n"
        "s_sleep 2\n"
        "s_branch Lpl62_%=\n"
        "Lrt61_%=:\n"
        "s_setprio 3\n"
        "ds_read_b128 v[160:163], v16 offset:4096\n"
        "ds_read_b128 v[164:167], v16 offset:4224\n"
        "ds_read_b128 v[168:171], v16 offset:4352\n"
        "s_waitcnt lgkmcnt(2)\n"
        "v_smfmac_f32_16x16x64_bf16 v[136:139], v[160:163], v[40:47], v17\n"
        "s_waitcnt lgkmcnt(1)\n"
        "v_smfmac_f32_16x16x64_bf16 v[136:139], v[164:167], v[48:55], v17\n"
        "s_waitcnt lgkmcnt(0)\n"
        "v_smfmac_f32_16x16x64_bf16 v[136:139], v[168:171], v[56:63], v17\n"
        "v_smfmac_f32_16x16x64_bf16 v[140:143], v[160:163], v[64:71], v17\n"
        "v_smfmac_f32_16x16x64_bf16 v[140:143], v[164:167], v[72:79], v17\n"
        "v_smfmac_f32_16x16x64_bf16 v[140:143], v[168:171], v[80:87], v17\n"
        "v_smfmac_f32_16x16x64_bf16 v[144:147], v[160:163], v[88:95], v17\n"
        "v_smfmac_f32_16x16x64_bf16 v[144:147], v[164:167], v[96:103], v17\n"
        "v_smfmac_f32_16x16x64_bf16 v[144:147], v[168:171], v[104:111], v17\n"
        "v_smfmac_f32_16x16x64_bf16 v[148:151], v[160:163], v[112:119], v17\n"
        "v_smfmac_f32_16x16x64_bf16 v[148:151], v[164:167], v[120:127], v17\n"
        "v_smfmac_f32_16x16x64_bf16 v[148:151], v[168:171], v[128:135], v17\n"
        "v_add_f32 v136, v136, v137\n"
        "s_nop 0\n"
        "v_add_f32 v140, v140, v141\n"
        "s_nop 1\n"
        "v_add_f32 v144, v144, v145\n"
        "s_nop 1\n"
        "v_add_f32 v148, v148, v149\n"
        "v_cvt_pk_bf16_f32 v16, v136, v140\n"
        "v_cvt_pk_bf16_f32 v17, v144, v148\n"
        "buffer_store_dwordx2 v[16:17], v29, s[28:31], s89 offen sc1\n"
        "Lhend_%=:\n"
        "s_endpgm\n"
        "Lpriv_%=:\n"
        "s_mov_b32 s40, s3\n"
        "s_mov_b32 s41, 72\n"
        "s_branch Lp1_%=\n"
        "Lp2p_%=:\n"
        "s_mov_b32 s40, s3\n"
        "Lp2_%=:\n"
        "s_mov_b32 s38, 0x12000\n"
        "s_mov_b32 s39, 0x20000\n"
        "s_mul_i32 s42, s40, 43\n"
        "v_and_b32 v10, 12, v0\n"
        "s_lshr_b32 s42, s42, 8\n"
        "v_and_b32 v11, 15, v0\n"
        "s_mul_i32 s43, s42, 6\n"
        "v_add_u32 v10, v10, v11\n"
        "s_sub_u32 s43, s40, s43\n"
        "v_and_b32 v12, 48, v0\n"
        "v_mul_u32_u24 v10, 384, v10\n"
        "v_mul_u32_u24 v13, 1152, v11\n"
        "v_add_u32 v10, v10, v12\n"
        "v_add_u32 v13, v13, v12\n"
        "s_lshr_b32 s44, s42, 1\n"
        "s_lshl_b32 s44, s44, 6\n"
        "s_mul_i32 s45, s43, 36864\n"
        "s_add_u32 s46, s45, 18432\n"
        "buffer_load_dwordx4 v[40:43], v10, s[36:39], s44 offen\n"
        "buffer_load_dwordx4 v[44:47], v10, s[36:39], s44 offen offset:1536\n"
        "buffer_load_dwordx4 v[48:51], v13, s[32:35], s45 offen offset:768\n"
        "buffer_load_dwordx4 v[52:55], v13, s[32:35], s46 offen offset:768\n"
        "s_add_u32 s44, s44, 12288\n"
        "buffer_load_dwordx4 v[56:59], v10, s[36:39], s44 offen\n"
        "buffer_load_dwordx4 v[60:63], v10, s[36:39], s44 offen offset:1536\n"
        "buffer_load_dwordx4 v[64:67], v13, s[32:35], s45 offen offset:832\n"
        "buffer_load_dwordx4 v[68:71], v13, s[32:35], s46 offen offset:832\n"
        "s_add_u32 s44, s44, 12288\n"
        "buffer_load_dwordx4 v[72:75], v10, s[36:39], s44 offen\n"
        "buffer_load_dwordx4 v[76:79], v10, s[36:39], s44 offen offset:1536\n"
        "buffer_load_dwordx4 v[80:83], v13, s[32:35], s45 offen offset:896\n"
        "buffer_load_dwordx4 v[84:87], v13, s[32:35], s46 offen offset:896\n"
        "s_add_u32 s44, s44, 12288\n"
        "buffer_load_dwordx4 v[88:91], v10, s[36:39], s44 offen\n"
        "buffer_load_dwordx4 v[92:95], v10, s[36:39], s44 offen offset:1536\n"
        "buffer_load_dwordx4 v[96:99], v13, s[32:35], s45 offen offset:960\n"
        "buffer_load_dwordx4 v[100:103], v13, s[32:35], s46 offen offset:960\n"
        "s_add_u32 s44, s44, 12288\n"
        "buffer_load_dwordx4 v[104:107], v10, s[36:39], s44 offen\n"
        "buffer_load_dwordx4 v[108:111], v10, s[36:39], s44 offen offset:1536\n"
        "buffer_load_dwordx4 v[112:115], v13, s[32:35], s45 offen offset:1024\n"
        "buffer_load_dwordx4 v[116:119], v13, s[32:35], s46 offen offset:1024\n"
        "s_add_u32 s44, s44, 12288\n"
        "buffer_load_dwordx4 v[120:123], v10, s[36:39], s44 offen\n"
        "buffer_load_dwordx4 v[124:127], v10, s[36:39], s44 offen offset:1536\n"
        "buffer_load_dwordx4 v[128:131], v13, s[32:35], s45 offen offset:1088\n"
        "buffer_load_dwordx4 v[132:135], v13, s[32:35], s46 offen offset:1088\n"
        "s_add_u32 s44, s44, 12288\n"
        "s_and_b32 s44, s42, 1\n"
        "s_lshl_b32 s44, s44, 4\n"
        "v_add_u32 v14, s44, v11\n"
        "v_lshrrev_b32 v15, 1, v12\n"
        "v_sub_u32 v14, v14, v15\n"
        "v_and_b32 v15, 1, v14\n"
        "v_lshlrev_b32 v15, 4, v15\n"
        "v_mov_b32 v22, 0x3f80\n"
        "v_lshlrev_b32 v15, v15, v22\n"
        "v_ashrrev_i32 v14, 1, v14\n"
        "v_cmp_eq_u32 vcc, 0, v14\n"
        "s_nop 1\n"
        "v_cndmask_b32 v136, 0, v15, vcc\n"
        "v_cmp_eq_u32 vcc, 1, v14\n"
        "s_nop 1\n"
        "v_cndmask_b32 v137, 0, v15, vcc\n"
        "v_cmp_eq_u32 vcc, 2, v14\n"
        "s_nop 1\n"
        "v_cndmask_b32 v138, 0, v15, vcc\n"
        "v_cmp_eq_u32 vcc, 3, v14\n"
        "s_nop 1\n"
        "v_cndmask_b32 v139, 0, v15, vcc\n"
        "v_and_b32 v14, 3, v0\n"
        "v_mul_u32_u24 v14, 6144, v14\n"
        "v_lshl_add_u32 v14, v12, 4, v14\n"
        "v_and_b32 v15, 12, v0\n"
        "v_lshl_add_u32 v14, v15, 2, v14\n"
        "s_lshr_b32 s44, s42, 2\n"
        "s_mul_i32 s44, s44, 24\n"
        "s_add_u32 s44, s44, s43\n"
        "s_lshl_b32 s44, s44, 10\n"
        "s_and_b32 s45, s42, 3\n"
        "s_lshl_b32 s45, s45, 6\n"
        "s_add_u32 s44, s44, s45\n"
        "s_add_u32 s44, s44, 0x14800\n"
        "v_add_u32 v140, s44, v14\n"
        "s_waitcnt vmcnt(0)\n"
        "v_mfma_f32_16x16x32_bf16 v[224:227], v[40:43], v[136:139], 0\n"
        "v_mfma_f32_16x16x32_bf16 v[228:231], v[44:47], v[136:139], 0\n"
        "v_mfma_f32_16x16x32_bf16 v[232:235], v[56:59], v[136:139], 0\n"
        "v_mfma_f32_16x16x32_bf16 v[236:239], v[60:63], v[136:139], 0\n"
        "v_mfma_f32_16x16x32_bf16 v[240:243], v[72:75], v[136:139], 0\n"
        "v_mfma_f32_16x16x32_bf16 v[244:247], v[76:79], v[136:139], 0\n"
        "v_mfma_f32_16x16x32_bf16 v[248:251], v[88:91], v[136:139], 0\n"
        "v_mfma_f32_16x16x32_bf16 v[252:255], v[92:95], v[136:139], 0\n"
        "v_mfma_f32_16x16x32_bf16 v[144:147], v[104:107], v[136:139], 0\n"
        "v_mfma_f32_16x16x32_bf16 v[148:151], v[108:111], v[136:139], 0\n"
        "v_mfma_f32_16x16x32_bf16 v[192:195], v[120:123], v[136:139], 0\n"
        "v_mfma_f32_16x16x32_bf16 v[196:199], v[124:127], v[136:139], 0\n"
        "v_cvt_pk_bf16_f32 v40, v224, v225\n"
        "v_cvt_pk_bf16_f32 v41, v226, v227\n"
        "v_cvt_pk_bf16_f32 v42, v228, v229\n"
        "v_cvt_pk_bf16_f32 v43, v230, v231\n"
        "v_cvt_pk_bf16_f32 v56, v232, v233\n"
        "v_cvt_pk_bf16_f32 v57, v234, v235\n"
        "v_cvt_pk_bf16_f32 v58, v236, v237\n"
        "v_cvt_pk_bf16_f32 v59, v238, v239\n"
        "v_cvt_pk_bf16_f32 v72, v240, v241\n"
        "v_cvt_pk_bf16_f32 v73, v242, v243\n"
        "v_cvt_pk_bf16_f32 v74, v244, v245\n"
        "v_cvt_pk_bf16_f32 v75, v246, v247\n"
        "v_cvt_pk_bf16_f32 v88, v248, v249\n"
        "v_cvt_pk_bf16_f32 v89, v250, v251\n"
        "v_cvt_pk_bf16_f32 v90, v252, v253\n"
        "v_cvt_pk_bf16_f32 v91, v254, v255\n"
        "v_cvt_pk_bf16_f32 v104, v144, v145\n"
        "v_cvt_pk_bf16_f32 v105, v146, v147\n"
        "v_cvt_pk_bf16_f32 v106, v148, v149\n"
        "v_cvt_pk_bf16_f32 v107, v150, v151\n"
        "v_cvt_pk_bf16_f32 v120, v192, v193\n"
        "v_cvt_pk_bf16_f32 v121, v194, v195\n"
        "v_cvt_pk_bf16_f32 v122, v196, v197\n"
        "v_cvt_pk_bf16_f32 v123, v198, v199\n"
        "v_mfma_f32_16x16x32_bf16 v[224:227], v[48:51], v[40:43], 0\n"
        "v_mfma_f32_16x16x32_bf16 v[228:231], v[52:55], v[40:43], 0\n"
        "v_mfma_f32_16x16x32_bf16 v[224:227], v[64:67], v[56:59], v[224:227]\n"
        "v_mfma_f32_16x16x32_bf16 v[228:231], v[68:71], v[56:59], v[228:231]\n"
        "v_mfma_f32_16x16x32_bf16 v[224:227], v[80:83], v[72:75], v[224:227]\n"
        "v_mfma_f32_16x16x32_bf16 v[228:231], v[84:87], v[72:75], v[228:231]\n"
        "v_mfma_f32_16x16x32_bf16 v[224:227], v[96:99], v[88:91], v[224:227]\n"
        "v_mfma_f32_16x16x32_bf16 v[228:231], v[100:103], v[88:91], v[228:231]\n"
        "v_mfma_f32_16x16x32_bf16 v[224:227], v[112:115], v[104:107], v[224:227]\n"
        "v_mfma_f32_16x16x32_bf16 v[228:231], v[116:119], v[104:107], v[228:231]\n"
        "v_mfma_f32_16x16x32_bf16 v[224:227], v[128:131], v[120:123], v[224:227]\n"
        "v_mfma_f32_16x16x32_bf16 v[228:231], v[132:135], v[120:123], v[228:231]\n"
        "s_nop 6\n"
        "v_cvt_pk_bf16_f32 v232, v224, v225\n"
        "v_cvt_pk_bf16_f32 v233, v226, v227\n"
        "v_cvt_pk_bf16_f32 v234, v228, v229\n"
        "v_cvt_pk_bf16_f32 v235, v230, v231\n"
        "buffer_store_dwordx4 v[232:235], v140, s[24:27], s19 offen sc1\n"
        "s_add_u32 s40, s40, 8\n"
        "s_cmp_lt_u32 s40, s41\n"
        "s_cbranch_scc1 Lp2_%=\n"
        "s_waitcnt vmcnt(0)\n"
        "s_barrier\n"
        "s_branch Lpdone_%=\n"
        "Lrelight_%=:\n"
        "s_nop 7\n"
        "s_waitcnt vmcnt(0) lgkmcnt(0)\n"
        "s_barrier\n"
        "s_add_u32 s18, s18, 0x100\n"
        "s_bitcmp1_b32 s18, 10\n"
        "s_cbranch_scc1 Lfallback_%=\n"
        "s_cmp_ge_u32 s3, 4\n"
        "s_cselect_b32 s90, 40, s90\n"
        "s_cbranch_scc1 Lgre2_%=\n"
        "s_cmp_lg_u32 s3, 1\n"
        "s_cbranch_scc1 Lcre_%=\n"
        "v_mov_b32 v200, 0\n"
        "v_and_b32 v201, 63, v0\n"
        "v_lshl_add_u32 v201, v201, 2, v2\n"
        "ds_write_b32 v201, v200\n"
        "s_branch Lcre_%=\n"
        "Lfallback_%=:\n"
        "s_nop 7\n"
        "s_setprio 0\n"
        "s_waitcnt vmcnt(0) lgkmcnt(0)\n"
        "s_barrier\n"
        "s_andn2_b32 s18, s18, 0xff00\n"
        "s_add_u32 s18, s18, 1\n"
        "s_and_b32 s86, s18, 0xff\n"
        "s_cmp_lt_u32 s86, 3\n"
        "s_cbranch_scc1 Ltop_%=\n"
        "s_mul_i32 s19, s2, 196608\n"
        "s_add_u32 s19, s19, 0x100000\n"
        "s_cmp_lt_u32 s86, 100\n"
        "s_cbranch_scc1 Ltop_%=\n"
        "s_endpgm\n"
        "Lsl4_%=:\n"
        "s_mov_b32 s87, 0x200000\n"
        "Lpl6_%=:\n"
        "ds_read_b32 v5, v2 offset:0\n"
        "s_waitcnt lgkmcnt(0)\n"
        "v_readfirstlane_b32 s86, v5\n"
        "s_cmp_eq_u32 s86, 0x1010101\n"
        "s_cbranch_scc1 Lrt5_%=\n"
        "s_sub_u32 s87, s87, 1\n"
        "s_cbranch_scc1 Lrt5_%=\n"
        "s_branch Lpl6_%=\n"
        "Lsl7_%=:\n"
        "s_mov_b32 s87, 0x200000\n"
        "Lpl9_%=:\n"
        "ds_read_b32 v5, v2 offset:68\n"
        "s_waitcnt lgkmcnt(0)\n"
        "v_readfirstlane_b32 s86, v5\n"
        "s_cmp_eq_u32 s86, 0x1010101\n"
        "s_cbranch_scc1 Lrt8_%=\n"
        "s_sub_u32 s87, s87, 1\n"
        "s_cbranch_scc1 Lrt8_%=\n"
        "s_branch Lpl9_%=\n"
        "Lsl10_%=:\n"
        "s_mov_b32 s87, 0x200000\n"
        "Lpl12_%=:\n"
        "ds_read_b32 v5, v2 offset:4\n"
        "s_waitcnt lgkmcnt(0)\n"
        "v_readfirstlane_b32 s86, v5\n"
        "s_cmp_eq_u32 s86, 0x1010101\n"
        "s_cbranch_scc1 Lrt11_%=\n"
        "s_and_b32 s86, s86, 0x02020202\n"
        "s_cbranch_scc1 Lrelight_%=\n"
        "s_sub_u32 s87, s87, 1\n"
        "s_cbranch_scc1 Lrt11_%=\n"
        "s_branch Lpl12_%=\n"
        "Lsl13_%=:\n"
        "s_mov_b32 s87, 0x200000\n"
        "Lpl15_%=:\n"
        "ds_read_b32 v5, v2 offset:20\n"
        "s_waitcnt lgkmcnt(0)\n"
        "v_readfirstlane_b32 s86, v5\n"
        "s_cmp_eq_u32 s86, 0x1010101\n"
        "s_cbranch_scc1 Lrt14_%=\n"
        "s_sub_u32 s87, s87, 1\n"
        "s_cbranch_scc1 Lrt14_%=\n"
        "s_branch Lpl15_%=\n"
        "Lsl16_%=:\n"
        "s_mov_b32 s87, 0x200000\n"
        "Lpl18_%=:\n"
        "ds_read_b32 v5, v2 offset:24\n"
        "s_waitcnt lgkmcnt(0)\n"
        "v_readfirstlane_b32 s86, v5\n"
        "s_cmp_eq_u32 s86, 0x1010101\n"
        "s_cbranch_scc1 Lrt17_%=\n"
        "s_sub_u32 s87, s87, 1\n"
        "s_cbranch_scc1 Lrt17_%=\n"
        "s_branch Lpl18_%=\n"
        "Lsl19_%=:\n"
        "s_mov_b32 s87, 0x200000\n"
        "Lpl21_%=:\n"
        "ds_read_b32 v5, v2 offset:28\n"
        "s_waitcnt lgkmcnt(0)\n"
        "v_readfirstlane_b32 s86, v5\n"
        "s_cmp_eq_u32 s86, 0x1010101\n"
        "s_cbranch_scc1 Lrt20_%=\n"
        "s_sub_u32 s87, s87, 1\n"
        "s_cbranch_scc1 Lrt20_%=\n"
        "s_branch Lpl21_%=\n"
        "Lsl22_%=:\n"
        "s_mov_b32 s87, 0x200000\n"
        "Lpl24_%=:\n"
        "ds_read_b32 v5, v2 offset:44\n"
        "s_waitcnt lgkmcnt(0)\n"
        "v_readfirstlane_b32 s86, v5\n"
        "s_cmp_eq_u32 s86, 0x1010100\n"
        "s_cbranch_scc1 Lrt23_%=\n"
        "s_and_b32 s86, s86, 0x02020202\n"
        "s_cbranch_scc1 Lfallback_%=\n"
        "s_sub_u32 s87, s87, 1\n"
        "s_cbranch_scc1 Lrt23_%=\n"
        "s_branch Lpl24_%=\n"
        "Lsl25_%=:\n"
        "s_mov_b32 s87, 0x200000\n"
        "Lpl27_%=:\n"
        "ds_read_b32 v5, v2 offset:48\n"
        "s_waitcnt lgkmcnt(0)\n"
        "v_readfirstlane_b32 s86, v5\n"
        "s_cmp_eq_u32 s86, 0x1010100\n"
        "s_cbranch_scc1 Lrt26_%=\n"
        "s_sub_u32 s87, s87, 1\n"
        "s_cbranch_scc1 Lrt26_%=\n"
        "s_branch Lpl27_%=\n"
        "Lsl28_%=:\n"
        "s_mov_b32 s87, 0x200000\n"
        "Lpl30_%=:\n"
        "ds_read_b32 v5, v2 offset:52\n"
        "s_waitcnt lgkmcnt(0)\n"
        "v_readfirstlane_b32 s86, v5\n"
        "s_cmp_eq_u32 s86, 0x1010100\n"
        "s_cbranch_scc1 Lrt29_%=\n"
        "s_sub_u32 s87, s87, 1\n"
        "s_cbranch_scc1 Lrt29_%=\n"
        "s_branch Lpl30_%=\n"
        "Lgret_%=:\n"
        "s_sub_u32 s90, s90, 1\n"
        "s_cbranch_scc1 Lgok_%=\n"
        "s_sleep 4\n"
        "buffer_load_dwordx2 v[36:37], v15, s[24:27], s10 offen offset:1024 sc1\n"
        "s_waitcnt vmcnt(0)\n"
        "v_cmp_ne_u32 vcc, s7, v37\n"
        "s_nop 4\n"
        "s_cbranch_vccnz Lgret_%=\n"
        "Lgre2_%=:\n"
        "buffer_load_dwordx2 v[36:37], v15, s[24:27], s10 offen offset:1024 sc1\n"
        "s_mov_b32 s87, 0\n"
        "s_add_u32 m0, s11, 0x12c00\n"
        "s_nop 0\n"
        "buffer_load_dwordx4 v13, s[24:27], s87 offen sc1 lds\n"
        "s_mov_b32 s87, 1152\n"
        "s_add_u32 m0, s11, 0x13000\n"
        "s_nop 0\n"
        "buffer_load_dwordx4 v13, s[24:27], s87 offen sc1 lds\n"
        "s_mov_b32 s87, 2304\n"
        "s_add_u32 m0, s11, 0x13400\n"
        "s_nop 0\n"
        "buffer_load_dwordx4 v13, s[24:27], s87 offen sc1 lds\n"
        "s_mov_b32 s87, 3456\n"
        "s_add_u32 m0, s11, 0x13800\n"
        "s_nop 0\n"
        "buffer_load_dwordx4 v13, s[24:27], s87 offen sc1 lds\n"
        "s_mov_b32 s87, 4608\n"
        "s_add_u32 m0, s11, 0x13c00\n"
        "s_nop 0\n"
        "buffer_load_dwordx4 v13, s[24:27], s87 offen sc1 lds\n"
        "s_mov_b32 s87, 5760\n"
        "s_add_u32 m0, s11, 0x14000\n"
        "s_nop 0\n"
        "buffer_load_dwordx4 v13, s[24:27], s87 offen sc1 lds\n"
        "s_sleep 4\n"
        "s_mov_b32 s87, 27648\n"
        "s_add_u32 m0, s11, 0x19000\n"
        "s_nop 0\n"
        "buffer_load_dwordx4 v13, s[24:27], s87 offen sc1 lds\n"
        "s_mov_b32 s87, 28800\n"
        "s_add_u32 m0, s11, 0x19400\n"
        "s_nop 0\n"
        "buffer_load_dwordx4 v13, s[24:27], s87 offen sc1 lds\n"
        "s_mov_b32 s87, 29952\n"
        "s_add_u32 m0, s11, 0x19800\n"
        "s_nop 0\n"
        "buffer_load_dwordx4 v13, s[24:27], s87 offen sc1 lds\n"
        "s_mov_b32 s87, 31104\n"
        "s_add_u32 m0, s11, 0x19c00\n"
        "s_nop 0\n"
        "buffer_load_dwordx4 v13, s[24:27], s87 offen sc1 lds\n"
        "s_mov_b32 s87, 32256\n"
        "s_add_u32 m0, s11, 0x1a000\n"
        "s_nop 0\n"
        "buffer_load_dwordx4 v13, s[24:27], s87 offen sc1 lds\n"
        "s_mov_b32 s87, 33408\n"
        "s_add_u32 m0, s11, 0x1a400\n"
        "s_nop 0\n"
        "buffer_load_dwordx4 v13, s[24:27], s87 offen sc1 lds\n"
        "s_branch Lgchk_%=\n"
        :
        : "{s[0:1]}"(__builtin_amdgcn_kernarg_segment_ptr()), "{s2}"(blockIdx.x), "{v0}"(threadIdx.x), "{s[4:5]}"(na_tab), "{m0}"((unsigned)(size_t)smem)
        : "memory", "scc", "vcc", "v1", "v2", "v3", "v4", "v5", "v6", "v7", "v8", "v9", "v10", "v11", "v12", "v13", "v14", "v15", "v16", "v17", "v18", "v19", "v20", "v21", "v22", "v23", "v24", "v25", "v26", "v27", "v28", "v29", "v30", "v31", "v32", "v33", "v34", "v35", "v36", "v37", "v38", "v39", "v40", "v41", "v42", "v43", "v44", "v45", "v46", "v47", "v48", "v49", "v50", "v51", "v52", "v53", "v54", "v55", "v56", "v57", "v58", "v59", "v60", "v61", "v62", "v63", "v64", "v65", "v66", "v67", "v68", "v69", "v70", "v71", "v72", "v73", "v74", "v75", "v76", "v77", "v78", "v79", "v80", "v81", "v82", "v83", "v84", "v85", "v86", "v87", "v88", "v89", "v90", "v91", "v92", "v93", "v94", "v95", "v96", "v97", "v98", "v99", "v100", "v101", "v102", "v103", "v104", "v105", "v106", "v107", "v108", "v109", "v110", "v111", "v112", "v113", "v114", "v115", "v116", "v117", "v118", "v119", "v120", "v121", "v122", "v123", "v124", "v125", "v126", "v127", "v128", "v129", "v130", "v131", "v132", "v133", "v134", "v135", "v136", "v137", "v138", "v139", "v140", "v141", "v142", "v143", "v144", "v145", "v146", "v147", "v148", "v149", "v150", "v151", "v152", "v153", "v154", "v155", "v156", "v157", "v158", "v159", "v160", "v161", "v162", "v163", "v164", "v165", "v166", "v167", "v168", "v169", "v170", "v171", "v172", "v173", "v174", "v175", "v176", "v177", "v178", "v179", "v180", "v181", "v182", "v183", "v184", "v185", "v186", "v187", "v188", "v189", "v190", "v191", "v192", "v193", "v194", "v195", "v196", "v197", "v198", "v199", "v200", "v201", "v202", "v203", "v204", "v205", "v206", "v207", "v208", "v209", "v210", "v211", "v212", "v213", "v214", "v215", "v216", "v217", "v218", "v219", "v220", "v221", "v222", "v223", "v224", "v225", "v226", "v227", "v228", "v229", "v230", "v231", "v232", "v233", "v234", "v235", "v236", "v237", "v238", "v239", "v240", "v241", "v242", "v243", "v244", "v245", "v246", "v247", "v248", "v249", "v250", "v251", "v252", "v253", "v254", "v255", "s3", "s6", "s7", "s8", "s9", "s10", "s11", "s12", "s13", "s14", "s15", "s16", "s17", "s18", "s19", "s20", "s21", "s22", "s23", "s24", "s25", "s26", "s27", "s28", "s29", "s30", "s31", "s32", "s33", "s34", "s35", "s36", "s37", "s38", "s39", "s40", "s41", "s42", "s43", "s44", "s45", "s46", "s47", "s48", "s49", "s50", "s51", "s52", "s53", "s54", "s55", "s56", "s57", "s58", "s59", "s60", "s61", "s62", "s63", "s64", "s65", "s66", "s67", "s68", "s69", "s70", "s71", "s72", "s73", "s74", "s75", "s76", "s77", "s78", "s79", "s80", "s81", "s82", "s83", "s84", "s85", "s86", "s87", "s88", "s89", "s90", "s91", "s92", "s93", "s94", "s95", "s96", "s97", "s98", "s99", "s100", "s101");
}

extern "C" void kernel_launch(void* const* d_in, const int* in_sizes, int n_in,
                              void* d_out, int out_size, void* d_ws, size_t ws_size,
                              hipStream_t stream) {
    na_main<<<256, 512, 0, stream>>>(d_in[0], d_in[1], d_in[2], d_ws, d_out);
}
```

```cpp
#include <hip/hip_runtime.h>

__device__ const unsigned na_tab[3840] = {
    0x12500b4, 0x2050194, 0x16a, 0x0, 0xf110711, 0x9120112, 0x5131112, 0x10f010f, 0x15f00ee, 0x23f01ce, 0x16a, 0x0,
    0xf110711, 0x9120112, 0x5131112, 0x10f010f, 0x12900b8, 0x2090198, 0x16a, 0x0, 0x10110811, 0xa120212, 0x6131212, 0x20f020f,
    0x16300f2, 0x24301d2, 0x16a, 0x0, 0x10110811, 0xa120212, 0x6131212, 0x20f020f, 0x12c00bd, 0x20c019d, 0x16f, 0x0,
    0x11110911, 0xb120312, 0x7131312, 0x11101110, 0x16600f7, 0x24601d7, 0x16f, 0x0, 0x11110911, 0xb120312, 0x7131312, 0x11101110,
    0x13000c1, 0x21001a1, 0x16f, 0x0, 0x12110a11, 0xc120412, 0x8130012, 0x12101210, 0x16a00fb, 0x24a01db, 0x16f, 0x0,
    0x12110a11, 0xc120412, 0x8130012, 0x12101210, 0x13500c4, 0x21501a4, 0x1a1, 0x0, 0x13110b11, 0xd120512, 0x9130113, 0x13101310,
    0x16f00fe, 0x24f01de, 0x1a1, 0x0, 0x13110b11, 0xd120512, 0x9130113, 0x13101310, 0x13900c8, 0x21901a8, 0x1a1, 0x0,
    0x110c11, 0xe120612, 0xa130213, 0x100010, 0x1730102, 0x25301e2, 0x1a1, 0x0, 0x110c11, 0xe120612, 0xa130213, 0x100010,
    0x13c00cd, 0x21c01ad, 0x1a4, 0x0, 0x1110d11, 0xf120712, 0xb130313, 0x1100110, 0x1760107, 0x25601e7, 0x1a4, 0x0,
    0x1110d11, 0xf120712, 0xb130313, 0x1100110, 0x14000d1, 0x22001b1, 0x1a4, 0x0, 0x2110e11, 0x10120812, 0xc130413, 0x2100210,
    0x17a010b, 0x25a01eb, 0x1a4, 0x0, 0x2110e11, 0x10120812, 0xc130413, 0x2100210, 0x3c000, 0x5557d555, 0x3c000, 0x3c000,
    0x3c000, 0x3c000, 0xaaabeaaa, 0x3c000, 0x0, 0x0, 0x0, 0x0, 0x0, 0x0, 0x0, 0x0,
    0x0, 0x0, 0x0, 0x0, 0x0, 0x0, 0x0, 0x0, 0x0, 0x0, 0x0, 0x0,
    0x0, 0x0, 0x0, 0x0, 0x0, 0x0, 0x0, 0x0, 0x0, 0x0, 0x0, 0x0,
    0x0, 0x0, 0x0, 0x0, 0x0, 0x0, 0x0, 0x0, 0x0, 0x0, 0x0, 0x0,
    0x0, 0x0, 0x0, 0x0, 0x0, 0x0, 0x0, 0x0, 0x0, 0x0, 0x0, 0x0,
    0x12500b4, 0x2050194, 0x16a, 0x0, 0x1000d13, 0x11000900, 0xd010501, 0x10f010f, 0x15f00ee, 0x23f01ce, 0x16a, 0x0,
    0x1000d13, 0x11000900, 0xd010501, 0x10f010f, 0x12900b8, 0x2090198, 0x16a, 0x0, 0x2000e13, 0x12000a00, 0xe010601, 0x20f020f,
    0x16300f2, 0x24301d2, 0x16a, 0x0, 0x2000e13, 0x12000a00, 0xe010601, 0x20f020f, 0x12c00bd, 0x20c019d, 0x16f, 0x0,
    0x3000f13, 0x13000b00, 0xf010701, 0x11101110, 0x16600f7, 0x24601d7, 0x16f, 0x0, 0x3000f13, 0x13000b00, 0xf010701, 0x11101110,
    0x13000c1, 0x21001a1, 0x16f, 0x0, 0x4001013, 0xc00, 0x10010801, 0x12101210, 0x16a00fb, 0x24a01db, 0x16f, 0x0,
    0x4001013, 0xc00, 0x10010801, 0x12101210, 0x13500c4, 0x21501a4, 0x1a1, 0x0, 0x5001113, 0x1010d00, 0x11010901, 0x13101310,
    0x16f00fe, 0x24f01de, 0x1a1, 0x0, 0x5001113, 0x1010d00, 0x11010901, 0x13101310, 0x13900c8, 0x21901a8, 0x1a1, 0x0,
    0x6001213, 0x2010e00, 0x12010a01, 0x100010, 0x1730102, 0x25301e2, 0x1a1, 0x0, 0x6001213, 0x2010e00, 0x12010a01, 0x100010,
    0x13c00cd, 0x21c01ad, 0x1a4, 0x0, 0x7001313, 0x3010f00, 0x13010b01, 0x1100110, 0x1760107, 0x25601e7, 0x1a4, 0x0,
    0x7001313, 0x3010f00, 0x13010b01, 0x1100110, 0x14000d1, 0x22001b1, 0x1a4, 0x0, 0x8000013, 0x4011000, 0x10c01, 0x2100210,
    0x17a010b, 0x25a01eb, 0x1a4, 0x0, 0x8000013, 0x4011000, 0x10c01, 0x2100210, 0x3c000, 0x5557d555, 0x3c000, 0x3c000,
    0x3c000, 0x3c000, 0xaaabeaaa, 0x3c000, 0x0, 0x0, 0x0, 0x0, 0x0, 0x0, 0x0, 0x0,
    0x0, 0x0, 0x0, 0x0, 0x0, 0x0, 0x0, 0x0, 0x0, 0x0, 0x0, 0x0,
    0x0, 0x0, 0x0, 0x0, 0x0, 0x0, 0x0, 0x0, 0x0, 0x0, 0x0, 0x0,
    0x0, 0x0, 0x0, 0x0, 0x0, 0x0, 0x0, 0x0, 0x0, 0x0, 0x0, 0x0,
    0x0, 0x0, 0x0, 0x0, 0x0, 0x0, 0x0, 0x0, 0x0, 0x0, 0x0, 0x0,
    0x12500b4, 0x2050194, 0x16a, 0x0, 0x9020102, 0x1031102, 0x13061105, 0x10f010f, 0x15f00ee, 0x23f01ce, 0x16a, 0x0,
    0x9020102, 0x1031102, 0x13061105, 0x10f010f, 0x12900b8, 0x2090198, 0x16a, 0x0, 0xa020202, 0x2031202, 0x61205, 0x20f020f,
    0x16300f2, 0x24301d2, 0x16a, 0x0, 0xa020202, 0x2031202, 0x61205, 0x20f020f, 0x12c00bd, 0x20c019d, 0x16f, 0x0,
    0xb020302, 0x11041302, 0x1061305, 0x11101110, 0x16600f7, 0x24601d7, 0x16f, 0x0, 0xb020302, 0x11041302, 0x1061305, 0x11101110,
    0x13000c1, 0x21001a1, 0x16f, 0x0, 0xc020402, 0x12040002, 0x2060005, 0x12101210, 0x16a00fb, 0x24a01db, 0x16f, 0x0,
    0xc020402, 0x12040002, 0x2060005, 0x12101210, 0x13500c4, 0x21501a4, 0x1a1, 0x0, 0xd020502, 0x13041103, 0x11070105, 0x13101310,
    0x16f00fe, 0x24f01de, 0x1a1, 0x0, 0xd020502, 0x13041103, 0x11070105, 0x13101310, 0x13900c8, 0x21901a8, 0x1a1, 0x0,
    0xe020602, 0x41203, 0x12070205, 0x100010, 0x1730102, 0x25301e2, 0x1a1, 0x0, 0xe020602, 0x41203, 0x12070205, 0x100010,
    0x13c00cd, 0x21c01ad, 0x1a4, 0x0, 0xf020702, 0x1041303, 0x13071106, 0x1100110, 0x1760107, 0x25601e7, 0x1a4, 0x0,
    0xf020702, 0x1041303, 0x13071106, 0x1100110, 0x14000d1, 0x22001b1, 0x1a4, 0x0, 0x10020802, 0x2040003, 0x71206, 0x2100210,
    0x17a010b, 0x25a01eb, 0x1a4, 0x0, 0x10020802, 0x2040003, 0x71206, 0x2100210, 0x3c000, 0x5557d555, 0x3c000, 0x3c000,
    0x3c000, 0x3c000, 0xaaabeaaa, 0x3c000, 0x0, 0x0, 0x0, 0x0, 0x0, 0x0, 0x0, 0x0,
    0x0, 0x0, 0x0, 0x0, 0x0, 0x0, 0x0, 0x0, 0x0, 0x0, 0x0, 0x0,
    0x0, 0x0, 0x0, 0x0, 0x0, 0x0, 0x0, 0x0, 0x0, 0x0, 0x0, 0x0,
    0x0, 0x0, 0x0, 0x0, 0x0, 0x0, 0x0, 0x0, 0x0, 0x0, 0x0, 0x0,
    0x0, 0x0, 0x0, 0x0, 0x0, 0x0, 0x0, 0x0, 0x0, 0x0, 0x0, 0x0,
    0x12500b4, 0x2050194, 0x16a, 0x0, 0x11090107, 0x10b130a, 0x130e110d, 0x10f010f, 0x15f00ee, 0x23f01ce, 0x16a, 0x0,
    0x11090107, 0x10b130a, 0x130e110d, 0x10f010f, 0x12900b8, 0x2090198, 0x16a, 0x0, 0x12090207, 0x20b000a, 0xe120d, 0x20f020f,
    0x16300f2, 0x24301d2, 0x16a, 0x0, 0x12090207, 0x20b000a, 0xe120d, 0x20f020f, 0x12c00bd, 0x20c019d, 0x16f, 0x0,
    0x13091108, 0x110c010a, 0x10e130d, 0x11101110, 0x16600f7, 0x24601d7, 0x16f, 0x0, 0x13091108, 0x110c010a, 0x10e130d, 0x11101110,
    0x13000c1, 0x21001a1, 0x16f, 0x0, 0x91208, 0x120c020a, 0x20e000d, 0x12101210, 0x16a00fb, 0x24a01db, 0x16f, 0x0,
    0x91208, 0x120c020a, 0x20e000d, 0x12101210, 0x13500c4, 0x21501a4, 0x1a1, 0x0, 0x1091308, 0x130c110b, 0x110f010d, 0x13101310,
    0x16f00fe, 0x24f01de, 0x1a1, 0x0, 0x1091308, 0x130c110b, 0x110f010d, 0x13101310, 0x13900c8, 0x21901a8, 0x1a1, 0x0,
    0x2090008, 0xc120b, 0x120f020d, 0x100010, 0x1730102, 0x25301e2, 0x1a1, 0x0, 0x2090008, 0xc120b, 0x120f020d, 0x100010,
    0x13c00cd, 0x21c01ad, 0x1a4, 0x0, 0x110a0108, 0x10c130b, 0x130f110e, 0x1100110, 0x1760107, 0x25601e7, 0x1a4, 0x0,
    0x110a0108, 0x10c130b, 0x130f110e, 0x1100110, 0x14000d1, 0x22001b1, 0x1a4, 0x0, 0x120a0208, 0x20c000b, 0xf120e, 0x2100210,
    0x17a010b, 0x25a01eb, 0x1a4, 0x0, 0x120a0208, 0x20c000b, 0xf120e, 0x2100210, 0x3c000, 0x5557d555, 0x3c000, 0x3c000,
    0x3c000, 0x3c000, 0xaaabeaaa, 0x3c000, 0x0, 0x0, 0x0, 0x0, 0x0, 0x0, 0x0, 0x0,
    0x0, 0x0, 0x0, 0x0, 0x0, 0x0, 0x0, 0x0, 0x0, 0x0, 0x0, 0x0,
    0x0, 0x0, 0x0, 0x0, 0x0, 0x0, 0x0, 0x0, 0x0, 0x0, 0x0, 0x0,
    0x0, 0x0, 0x0, 0x0, 0x0, 0x0, 0x0, 0x0, 0x0, 0x0, 0x0, 0x0,
    0x0, 0x0, 0x0, 0x0, 0x0, 0x0, 0x0, 0x0, 0x0, 0x0, 0x0, 0x0,
    0xa590a8c, 0xaf80b2d, 0x1290bb4, 0x1980be5, 0x490b54, 0x20b5d, 0xe20073, 0x0, 0xaab0ade, 0x120b3f, 0x1630bce, 0x1d20bff,
    0x830b6e, 0x390b77, 0x11900a8, 0x0, 0xa5c0a40, 0xafd0ae1, 0x12c0bb8, 0x19d0be9, 0x4c0b58, 0x70050, 0xe70076, 0x0,
    0xaae0a92, 0x170b43, 0x1660bd2, 0x1d70c03, 0x860b72, 0x3c008a, 0x11c00ad, 0x0, 0xa600a45, 0xb010ae4, 0x1300bbd, 0x1a10bec,
    0xb80b85, 0xb0b8c, 0xeb007a, 0x0, 0xab20a97, 0x1b0b46, 0x16a0bd7, 0x1db0c06, 0xf20b9f, 0x400ba6, 0x12000b1, 0x0,
    0xa650a49, 0xb040ae8, 0x1350119, 0x1a40188, 0xbd0b89, 0xe00c1, 0xee007f, 0x0, 0xab70a9b, 0x1e0002, 0x16f0153, 0x1de01c2,
    0xf70ba3, 0x4500fb, 0x12500b4, 0x0, 0xa690a4c, 0xb080aed, 0x139011c, 0x1a8018d, 0x59003c, 0x120045, 0xf20083, 0x0,
    0xabb0a9e, 0x220007, 0x1730156, 0x1e201c7, 0x930076, 0x49007f, 0x12900b8, 0x0, 0xa6c0a50, 0xb0d0af1, 0x13c0120, 0x1ad0191,
    0x5c0040, 0x170060, 0xf70086, 0x0, 0xabe0aa2, 0x27000b, 0x176015a, 0x1e701cb, 0x96007a, 0x4c009a, 0x12c00bd, 0x0,
    0xa700a55, 0xb110af4, 0x1400125, 0x1b10194, 0xc800ad, 0x1b00b4, 0xfb008a, 0x0, 0xac20aa7, 0x2b000e, 0x17a015f, 0x1eb01ce,
    0x10200e7, 0x5000ee, 0x13000c1, 0x0, 0x550039, 0xc400a8, 0x550039, 0xc400a8, 0xcd00b1, 0x1e00d1, 0xfe008f, 0x0,
    0x8f0073, 0xfe00e2, 0x8f0073, 0xfe00e2, 0x10700eb, 0x55010b, 0x13500c4, 0x0, 0x3f0003, 0x3fff03ff, 0xff000f, 0xff0fff,
    0x30003, 0x3ff03ff, 0xf000f, 0xfff0fff, 0x3f003f, 0x3fff3fff, 0xaaaaaaaa, 0xaaaaaaaa, 0xaaaaaaaa, 0xaaaa, 0x0, 0x55550000,
    0x55550000, 0x55550000, 0xaaaaaaaa, 0xaaaaaaaa, 0xaaaaaaaa, 0xaaaa, 0x55555555, 0x55555555, 0x55555555, 0x5555, 0x0, 0x0,
    0x0, 0xaaaa0000, 0x0, 0x0, 0x55555555, 0x55555555, 0x55555555, 0x5555, 0xaaaa, 0xaaaa, 0x0, 0x0,
    0x0, 0x0, 0x0, 0x0, 0x0, 0x0, 0x0, 0x0, 0x0, 0x0, 0x0, 0x0,
    0x0, 0x0, 0x0, 0x0, 0x0, 0x0, 0x0, 0x0, 0x0, 0x0, 0x0, 0x0,
    0xf70a5c, 0xfe0a65, 0x1170a7c, 0xb960a85, 0x1070a6c, 0x220b0d, 0x32002b, 0x0, 0xfb0a60, 0x1020a69, 0xb930a80, 0xb9a0a89,
    0x10b0a70, 0x270b11, 0x37002e, 0x0, 0x12c0aae, 0x1350ab7, 0x14c0ace, 0xbad0ad7, 0x13c0abe, 0x590176, 0x690060, 0x0,
    0x1300ab2, 0x1390abb, 0xba80ad2, 0xbb10adb, 0x1400ac2, 0x5c017a, 0x6c0065, 0x0, 0x1660afd, 0x16f0b04, 0x1860b1d, 0xbc70b24,
    0x10e0a75, 0x930b14, 0xa3009a, 0x0, 0x16a0b01, 0x1730b08, 0xbc20b21, 0xbcb0b28, 0x1120a79, 0x960b18, 0xa6009f, 0x0,
    0x19d0017, 0x1a4001e, 0x1bd0037, 0xbdc0b36, 0x1450ac7, 0xc8017f, 0xd800d1, 0x0, 0x1a1001b, 0x1a80022, 0xbd90b33, 0xbe00b3a,
    0x1490acb, 0xcd0183, 0xdd00d4, 0x0, 0x1d7004c, 0x1de0055, 0x1f7006c, 0xbf60b4d, 0x1e7005c, 0x10200cd, 0x112010b, 0x0,
    0x1db0050, 0x1e20059, 0xbf30b48, 0xbfa0b51, 0x1eb0060, 0x10700d1, 0x117010e, 0x0, 0x20c0086, 0x215008f, 0x22c00a6, 0xc0d0b67,
    0x21c0096, 0x1390256, 0x1490140, 0x0, 0x210008a, 0x2190093, 0xc080b62, 0xc110b6b, 0x220009a, 0x13c025a, 0x14c0145, 0x0,
    0x24600bd, 0x24f00c4, 0x26600dd, 0xc270b7c, 0x1ee0065, 0x17300d4, 0x183017a, 0x0, 0x24a00c1, 0x25300c8, 0xc220b79, 0xc2b0b80,
    0x1f20069, 0x17600d8, 0x186017f, 0x0, 0x1ad0027, 0x1b4002e, 0x1ad0027, 0x1b4002e, 0x225009f, 0x1a8025f, 0x1b801b1, 0x0,
    0x1b1002b, 0x1b80032, 0x1b1002b, 0x1b80032, 0x22900a3, 0x1ad0263, 0x1bd01b4, 0x0, 0x3f0003, 0x3fff03ff, 0xff000f, 0xff0fff,
    0x30003, 0x3ff03ff, 0xf000f, 0xfff0fff, 0x3f003f, 0x3fff3fff, 0xaaaaaaaa, 0xaaaaaaaa, 0xaaaaaaaa, 0xaaaa, 0x0, 0x55550000,
    0x55550000, 0x55550000, 0xaaaaaaaa, 0xaaaaaaaa, 0xaaaaaaaa, 0xaaaa, 0x55555555, 0x55555555, 0x55555555, 0x5555, 0x0, 0x0,
    0x0, 0xaaaa0000, 0x0, 0x0, 0x55555555, 0x55555555, 0x55555555, 0x5555, 0xaaaa, 0xaaaa, 0x0, 0x0,
    0x0, 0x0, 0x0, 0x0, 0x0, 0x0, 0x0, 0x0, 0x0, 0x0, 0x0, 0x0,
    0x0, 0x0, 0x0, 0x0, 0x0, 0x0, 0x0, 0x0, 0x0, 0x0, 0x0, 0x0,
    0x1400125, 0x1b10194, 0x30002e5, 0x9d109b4, 0x2200205, 0x1db020c, 0x2bb024a, 0x0, 0x17a015f, 0x1eb01ce, 0x97a031f, 0xa230a06,
    0x25a023f, 0x2100246, 0x2f00281, 0x0, 0x1450129, 0x1b40198, 0x30502e9, 0x9d409b8, 0x2250209, 0x1de0229, 0x2be024f, 0x0,
    0x17f0163, 0x1ee01d2, 0x97f0963, 0xa260a0a, 0x25f0243, 0x2150263, 0x2f50284, 0x0, 0x149012c, 0x1b8019d, 0x30902ec, 0x9d809bd,
    0x2910274, 0x1e2027d, 0x2c20253, 0x0, 0x1830166, 0x1f201d7, 0x9830966, 0xa2a0a0f, 0x2cb02ae, 0x21902b7, 0x2f90288, 0x0,
    0x14c0130, 0x1bd01a1, 0x30c02f0, 0x9dd09c1, 0x2940278, 0x1e70298, 0x2c70256, 0x0, 0x186016a, 0x1f701db, 0x986096a, 0xa2f0a13,
    0x2ce02b2, 0x21c02d2, 0x2fc028d, 0x0, 0xba80135, 0xbd901a4, 0xc6802f5, 0x9e109c4, 0xc080215, 0x1eb021c, 0x2cb025a, 0x0,
    0xbc2016f, 0xbf301de, 0x98a096f, 0xa330a16, 0xc22024f, 0x2200256, 0x3000291, 0x0, 0xbad0139, 0xbdc01a8, 0xc6d02f9, 0x9e409c8,
    0xc0d0219, 0x1ee0c11, 0x2ce025f, 0x0, 0xbc70173, 0xbf601e2, 0x98f0973, 0xa360a1a, 0xc270253, 0x2250c2b, 0x3050294, 0x0,
    0xbb1013c, 0xbe001ad, 0xc7102fc, 0x9e809cd, 0xc390284, 0x1f2028d, 0x2d20263, 0x0, 0xbcb0176, 0xbfa01e7, 0x9930976, 0xa3a0a1f,
    0xc5302be, 0x22902c7, 0x3090298, 0x0, 0x22c0210, 0x29d0281, 0x22c0210, 0x29d0281, 0xc3c0288, 0x1f70c40, 0x2d70266, 0x0,
    0x266024a, 0x2d702bb, 0x266024a, 0x2d702bb, 0xc5602c2, 0x22c0c5a, 0x30c029d, 0x0, 0x3f0003, 0x3fff03ff, 0xff000f, 0xff0fff,
    0x30003, 0x3ff03ff, 0xf000f, 0xfff0fff, 0x3f003f, 0x3fff3fff, 0xaaaaaaaa, 0xaaaaaaaa, 0xaaaaaaaa, 0xaaaa, 0x0, 0x55550000,
    0x55550000, 0x55550000, 0xaaaaaaaa, 0xaaaaaaaa, 0xaaaaaaaa, 0xaaaa, 0x55555555, 0x55555555, 0x55555555, 0x5555, 0x0, 0x0,
    0x0, 0xaaaa0000, 0x0, 0x0, 0x55555555, 0x55555555, 0x55555555, 0x5555, 0xaaaa, 0xaaaa, 0x0, 0x0,
    0x0, 0x0, 0x0, 0x0, 0x0, 0x0, 0x0, 0x0, 0x0, 0x0, 0x0, 0x0,
    0x0, 0x0, 0x0, 0x0, 0x0, 0x0, 0x0, 0x0, 0x0, 0x0, 0x0, 0x0,
    0xc2e0b85, 0xc370b8c, 0x24600bd, 0x24f00c4, 0x23600ad, 0x153011c, 0x163015a, 0x0, 0xc320b89, 0x23300a8, 0x24a00c1, 0x25300c8,
    0x23a00b1, 0x1560120, 0x166015f, 0x0, 0xc450b9f, 0xc4c0ba6, 0x27d00f7, 0x28400fe, 0x26d00e7, 0x18802a7, 0x1980191, 0x0,
    0xc490ba3, 0x26800e2, 0x28100fb, 0x2880102, 0x27100eb, 0x18d02ab, 0x19d0194, 0x0, 0xc5f0bb4, 0xc660bbd, 0x2b7012c, 0x2be0135,
    0x23f00b4, 0x1c20125, 0x1d201cb, 0x0, 0xc630bb8, 0x2a20119, 0x2bb0130, 0x2c20139, 0x24300b8, 0x1c70129, 0x1d701ce, 0x0,
    0xc740bce, 0xc7d0bd7, 0x2ec0166, 0x2f5016f, 0x27400ee, 0x1f902ae, 0x2090200, 0x0, 0xc780bd2, 0x2d90153, 0x2f0016a, 0x2f90173,
    0x27800f2, 0x1fc02b2, 0x20c0205, 0x0, 0x9960be5, 0x99f0bec, 0x966019d, 0x96f01a4, 0x316018d, 0x23301fc, 0x243023a, 0x0,
    0x99a0be9, 0x3130188, 0x96a01a1, 0x97301a8, 0x31a0191, 0x2360200, 0x246023f, 0x0, 0x9ed0bff, 0x9a40c06, 0x9bd01d7, 0x9c401de,
    0x9ad01c7, 0x26809ff, 0x2780271, 0x0, 0x9a10c03, 0x9a801c2, 0x9c101db, 0x9c801e2, 0x9b101cb, 0x26d0a03, 0x27d0274, 0x0,
    0xa3f0c14, 0x9f60c1d, 0xa0f020c, 0xa160215, 0x31f0194, 0x2a20205, 0x2b202ab, 0x0, 0x9f30c18, 0x9fa01f9, 0xa130210, 0xa1a0219,
    0x9630198, 0x2a70209, 0x2b702ae, 0x0, 0x2dc0156, 0x2e5015f, 0x2dc0156, 0x2e5015f, 0x9b401ce, 0x2d90a06, 0x2e902e0, 0x0,
    0x2e0015a, 0x2e90163, 0x2e0015a, 0x2e90163, 0x9b801d2, 0x2dc0a0a, 0x2ec02e5, 0x0, 0x3f0003, 0x3fff03ff, 0xff000f, 0xff0fff,
    0x30003, 0x3ff03ff, 0xf000f, 0xfff0fff, 0x3f003f, 0x3fff3fff, 0xaaaaaaaa, 0xaaaaaaaa, 0xaaaaaaaa, 0xaaaa, 0x0, 0x55550000,
    0x55550000, 0x55550000, 0xaaaaaaaa, 0xaaaaaaaa, 0xaaaaaaaa, 0xaaaa, 0x55555555, 0x55555555, 0x55555555, 0x5555, 0x0, 0x0,
    0x0, 0xaaaa0000, 0x0, 0x0, 0x55555555, 0x55555555, 0x55555555, 0x5555, 0xaaaa, 0xaaaa, 0x0, 0x0,
    0x0, 0x0, 0x0, 0x0, 0x0, 0x0, 0x0, 0x0, 0x0, 0x0, 0x0, 0x0,
    0x0, 0x0, 0x0, 0x0, 0x0, 0x0, 0x0, 0x0, 0x0, 0x0, 0x0, 0x0,
    0xa590a8c, 0xaf80b2d, 0x1290bb4, 0x1980be5, 0x490b54, 0xe0b5d, 0xee007f, 0x0, 0xaab0ade, 0x120b3f, 0x1630bce, 0x1d20bff,
    0x830b6e, 0x450b77, 0x12500b4, 0x0, 0xa5c0a40, 0xafd0ae1, 0x12c0bb8, 0x19d0be9, 0x4c0b58, 0x20050, 0xe20073, 0x0,
    0xaae0a92, 0x170b43, 0x1660bd2, 0x1d70c03, 0x860b72, 0x39008a, 0x11900a8, 0x0, 0xa600a45, 0xb010ae4, 0x1300bbd, 0x1a10bec,
    0xb80b85, 0x120b8c, 0xf20083, 0x0, 0xab20a97, 0x1b0b46, 0x16a0bd7, 0x1db0c06, 0xf20b9f, 0x490ba6, 0x12900b8, 0x0,
    0xa650a49, 0xb040ae8, 0x1350119, 0x1a40188, 0xbd0b89, 0x700c1, 0xe70076, 0x0, 0xab70a9b, 0x1e0002, 0x16f0153, 0x1de01c2,
    0xf70ba3, 0x3c00fb, 0x11c00ad, 0x0, 0xa690a4c, 0xb080aed, 0x139011c, 0x1a8018d, 0x59003c, 0x170045, 0xf70086, 0x0,
    0xabb0a9e, 0x220007, 0x1730156, 0x1e201c7, 0x930076, 0x4c007f, 0x12c00bd, 0x0, 0xa6c0a50, 0xb0d0af1, 0x13c0120, 0x1ad0191,
    0x5c0040, 0xb0060, 0xeb007a, 0x0, 0xabe0aa2, 0x27000b, 0x176015a, 0x1e701cb, 0x96007a, 0x40009a, 0x12000b1, 0x0,
    0xa700a55, 0xb110af4, 0x1400125, 0x1b10194, 0xc800ad, 0x1b00b4, 0xfb008a, 0x0, 0xac20aa7, 0x2b000e, 0x17a015f, 0x1eb01ce,
    0x10200e7, 0x5000ee, 0x13000c1, 0x0, 0x550039, 0xc400a8, 0x550039, 0xc400a8, 0xcd00b1, 0x1e00d1, 0xfe008f, 0x0,
    0x8f0073, 0xfe00e2, 0x8f0073, 0xfe00e2, 0x10700eb, 0x55010b, 0x13500c4, 0x0, 0x0, 0x3fff0000, 0x0, 0x0,
    0x0, 0x0, 0x0, 0x0, 0x0, 0x3fff3fff, 0xaaaaaaaa, 0xaaaaaaaa, 0xaaaaaaaa, 0xaaaa, 0x0, 0x55550000,
    0x55550000, 0x55550000, 0xaaaaaaaa, 0xaaaaaaaa, 0xaaaaaaaa, 0xaaaa, 0x55555555, 0x55555555, 0x55555555, 0x5555, 0x0, 0x0,
    0x0, 0xaaaa0000, 0x0, 0x0, 0x55555555, 0x55555555, 0x55555555, 0x5555, 0xaaaa, 0xaaaa, 0x0, 0x0,
    0x0, 0x0, 0x0, 0x0, 0x0, 0x0, 0x0, 0x0, 0x0, 0x0, 0x0, 0x0,
    0x0, 0x0, 0x0, 0x0, 0x0, 0x0, 0x0, 0x0, 0x0, 0x0, 0x0, 0x0,
    0xf70a5c, 0xfe0a65, 0x1170a7c, 0xb960a85, 0x1070a6c, 0x220b0d, 0x32002b, 0x0, 0xfb0a60, 0x1020a69, 0xb930a80, 0xb9a0a89,
    0x10b0a70, 0x270b11, 0x37002e, 0x0, 0x12c0aae, 0x1350ab7, 0x14c0ace, 0xbad0ad7, 0x13c0abe, 0x590176, 0x690060, 0x0,
    0x1300ab2, 0x1390abb, 0xba80ad2, 0xbb10adb, 0x1400ac2, 0x5c017a, 0x6c0065, 0x0, 0x1660afd, 0x16f0b04, 0x1860b1d, 0xbc70b24,
    0x10e0a75, 0x930b14, 0xa3009a, 0x0, 0x16a0b01, 0x1730b08, 0xbc20b21, 0xbcb0b28, 0x1120a79, 0x960b18, 0xa6009f, 0x0,
    0x19d0017, 0x1a4001e, 0x1bd0037, 0xbdc0b36, 0x1450ac7, 0xc8017f, 0xd800d1, 0x0, 0x1a1001b, 0x1a80022, 0xbd90b33, 0xbe00b3a,
    0x1490acb, 0xcd0183, 0xdd00d4, 0x0, 0x1d7004c, 0x1de0055, 0x1f7006c, 0xbf60b4d, 0x1e7005c, 0x10200cd, 0x112010b, 0x0,
    0x1db0050, 0x1e20059, 0xbf30b48, 0xbfa0b51, 0x1eb0060, 0x10700d1, 0x117010e, 0x0, 0x20c0086, 0x215008f, 0x22c00a6, 0xc0d0b67,
    0x21c0096, 0x1390256, 0x1490140, 0x0, 0x210008a, 0x2190093, 0xc080b62, 0xc110b6b, 0x220009a, 0x13c025a, 0x14c0145, 0x0,
    0x24600bd, 0x24f00c4, 0x26600dd, 0xc270b7c, 0x1ee0065, 0x17300d4, 0x183017a, 0x0, 0x24a00c1, 0x25300c8, 0xc220b79, 0xc2b0b80,
    0x1f20069, 0x17600d8, 0x186017f, 0x0, 0x1ad0027, 0x1b4002e, 0x1ad0027, 0x1b4002e, 0x225009f, 0x1a8025f, 0x1b801b1, 0x0,
    0x1b1002b, 0x1b80032, 0x1b1002b, 0x1b80032, 0x22900a3, 0x1ad0263, 0x1bd01b4, 0x0, 0x3f0003, 0x3fff03ff, 0xff000f, 0xff0fff,
    0x30003, 0x3ff03ff, 0xf000f, 0xfff0fff, 0x3f003f, 0x3fff3fff, 0xaaaaaaaa, 0xaaaaaaaa, 0xaaaaaaaa, 0xaaaa, 0x0, 0x55550000,
    0x55550000, 0x55550000, 0xaaaaaaaa, 0xaaaaaaaa, 0xaaaaaaaa, 0xaaaa, 0x55555555, 0x55555555, 0x55555555, 0x5555, 0x0, 0x0,
    0x0, 0xaaaa0000, 0x0, 0x0, 0x55555555, 0x55555555, 0x55555555, 0x5555, 0xaaaa, 0xaaaa, 0x0, 0x0,
    0x0, 0x0, 0x0, 0x0, 0x0, 0x0, 0x0, 0x0, 0x0, 0x0, 0x0, 0x0,
    0x0, 0x0, 0x0, 0x0, 0x0, 0x0, 0x0, 0x0, 0x0, 0x0, 0x0, 0x0,
    0x1400125, 0x1b10194, 0x30002e5, 0x9d109b4, 0x2200205, 0x1db020c, 0x2bb024a, 0x0, 0x17a015f, 0x1eb01ce, 0x97a031f, 0xa230a06,
    0x25a023f, 0x2100246, 0x2f00281, 0x0, 0x1450129, 0x1b40198, 0x30502e9, 0x9d409b8, 0x2250209, 0x1de0229, 0x2be024f, 0x0,
    0x17f0163, 0x1ee01d2, 0x97f0963, 0xa260a0a, 0x25f0243, 0x2150263, 0x2f50284, 0x0, 0x149012c, 0x1b8019d, 0x30902ec, 0x9d809bd,
    0x2910274, 0x1e2027d, 0x2c20253, 0x0, 0x1830166, 0x1f201d7, 0x9830966, 0xa2a0a0f, 0x2cb02ae, 0x21902b7, 0x2f90288, 0x0,
    0x14c0130, 0x1bd01a1, 0x30c02f0, 0x9dd09c1, 0x2940278, 0x1e70298, 0x2c70256, 0x0, 0x186016a, 0x1f701db, 0x986096a, 0xa2f0a13,
    0x2ce02b2, 0x21c02d2, 0x2fc028d, 0x0, 0xba80135, 0xbd901a4, 0xc6802f5, 0x9e109c4, 0xc080215, 0x1eb021c, 0x2cb025a, 0x0,
    0xbc2016f, 0xbf301de, 0x98a096f, 0xa330a16, 0xc22024f, 0x2200256, 0x3000291, 0x0, 0xbad0139, 0xbdc01a8, 0xc6d02f9, 0x9e409c8,
    0xc0d0219, 0x1ee0c11, 0x2ce025f, 0x0, 0xbc70173, 0xbf601e2, 0x98f0973, 0xa360a1a, 0xc270253, 0x2250c2b, 0x3050294, 0x0,
    0xbb1013c, 0xbe001ad, 0xc7102fc, 0x9e809cd, 0xc390284, 0x1f2028d, 0x2d20263, 0x0, 0xbcb0176, 0xbfa01e7, 0x9930976, 0xa3a0a1f,
    0xc5302be, 0x22902c7, 0x3090298, 0x0, 0x22c0210, 0x29d0281, 0x22c0210, 0x29d0281, 0xc3c0288, 0x1f70c40, 0x2d70266, 0x0,
    0x266024a, 0x2d702bb, 0x266024a, 0x2d702bb, 0xc5602c2, 0x22c0c5a, 0x30c029d, 0x0, 0x3f0003, 0x3fff03ff, 0xff000f, 0xff0fff,
    0x30003, 0x3ff03ff, 0xf000f, 0xfff0fff, 0x3f003f, 0x3fff3fff, 0xaaaaaaaa, 0xaaaaaaaa, 0xaaaaaaaa, 0xaaaa, 0x0, 0x55550000,
    0x55550000, 0x55550000, 0xaaaaaaaa, 0xaaaaaaaa, 0xaaaaaaaa, 0xaaaa, 0x55555555, 0x55555555, 0x55555555, 0x5555, 0x0, 0x0,
    0x0, 0xaaaa0000, 0x0, 0x0, 0x55555555, 0x55555555, 0x55555555, 0x5555, 0xaaaa, 0xaaaa, 0x0, 0x0,
    0x0, 0x0, 0x0, 0x0, 0x0, 0x0, 0x0, 0x0, 0x0, 0x0, 0x0, 0x0,
    0x0, 0x0, 0x0, 0x0, 0x0, 0x0, 0x0, 0x0, 0x0, 0x0, 0x0, 0x0,
    0x25a00d1, 0x23f00b4, 0x25600cd, 0x23f00b4, 0x24600bd, 0x15f012c, 0x1660163, 0x0, 0x25600cd, 0x24300b8, 0x25a00d1, 0x24300b8,
    0x24a00c1, 0x1530130, 0x15a0156, 0x0, 0x291010b, 0x27400ee, 0x28d0107, 0x27400ee, 0x27d00f7, 0x19402b7, 0x19d0198, 0x0,
    0x28d0107, 0x27800f2, 0x291010b, 0x27800f2, 0x28100fb, 0x18802bb, 0x191018d, 0x0, 0x2cb0140, 0x2ae0125, 0x2c7013c, 0x2ae0125,
    0x24f00c4, 0x1ce0135, 0x1d701d2, 0x0, 0x2c7013c, 0x2b20129, 0x2cb0140, 0x2b20129, 0x25300c8, 0x1c20139, 0x1cb01c7, 0x0,
    0x300017a, 0x2e5015f, 0x2fc0176, 0x2e5015f, 0x28400fe, 0x20502be, 0x20c0209, 0x0, 0x2fc0176, 0x2e90163, 0x300017a, 0x2e90163,
    0x2880102, 0x1f902c2, 0x20001fc, 0x0, 0x97a01b1, 0x31f0194, 0x97601ad, 0x31f0194, 0x966019d, 0x23f020c, 0x2460243, 0x0,
    0x97601ad, 0x9630198, 0x97a01b1, 0x9630198, 0x96a01a1, 0x2330210, 0x23a0236, 0x0, 0x9d101eb, 0x9b401ce, 0x9cd01e7, 0x9b401ce,
    0x9bd01d7, 0x2740a0f, 0x27d0278, 0x0, 0x9cd01e7, 0x9b801d2, 0x9d101eb, 0x9b801d2, 0x9c101db, 0x2680a13, 0x271026d, 0x0,
    0xa230220, 0xa060205, 0xa1f021c, 0xa060205, 0x96f01a4, 0x2ae0215, 0x2b702b2, 0x0, 0xa1f021c, 0xa0a0209, 0xa230220, 0xa0a0209,
    0x97301a8, 0x2a20219, 0x2ab02a7, 0x0, 0x2ec0166, 0x2f5016f, 0x2ec0166, 0x2f5016f, 0x9c401de, 0x2e50a16, 0x2ec02e9, 0x0,
    0x2f0016a, 0x2f90173, 0x2f0016a, 0x2f90173, 0x9c801e2, 0x2d90a1a, 0x2e002dc, 0x0, 0x3f0003, 0x3fff03ff, 0xff000f, 0xff0fff,
    0x30003, 0x3ff03ff, 0xf000f, 0xfff0fff, 0x3f003f, 0x3fff3fff, 0xffffffff, 0xffffffff, 0xffffffff, 0xffff, 0x0, 0x0,
    0x0, 0x0, 0x0, 0x0, 0x0, 0x0, 0xffffffff, 0xffffffff, 0xffffffff, 0xffff, 0xffffffff, 0xffffffff,
    0x0, 0x0, 0xffffffff, 0xffff, 0x0, 0x0, 0x0, 0x0, 0x0, 0x0, 0x0, 0x0,
    0x0, 0x0, 0x0, 0x0, 0x0, 0x0, 0x0, 0x0, 0x0, 0x0, 0x0, 0x0,
    0x0, 0x0, 0x0, 0x0, 0x0, 0x0, 0x0, 0x0, 0x0, 0x0, 0x0, 0x0,
    0x2430c2e, 0xb80b85, 0x2090c14, 0xb80b85, 0x1290bb4, 0xa80bbd, 0x11900e2, 0x0, 0x2090c14, 0xf20b9f, 0x2430c2e, 0xf20b9f,
    0x1630bce, 0x20bd7, 0x730039, 0x0, 0x2460c32, 0xbd0b89, 0x20c0c18, 0xbd0b89, 0x12c0bb8, 0xad0130, 0x11c00e7, 0x0,
    0x20c0c18, 0xf70ba3, 0x2460c32, 0xf70ba3, 0x1660bd2, 0x7016a, 0x76003c, 0x0, 0x24a0c37, 0xc10b8c, 0x2100c1d, 0xc10b8c,
    0x1980be5, 0xb10bec, 0x12000eb, 0x0, 0x2100c1d, 0xfb0ba6, 0x24a0c37, 0xfb0ba6, 0x1d20bff, 0xb0c06, 0x7a0040, 0x0,
    0x24f0233, 0xc400a8, 0x21501f9, 0xc400a8, 0x19d0be9, 0xb401a1, 0x12500ee, 0x0, 0x21501f9, 0xfe00e2, 0x24f0233, 0xfe00e2,
    0x1d70c03, 0xe01db, 0x7f0045, 0x0, 0x2530236, 0xc800ad, 0x21901fc, 0xc800ad, 0x139011c, 0xb80125, 0x12900f2, 0x0,
    0x21901fc, 0x10200e7, 0x2530236, 0x10200e7, 0x1730156, 0x12015f, 0x830049, 0x0, 0x256023a, 0xcd00b1, 0x21c0200, 0xcd00b1,
    0x13c0120, 0xbd0140, 0x12c00f7, 0x0, 0x21c0200, 0x10700eb, 0x256023a, 0x10700eb, 0x176015a, 0x17017a, 0x86004c, 0x0,
    0x25a023f, 0xd100b4, 0x2200205, 0xd100b4, 0x1a8018d, 0xc10194, 0x13000fb, 0x0, 0x2200205, 0x10b00ee, 0x25a023f, 0x10b00ee,
    0x1e201c7, 0x1b01ce, 0x8a0050, 0x0, 0x1350119, 0x1a40188, 0x1350119, 0x1a40188, 0x1ad0191, 0xc401b1, 0x13500fe, 0x0,
    0x16f0153, 0x1de01c2, 0x16f0153, 0x1de01c2, 0x1e701cb, 0x1e01eb, 0x8f0055, 0x0, 0x3f0003, 0x3fff03ff, 0xff000f, 0xff0fff,
    0x30003, 0x3ff03ff, 0xf000f, 0xfff0fff, 0x3f003f, 0x3fff3fff, 0xffffffff, 0xffffffff, 0xffffffff, 0xffff, 0x0, 0x0,
    0x0, 0x0, 0x0, 0x0, 0x0, 0x0, 0xffffffff, 0xffffffff, 0xffffffff, 0xffff, 0xffffffff, 0xffffffff,
    0x0, 0x0, 0xffffffff, 0xffff, 0x0, 0x0, 0x0, 0x0, 0x0, 0x0, 0x0, 0x0,
    0x0, 0x0, 0x0, 0x0, 0x0, 0x0, 0x0, 0x0, 0x0, 0x0, 0x0, 0x0,
    0x0, 0x0, 0x0, 0x0, 0x0, 0x0, 0x0, 0x0, 0x0, 0x0, 0x0, 0x0,
    0xf70a5c, 0xfe0a65, 0x1170a7c, 0xb960a85, 0x1070a6c, 0xc80b0d, 0xd800d1, 0x0, 0xfb0a60, 0x1020a69, 0xb930a80, 0xb9a0a89,
    0x10b0a70, 0xcd0b11, 0xdd00d4, 0x0, 0x12c0aae, 0x1350ab7, 0x14c0ace, 0xbad0ad7, 0x13c0abe, 0x220176, 0x32002b, 0x0,
    0x1300ab2, 0x1390abb, 0xba80ad2, 0xbb10adb, 0x1400ac2, 0x27017a, 0x37002e, 0x0, 0x1660afd, 0x16f0b04, 0x1860b1d, 0xbc70b24,
    0x10e0a75, 0x1020b14, 0x112010b, 0x0, 0x16a0b01, 0x1730b08, 0xbc20b21, 0xbcb0b28, 0x1120a79, 0x1070b18, 0x117010e, 0x0,
    0x19d0017, 0x1a4001e, 0x1bd0037, 0xbdc0b36, 0x1450ac7, 0x59017f, 0x690060, 0x0, 0x1a1001b, 0x1a80022, 0xbd90b33, 0xbe00b3a,
    0x1490acb, 0x5c0183, 0x6c0065, 0x0, 0x1d7004c, 0x1de0055, 0x1f7006c, 0xbf60b4d, 0x1e7005c, 0x13900cd, 0x1490140, 0x0,
    0x1db0050, 0x1e20059, 0xbf30b48, 0xbfa0b51, 0x1eb0060, 0x13c00d1, 0x14c0145, 0x0, 0x20c0086, 0x215008f, 0x22c00a6, 0xc0d0b67,
    0x21c0096, 0x930256, 0xa3009a, 0x0, 0x210008a, 0x2190093, 0xc080b62, 0xc110b6b, 0x220009a, 0x96025a, 0xa6009f, 0x0,
    0x24600bd, 0x24f00c4, 0x26600dd, 0xc270b7c, 0x1ee0065, 0x17300d4, 0x183017a, 0x0, 0x24a00c1, 0x25300c8, 0xc220b79, 0xc2b0b80,
    0x1f20069, 0x17600d8, 0x186017f, 0x0, 0x1ad0027, 0x1b4002e, 0x1ad0027, 0x1b4002e, 0x225009f, 0x1a8025f, 0x1b801b1, 0x0,
    0x1b1002b, 0x1b80032, 0x1b1002b, 0x1b80032, 0x22900a3, 0x1ad0263, 0x1bd01b4, 0x0, 0x0, 0x3fff0000, 0x0, 0x0,
    0x0, 0x0, 0x0, 0x0, 0x0, 0x3fff3fff, 0xaaaaaaaa, 0xaaaaaaaa, 0xaaaaaaaa, 0xaaaa, 0x0, 0x55550000,
    0x55550000, 0x55550000, 0xaaaaaaaa, 0xaaaaaaaa, 0xaaaaaaaa, 0xaaaa, 0x55555555, 0x55555555, 0x55555555, 0x5555, 0x0, 0x0,
    0x0, 0xaaaa0000, 0x0, 0x0, 0x55555555, 0x55555555, 0x55555555, 0x5555, 0xaaaa, 0xaaaa, 0x0, 0x0,
    0x0, 0x0, 0x0, 0x0, 0x0, 0x0, 0x0, 0x0, 0x0, 0x0, 0x0, 0x0,
    0x0, 0x0, 0x0, 0x0, 0x0, 0x0, 0x0, 0x0, 0x0, 0x0, 0x0, 0x0,
    0x1400125, 0x1b10194, 0x30002e5, 0x9d109b4, 0x2200205, 0x1db020c, 0x2bb024a, 0x0, 0x17a015f, 0x1eb01ce, 0x97a031f, 0xa230a06,
    0x25a023f, 0x2100246, 0x2f00281, 0x0, 0x1450129, 0x1b40198, 0x30502e9, 0x9d409b8, 0x2250209, 0x1de0229, 0x2be024f, 0x0,
    0x17f0163, 0x1ee01d2, 0x97f0963, 0xa260a0a, 0x25f0243, 0x2150263, 0x2f50284, 0x0, 0x149012c, 0x1b8019d, 0x30902ec, 0x9d809bd,
    0x2910274, 0x1e2027d, 0x2c20253, 0x0, 0x1830166, 0x1f201d7, 0x9830966, 0xa2a0a0f, 0x2cb02ae, 0x21902b7, 0x2f90288, 0x0,
    0x14c0130, 0x1bd01a1, 0x30c02f0, 0x9dd09c1, 0x2940278, 0x1e70298, 0x2c70256, 0x0, 0x186016a, 0x1f701db, 0x986096a, 0xa2f0a13,
    0x2ce02b2, 0x21c02d2, 0x2fc028d, 0x0, 0xba80135, 0xbd901a4, 0xc6802f5, 0x9e109c4, 0xc080215, 0x1eb021c, 0x2cb025a, 0x0,
    0xbc2016f, 0xbf301de, 0x98a096f, 0xa330a16, 0xc22024f, 0x2200256, 0x3000291, 0x0, 0xbad0139, 0xbdc01a8, 0xc6d02f9, 0x9e409c8,
    0xc0d0219, 0x1ee0c11, 0x2ce025f, 0x0, 0xbc70173, 0xbf601e2, 0x98f0973, 0xa360a1a, 0xc270253, 0x2250c2b, 0x3050294, 0x0,
    0xbb1013c, 0xbe001ad, 0xc7102fc, 0x9e809cd, 0xc390284, 0x1f2028d, 0x2d20263, 0x0, 0xbcb0176, 0xbfa01e7, 0x9930976, 0xa3a0a1f,
    0xc5302be, 0x22902c7, 0x3090298, 0x0, 0x22c0210, 0x29d0281, 0x22c0210, 0x29d0281, 0xc3c0288, 0x1f70c40, 0x2d70266, 0x0,
    0x266024a, 0x2d702bb, 0x266024a, 0x2d702bb, 0xc5602c2, 0x22c0c5a, 0x30c029d, 0x0, 0x3f0003, 0x3fff03ff, 0xff000f, 0xff0fff,
    0x30003, 0x3ff03ff, 0xf000f, 0xfff0fff, 0x3f003f, 0x3fff3fff, 0xaaaaaaaa, 0xaaaaaaaa, 0xaaaaaaaa, 0xaaaa, 0x0, 0x55550000,
    0x55550000, 0x55550000, 0xaaaaaaaa, 0xaaaaaaaa, 0xaaaaaaaa, 0xaaaa, 0x55555555, 0x55555555, 0x55555555, 0x5555, 0x0, 0x0,
    0x0, 0xaaaa0000, 0x0, 0x0, 0x55555555, 0x55555555, 0x55555555, 0x5555, 0xaaaa, 0xaaaa, 0x0, 0x0,
    0x0, 0x0, 0x0, 0x0, 0x0, 0x0, 0x0, 0x0, 0x0, 0x0, 0x0, 0x0,
    0x0, 0x0, 0x0, 0x0, 0x0, 0x0, 0x0, 0x0, 0x0, 0x0, 0x0, 0x0,
    0xc2e0b85, 0xc370b8c, 0x24600bd, 0x24f00c4, 0x23600ad, 0x153011c, 0x163015a, 0x0, 0xc320b89, 0x23300a8, 0x24a00c1, 0x25300c8,
    0x23a00b1, 0x1560120, 0x166015f, 0x0, 0xc450b9f, 0xc4c0ba6, 0x27d00f7, 0x28400fe, 0x26d00e7, 0x18802a7, 0x1980191, 0x0,
    0xc490ba3, 0x26800e2, 0x28100fb, 0x2880102, 0x27100eb, 0x18d02ab, 0x19d0194, 0x0, 0xc5f0bb4, 0xc660bbd, 0x2b7012c, 0x2be0135,
    0x23f00b4, 0x1c20125, 0x1d201cb, 0x0, 0xc630bb8, 0x2a20119, 0x2bb0130, 0x2c20139, 0x24300b8, 0x1c70129, 0x1d701ce, 0x0,
    0xc740bce, 0xc7d0bd7, 0x2ec0166, 0x2f5016f, 0x27400ee, 0x1f902ae, 0x2090200, 0x0, 0xc780bd2, 0x2d90153, 0x2f0016a, 0x2f90173,
    0x27800f2, 0x1fc02b2, 0x20c0205, 0x0, 0x9960be5, 0x99f0bec, 0x966019d, 0x96f01a4, 0x316018d, 0x23301fc, 0x243023a, 0x0,
    0x99a0be9, 0x3130188, 0x96a01a1, 0x97301a8, 0x31a0191, 0x2360200, 0x246023f, 0x0, 0x9ed0bff, 0x9a40c06, 0x9bd01d7, 0x9c401de,
    0x9ad01c7, 0x26809ff, 0x2780271, 0x0, 0x9a10c03, 0x9a801c2, 0x9c101db, 0x9c801e2, 0x9b101cb, 0x26d0a03, 0x27d0274, 0x0,
    0xa3f0c14, 0x9f60c1d, 0xa0f020c, 0xa160215, 0x31f0194, 0x2a20205, 0x2b202ab, 0x0, 0x9f30c18, 0x9fa01f9, 0xa130210, 0xa1a0219,
    0x9630198, 0x2a70209, 0x2b702ae, 0x0, 0x2dc0156, 0x2e5015f, 0x2dc0156, 0x2e5015f, 0x9b401ce, 0x2d90a06, 0x2e902e0, 0x0,
    0x2e0015a, 0x2e90163, 0x2e0015a, 0x2e90163, 0x9b801d2, 0x2dc0a0a, 0x2ec02e5, 0x0, 0x3f0003, 0x3fff03ff, 0xff000f, 0xff0fff,
    0x30003, 0x3ff03ff, 0xf000f, 0xfff0fff, 0x3f003f, 0x3fff3fff, 0xaaaaaaaa, 0xaaaaaaaa, 0xaaaaaaaa, 0xaaaa, 0x0, 0x55550000,
    0x55550000, 0x55550000, 0xaaaaaaaa, 0xaaaaaaaa, 0xaaaaaaaa, 0xaaaa, 0x55555555, 0x55555555, 0x55555555, 0x5555, 0x0, 0x0,
    0x0, 0xaaaa0000, 0x0, 0x0, 0x55555555, 0x55555555, 0x55555555, 0x5555, 0xaaaa, 0xaaaa, 0x0, 0x0,
    0x0, 0x0, 0x0, 0x0, 0x0, 0x0, 0x0, 0x0, 0x0, 0x0, 0x0, 0x0,
    0x0, 0x0, 0x0, 0x0, 0x0, 0x0, 0x0, 0x0, 0x0, 0x0, 0x0, 0x0,
    0x2430c2e, 0xb80b85, 0x2090c14, 0xb80b85, 0x1290bb4, 0xb40bbd, 0x12500ee, 0x0, 0x2090c14, 0xf20b9f, 0x2430c2e, 0xf20b9f,
    0x1630bce, 0xe0bd7, 0x7f0045, 0x0, 0x2460c32, 0xbd0b89, 0x20c0c18, 0xbd0b89, 0x12c0bb8, 0xa80130, 0x11900e2, 0x0,
    0x20c0c18, 0xf70ba3, 0x2460c32, 0xf70ba3, 0x1660bd2, 0x2016a, 0x730039, 0x0, 0x24a0c37, 0xc10b8c, 0x2100c1d, 0xc10b8c,
    0x1980be5, 0xb80bec, 0x12900f2, 0x0, 0x2100c1d, 0xfb0ba6, 0x24a0c37, 0xfb0ba6, 0x1d20bff, 0x120c06, 0x830049, 0x0,
    0x24f0233, 0xc400a8, 0x21501f9, 0xc400a8, 0x19d0be9, 0xad01a1, 0x11c00e7, 0x0, 0x21501f9, 0xfe00e2, 0x24f0233, 0xfe00e2,
    0x1d70c03, 0x701db, 0x76003c, 0x0, 0x2530236, 0xc800ad, 0x21901fc, 0xc800ad, 0x139011c, 0xbd0125, 0x12c00f7, 0x0,
    0x21901fc, 0x10200e7, 0x2530236, 0x10200e7, 0x1730156, 0x17015f, 0x86004c, 0x0, 0x256023a, 0xcd00b1, 0x21c0200, 0xcd00b1,
    0x13c0120, 0xb10140, 0x12000eb, 0x0, 0x21c0200, 0x10700eb, 0x256023a, 0x10700eb, 0x176015a, 0xb017a, 0x7a0040, 0x0,
    0x25a023f, 0xd100b4, 0x2200205, 0xd100b4, 0x1a8018d, 0xc10194, 0x13000fb, 0x0, 0x2200205, 0x10b00ee, 0x25a023f, 0x10b00ee,
    0x1e201c7, 0x1b01ce, 0x8a0050, 0x0, 0x1350119, 0x1a40188, 0x1350119, 0x1a40188, 0x1ad0191, 0xc401b1, 0x13500fe, 0x0,
    0x16f0153, 0x1de01c2, 0x16f0153, 0x1de01c2, 0x1e701cb, 0x1e01eb, 0x8f0055, 0x0, 0x0, 0x3fff0000, 0x0, 0x0,
    0x0, 0x0, 0x0, 0x0, 0x0, 0x3fff3fff, 0xffffffff, 0xffffffff, 0xffffffff, 0xffff, 0x0, 0x0,
    0x0, 0x0, 0x0, 0x0, 0x0, 0x0, 0xffffffff, 0xffffffff, 0xffffffff, 0xffff, 0xffffffff, 0xffffffff,
    0x0, 0x0, 0xffffffff, 0xffff, 0x0, 0x0, 0x0, 0x0, 0x0, 0x0, 0x0, 0x0,
    0x0, 0x0, 0x0, 0x0, 0x0, 0x0, 0x0, 0x0, 0x0, 0x0, 0x0, 0x0,
    0x0, 0x0, 0x0, 0x0, 0x0, 0x0, 0x0, 0x0, 0x0, 0x0, 0x0, 0x0,
    0xf70a5c, 0xfe0a65, 0x1170a7c, 0xb960a85, 0x1070a6c, 0xc80b0d, 0xd800d1, 0x0, 0xfb0a60, 0x1020a69, 0xb930a80, 0xb9a0a89,
    0x10b0a70, 0xcd0b11, 0xdd00d4, 0x0, 0x12c0aae, 0x1350ab7, 0x14c0ace, 0xbad0ad7, 0x13c0abe, 0x220176, 0x32002b, 0x0,
    0x1300ab2, 0x1390abb, 0xba80ad2, 0xbb10adb, 0x1400ac2, 0x27017a, 0x37002e, 0x0, 0x1660afd, 0x16f0b04, 0x1860b1d, 0xbc70b24,
    0x10e0a75, 0x1020b14, 0x112010b, 0x0, 0x16a0b01, 0x1730b08, 0xbc20b21, 0xbcb0b28, 0x1120a79, 0x1070b18, 0x117010e, 0x0,
    0x19d0017, 0x1a4001e, 0x1bd0037, 0xbdc0b36, 0x1450ac7, 0x59017f, 0x690060, 0x0, 0x1a1001b, 0x1a80022, 0xbd90b33, 0xbe00b3a,
    0x1490acb, 0x5c0183, 0x6c0065, 0x0, 0x1d7004c, 0x1de0055, 0x1f7006c, 0xbf60b4d, 0x1e7005c, 0x13900cd, 0x1490140, 0x0,
    0x1db0050, 0x1e20059, 0xbf30b48, 0xbfa0b51, 0x1eb0060, 0x13c00d1, 0x14c0145, 0x0, 0x20c0086, 0x215008f, 0x22c00a6, 0xc0d0b67,
    0x21c0096, 0x930256, 0xa3009a, 0x0, 0x210008a, 0x2190093, 0xc080b62, 0xc110b6b, 0x220009a, 0x96025a, 0xa6009f, 0x0,
    0x24600bd, 0x24f00c4, 0x26600dd, 0xc270b7c, 0x1ee0065, 0x17300d4, 0x183017a, 0x0, 0x24a00c1, 0x25300c8, 0xc220b79, 0xc2b0b80,
    0x1f20069, 0x17600d8, 0x186017f, 0x0, 0x1ad0027, 0x1b4002e, 0x1ad0027, 0x1b4002e, 0x225009f, 0x1a8025f, 0x1b801b1, 0x0,
    0x1b1002b, 0x1b80032, 0x1b1002b, 0x1b80032, 0x22900a3, 0x1ad0263, 0x1bd01b4, 0x0, 0x0, 0x3fff0000, 0x0, 0x0,
    0x0, 0x0, 0x0, 0x0, 0x0, 0x3fff3fff, 0xaaaaaaaa, 0xaaaaaaaa, 0xaaaaaaaa, 0xaaaa, 0x0, 0x55550000,
    0x55550000, 0x55550000, 0xaaaaaaaa, 0xaaaaaaaa, 0xaaaaaaaa, 0xaaaa, 0x55555555, 0x55555555, 0x55555555, 0x5555, 0x0, 0x0,
    0x0, 0xaaaa0000, 0x0, 0x0, 0x55555555, 0x55555555, 0x55555555, 0x5555, 0xaaaa, 0xaaaa, 0x0, 0x0,
    0x0, 0x0, 0x0, 0x0, 0x0, 0x0, 0x0, 0x0, 0x0, 0x0, 0x0, 0x0,
    0x0, 0x0, 0x0, 0x0, 0x0, 0x0, 0x0, 0x0, 0x0, 0x0, 0x0, 0x0,
    0x1400125, 0x1b10194, 0x30002e5, 0x9d109b4, 0x2200205, 0x1db020c, 0x2bb024a, 0x0, 0x17a015f, 0x1eb01ce, 0x97a031f, 0xa230a06,
    0x25a023f, 0x2100246, 0x2f00281, 0x0, 0x1450129, 0x1b40198, 0x30502e9, 0x9d409b8, 0x2250209, 0x1de0229, 0x2be024f, 0x0,
    0x17f0163, 0x1ee01d2, 0x97f0963, 0xa260a0a, 0x25f0243, 0x2150263, 0x2f50284, 0x0, 0x149012c, 0x1b8019d, 0x30902ec, 0x9d809bd,
    0x2910274, 0x1e2027d, 0x2c20253, 0x0, 0x1830166, 0x1f201d7, 0x9830966, 0xa2a0a0f, 0x2cb02ae, 0x21902b7, 0x2f90288, 0x0,
    0x14c0130, 0x1bd01a1, 0x30c02f0, 0x9dd09c1, 0x2940278, 0x1e70298, 0x2c70256, 0x0, 0x186016a, 0x1f701db, 0x986096a, 0xa2f0a13,
    0x2ce02b2, 0x21c02d2, 0x2fc028d, 0x0, 0xba80135, 0xbd901a4, 0xc6802f5, 0x9e109c4, 0xc080215, 0x1eb021c, 0x2cb025a, 0x0,
    0xbc2016f, 0xbf301de, 0x98a096f, 0xa330a16, 0xc22024f, 0x2200256, 0x3000291, 0x0, 0xbad0139, 0xbdc01a8, 0xc6d02f9, 0x9e409c8,
    0xc0d0219, 0x1ee0c11, 0x2ce025f, 0x0, 0xbc70173, 0xbf601e2, 0x98f0973, 0xa360a1a, 0xc270253, 0x2250c2b, 0x3050294, 0x0,
    0xbb1013c, 0xbe001ad, 0xc7102fc, 0x9e809cd, 0xc390284, 0x1f2028d, 0x2d20263, 0x0, 0xbcb0176, 0xbfa01e7, 0x9930976, 0xa3a0a1f,
    0xc5302be, 0x22902c7, 0x3090298, 0x0, 0x22c0210, 0x29d0281, 0x22c0210, 0x29d0281, 0xc3c0288, 0x1f70c40, 0x2d70266, 0x0,
    0x266024a, 0x2d702bb, 0x266024a, 0x2d702bb, 0xc5602c2, 0x22c0c5a, 0x30c029d, 0x0, 0x3f0003, 0x3fff03ff, 0xff000f, 0xff0fff,
    0x30003, 0x3ff03ff, 0xf000f, 0xfff0fff, 0x3f003f, 0x3fff3fff, 0xaaaaaaaa, 0xaaaaaaaa, 0xaaaaaaaa, 0xaaaa, 0x0, 0x55550000,
    0x55550000, 0x55550000, 0xaaaaaaaa, 0xaaaaaaaa, 0xaaaaaaaa, 0xaaaa, 0x55555555, 0x55555555, 0x55555555, 0x5555, 0x0, 0x0,
    0x0, 0xaaaa0000, 0x0, 0x0, 0x55555555, 0x55555555, 0x55555555, 0x5555, 0xaaaa, 0xaaaa, 0x0, 0x0,
    0x0, 0x0, 0x0, 0x0, 0x0, 0x0, 0x0, 0x0, 0x0, 0x0, 0x0, 0x0,
    0x0, 0x0, 0x0, 0x0, 0x0, 0x0, 0x0, 0x0, 0x0, 0x0, 0x0, 0x0,
    0x25a00d1, 0x23f00b4, 0x25600cd, 0x23f00b4, 0x24600bd, 0x15f012c, 0x1660163, 0x0, 0x25600cd, 0x24300b8, 0x25a00d1, 0x24300b8,
    0x24a00c1, 0x1530130, 0x15a0156, 0x0, 0x291010b, 0x27400ee, 0x28d0107, 0x27400ee, 0x27d00f7, 0x19402b7, 0x19d0198, 0x0,
    0x28d0107, 0x27800f2, 0x291010b, 0x27800f2, 0x28100fb, 0x18802bb, 0x191018d, 0x0, 0x2cb0140, 0x2ae0125, 0x2c7013c, 0x2ae0125,
    0x24f00c4, 0x1ce0135, 0x1d701d2, 0x0, 0x2c7013c, 0x2b20129, 0x2cb0140, 0x2b20129, 0x25300c8, 0x1c20139, 0x1cb01c7, 0x0,
    0x300017a, 0x2e5015f, 0x2fc0176, 0x2e5015f, 0x28400fe, 0x20502be, 0x20c0209, 0x0, 0x2fc0176, 0x2e90163, 0x300017a, 0x2e90163,
    0x2880102, 0x1f902c2, 0x20001fc, 0x0, 0x97a01b1, 0x31f0194, 0x97601ad, 0x31f0194, 0x966019d, 0x23f020c, 0x2460243, 0x0,
    0x97601ad, 0x9630198, 0x97a01b1, 0x9630198, 0x96a01a1, 0x2330210, 0x23a0236, 0x0, 0x9d101eb, 0x9b401ce, 0x9cd01e7, 0x9b401ce,
    0x9bd01d7, 0x2740a0f, 0x27d0278, 0x0, 0x9cd01e7, 0x9b801d2, 0x9d101eb, 0x9b801d2, 0x9c101db, 0x2680a13, 0x271026d, 0x0,
    0xa230220, 0xa060205, 0xa1f021c, 0xa060205, 0x96f01a4, 0x2ae0215, 0x2b702b2, 0x0, 0xa1f021c, 0xa0a0209, 0xa230220, 0xa0a0209,
    0x97301a8, 0x2a20219, 0x2ab02a7, 0x0, 0x2ec0166, 0x2f5016f, 0x2ec0166, 0x2f5016f, 0x9c401de, 0x2e50a16, 0x2ec02e9, 0x0,
    0x2f0016a, 0x2f90173, 0x2f0016a, 0x2f90173, 0x9c801e2, 0x2d90a1a, 0x2e002dc, 0x0, 0x3f0003, 0x3fff03ff, 0xff000f, 0xff0fff,
    0x30003, 0x3ff03ff, 0xf000f, 0xfff0fff, 0x3f003f, 0x3fff3fff, 0xffffffff, 0xffffffff, 0xffffffff, 0xffff, 0x0, 0x0,
    0x0, 0x0, 0x0, 0x0, 0x0, 0x0, 0xffffffff, 0xffffffff, 0xffffffff, 0xffff, 0xffffffff, 0xffffffff,
    0x0, 0x0, 0xffffffff, 0xffff, 0x0, 0x0, 0x0, 0x0, 0x0, 0x0, 0x0, 0x0,
    0x0, 0x0, 0x0, 0x0, 0x0, 0x0, 0x0, 0x0, 0x0, 0x0, 0x0, 0x0,
    0x0, 0x0, 0x0, 0x0, 0x0, 0x0, 0x0, 0x0, 0x0, 0x0, 0x0, 0x0
};

__global__ __launch_bounds__(512, 2) void na_main(const void* x, const void* wqkv, const void* wo, void* ws, void* out) {
    __shared__ __attribute__((aligned(1024))) char smem[163840];
    asm volatile(
        "s_mov_b32 s18, 0\n"
        "s_mov_b32 s19, 0\n"
        "s_memtime s[8:9]\n"
        "Ltop_%=:\n"
        "s_load_dwordx2 s[24:25], s[0:1], 24\n"
        "s_load_dwordx2 s[32:33], s[0:1], 8\n"
        "s_load_dwordx2 s[20:21], s[0:1], 0\n"
        "s_load_dwordx2 s[36:37], s[0:1], 16\n"
        "s_load_dwordx2 s[28:29], s[0:1], 32\n"
        "s_getpc_b64 s[10:11]\n"
        "v_and_b32 v9, 0x1c0, v0\n"
        "v_bfe_u32 v1, v0, 5, 1\n"
        "s_lshr_b32 s88, s2, 3\n"
        "v_lshl_add_u32 v9, v1, 14, v9\n"
        "s_lshl_b32 s86, s88, 9\n"
        "v_readfirstlane_b32 s3, v0\n"
        "v_add_u32 v9, s86, v9\n"
        "v_min_u32 v9, 28664, v9\n"
        "global_load_dword v21, v9, s[10:11]\n"
        "s_lshr_b32 s3, s3, 6\n"
        "s_xor_b32 s3, s3, 4\n"
        "s_mov_b32 s34, 0x36000\n"
        "s_mov_b32 s35, 0x20000\n"
        "s_mov_b32 s26, 0x3100000\n"
        "s_mov_b32 s27, 0x20000\n"
        "v_and_b32 v8, 63, v0\n"
        "s_lshr_b32 s40, s2, 0\n"
        "s_mov_b32 s41, 0\n"
        "s_cmp_lg_u32 s3, 4\n"
        "s_cbranch_scc1 Lnoad_%=\n"
        "v_mov_b32 v10, 0\n"
        "v_lshlrev_b32 v11, 2, v8\n"
        "v_add_u32 v11, 0x27000, v11\n"
        "ds_write_b32 v11, v10\n"
        "s_cmp_lt_u32 s40, 24\n"
        "s_cselect_b32 s47, 3, 2\n"
        "v_bfe_u32 v10, v0, 3, 2\n"
        "v_bfe_u32 v11, v0, 5, 1\n"
        "v_lshl_add_u32 v10, v11, s47, v10\n"
        "s_lshl_b32 s47, 2304, s47\n"
        "s_lshr_b32 s42, s40, 2\n"
        "v_lshrrev_b32 v11, 3, v0\n"
        "s_mul_i32 s42, s42, 36864\n"
        "v_and_b32 v11, 6, v11\n"
        "s_bfe_u32 s44, s40, 0x10001\n"
        "v_xor_b32 v11, v11, v0\n"
        "s_mul_i32 s44, s44, 36864\n"
        "v_and_b32 v11, 7, v11\n"
        "s_bitcmp1_b32 s40, 0\n"
        "v_mul_u32_u24 v10, 1152, v10\n"
        "s_cselect_b32 s45, 12, 0\n"
        "v_lshl_add_u32 v10, v11, 4, v10\n"
        "s_add_u32 s45, s45, 1\n"
        "v_add_u32 v22, s47, v10\n"
        "s_mul_i32 s45, s45, 384\n"
        "v_mov_b32 v12, v10\n"
        "s_add_u32 s44, s44, s45\n"
        "v_mov_b32 v13, v22\n"
        "s_add_u32 s43, s42, 4608\n"
        "s_add_u32 s45, s44, 73728\n"
        "s_add_u32 s46, s44, 147456\n"
        "s_mov_b32 s47, 0\n"
        "s_cmp_lt_u32 s40, 24\n"
        "s_cbranch_scc1 Lpa1_%=\n"
        "s_mul_i32 s48, s40, 43\n"
        "v_bfe_u32 v12, v0, 2, 4\n"
        "s_lshr_b32 s48, s48, 8\n"
        "v_and_b32 v14, 3, v0\n"
        "s_mul_i32 s49, s48, 6\n"
        "v_mul_u32_u24 v12, 384, v12\n"
        "s_sub_u32 s49, s40, s49\n"
        "v_lshl_add_u32 v12, v14, 4, v12\n"
        "s_sub_u32 s48, s48, 4\n"
        "v_add_u32 v13, 6144, v12\n"
        "s_mul_i32 s42, s49, 36864\n"
        "s_lshl_b32 s44, s48, 6\n"
        "s_add_u32 s42, s42, 768\n"
        "s_add_u32 s45, s44, 12288\n"
        "s_add_u32 s43, s42, 18432\n"
        "s_mov_b32 s47, 24448\n"
        "Lpa1_%=:\n"
        "Lnoad_%=:\n"
        "s_waitcnt lgkmcnt(0)\n"
        "s_cmp_lg_u32 s18, 0\n"
        "s_cbranch_scc1 Lnoep_%=\n"
        "s_load_dwordx2 s[6:7], s[24:25], 0xb4000\n"
        "Lnoep_%=:\n"
        "s_cmp_lg_u32 s19, 0\n"
        "s_cbranch_scc1 Lpriv_%=\n"
        "s_cmp_lg_u32 s3, 4\n"
        "s_cbranch_scc1 Lpdone_%=\n"
        "s_cmp_ge_u32 s2, 60\n"
        "s_cbranch_scc1 Lp2n_%=\n"
        "Lp1_%=:\n"
        "s_mov_b32 m0, 0x1f400\n"
        "s_nop 0\n"
        "buffer_load_dwordx4 v10, s[32:35], s42 offen lds\n"
        "s_mov_b32 m0, 0x1f800\n"
        "s_nop 0\n"
        "buffer_load_dwordx4 v22, s[32:35], s42 offen lds\n"
        "s_cmp_lt_u32 s40, 24\n"
        "s_cselect_b64 s[52:53], s[32:33], s[36:37]\n"
        "s_cselect_b32 s54, s34, 0x12000\n"
        "s_mov_b32 s55, 0x20000\n"
        "s_mov_b32 m0, 0x20400\n"
        "s_nop 0\n"
        "buffer_load_dwordx4 v12, s[52:55], s44 offen lds\n"
        "s_mov_b32 m0, 0x20800\n"
        "s_nop 0\n"
        "buffer_load_dwordx4 v13, s[52:55], s44 offen lds\n"
        "s_mov_b32 m0, 0x1fc00\n"
        "s_nop 0\n"
        "buffer_load_dwordx4 v10, s[32:35], s43 offen lds\n"
        "s_mov_b32 m0, 0x20000\n"
        "s_nop 0\n"
        "buffer_load_dwordx4 v22, s[32:35], s43 offen lds\n"
        "s_mov_b32 m0, 0x20c00\n"
        "s_nop 0\n"
        "buffer_load_dwordx4 v12, s[52:55], s45 offen lds\n"
        "s_mov_b32 m0, 0x21000\n"
        "s_nop 0\n"
        "buffer_load_dwordx4 v13, s[52:55], s45 offen lds\n"
        "s_add_u32 s44, s44, s47\n"
        "s_add_u32 s45, s45, s47\n"
        "s_mov_b32 m0, 0x21b80\n"
        "s_nop 0\n"
        "buffer_load_dwordx4 v10, s[32:35], s42 offen offset:128 lds\n"
        "s_mov_b32 m0, 0x21f80\n"
        "s_nop 0\n"
        "buffer_load_dwordx4 v22, s[32:35], s42 offen offset:128 lds\n"
        "s_mov_b32 m0, 0x22b80\n"
        "s_nop 0\n"
        "buffer_load_dwordx4 v12, s[52:55], s44 offen offset:128 lds\n"
        "s_mov_b32 m0, 0x22f80\n"
        "s_nop 0\n"
        "buffer_load_dwordx4 v13, s[52:55], s44 offen offset:128 lds\n"
        "s_mov_b32 m0, 0x22380\n"
        "s_nop 0\n"
        "buffer_load_dwordx4 v10, s[32:35], s43 offen offset:128 lds\n"
        "s_mov_b32 m0, 0x22780\n"
        "s_nop 0\n"
        "buffer_load_dwordx4 v22, s[32:35], s43 offen offset:128 lds\n"
        "s_mov_b32 m0, 0x23380\n"
        "s_nop 0\n"
        "buffer_load_dwordx4 v12, s[52:55], s45 offen offset:128 lds\n"
        "s_mov_b32 m0, 0x23780\n"
        "s_nop 0\n"
        "buffer_load_dwordx4 v13, s[52:55], s45 offen offset:128 lds\n"
        "s_add_u32 s44, s44, s47\n"
        "s_add_u32 s45, s45, s47\n"
        "s_mov_b32 m0, 0x24300\n"
        "s_nop 0\n"
        "buffer_load_dwordx4 v10, s[32:35], s42 offen offset:256 lds\n"
        "s_mov_b32 m0, 0x24700\n"
        "s_nop 0\n"
        "buffer_load_dwordx4 v22, s[32:35], s42 offen offset:256 lds\n"
        "s_mov_b32 m0, 0x25300\n"
        "s_nop 0\n"
        "buffer_load_dwordx4 v12, s[52:55], s44 offen offset:256 lds\n"
        "s_mov_b32 m0, 0x25700\n"
        "s_nop 0\n"
        "buffer_load_dwordx4 v13, s[52:55], s44 offen offset:256 lds\n"
        "s_mov_b32 m0, 0x24b00\n"
        "s_nop 0\n"
        "buffer_load_dwordx4 v10, s[32:35], s43 offen offset:256 lds\n"
        "s_mov_b32 m0, 0x24f00\n"
        "s_nop 0\n"
        "buffer_load_dwordx4 v22, s[32:35], s43 offen offset:256 lds\n"
        "s_mov_b32 m0, 0x25b00\n"
        "s_nop 0\n"
        "buffer_load_dwordx4 v12, s[52:55], s45 offen offset:256 lds\n"
        "s_mov_b32 m0, 0x25f00\n"
        "s_nop 0\n"
        "buffer_load_dwordx4 v13, s[52:55], s45 offen offset:256 lds\n"
        "s_cmp_ge_u32 s40, 24\n"
        "s_cbranch_scc1 Lp2g_%=\n"
        "s_mov_b32 m0, 0x21400\n"
        "s_nop 0\n"
        "buffer_load_dwordx4 v10, s[32:35], s46 offen lds\n"
        "s_mov_b32 m0, 0x21800\n"
        "s_nop 0\n"
        "buffer_load_dwordx4 v22, s[32:35], s46 offen lds\n"
        "s_mov_b32 m0, 0x23b80\n"
        "s_nop 0\n"
        "buffer_load_dwordx4 v10, s[32:35], s46 offen offset:128 lds\n"
        "s_mov_b32 m0, 0x23f80\n"
        "s_nop 0\n"
        "buffer_load_dwordx4 v22, s[32:35], s46 offen offset:128 lds\n"
        "s_mov_b32 m0, 0x26300\n"
        "s_nop 0\n"
        "buffer_load_dwordx4 v10, s[32:35], s46 offen offset:256 lds\n"
        "s_mov_b32 m0, 0x26700\n"
        "s_nop 0\n"
        "buffer_load_dwordx4 v22, s[32:35], s46 offen offset:256 lds\n"
        "s_mov_b64 s[10:11], s[4:5]\n"
        "Lprod_%=:\n"
        "v_and_b32 v216, 15, v0\n"
        "v_bfe_u32 v217, v0, 2, 4\n"
        "v_lshlrev_b32 v216, 5, v216\n"
        "v_lshlrev_b32 v217, 5, v217\n"
        "global_load_dwordx4 v[200:203], v216, s[10:11]\n"
        "global_load_dwordx4 v[204:207], v216, s[10:11] offset:16\n"
        "global_load_dwordx4 v[208:211], v217, s[10:11]\n"
        "global_load_dwordx4 v[212:215], v217, s[10:11] offset:16\n"
        "s_setprio 3\n"
        "s_sub_u32 s80, s3, 4\n"
        "v_and_b32 v23, 15, v0\n"
        "v_bfe_u32 v18, v0, 4, 2\n"
        "v_and_b32 v19, 6, v0\n"
        "v_xor_b32 v18, v18, v19\n"
        "v_lshlrev_b32 v23, 7, v23\n"
        "v_lshl_add_u32 v18, v18, 4, v23\n"
        "v_add_u32 v18, 0x1f400, v18\n"
        "v_xor_b32 v19, 64, v18\n"
        "s_and_b32 s81, s2, 0\n"
        "s_mul_i32 s81, s81, 76800\n"
        "s_cmp_lg_u32 s19, 0\n"
        "s_cselect_b32 s81, s19, s81\n"
        "s_add_u32 s81, s81, 81920\n"
        "s_mul_i32 s82, s40, 3200\n"
        "s_add_u32 s81, s81, s82\n"
        "s_mov_b32 s84, 0x3dd53b94\n"
        "s_mov_b32 s85, 0x3dd53b94\n"
        "s_mov_b32 s87, 0x9e3779b1\n"
        "v_add_u32 v15, 0x51ed27, v8\n"
        "v_mul_lo_u32 v12, v15, s87\n"
        "v_mul_lo_u32 v13, v12, s87\n"
        "v_mul_lo_u32 v14, v13, s87\n"
        "v_mul_lo_u32 v15, v14, s87\n"
        "v_or_b32 v12, 1, v12\n"
        "v_or_b32 v13, 1, v13\n"
        "v_or_b32 v14, 1, v14\n"
        "v_or_b32 v15, 1, v15\n"
        "v_lshlrev_b32 v11, 4, v8\n"
        "s_waitcnt lgkmcnt(0)\n"
        "s_bitcmp1_b32 s18, 31\n"
        "s_cbranch_scc1 Ltg2_%=\n"
        "s_bitset1_b32 s18, 31\n"
        "s_mul_i32 s7, s7, 0x4c957f2d\n"
        "s_mul_i32 s86, s6, 0x5851f42d\n"
        "s_add_u32 s7, s7, s86\n"
        "s_mul_hi_u32 s86, s6, 0x4c957f2d\n"
        "s_add_u32 s7, s7, s86\n"
        "s_mul_i32 s6, s6, 0x4c957f2d\n"
        "s_add_u32 s6, s6, 0xf767814f\n"
        "s_addc_u32 s7, s7, 0x14057b7e\n"
        "Ltg2_%=:\n"
        "v_mov_b32 v17, s7\n"
        "Lprl_%=:\n"
        "s_lshl_b32 s82, s80, 11\n"
        "v_add_u32 v22, s82, v18\n"
        "v_add_u32 v23, s82, v19\n"
        "s_lshl_b32 s82, s80, 10\n"
        "s_add_u32 s82, s82, s81\n"
        "s_lshl4_add_u32 s83, s80, s81\n"
        "s_cmp_lg_u32 s3, 4\n"
        "s_cbranch_scc1 Lprw_%=\n"
        "s_waitcnt vmcnt(4)\n"
        "s_cmp_lg_u32 s19, 0\n"
        "s_cbranch_scc1 Lprb_%=\n"
        "Lprw_%=:\n"
        "s_barrier\n"
        "Lprb_%=:\n"
        "ds_read_b128 v[48:51], v18 offset:0\n"
        "ds_read_b128 v[56:59], v22 offset:4096\n"
        "ds_read_b128 v[52:55], v18 offset:2048\n"
        "ds_read_b128 v[60:63], v19 offset:0\n"
        "ds_read_b128 v[68:71], v23 offset:4096\n"
        "ds_read_b128 v[64:67], v19 offset:2048\n"
        "ds_read_b128 v[72:75], v18 offset:10240\n"
        "ds_read_b128 v[80:83], v22 offset:14336\n"
        "ds_read_b128 v[76:79], v18 offset:12288\n"
        "ds_read_b128 v[84:87], v19 offset:10240\n"
        "ds_read_b128 v[92:95], v23 offset:14336\n"
        "ds_read_b128 v[88:91], v19 offset:12288\n"
        "ds_read_b128 v[96:99], v18 offset:20480\n"
        "ds_read_b128 v[104:107], v22 offset:24576\n"
        "ds_read_b128 v[100:103], v18 offset:22528\n"
        "s_waitcnt lgkmcnt(13)\n"
        "v_mfma_f32_16x16x32_bf16 v[24:27], v[48:51], v[56:59], 0\n"
        "s_waitcnt lgkmcnt(12)\n"
        "v_mfma_f32_16x16x32_bf16 v[28:31], v[52:55], v[56:59], 0\n"
        "ds_read_b128 v[108:111], v19 offset:20480\n"
        "ds_read_b128 v[116:119], v23 offset:24576\n"
        "ds_read_b128 v[112:115], v19 offset:22528\n"
        "s_waitcnt lgkmcnt(13)\n"
        "v_mfma_f32_16x16x32_bf16 v[24:27], v[60:63], v[68:71], v[24:27]\n"
        "s_waitcnt lgkmcnt(12)\n"
        "v_mfma_f32_16x16x32_bf16 v[28:31], v[64:67], v[68:71], v[28:31]\n"
        "s_waitcnt lgkmcnt(10)\n"
        "v_mfma_f32_16x16x32_bf16 v[24:27], v[72:75], v[80:83], v[24:27]\n"
        "s_waitcnt lgkmcnt(9)\n"
        "v_mfma_f32_16x16x32_bf16 v[28:31], v[76:79], v[80:83], v[28:31]\n"
        "s_waitcnt lgkmcnt(7)\n"
        "v_mfma_f32_16x16x32_bf16 v[24:27], v[84:87], v[92:95], v[24:27]\n"
        "s_waitcnt lgkmcnt(6)\n"
        "v_mfma_f32_16x16x32_bf16 v[28:31], v[88:91], v[92:95], v[28:31]\n"
        "s_waitcnt lgkmcnt(4)\n"
        "v_mfma_f32_16x16x32_bf16 v[24:27], v[96:99], v[104:107], v[24:27]\n"
        "s_waitcnt lgkmcnt(3)\n"
        "v_mfma_f32_16x16x32_bf16 v[28:31], v[100:103], v[104:107], v[28:31]\n"
        "s_waitcnt lgkmcnt(1)\n"
        "v_mfma_f32_16x16x32_bf16 v[24:27], v[108:111], v[116:119], v[24:27]\n"
        "s_waitcnt lgkmcnt(0)\n"
        "v_mfma_f32_16x16x32_bf16 v[28:31], v[112:115], v[116:119], v[28:31]\n"
        "s_waitcnt vmcnt(0)\n"
        "s_nop 4\n"
        "v_pk_mul_f32 v[24:25], v[24:25], s[84:85]\n"
        "v_pk_mul_f32 v[26:27], v[26:27], s[84:85]\n"
        "v_pk_mul_f32 v[28:29], v[28:29], s[84:85]\n"
        "v_pk_mul_f32 v[30:31], v[30:31], s[84:85]\n"
        "v_cvt_pk_bf16_f32 v32, v24, v25\n"
        "v_cvt_pk_bf16_f32 v33, v26, v27\n"
        "v_cvt_pk_bf16_f32 v34, v28, v29\n"
        "v_cvt_pk_bf16_f32 v35, v30, v31\n"
        "buffer_store_dwordx4 v[32:35], v11, s[24:27], s82 offen sc1\n"
        "v_mul_lo_u32 v36, v32, v12\n"
        "v_mul_lo_u32 v37, v33, v13\n"
        "v_mul_lo_u32 v38, v34, v14\n"
        "v_mul_lo_u32 v39, v35, v15\n"
        "v_add3_u32 v16, v36, v37, v38\n"
        "v_add_u32 v16, v16, v39\n"
        "s_nop 1\n"
        "v_add_u32_dpp v16, v16, v16 row_shr:1 row_mask:0xf bank_mask:0xf\n"
        "s_nop 1\n"
        "v_add_u32_dpp v16, v16, v16 row_shr:2 row_mask:0xf bank_mask:0xf\n"
        "s_nop 1\n"
        "v_add_u32_dpp v16, v16, v16 row_shr:4 row_mask:0xf bank_mask:0xf\n"
        "s_nop 1\n"
        "v_add_u32_dpp v16, v16, v16 row_shr:8 row_mask:0xf bank_mask:0xf\n"
        "s_nop 1\n"
        "v_add_u32_dpp v16, v16, v16 row_bcast:15 row_mask:0xa bank_mask:0xf\n"
        "s_nop 1\n"
        "v_add_u32_dpp v16, v16, v16 row_bcast:31 row_mask:0xc bank_mask:0xf\n"
        "s_mov_b64 exec, 1\n"
        "s_lshl_b64 exec, exec, 63\n"
        "v_add_u32 v16, s6, v16\n"
        "buffer_store_dwordx2 v[16:17], v11, s[24:27], s83 offen offset:2064 sc1\n"
        "s_mov_b64 exec, -1\n"
        "s_setprio 0\n"
        "s_cmp_lg_u32 s19, 0\n"
        "s_cbranch_scc1 Lprn_%=\n"
        "s_barrier\n"
        "s_mov_b32 s80, 0x1000\n"
        "s_cmp_lg_u32 s3, 4\n"
        "s_cbranch_scc1 Lnopr_%=\n"
        "s_branch Lpdone_%=\n"
        "Lp2n_%=:\n"
        "v_and_b32 v216, 15, v0\n"
        "v_bfe_u32 v217, v0, 2, 4\n"
        "v_lshlrev_b32 v216, 5, v216\n"
        "v_lshlrev_b32 v217, 5, v217\n"
        "global_load_dwordx4 v[200:203], v216, s[4:5]\n"
        "global_load_dwordx4 v[204:207], v216, s[4:5] offset:16\n"
        "global_load_dwordx4 v[208:211], v217, s[4:5]\n"
        "global_load_dwordx4 v[212:215], v217, s[4:5] offset:16\n"
        "s_waitcnt vmcnt(0)\n"
        "s_branch Lpdone_%=\n"
        "Lp2g_%=:\n"
        "v_and_b32 v216, 15, v0\n"
        "v_bfe_u32 v217, v0, 2, 4\n"
        "v_lshlrev_b32 v216, 5, v216\n"
        "v_lshlrev_b32 v217, 5, v217\n"
        "global_load_dwordx4 v[200:203], v216, s[4:5]\n"
        "global_load_dwordx4 v[204:207], v216, s[4:5] offset:16\n"
        "global_load_dwordx4 v[208:211], v217, s[4:5]\n"
        "global_load_dwordx4 v[212:215], v217, s[4:5] offset:16\n"
        "v_and_b32 v23, 15, v0\n"
        "v_bfe_u32 v18, v0, 4, 2\n"
        "v_and_b32 v19, 6, v0\n"
        "v_xor_b32 v18, v18, v19\n"
        "v_lshlrev_b32 v23, 7, v23\n"
        "v_lshl_add_u32 v18, v18, 4, v23\n"
        "v_add_u32 v240, 0x1f400, v18\n"
        "v_xor_b32 v241, 64, v240\n"
        "v_and_b32 v12, 48, v0\n"
        "v_and_b32 v15, 12, v0\n"
        "v_lshlrev_b32 v22, 3, v14\n"
        "v_lshl_add_u32 v22, v15, 4, v22\n"
        "v_lshl_add_u32 v22, v12, 5, v22\n"
        "v_add_u32 v242, 0x20400, v22\n"
        "v_mul_u32_u24 v14, 6144, v14\n"
        "v_lshl_add_u32 v14, v12, 4, v14\n"
        "v_lshl_add_u32 v14, v15, 2, v14\n"
        "s_lshr_b32 s44, s48, 1\n"
        "s_mul_i32 s44, s44, 24\n"
        "s_add_u32 s44, s44, s49\n"
        "s_lshl_b32 s44, s44, 10\n"
        "s_and_b32 s45, s48, 1\n"
        "s_lshl_b32 s45, s45, 7\n"
        "s_add_u32 s44, s44, s45\n"
        "v_add_u32 v140, s44, v14\n"
        "s_waitcnt vmcnt(4) lgkmcnt(0)\n"
        "ds_read_b128 v[40:43], v240 offset:0\n"
        "ds_read_b128 v[44:47], v240 offset:2048\n"
        "ds_read_b64_tr_b16 v[48:49], v242 offset:0\n"
        "ds_read_b64_tr_b16 v[50:51], v242 offset:256\n"
        "ds_read_b64_tr_b16 v[52:53], v242 offset:32\n"
        "ds_read_b64_tr_b16 v[54:55], v242 offset:288\n"
        "ds_read_b128 v[56:59], v241 offset:0\n"
        "ds_read_b128 v[60:63], v241 offset:2048\n"
        "ds_read_b64_tr_b16 v[64:65], v242 offset:2048\n"
        "ds_read_b64_tr_b16 v[66:67], v242 offset:2304\n"
        "ds_read_b64_tr_b16 v[68:69], v242 offset:2080\n"
        "ds_read_b64_tr_b16 v[70:71], v242 offset:2336\n"
        "s_waitcnt lgkmcnt(8)\n"
        "v_mfma_f32_16x16x32_bf16 v[148:151], v[40:43], v[48:51], 0\n"
        "v_mfma_f32_16x16x32_bf16 v[152:155], v[44:47], v[48:51], 0\n"
        "s_waitcnt lgkmcnt(6)\n"
        "v_mfma_f32_16x16x32_bf16 v[156:159], v[40:43], v[52:55], 0\n"
        "v_mfma_f32_16x16x32_bf16 v[160:163], v[44:47], v[52:55], 0\n"
        "ds_read_b128 v[72:75], v240 offset:10240\n"
        "ds_read_b128 v[76:79], v240 offset:12288\n"
        "ds_read_b64_tr_b16 v[80:81], v242 offset:10240\n"
        "ds_read_b64_tr_b16 v[82:83], v242 offset:10496\n"
        "ds_read_b64_tr_b16 v[84:85], v242 offset:10272\n"
        "ds_read_b64_tr_b16 v[86:87], v242 offset:10528\n"
        "s_waitcnt lgkmcnt(8)\n"
        "v_mfma_f32_16x16x32_bf16 v[148:151], v[56:59], v[64:67], v[148:151]\n"
        "v_mfma_f32_16x16x32_bf16 v[152:155], v[60:63], v[64:67], v[152:155]\n"
        "s_waitcnt lgkmcnt(6)\n"
        "v_mfma_f32_16x16x32_bf16 v[156:159], v[56:59], v[68:71], v[156:159]\n"
        "v_mfma_f32_16x16x32_bf16 v[160:163], v[60:63], v[68:71], v[160:163]\n"
        "ds_read_b128 v[88:91], v241 offset:10240\n"
        "ds_read_b128 v[92:95], v241 offset:12288\n"
        "ds_read_b64_tr_b16 v[96:97], v242 offset:12288\n"
        "ds_read_b64_tr_b16 v[98:99], v242 offset:12544\n"
        "ds_read_b64_tr_b16 v[100:101], v242 offset:12320\n"
        "ds_read_b64_tr_b16 v[102:103], v242 offset:12576\n"
        "s_waitcnt lgkmcnt(8)\n"
        "v_mfma_f32_16x16x32_bf16 v[148:151], v[72:75], v[80:83], v[148:151]\n"
        "v_mfma_f32_16x16x32_bf16 v[152:155], v[76:79], v[80:83], v[152:155]\n"
        "s_waitcnt lgkmcnt(6)\n"
        "v_mfma_f32_16x16x32_bf16 v[156:159], v[72:75], v[84:87], v[156:159]\n"
        "v_mfma_f32_16x16x32_bf16 v[160:163], v[76:79], v[84:87], v[160:163]\n"
        "ds_read_b128 v[104:107], v240 offset:20480\n"
        "ds_read_b128 v[108:111], v240 offset:22528\n"
        "ds_read_b64_tr_b16 v[112:113], v242 offset:20480\n"
        "ds_read_b64_tr_b16 v[114:115], v242 offset:20736\n"
        "ds_read_b64_tr_b16 v[116:117], v242 offset:20512\n"
        "ds_read_b64_tr_b16 v[118:119], v242 offset:20768\n"
        "s_waitcnt lgkmcnt(8)\n"
        "v_mfma_f32_16x16x32_bf16 v[148:151], v[88:91], v[96:99], v[148:151]\n"
        "v_mfma_f32_16x16x32_bf16 v[152:155], v[92:95], v[96:99], v[152:155]\n"
        "s_waitcnt lgkmcnt(6)\n"
        "v_mfma_f32_16x16x32_bf16 v[156:159], v[88:91], v[100:103], v[156:159]\n"
        "v_mfma_f32_16x16x32_bf16 v[160:163], v[92:95], v[100:103], v[160:163]\n"
        "ds_read_b128 v[120:123], v241 offset:20480\n"
        "ds_read_b128 v[124:127], v241 offset:22528\n"
        "ds_read_b64_tr_b16 v[128:129], v242 offset:22528\n"
        "ds_read_b64_tr_b16 v[130:131], v242 offset:22784\n"
        "ds_read_b64_tr_b16 v[132:133], v242 offset:22560\n"
        "ds_read_b64_tr_b16 v[134:135], v242 offset:22816\n"
        "s_waitcnt lgkmcnt(8)\n"
        "v_mfma_f32_16x16x32_bf16 v[148:151], v[104:107], v[112:115], v[148:151]\n"
        "v_mfma_f32_16x16x32_bf16 v[152:155], v[108:111], v[112:115], v[152:155]\n"
        "s_waitcnt lgkmcnt(6)\n"
        "v_mfma_f32_16x16x32_bf16 v[156:159], v[104:107], v[116:119], v[156:159]\n"
        "v_mfma_f32_16x16x32_bf16 v[160:163], v[108:111], v[116:119], v[160:163]\n"
        "s_waitcnt lgkmcnt(2)\n"
        "v_mfma_f32_16x16x32_bf16 v[148:151], v[120:123], v[128:131], v[148:151]\n"
        "v_mfma_f32_16x16x32_bf16 v[152:155], v[124:127], v[128:131], v[152:155]\n"
        "s_waitcnt lgkmcnt(0)\n"
        "v_mfma_f32_16x16x32_bf16 v[156:159], v[120:123], v[132:135], v[156:159]\n"
        "v_mfma_f32_16x16x32_bf16 v[160:163], v[124:127], v[132:135], v[160:163]\n"
        "s_nop 3\n"
        "v_cvt_pk_bf16_f32 v232, v148, v149\n"
        "v_cvt_pk_bf16_f32 v233, v150, v151\n"
        "v_cvt_pk_bf16_f32 v234, v152, v153\n"
        "v_cvt_pk_bf16_f32 v235, v154, v155\n"
        "v_cvt_pk_bf16_f32 v236, v156, v157\n"
        "v_cvt_pk_bf16_f32 v237, v158, v159\n"
        "v_cvt_pk_bf16_f32 v238, v160, v161\n"
        "v_cvt_pk_bf16_f32 v239, v162, v163\n"
        "s_waitcnt vmcnt(0)\n"
        "s_cmp_lg_u32 s19, 0\n"
        "s_cbranch_scc1 Lp2_%=\n"
        "Lpdone_%=:\n"
        "s_and_b32 s86, s2, 1\n"
        "s_lshr_b32 s87, s2, 6\n"
        "s_lshl_b32 s86, s86, 2\n"
        "s_add_u32 s46, s86, s87\n"
        "s_bfe_u32 s47, s2, 0x30003\n"
        "s_mul_i32 s12, s46, 28\n"
        "s_mul_i32 s13, s47, 28\n"
        "s_sub_u32 s12, s12, 12\n"
        "s_sub_u32 s13, s13, 12\n"
        "s_bitcmp1_b32 s2, 2\n"
        "s_cselect_b32 s14, -1, 1\n"
        "s_cselect_b32 s16, 0x124b000, 0\n"
        "s_mul_i32 s14, s14, 86016\n"
        "s_bitcmp1_b32 s2, 1\n"
        "s_cselect_b32 s15, -1, 1\n"
        "s_cselect_b32 s17, 0x14e80, 0\n"
        "s_mul_i32 s15, s15, 384\n"
        "s_add_u32 s86, s16, s17\n"
        "s_cmp_gt_u32 s3, 4\n"
        "s_subb_u32 s91, s3, 0\n"
        "v_mov_b32 v9, s86\n"
        "v_and_b32 v7, 7, v0\n"
        "v_lshrrev_b32 v6, 3, v8\n"
        "v_lshl_add_u32 v6, s91, 3, v6\n"
        "s_mov_b32 s22, 0x1260000\n"
        "s_mov_b32 s23, 0x20000\n"
        "s_lshl_b32 s91, s91, 10\n"
        "s_cmp_eq_u32 s46, 0\n"
        "s_cselect_b32 s86, 12, 4\n"
        "s_cmp_eq_u32 s47, 0\n"
        "s_cselect_b32 s87, 4, 0\n"
        "s_add_u32 s86, s86, s87\n"
        "s_cmp_ge_u32 s3, 4\n"
        "s_cselect_b32 s86, -4, s86\n"
        "s_add_u32 s86, s86, s3\n"
        "s_mul_i32 s86, s86, 768\n"
        "s_add_u32 s10, s4, s86\n"
        "s_addc_u32 s11, s5, 0\n"
        "s_cmp_eq_u32 s3, 4\n"
        "s_cbranch_scc1 Lwave4_%=\n"
        "v_add_u32 v10, 42, v6\n"
        "v_mul_u32_u24 v11, 293, v10\n"
        "v_lshrrev_b32 v11, 12, v11\n"
        "v_mad_i32_i24 v10, v11, -14, v10\n"
        "v_add_u32 v10, 3, v10\n"
        "v_lshl_add_u32 v12, v11, 1, s12\n"
        "v_add_u32 v13, 7, v12\n"
        "v_min_u32 v12, v12, v13\n"
        "v_mad_i32_i24 v248, v12, s14, v9\n"
        "v_lshl_add_u32 v12, v10, 1, s13\n"
        "v_add_u32 v13, 7, v12\n"
        "v_min_u32 v12, v12, v13\n"
        "v_mad_i32_i24 v248, v12, s15, v248\n"
        "v_xor_b32 v12, v11, v10\n"
        "v_and_b32 v12, 2, v12\n"
        "v_and_b32 v13, 1, v11\n"
        "v_lshl_or_b32 v12, v13, 2, v12\n"
        "v_xor_b32 v12, v12, v7\n"
        "v_lshl_add_u32 v248, v12, 4, v248\n"
        "s_cmp_lg_u32 s19, 0\n"
        "s_cselect_b32 s80, 24, s2\n"
        "s_cmp_ge_u32 s80, 24\n"
        "s_cbranch_scc1 Lnopr_%=\n"
        "s_sub_u32 s86, s3, 5\n"
        "s_cmp_lt_u32 s86, 2\n"
        "s_cbranch_scc1 Lprod_%=\n"
        "s_barrier\n"
        "Lnopr_%=:\n"
        "s_add_u32 m0, s91, 0x0\n"
        "s_nop 0\n"
        "buffer_load_dwordx4 v248, s[20:23], 0 offen lds\n"
        "s_cmp_eq_u32 s80, 0x1000\n"
        "s_cbranch_scc1 Lprx_%=\n"
        "v_and_b32 v216, 15, v0\n"
        "v_bfe_u32 v217, v0, 2, 4\n"
        "v_lshlrev_b32 v216, 5, v216\n"
        "v_lshlrev_b32 v217, 5, v217\n"
        "global_load_dwordx4 v[200:203], v216, s[10:11]\n"
        "global_load_dwordx4 v[204:207], v216, s[10:11] offset:16\n"
        "global_load_dwordx4 v[208:211], v217, s[10:11]\n"
        "global_load_dwordx4 v[212:215], v217, s[10:11] offset:16\n"
        "Lprx_%=:\n"
        "v_add_u32 v10, 98, v6\n"
        "v_mul_u32_u24 v11, 293, v10\n"
        "v_lshrrev_b32 v11, 12, v11\n"
        "v_mad_i32_i24 v10, v11, -14, v10\n"
        "v_add_u32 v10, 3, v10\n"
        "v_lshl_add_u32 v12, v11, 1, s12\n"
        "v_add_u32 v13, 7, v12\n"
        "v_min_u32 v12, v12, v13\n"
        "v_mad_i32_i24 v249, v12, s14, v9\n"
        "v_lshl_add_u32 v12, v10, 1, s13\n"
        "v_add_u32 v13, 7, v12\n"
        "v_min_u32 v12, v12, v13\n"
        "v_mad_i32_i24 v249, v12, s15, v249\n"
        "v_xor_b32 v12, v11, v10\n"
        "v_and_b32 v12, 2, v12\n"
        "v_and_b32 v13, 1, v11\n"
        "v_lshl_or_b32 v12, v13, 2, v12\n"
        "v_xor_b32 v12, v12, v7\n"
        "v_lshl_add_u32 v249, v12, 4, v249\n"
        "s_add_u32 m0, s91, 0x1c00\n"
        "s_nop 0\n"
        "buffer_load_dwordx4 v249, s[20:23], 0 offen lds\n"
        "v_add_u32 v10, 154, v6\n"
        "v_mul_u32_u24 v11, 293, v10\n"
        "v_lshrrev_b32 v11, 12, v11\n"
        "v_mad_i32_i24 v10, v11, -14, v10\n"
        "v_add_u32 v10, 3, v10\n"
        "v_lshl_add_u32 v12, v11, 1, s12\n"
        "v_add_u32 v13, 7, v12\n"
        "v_min_u32 v12, v12, v13\n"
        "v_mad_i32_i24 v250, v12, s14, v9\n"
        "v_lshl_add_u32 v12, v10, 1, s13\n"
        "v_add_u32 v13, 7, v12\n"
        "v_min_u32 v12, v12, v13\n"
        "v_mad_i32_i24 v250, v12, s15, v250\n"
        "v_xor_b32 v12, v11, v10\n"
        "v_and_b32 v12, 2, v12\n"
        "v_and_b32 v13, 1, v11\n"
        "v_lshl_or_b32 v12, v13, 2, v12\n"
        "v_xor_b32 v12, v12, v7\n"
        "v_lshl_add_u32 v250, v12, 4, v250\n"
        "s_add_u32 m0, s91, 0x3800\n"
        "s_nop 0\n"
        "buffer_load_dwordx4 v250, s[20:23], 0 offen lds\n"
        "s_add_u32 m0, s91, 0x6380\n"
        "s_nop 0\n"
        "buffer_load_dwordx4 v248, s[20:23], 0 offen offset:128 lds\n"
        "s_waitcnt lgkmcnt(0)\n"
        "s_bitcmp1_b32 s18, 31\n"
        "s_cbranch_scc1 Ltg3_%=\n"
        "s_bitset1_b32 s18, 31\n"
        "s_mul_i32 s7, s7, 0x4c957f2d\n"
        "s_mul_i32 s86, s6, 0x5851f42d\n"
        "s_add_u32 s7, s7, s86\n"
        "s_mul_hi_u32 s86, s6, 0x4c957f2d\n"
        "s_add_u32 s7, s7, s86\n"
        "s_mul_i32 s6, s6, 0x4c957f2d\n"
        "s_add_u32 s6, s6, 0xf767814f\n"
        "s_addc_u32 s7, s7, 0x14057b7e\n"
        "Ltg3_%=:\n"
        "s_mov_b32 s88, s46\n"
        "s_mov_b32 s89, s47\n"
        "s_cmp_ge_u32 s3, 4\n"
        "s_cbranch_scc1 Lnomk_%=\n"
        "s_load_dwordx16 s[40:55], s[10:11], 512\n"
        "s_load_dwordx16 s[56:71], s[10:11], 576\n"
        "s_load_dwordx4 s[72:75], s[10:11], 640\n"
        "s_load_dwordx2 s[76:77], s[10:11], 656\n"
        "Lnomk_%=:\n"
        "v_mov_b32 v2, 0x27000\n"
        "s_and_b32 s86, s3, 3\n"
        "v_mov_b32 v4, 1\n"
        "v_add_u32 v3, s86, v2\n"
        "s_mov_b32 s30, 0x1260000\n"
        "s_mov_b32 s31, 0x20000\n"
        "s_cmp_ge_u32 s80, 24\n"
        "s_cbranch_scc1 Lnob0_%=\n"
        "s_barrier\n"
        "Lnob0_%=:\n"
        "s_cmp_ge_u32 s3, 4\n"
        "s_cbranch_scc1 Lhelper_%=\n"
        "s_add_u32 m0, s91, 0x7f80\n"
        "s_nop 0\n"
        "buffer_load_dwordx4 v249, s[20:23], 0 offen offset:128 lds\n"
        "v_add_u32 v10, 210, v6\n"
        "v_mul_u32_u24 v11, 293, v10\n"
        "v_lshrrev_b32 v11, 12, v11\n"
        "v_mad_i32_i24 v10, v11, -14, v10\n"
        "v_add_u32 v10, 3, v10\n"
        "v_lshl_add_u32 v12, v11, 1, s12\n"
        "v_add_u32 v13, 7, v12\n"
        "v_min_u32 v12, v12, v13\n"
        "v_mad_i32_i24 v251, v12, s14, v9\n"
        "v_lshl_add_u32 v12, v10, 1, s13\n"
        "v_add_u32 v13, 7, v12\n"
        "v_min_u32 v12, v12, v13\n"
        "v_mad_i32_i24 v251, v12, s15, v251\n"
        "v_xor_b32 v12, v11, v10\n"
        "v_and_b32 v12, 2, v12\n"
        "v_and_b32 v13, 1, v11\n"
        "v_lshl_or_b32 v12, v13, 2, v12\n"
        "v_xor_b32 v12, v12, v7\n"
        "v_lshl_add_u32 v251, v12, 4, v251\n"
        "s_add_u32 m0, s91, 0x5400\n"
        "s_nop 0\n"
        "buffer_load_dwordx4 v251, s[20:23], 0 offen lds\n"
        "s_add_u32 m0, s91, 0x9b80\n"
        "s_nop 0\n"
        "buffer_load_dwordx4 v250, s[20:23], 0 offen offset:128 lds\n"
        "s_add_u32 m0, s91, 0xb780\n"
        "s_nop 0\n"
        "buffer_load_dwordx4 v251, s[20:23], 0 offen offset:128 lds\n"
        "s_bitcmp1_b32 s3, 0\n"
        "s_cselect_b32 s80, s89, s88\n"
        "s_cselect_b32 s81, s88, s89\n"
        "s_cselect_b32 s88, s15, s14\n"
        "s_cselect_b32 s89, s17, s16\n"
        "s_cselect_b32 s82, s14, s15\n"
        "s_cselect_b32 s83, s16, s17\n"
        "s_mul_i32 s81, s81, 14\n"
        "s_cmp_ge_u32 s3, 2\n"
        "s_cselect_b32 s86, 6, 0\n"
        "s_add_u32 s81, s81, s86\n"
        "v_bfe_u32 v10, v0, 4, 2\n"
        "v_lshl_add_u32 v10, v10, 1, s81\n"
        "v_and_b32 v12, 15, v0\n"
        "v_lshl_add_u32 v12, v12, 3, s83\n"
        "v_lshl_add_u32 v11, v10, 1, -6\n"
        "v_max_i32 v11, v11, v10\n"
        "v_mad_i32_i24 v6, v11, s82, v12\n"
        "v_add_u32 v10, 1, v10\n"
        "v_lshl_add_u32 v11, v10, 1, -6\n"
        "v_max_i32 v11, v11, v10\n"
        "v_mad_i32_i24 v7, v11, s82, v12\n"
        "s_mul_i32 s86, s80, 14\n"
        "s_add_u32 s87, s3, 1\n"
        "s_and_b32 s87, s87, 2\n"
        "s_lshl_b32 s87, s87, 2\n"
        "s_add_u32 s86, s86, s87\n"
        "s_lshl_b32 s87, s86, 1\n"
        "s_sub_u32 s87, s87, 6\n"
        "s_max_i32 s87, s87, s86\n"
        "s_mul_i32 s87, s87, s88\n"
        "s_add_u32 s80, s87, s89\n"
        "s_add_u32 s86, s86, 1\n"
        "s_lshl_b32 s87, s86, 1\n"
        "s_sub_u32 s87, s87, 6\n"
        "s_max_i32 s87, s87, s86\n"
        "s_mul_i32 s87, s87, s88\n"
        "s_add_u32 s81, s87, s89\n"
        "s_add_u32 s86, s86, 1\n"
        "s_lshl_b32 s87, s86, 1\n"
        "s_sub_u32 s87, s87, 6\n"
        "s_max_i32 s87, s87, s86\n"
        "s_mul_i32 s87, s87, s88\n"
        "s_add_u32 s82, s87, s89\n"
        "s_add_u32 s86, s86, 1\n"
        "s_lshl_b32 s87, s86, 1\n"
        "s_sub_u32 s87, s87, 6\n"
        "s_max_i32 s87, s87, s86\n"
        "s_mul_i32 s87, s87, s88\n"
        "s_add_u32 s83, s87, s89\n"
        "s_add_u32 s86, s86, 1\n"
        "s_lshl_b32 s87, s86, 1\n"
        "s_sub_u32 s87, s87, 6\n"
        "s_max_i32 s87, s87, s86\n"
        "s_mul_i32 s87, s87, s88\n"
        "s_add_u32 s84, s87, s89\n"
        "s_add_u32 s86, s86, 1\n"
        "s_lshl_b32 s87, s86, 1\n"
        "s_sub_u32 s87, s87, 6\n"
        "s_max_i32 s87, s87, s86\n"
        "s_mul_i32 s87, s87, s88\n"
        "s_add_u32 s85, s87, s89\n"
        "s_add_u32 s86, s86, 1\n"
        "s_waitcnt vmcnt(7)\n"
        "v_mov_b32 v218, 5\n"
        "v_and_b32 v219, 48, v0\n"
        "v_and_b32 v220, 3, v0\n"
        "v_lshlrev_b32 v220, 3, v220\n"
        "v_lshlrev_b32_sdwa v22, v218, v200 dst_sel:DWORD dst_unused:UNUSED_PAD src0_sel:DWORD src1_sel:WORD_0\n"
        "v_xor_b32 v22, v22, v219\n"
        "v_xor_b32 v23, 64, v22\n"
        "v_lshlrev_b32_sdwa v24, v218, v200 dst_sel:DWORD dst_unused:UNUSED_PAD src0_sel:DWORD src1_sel:WORD_1\n"
        "v_xor_b32 v24, v24, v219\n"
        "v_xor_b32 v25, 64, v24\n"
        "v_lshlrev_b32_sdwa v26, v218, v201 dst_sel:DWORD dst_unused:UNUSED_PAD src0_sel:DWORD src1_sel:WORD_0\n"
        "v_xor_b32 v26, v26, v219\n"
        "v_xor_b32 v27, 64, v26\n"
        "v_lshlrev_b32_sdwa v28, v218, v201 dst_sel:DWORD dst_unused:UNUSED_PAD src0_sel:DWORD src1_sel:WORD_1\n"
        "v_xor_b32 v28, v28, v219\n"
        "v_xor_b32 v29, 64, v28\n"
        "v_lshlrev_b32_sdwa v30, v218, v202 dst_sel:DWORD dst_unused:UNUSED_PAD src0_sel:DWORD src1_sel:WORD_0\n"
        "v_xor_b32 v30, v30, v219\n"
        "v_lshlrev_b32_sdwa v32, v218, v202 dst_sel:DWORD dst_unused:UNUSED_PAD src0_sel:DWORD src1_sel:WORD_1\n"
        "v_xor_b32 v32, v32, v219\n"
        "v_lshlrev_b32_sdwa v34, v218, v203 dst_sel:DWORD dst_unused:UNUSED_PAD src0_sel:DWORD src1_sel:WORD_0\n"
        "v_xor_b32 v34, v34, v219\n"
        "v_lshlrev_b32_sdwa v36, v218, v203 dst_sel:DWORD dst_unused:UNUSED_PAD src0_sel:DWORD src1_sel:WORD_1\n"
        "v_xor_b32 v36, v36, v219\n"
        "v_lshlrev_b32_sdwa v38, v218, v204 dst_sel:DWORD dst_unused:UNUSED_PAD src0_sel:DWORD src1_sel:WORD_0\n"
        "v_xor_b32 v38, v38, v219\n"
        "v_xor_b32 v39, 64, v38\n"
        "v_lshlrev_b32_sdwa v40, v218, v204 dst_sel:DWORD dst_unused:UNUSED_PAD src0_sel:DWORD src1_sel:WORD_1\n"
        "v_xor_b32 v40, v40, v219\n"
        "v_lshlrev_b32_sdwa v42, v218, v205 dst_sel:DWORD dst_unused:UNUSED_PAD src0_sel:DWORD src1_sel:WORD_0\n"
        "v_xor_b32 v42, v42, v219\n"
        "v_lshlrev_b32_sdwa v31, v218, v205 dst_sel:DWORD dst_unused:UNUSED_PAD src0_sel:DWORD src1_sel:WORD_1\n"
        "v_xor_b32 v31, v31, v219\n"
        "v_xor_b32 v33, 64, v31\n"
        "v_lshlrev_b32_sdwa v35, v218, v206 dst_sel:DWORD dst_unused:UNUSED_PAD src0_sel:DWORD src1_sel:WORD_0\n"
        "v_xor_b32 v35, v35, v219\n"
        "v_xor_b32 v37, 64, v35\n"
        "v_lshlrev_b32_sdwa v41, v218, v206 dst_sel:DWORD dst_unused:UNUSED_PAD src0_sel:DWORD src1_sel:WORD_1\n"
        "v_xor_b32 v41, v41, v219\n"
        "v_xor_b32 v43, 64, v41\n"
        "v_lshlrev_b32_sdwa v10, v218, v208 dst_sel:DWORD dst_unused:UNUSED_PAD src0_sel:DWORD src1_sel:WORD_0\n"
        "v_or_b32 v10, v10, v220\n"
        "v_lshlrev_b32_sdwa v11, v218, v208 dst_sel:DWORD dst_unused:UNUSED_PAD src0_sel:DWORD src1_sel:WORD_1\n"
        "v_or_b32 v11, v11, v220\n"
        "v_lshlrev_b32_sdwa v12, v218, v209 dst_sel:DWORD dst_unused:UNUSED_PAD src0_sel:DWORD src1_sel:WORD_0\n"
        "v_or_b32 v12, v12, v220\n"
        "v_lshlrev_b32_sdwa v13, v218, v209 dst_sel:DWORD dst_unused:UNUSED_PAD src0_sel:DWORD src1_sel:WORD_1\n"
        "v_or_b32 v13, v13, v220\n"
        "v_lshlrev_b32_sdwa v14, v218, v210 dst_sel:DWORD dst_unused:UNUSED_PAD src0_sel:DWORD src1_sel:WORD_0\n"
        "v_or_b32 v14, v14, v220\n"
        "v_lshlrev_b32_sdwa v15, v218, v210 dst_sel:DWORD dst_unused:UNUSED_PAD src0_sel:DWORD src1_sel:WORD_1\n"
        "v_or_b32 v15, v15, v220\n"
        "v_lshlrev_b32_sdwa v16, v218, v211 dst_sel:DWORD dst_unused:UNUSED_PAD src0_sel:DWORD src1_sel:WORD_0\n"
        "v_or_b32 v16, v16, v220\n"
        "v_lshlrev_b32_sdwa v17, v218, v211 dst_sel:DWORD dst_unused:UNUSED_PAD src0_sel:DWORD src1_sel:WORD_1\n"
        "v_or_b32 v17, v17, v220\n"
        "v_lshlrev_b32_sdwa v18, v218, v212 dst_sel:DWORD dst_unused:UNUSED_PAD src0_sel:DWORD src1_sel:WORD_0\n"
        "v_or_b32 v18, v18, v220\n"
        "v_lshlrev_b32_sdwa v19, v218, v212 dst_sel:DWORD dst_unused:UNUSED_PAD src0_sel:DWORD src1_sel:WORD_1\n"
        "v_or_b32 v19, v19, v220\n"
        "v_lshlrev_b32_sdwa v20, v218, v213 dst_sel:DWORD dst_unused:UNUSED_PAD src0_sel:DWORD src1_sel:WORD_0\n"
        "v_or_b32 v20, v20, v220\n"
        "v_and_b32 v1, 63, v0\n"
        "v_lshlrev_b32 v1, 4, v1\n"
        "v_add_u32 v1, 0x12c00, v1\n"
        "v_add_u32 v9, 0xc800, v1\n"
        "v_mov_b32 v8, 0xf149f2ca\n"
        "s_mov_b32 s90, 0x3f803f80\n"
        "s_mov_b32 s91, 0x3f803f80\n"
        "s_setprio 2\n"
        "s_waitcnt vmcnt(0)\n"
        "Lcre_%=:\n"
        "s_waitcnt lgkmcnt(0)\n"
        "s_barrier\n"
        "ds_read_b128 v[232:235], v31 offset:0\n"
        "ds_read_b128 v[236:239], v33 offset:0\n"
        "ds_read_b128 v[200:203], v1 offset:0\n"
        "ds_read_b128 v[204:207], v1 offset:1024\n"
        "ds_read_b128 v[208:211], v1 offset:6144\n"
        "ds_read_b128 v[212:215], v1 offset:7168\n"
        "ds_read_b128 v[240:243], v35 offset:0\n"
        "ds_read_b128 v[244:247], v37 offset:0\n"
        "ds_read_b128 v[188:191], v41 offset:0\n"
        "ds_read_b128 v[192:195], v43 offset:0\n"
        "s_waitcnt lgkmcnt(7)\n"
        "ds_read_b128 v[216:219], v1 offset:12288\n"
        "v_mfma_f32_16x16x32_bf16 v[44:47], v[200:203], v[232:235], 0\n"
        "s_waitcnt lgkmcnt(7)\n"
        "ds_read_b128 v[220:223], v1 offset:13312\n"
        "v_mfma_f32_16x16x32_bf16 v[44:47], v[204:207], v[236:239], v[44:47]\n"
        "s_waitcnt lgkmcnt(7)\n"
        "ds_read_b128 v[224:227], v1 offset:18432\n"
        "v_mfma_f32_16x16x32_bf16 v[116:119], v[208:211], v[232:235], 0\n"
        "s_waitcnt lgkmcnt(7)\n"
        "ds_read_b128 v[228:231], v1 offset:19456\n"
        "v_mfma_f32_16x16x32_bf16 v[116:119], v[212:215], v[236:239], v[116:119]\n"
        "s_waitcnt lgkmcnt(7)\n"
        "ds_read_b128 v[196:199], v1 offset:2048\n"
        "v_mfma_f32_16x16x32_bf16 v[68:71], v[200:203], v[240:243], 0\n"
        "s_waitcnt lgkmcnt(7)\n"
        "v_mfma_f32_16x16x32_bf16 v[68:71], v[204:207], v[244:247], v[68:71]\n"
        "v_mfma_f32_16x16x32_bf16 v[120:123], v[208:211], v[240:243], 0\n"
        "v_mfma_f32_16x16x32_bf16 v[120:123], v[212:215], v[244:247], v[120:123]\n"
        "s_waitcnt lgkmcnt(6)\n"
        "ds_read_b128 v[248:251], v31 offset:25600\n"
        "v_mfma_f32_16x16x32_bf16 v[92:95], v[200:203], v[188:191], 0\n"
        "s_waitcnt lgkmcnt(6)\n"
        "ds_read_b128 v[200:203], v1 offset:3072\n"
        "v_mfma_f32_16x16x32_bf16 v[92:95], v[204:207], v[192:195], v[92:95]\n"
        "ds_read_b128 v[204:207], v1 offset:8192\n"
        "v_mfma_f32_16x16x32_bf16 v[124:127], v[208:211], v[188:191], 0\n"
        "ds_read_b128 v[208:211], v1 offset:9216\n"
        "v_mfma_f32_16x16x32_bf16 v[124:127], v[212:215], v[192:195], v[124:127]\n"
        "s_waitcnt lgkmcnt(8)\n"
        "ds_read_b128 v[212:215], v1 offset:14336\n"
        "v_mfma_f32_16x16x32_bf16 v[48:51], v[216:219], v[232:235], 0\n"
        "s_waitcnt lgkmcnt(8)\n"
        "ds_read_b128 v[252:255], v33 offset:25600\n"
        "v_mfma_f32_16x16x32_bf16 v[48:51], v[220:223], v[236:239], v[48:51]\n"
        "v_mfma_f32_16x16x32_bf16 v[72:75], v[216:219], v[240:243], 0\n"
        "v_mfma_f32_16x16x32_bf16 v[72:75], v[220:223], v[244:247], v[72:75]\n"
        "v_mfma_f32_16x16x32_bf16 v[96:99], v[216:219], v[188:191], 0\n"
        "ds_read_b128 v[216:219], v1 offset:15360\n"
        "v_mfma_f32_16x16x32_bf16 v[96:99], v[220:223], v[192:195], v[96:99]\n"
        "s_waitcnt lgkmcnt(9)\n"
        "ds_read_b128 v[220:223], v1 offset:20480\n"
        "v_mfma_f32_16x16x32_bf16 v[144:147], v[224:227], v[232:235], 0\n"
        "s_waitcnt lgkmcnt(9)\n"
        "v_mfma_f32_16x16x32_bf16 v[144:147], v[228:231], v[236:239], v[144:147]\n"
        "v_mfma_f32_16x16x32_bf16 v[128:131], v[224:227], v[240:243], 0\n"
        "v_mfma_f32_16x16x32_bf16 v[128:131], v[228:231], v[244:247], v[128:131]\n"
        "v_mfma_f32_16x16x32_bf16 v[148:151], v[224:227], v[188:191], 0\n"
        "ds_read_b128 v[224:227], v1 offset:21504\n"
        "v_mfma_f32_16x16x32_bf16 v[148:151], v[228:231], v[192:195], v[148:151]\n"
        "s_waitcnt lgkmcnt(9)\n"
        "ds_read_b128 v[228:231], v1 offset:4096\n"
        "v_mfma_f32_16x16x32_bf16 v[52:55], v[196:199], v[232:235], 0\n"
        "s_waitcnt lgkmcnt(8)\n"
        "v_mfma_f32_16x16x32_bf16 v[52:55], v[200:203], v[236:239], v[52:55]\n"
        "v_mfma_f32_16x16x32_bf16 v[76:79], v[196:199], v[240:243], 0\n"
        "v_mfma_f32_16x16x32_bf16 v[76:79], v[200:203], v[244:247], v[76:79]\n"
        "v_mfma_f32_16x16x32_bf16 v[100:103], v[196:199], v[188:191], 0\n"
        "ds_read_b128 v[196:199], v1 offset:5120\n"
        "v_mfma_f32_16x16x32_bf16 v[100:103], v[200:203], v[192:195], v[100:103]\n"
        "s_waitcnt lgkmcnt(8)\n"
        "ds_read_b128 v[200:203], v1 offset:10240\n"
        "v_mfma_f32_16x16x32_bf16 v[132:135], v[204:207], v[232:235], 0\n"
        "s_waitcnt lgkmcnt(8)\n"
        "v_mfma_f32_16x16x32_bf16 v[132:135], v[208:211], v[236:239], v[132:135]\n"
        "v_mfma_f32_16x16x32_bf16 v[160:163], v[204:207], v[240:243], 0\n"
        "v_mfma_f32_16x16x32_bf16 v[160:163], v[208:211], v[244:247], v[160:163]\n"
        "v_mfma_f32_16x16x32_bf16 v[136:139], v[204:207], v[188:191], 0\n"
        "ds_read_b128 v[204:207], v1 offset:11264\n"
        "v_mfma_f32_16x16x32_bf16 v[136:139], v[208:211], v[192:195], v[136:139]\n"
        "s_waitcnt lgkmcnt(8)\n"
        "ds_read_b128 v[208:211], v1 offset:16384\n"
        "v_mfma_f32_16x16x32_bf16 v[56:59], v[212:215], v[232:235], 0\n"
        "s_waitcnt lgkmcnt(7)\n"
        "v_mfma_f32_16x16x32_bf16 v[56:59], v[216:219], v[236:239], v[56:59]\n"
        "v_mfma_f32_16x16x32_bf16 v[80:83], v[212:215], v[240:243], 0\n"
        "v_mfma_f32_16x16x32_bf16 v[80:83], v[216:219], v[244:247], v[80:83]\n"
        "v_mfma_f32_16x16x32_bf16 v[104:107], v[212:215], v[188:191], 0\n"
        "ds_read_b128 v[212:215], v1 offset:17408\n"
        "v_mfma_f32_16x16x32_bf16 v[104:107], v[216:219], v[192:195], v[104:107]\n"
        "s_waitcnt lgkmcnt(7)\n"
        "ds_read_b128 v[216:219], v1 offset:22528\n"
        "v_mfma_f32_16x16x32_bf16 v[164:167], v[220:223], v[232:235], 0\n"
        "s_waitcnt lgkmcnt(7)\n"
        "v_mfma_f32_16x16x32_bf16 v[164:167], v[224:227], v[236:239], v[164:167]\n"
        "v_mfma_f32_16x16x32_bf16 v[140:143], v[220:223], v[240:243], 0\n"
        "ds_read_b32 v5, v2 offset:0\n"
        "v_mfma_f32_16x16x32_bf16 v[140:143], v[224:227], v[244:247], v[140:143]\n"
        "v_mfma_f32_16x16x32_bf16 v[168:171], v[220:223], v[188:191], 0\n"
        "ds_read_b128 v[220:223], v1 offset:23552\n"
        "v_mfma_f32_16x16x32_bf16 v[168:171], v[224:227], v[192:195], v[168:171]\n"
        "s_waitcnt lgkmcnt(8)\n"
        "ds_write_b8 v3, v4 offset:8\n"
        "v_mfma_f32_16x16x32_bf16 v[60:63], v[228:231], v[232:235], 0\n"
        "s_waitcnt lgkmcnt(8)\n"
        "v_mfma_f32_16x16x32_bf16 v[60:63], v[196:199], v[236:239], v[60:63]\n"
        "v_mfma_f32_16x16x32_bf16 v[84:87], v[228:231], v[240:243], 0\n"
        "s_waitcnt lgkmcnt(2)\n"
        "v_readfirstlane_b32 s86, v5\n"
        "v_mfma_f32_16x16x32_bf16 v[84:87], v[196:199], v[244:247], v[84:87]\n"
        "v_mfma_f32_16x16x32_bf16 v[108:111], v[228:231], v[188:191], 0\n"
        "v_mfma_f32_16x16x32_bf16 v[108:111], v[196:199], v[192:195], v[108:111]\n"
        "s_cmp_eq_u32 s86, 0x1010101\n"
        "v_mfma_f32_16x16x32_bf16 v[152:155], v[200:203], v[232:235], 0\n"
        "s_cbranch_scc0 Lsl4_%=\n"
        "Lrt5_%=:\n"
        "v_mfma_f32_16x16x32_bf16 v[152:155], v[204:207], v[236:239], v[152:155]\n"
        "ds_read_b128 v[224:227], v1 offset:25600\n"
        "v_mfma_f32_16x16x32_bf16 v[156:159], v[200:203], v[240:243], 0\n"
        "ds_read_b128 v[228:231], v1 offset:26624\n"
        "v_mfma_f32_16x16x32_bf16 v[156:159], v[204:207], v[244:247], v[156:159]\n"
        "ds_read_b128 v[196:199], v1 offset:31744\n"
        "v_mfma_f32_16x16x32_bf16 v[172:175], v[200:203], v[188:191], 0\n"
        "ds_read_b128 v[200:203], v1 offset:32768\n"
        "v_mfma_f32_16x16x32_bf16 v[172:175], v[204:207], v[192:195], v[172:175]\n"
        "ds_read_b128 v[204:207], v1 offset:37888\n"
        "v_mfma_f32_16x16x32_bf16 v[64:67], v[208:211], v[232:235], 0\n"
        "v_mfma_f32_16x16x32_bf16 v[64:67], v[212:215], v[236:239], v[64:67]\n"
        "v_mfma_f32_16x16x32_bf16 v[176:179], v[216:219], v[232:235], 0\n"
        "s_waitcnt lgkmcnt(6)\n"
        "v_mfma_f32_16x16x32_bf16 v[176:179], v[220:223], v[236:239], v[176:179]\n"
        "ds_read_b128 v[232:235], v35 offset:25600\n"
        "v_mfma_f32_16x16x32_bf16 v[88:91], v[208:211], v[240:243], 0\n"
        "ds_read_b128 v[236:239], v37 offset:25600\n"
        "v_mfma_f32_16x16x32_bf16 v[88:91], v[212:215], v[244:247], v[88:91]\n"
        "v_mfma_f32_16x16x32_bf16 v[180:183], v[216:219], v[240:243], 0\n"
        "v_mfma_f32_16x16x32_bf16 v[180:183], v[220:223], v[244:247], v[180:183]\n"
        "ds_read_b128 v[240:243], v41 offset:25600\n"
        "v_mfma_f32_16x16x32_bf16 v[112:115], v[208:211], v[188:191], 0\n"
        "ds_read_b128 v[244:247], v43 offset:25600\n"
        "v_mfma_f32_16x16x32_bf16 v[112:115], v[212:215], v[192:195], v[112:115]\n"
        "ds_read_b128 v[208:211], v1 offset:38912\n"
        "v_mfma_f32_16x16x32_bf16 v[184:187], v[216:219], v[188:191], 0\n"
        "ds_read_b128 v[212:215], v1 offset:44032\n"
        "v_mfma_f32_16x16x32_bf16 v[184:187], v[220:223], v[192:195], v[184:187]\n"
        "s_waitcnt lgkmcnt(10)\n"
        "ds_read_b128 v[216:219], v1 offset:45056\n"
        "v_mfma_f32_16x16x32_bf16 v[44:47], v[224:227], v[248:251], v[44:47]\n"
        "s_waitcnt lgkmcnt(10)\n"
        "ds_read_b128 v[220:223], v1 offset:27648\n"
        "v_mfma_f32_16x16x32_bf16 v[44:47], v[228:231], v[252:255], v[44:47]\n"
        "s_waitcnt lgkmcnt(10)\n"
        "v_mfma_f32_16x16x32_bf16 v[116:119], v[196:199], v[248:251], v[116:119]\n"
        "s_waitcnt lgkmcnt(9)\n"
        "v_mfma_f32_16x16x32_bf16 v[116:119], v[200:203], v[252:255], v[116:119]\n"
        "s_waitcnt lgkmcnt(7)\n"
        "v_mfma_f32_16x16x32_bf16 v[68:71], v[224:227], v[232:235], v[68:71]\n"
        "s_waitcnt lgkmcnt(6)\n"
        "v_mfma_f32_16x16x32_bf16 v[68:71], v[228:231], v[236:239], v[68:71]\n"
        "v_mfma_f32_16x16x32_bf16 v[120:123], v[196:199], v[232:235], v[120:123]\n"
        "v_mfma_f32_16x16x32_bf16 v[120:123], v[200:203], v[236:239], v[120:123]\n"
        "s_waitcnt lgkmcnt(5)\n"
        "v_mfma_f32_16x16x32_bf16 v[92:95], v[224:227], v[240:243], v[92:95]\n"
        "s_waitcnt lgkmcnt(4)\n"
        "ds_read_b128 v[224:227], v1 offset:28672\n"
        "v_mfma_f32_16x16x32_bf16 v[92:95], v[228:231], v[244:247], v[92:95]\n"
        "ds_read_b128 v[228:231], v1 offset:33792\n"
        "v_mfma_f32_16x16x32_bf16 v[124:127], v[196:199], v[240:243], v[124:127]\n"
        "ds_read_b128 v[196:199], v1 offset:34816\n"
        "v_mfma_f32_16x16x32_bf16 v[124:127], v[200:203], v[244:247], v[124:127]\n"
        "ds_read_b128 v[200:203], v1 offset:39936\n"
        "v_mfma_f32_16x16x32_bf16 v[48:51], v[204:207], v[248:251], v[48:51]\n"
        "s_waitcnt lgkmcnt(7)\n"
        "v_mfma_f32_16x16x32_bf16 v[48:51], v[208:211], v[252:255], v[48:51]\n"
        "v_mfma_f32_16x16x32_bf16 v[72:75], v[204:207], v[232:235], v[72:75]\n"
        "v_mfma_f32_16x16x32_bf16 v[72:75], v[208:211], v[236:239], v[72:75]\n"
        "v_mfma_f32_16x16x32_bf16 v[96:99], v[204:207], v[240:243], v[96:99]\n"
        "ds_read_b128 v[204:207], v1 offset:40960\n"
        "v_mfma_f32_16x16x32_bf16 v[96:99], v[208:211], v[244:247], v[96:99]\n"
        "s_waitcnt lgkmcnt(7)\n"
        "ds_read_b128 v[208:211], v1 offset:46080\n"
        "v_mfma_f32_16x16x32_bf16 v[144:147], v[212:215], v[248:251], v[144:147]\n"
        "s_waitcnt lgkmcnt(7)\n"
        "v_mfma_f32_16x16x32_bf16 v[144:147], v[216:219], v[252:255], v[144:147]\n"
        "v_mfma_f32_16x16x32_bf16 v[128:131], v[212:215], v[232:235], v[128:131]\n"
        "v_mfma_f32_16x16x32_bf16 v[128:131], v[216:219], v[236:239], v[128:131]\n"
        "v_mfma_f32_16x16x32_bf16 v[148:151], v[212:215], v[240:243], v[148:151]\n"
        "ds_read_b128 v[212:215], v1 offset:47104\n"
        "v_mfma_f32_16x16x32_bf16 v[148:151], v[216:219], v[244:247], v[148:151]\n"
        "s_waitcnt lgkmcnt(7)\n"
        "ds_read_b128 v[216:219], v1 offset:29696\n"
        "v_mfma_f32_16x16x32_bf16 v[52:55], v[220:223], v[248:251], v[52:55]\n"
        "s_waitcnt lgkmcnt(7)\n"
        "v_mfma_f32_16x16x32_bf16 v[52:55], v[224:227], v[252:255], v[52:55]\n"
        "v_mfma_f32_16x16x32_bf16 v[76:79], v[220:223], v[232:235], v[76:79]\n"
        "v_mfma_f32_16x16x32_bf16 v[76:79], v[224:227], v[236:239], v[76:79]\n"
        "v_mfma_f32_16x16x32_bf16 v[100:103], v[220:223], v[240:243], v[100:103]\n"
        "ds_read_b128 v[220:223], v1 offset:30720\n"
        "v_mfma_f32_16x16x32_bf16 v[100:103], v[224:227], v[244:247], v[100:103]\n"
        "s_waitcnt lgkmcnt(7)\n"
        "ds_read_b128 v[224:227], v1 offset:35840\n"
        "v_mfma_f32_16x16x32_bf16 v[132:135], v[228:231], v[248:251], v[132:135]\n"
        "s_waitcnt lgkmcnt(7)\n"
        "v_mfma_f32_16x16x32_bf16 v[132:135], v[196:199], v[252:255], v[132:135]\n"
        "v_mfma_f32_16x16x32_bf16 v[160:163], v[228:231], v[232:235], v[160:163]\n"
        "v_mfma_f32_16x16x32_bf16 v[160:163], v[196:199], v[236:239], v[160:163]\n"
        "v_mfma_f32_16x16x32_bf16 v[136:139], v[228:231], v[240:243], v[136:139]\n"
        "ds_read_b128 v[228:231], v1 offset:36864\n"
        "v_mfma_f32_16x16x32_bf16 v[136:139], v[196:199], v[244:247], v[136:139]\n"
        "s_waitcnt lgkmcnt(7)\n"
        "ds_read_b128 v[196:199], v1 offset:41984\n"
        "v_mfma_f32_16x16x32_bf16 v[56:59], v[200:203], v[248:251], v[56:59]\n"
        "s_waitcnt lgkmcnt(7)\n"
        "v_mfma_f32_16x16x32_bf16 v[56:59], v[204:207], v[252:255], v[56:59]\n"
        "v_mfma_f32_16x16x32_bf16 v[80:83], v[200:203], v[232:235], v[80:83]\n"
        "v_mfma_f32_16x16x32_bf16 v[80:83], v[204:207], v[236:239], v[80:83]\n"
        "v_mfma_f32_16x16x32_bf16 v[104:107], v[200:203], v[240:243], v[104:107]\n"
        "ds_read_b128 v[200:203], v1 offset:43008\n"
        "v_mfma_f32_16x16x32_bf16 v[104:107], v[204:207], v[244:247], v[104:107]\n"
        "s_waitcnt lgkmcnt(7)\n"
        "ds_read_b128 v[204:207], v1 offset:48128\n"
        "v_mfma_f32_16x16x32_bf16 v[164:167], v[208:211], v[248:251], v[164:167]\n"
        "s_waitcnt lgkmcnt(7)\n"
        "v_mfma_f32_16x16x32_bf16 v[164:167], v[212:215], v[252:255], v[164:167]\n"
        "v_mfma_f32_16x16x32_bf16 v[140:143], v[208:211], v[232:235], v[140:143]\n"
        "ds_read_b32 v5, v2 offset:4\n"
        "v_mfma_f32_16x16x32_bf16 v[140:143], v[212:215], v[236:239], v[140:143]\n"
        "v_mfma_f32_16x16x32_bf16 v[168:171], v[208:211], v[240:243], v[168:171]\n"
        "ds_read_b128 v[208:211], v1 offset:49152\n"
        "v_mfma_f32_16x16x32_bf16 v[168:171], v[212:215], v[244:247], v[168:171]\n"
        "s_waitcnt lgkmcnt(8)\n"
        "ds_write_b8 v3, v4 offset:12\n"
        "v_mfma_f32_16x16x32_bf16 v[60:63], v[216:219], v[248:251], v[60:63]\n"
        "s_waitcnt lgkmcnt(8)\n"
        "v_mfma_f32_16x16x32_bf16 v[60:63], v[220:223], v[252:255], v[60:63]\n"
        "v_mfma_f32_16x16x32_bf16 v[84:87], v[216:219], v[232:235], v[84:87]\n"
        "s_waitcnt lgkmcnt(2)\n"
        "v_readfirstlane_b32 s86, v5\n"
        "v_mfma_f32_16x16x32_bf16 v[84:87], v[220:223], v[236:239], v[84:87]\n"
        "v_mfma_f32_16x16x32_bf16 v[108:111], v[216:219], v[240:243], v[108:111]\n"
        "v_mfma_f32_16x16x32_bf16 v[108:111], v[220:223], v[244:247], v[108:111]\n"
        "s_cmp_eq_u32 s86, 0x1010101\n"
        "v_mfma_f32_16x16x32_bf16 v[152:155], v[224:227], v[248:251], v[152:155]\n"
        "s_cbranch_scc0 Lsl7_%=\n"
        "Lrt8_%=:\n"
        "v_mfma_f32_16x16x32_bf16 v[152:155], v[228:231], v[252:255], v[152:155]\n"
        "ds_read_b128 v[212:215], v9 offset:0\n"
        "v_mfma_f32_16x16x32_bf16 v[156:159], v[224:227], v[232:235], v[156:159]\n"
        "ds_read_b128 v[216:219], v9 offset:1024\n"
        "v_mfma_f32_16x16x32_bf16 v[156:159], v[228:231], v[236:239], v[156:159]\n"
        "ds_read_b128 v[188:191], v31 offset:51200\n"
        "v_mfma_f32_16x16x32_bf16 v[172:175], v[224:227], v[240:243], v[172:175]\n"
        "ds_read_b128 v[192:195], v33 offset:51200\n"
        "v_xor_b32 v31, 64, v30\n"
        "v_mfma_f32_16x16x32_bf16 v[172:175], v[228:231], v[244:247], v[172:175]\n"
        "ds_read_b128 v[220:223], v9 offset:6144\n"
        "v_xor_b32 v33, 64, v32\n"
        "v_mfma_f32_16x16x32_bf16 v[64:67], v[196:199], v[248:251], v[64:67]\n"
        "ds_read_b128 v[224:227], v9 offset:7168\n"
        "v_mfma_f32_16x16x32_bf16 v[64:67], v[200:203], v[252:255], v[64:67]\n"
        "ds_read_b128 v[228:231], v9 offset:12288\n"
        "v_mfma_f32_16x16x32_bf16 v[176:179], v[204:207], v[248:251], v[176:179]\n"
        "s_waitcnt lgkmcnt(8)\n"
        "v_mfma_f32_16x16x32_bf16 v[176:179], v[208:211], v[252:255], v[176:179]\n"
        "ds_read_b128 v[248:251], v35 offset:51200\n"
        "v_mfma_f32_16x16x32_bf16 v[88:91], v[196:199], v[232:235], v[88:91]\n"
        "ds_read_b128 v[252:255], v37 offset:51200\n"
        "v_xor_b32 v35, 64, v34\n"
        "v_mfma_f32_16x16x32_bf16 v[88:91], v[200:203], v[236:239], v[88:91]\n"
        "v_xor_b32 v37, 64, v36\n"
        "v_mfma_f32_16x16x32_bf16 v[180:183], v[204:207], v[232:235], v[180:183]\n"
        "v_mfma_f32_16x16x32_bf16 v[180:183], v[208:211], v[236:239], v[180:183]\n"
        "ds_read_b128 v[232:235], v41 offset:51200\n"
        "v_mfma_f32_16x16x32_bf16 v[112:115], v[196:199], v[240:243], v[112:115]\n"
        "ds_read_b128 v[236:239], v43 offset:51200\n"
        "v_xor_b32 v41, 64, v40\n"
        "v_mfma_f32_16x16x32_bf16 v[112:115], v[200:203], v[244:247], v[112:115]\n"
        "ds_read_b128 v[196:199], v9 offset:13312\n"
        "v_xor_b32 v43, 64, v42\n"
        "v_mfma_f32_16x16x32_bf16 v[184:187], v[204:207], v[240:243], v[184:187]\n"
        "ds_read_b128 v[200:203], v9 offset:18432\n"
        "v_mfma_f32_16x16x32_bf16 v[184:187], v[208:211], v[244:247], v[184:187]\n"
        "s_waitcnt lgkmcnt(10)\n"
        "ds_read_b128 v[204:207], v9 offset:19456\n"
        "v_mfma_f32_16x16x32_bf16 v[44:47], v[212:215], v[188:191], v[44:47]\n"
        "s_waitcnt lgkmcnt(10)\n"
        "ds_read_b128 v[208:211], v9 offset:2048\n"
        "v_mfma_f32_16x16x32_bf16 v[44:47], v[216:219], v[192:195], v[44:47]\n"
        "s_waitcnt lgkmcnt(10)\n"
        "ds_read_b128 v[240:243], v9 offset:3072\n"
        "v_mfma_f32_16x16x32_bf16 v[116:119], v[220:223], v[188:191], v[116:119]\n"
        "s_waitcnt lgkmcnt(10)\n"
        "ds_read_b128 v[244:247], v9 offset:8192\n"
        "v_mfma_f32_16x16x32_bf16 v[116:119], v[224:227], v[192:195], v[116:119]\n"
        "s_waitcnt lgkmcnt(9)\n"
        "v_mfma_f32_16x16x32_bf16 v[68:71], v[212:215], v[248:251], v[68:71]\n"
        "s_waitcnt lgkmcnt(8)\n"
        "v_cvt_pk_bf16_f32 v44, v44, v45\n"
        "v_cvt_pk_bf16_f32 v45, v46, v47\n"
        "v_mfma_f32_16x16x32_bf16 v[68:71], v[216:219], v[252:255], v[68:71]\n"
        "v_mfma_f32_16x16x32_bf16 v[120:123], v[220:223], v[248:251], v[120:123]\n"
        "v_mfma_f32_16x16x32_bf16 v[120:123], v[224:227], v[252:255], v[120:123]\n"
        "s_waitcnt lgkmcnt(7)\n"
        "v_cvt_pk_bf16_f32 v46, v116, v117\n"
        "v_cvt_pk_bf16_f32 v47, v118, v119\n"
        "v_mfma_f32_16x16x32_bf16 v[92:95], v[212:215], v[232:235], v[92:95]\n"
        "s_waitcnt lgkmcnt(6)\n"
        "ds_read_b128 v[212:215], v9 offset:9216\n"
        "v_mfma_f32_16x16x32_bf16 v[92:95], v[216:219], v[236:239], v[92:95]\n"
        "v_cvt_pk_bf16_f32 v68, v68, v69\n"
        "v_cvt_pk_bf16_f32 v69, v70, v71\n"
        "v_mfma_f32_16x16x32_bf16 v[124:127], v[220:223], v[232:235], v[124:127]\n"
        "v_cvt_pk_bf16_f32 v70, v120, v121\n"
        "v_cvt_pk_bf16_f32 v71, v122, v123\n"
        "v_mfma_f32_16x16x32_bf16 v[124:127], v[224:227], v[236:239], v[124:127]\n"
        "ds_read_b128 v[216:219], v9 offset:14336\n"
        "v_mfma_f32_16x16x32_bf16 v[48:51], v[228:231], v[188:191], v[48:51]\n"
        "s_waitcnt lgkmcnt(7)\n"
        "v_cvt_pk_bf16_f32 v92, v92, v93\n"
        "v_cvt_pk_bf16_f32 v93, v94, v95\n"
        "v_mfma_f32_16x16x32_bf16 v[48:51], v[196:199], v[192:195], v[48:51]\n"
        "ds_read_b128 v[220:223], v9 offset:15360\n"
        "v_mfma_f32_16x16x32_bf16 v[72:75], v[228:231], v[248:251], v[72:75]\n"
        "v_cvt_pk_bf16_f32 v94, v124, v125\n"
        "v_cvt_pk_bf16_f32 v95, v126, v127\n"
        "v_mfma_f32_16x16x32_bf16 v[72:75], v[196:199], v[252:255], v[72:75]\n"
        "ds_read_b128 v[224:227], v9 offset:20480\n"
        "v_mfma_f32_16x16x32_bf16 v[96:99], v[228:231], v[232:235], v[96:99]\n"
        "ds_read_b128 v[116:119], v9 offset:21504\n"
        "v_mfma_f32_16x16x32_bf16 v[96:99], v[196:199], v[236:239], v[96:99]\n"
        "s_waitcnt lgkmcnt(9)\n"
        "v_cvt_pk_bf16_f32 v48, v48, v49\n"
        "v_cvt_pk_bf16_f32 v49, v50, v51\n"
        "v_mfma_f32_16x16x32_bf16 v[144:147], v[200:203], v[188:191], v[144:147]\n"
        "s_waitcnt lgkmcnt(8)\n"
        "v_cvt_pk_bf16_f32 v72, v72, v73\n"
        "v_cvt_pk_bf16_f32 v73, v74, v75\n"
        "v_mfma_f32_16x16x32_bf16 v[144:147], v[204:207], v[192:195], v[144:147]\n"
        "v_cvt_pk_bf16_f32 v96, v96, v97\n"
        "v_cvt_pk_bf16_f32 v97, v98, v99\n"
        "v_mfma_f32_16x16x32_bf16 v[128:131], v[200:203], v[248:251], v[128:131]\n"
        "ds_read_b128 v[228:231], v9 offset:4096\n"
        "v_mfma_f32_16x16x32_bf16 v[128:131], v[204:207], v[252:255], v[128:131]\n"
        "ds_read_b128 v[196:199], v9 offset:5120\n"
        "v_mfma_f32_16x16x32_bf16 v[148:151], v[200:203], v[232:235], v[148:151]\n"
        "ds_read_b128 v[120:123], v9 offset:10240\n"
        "v_mfma_f32_16x16x32_bf16 v[148:151], v[204:207], v[236:239], v[148:151]\n"
        "s_waitcnt lgkmcnt(10)\n"
        "v_cvt_pk_bf16_f32 v50, v144, v145\n"
        "v_cvt_pk_bf16_f32 v51, v146, v147\n"
        "v_mfma_f32_16x16x32_bf16 v[52:55], v[208:211], v[188:191], v[52:55]\n"
        "s_waitcnt lgkmcnt(9)\n"
        "v_cvt_pk_bf16_f32 v74, v128, v129\n"
        "v_cvt_pk_bf16_f32 v75, v130, v131\n"
        "v_mfma_f32_16x16x32_bf16 v[52:55], v[240:243], v[192:195], v[52:55]\n"
        "v_cvt_pk_bf16_f32 v98, v148, v149\n"
        "v_cvt_pk_bf16_f32 v99, v150, v151\n"
        "v_mfma_f32_16x16x32_bf16 v[76:79], v[208:211], v[248:251], v[76:79]\n"
        "ds_read_b128 v[200:203], v9 offset:11264\n"
        "v_mfma_f32_16x16x32_bf16 v[76:79], v[240:243], v[252:255], v[76:79]\n"
        "ds_read_b128 v[204:207], v9 offset:16384\n"
        "v_mfma_f32_16x16x32_bf16 v[100:103], v[208:211], v[232:235], v[100:103]\n"
        "ds_read_b128 v[124:127], v9 offset:17408\n"
        "v_mfma_f32_16x16x32_bf16 v[100:103], v[240:243], v[236:239], v[100:103]\n"
        "s_waitcnt lgkmcnt(11)\n"
        "v_cvt_pk_bf16_f32 v52, v52, v53\n"
        "v_cvt_pk_bf16_f32 v53, v54, v55\n"
        "v_mfma_f32_16x16x32_bf16 v[132:135], v[244:247], v[188:191], v[132:135]\n"
        "s_waitcnt lgkmcnt(10)\n"
        "v_cvt_pk_bf16_f32 v76, v76, v77\n"
        "v_cvt_pk_bf16_f32 v77, v78, v79\n"
        "v_mfma_f32_16x16x32_bf16 v[132:135], v[212:215], v[192:195], v[132:135]\n"
        "v_cvt_pk_bf16_f32 v100, v100, v101\n"
        "v_cvt_pk_bf16_f32 v101, v102, v103\n"
        "v_mfma_f32_16x16x32_bf16 v[160:163], v[244:247], v[248:251], v[160:163]\n"
        "ds_read_b128 v[208:211], v9 offset:22528\n"
        "v_mfma_f32_16x16x32_bf16 v[160:163], v[212:215], v[252:255], v[160:163]\n"
        "ds_read_b128 v[240:243], v9 offset:23552\n"
        "v_mfma_f32_16x16x32_bf16 v[136:139], v[244:247], v[232:235], v[136:139]\n"
        "ds_write_b8 v3, v4 offset:16\n"
        "v_mfma_f32_16x16x32_bf16 v[136:139], v[212:215], v[236:239], v[136:139]\n"
        "s_waitcnt lgkmcnt(12)\n"
        "v_cvt_pk_bf16_f32 v54, v132, v133\n"
        "v_cvt_pk_bf16_f32 v55, v134, v135\n"
        "v_mfma_f32_16x16x32_bf16 v[56:59], v[216:219], v[188:191], v[56:59]\n"
        "s_waitcnt lgkmcnt(11)\n"
        "v_cvt_pk_bf16_f32 v78, v160, v161\n"
        "v_cvt_pk_bf16_f32 v79, v162, v163\n"
        "v_mfma_f32_16x16x32_bf16 v[56:59], v[220:223], v[192:195], v[56:59]\n"
        "v_cvt_pk_bf16_f32 v102, v136, v137\n"
        "v_cvt_pk_bf16_f32 v103, v138, v139\n"
        "v_mfma_f32_16x16x32_bf16 v[80:83], v[216:219], v[248:251], v[80:83]\n"
        "v_mfma_f32_16x16x32_bf16 v[80:83], v[220:223], v[252:255], v[80:83]\n"
        "v_mfma_f32_16x16x32_bf16 v[104:107], v[216:219], v[232:235], v[104:107]\n"
        "v_mfma_f32_16x16x32_bf16 v[104:107], v[220:223], v[236:239], v[104:107]\n"
        "s_waitcnt lgkmcnt(10)\n"
        "v_mfma_f32_16x16x32_bf16 v[164:167], v[224:227], v[188:191], v[164:167]\n"
        "s_waitcnt lgkmcnt(9)\n"
        "v_cvt_pk_bf16_f32 v56, v56, v57\n"
        "v_cvt_pk_bf16_f32 v57, v58, v59\n"
        "v_mfma_f32_16x16x32_bf16 v[164:167], v[116:119], v[192:195], v[164:167]\n"
        "v_cvt_pk_bf16_f32 v80, v80, v81\n"
        "v_cvt_pk_bf16_f32 v81, v82, v83\n"
        "v_mfma_f32_16x16x32_bf16 v[140:143], v[224:227], v[248:251], v[140:143]\n"
        "v_cvt_pk_bf16_f32 v104, v104, v105\n"
        "v_cvt_pk_bf16_f32 v105, v106, v107\n"
        "v_mfma_f32_16x16x32_bf16 v[140:143], v[116:119], v[252:255], v[140:143]\n"
        "v_mfma_f32_16x16x32_bf16 v[168:171], v[224:227], v[232:235], v[168:171]\n"
        "v_mfma_f32_16x16x32_bf16 v[168:171], v[116:119], v[236:239], v[168:171]\n"
        "s_waitcnt lgkmcnt(8)\n"
        "v_cvt_pk_bf16_f32 v58, v164, v165\n"
        "v_cvt_pk_bf16_f32 v59, v166, v167\n"
        "v_mfma_f32_16x16x32_bf16 v[60:63], v[228:231], v[188:191], v[60:63]\n"
        "s_waitcnt lgkmcnt(7)\n"
        "v_mfma_f32_16x16x32_bf16 v[60:63], v[196:199], v[192:195], v[60:63]\n"
        "v_cvt_pk_bf16_f32 v82, v140, v141\n"
        "v_cvt_pk_bf16_f32 v83, v142, v143\n"
        "v_mfma_f32_16x16x32_bf16 v[84:87], v[228:231], v[248:251], v[84:87]\n"
        "ds_read_b32 v5, v2 offset:20\n"
        "v_cvt_pk_bf16_f32 v106, v168, v169\n"
        "v_mfma_f32_16x16x32_bf16 v[84:87], v[196:199], v[252:255], v[84:87]\n"
        "v_cvt_pk_bf16_f32 v107, v170, v171\n"
        "v_mfma_f32_16x16x32_bf16 v[108:111], v[228:231], v[232:235], v[108:111]\n"
        "v_cvt_pk_bf16_f32 v60, v60, v61\n"
        "v_cvt_pk_bf16_f32 v61, v62, v63\n"
        "v_mfma_f32_16x16x32_bf16 v[108:111], v[196:199], v[236:239], v[108:111]\n"
        "s_waitcnt lgkmcnt(7)\n"
        "v_mfma_f32_16x16x32_bf16 v[152:155], v[120:123], v[188:191], v[152:155]\n"
        "s_waitcnt lgkmcnt(6)\n"
        "v_mfma_f32_16x16x32_bf16 v[152:155], v[200:203], v[192:195], v[152:155]\n"
        "v_cvt_pk_bf16_f32 v84, v84, v85\n"
        "v_cvt_pk_bf16_f32 v85, v86, v87\n"
        "v_mfma_f32_16x16x32_bf16 v[156:159], v[120:123], v[248:251], v[156:159]\n"
        "s_waitcnt lgkmcnt(0)\n"
        "v_readfirstlane_b32 s86, v5\n"
        "v_mfma_f32_16x16x32_bf16 v[156:159], v[200:203], v[252:255], v[156:159]\n"
        "v_cvt_pk_bf16_f32 v108, v108, v109\n"
        "v_cvt_pk_bf16_f32 v109, v110, v111\n"
        "v_mfma_f32_16x16x32_bf16 v[172:175], v[120:123], v[232:235], v[172:175]\n"
        "v_cvt_pk_bf16_f32 v62, v152, v153\n"
        "v_cvt_pk_bf16_f32 v63, v154, v155\n"
        "v_mfma_f32_16x16x32_bf16 v[172:175], v[200:203], v[236:239], v[172:175]\n"
        "s_cmp_eq_u32 s86, 0x1010101\n"
        "v_mfma_f32_16x16x32_bf16 v[64:67], v[204:207], v[188:191], v[64:67]\n"
        "s_cbranch_scc0 Lsl10_%=\n"
        "Lrt11_%=:\n"
        "v_mfma_f32_16x16x32_bf16 v[64:67], v[124:127], v[192:195], v[64:67]\n"
        "v_cvt_pk_bf16_f32 v86, v156, v157\n"
        "v_cvt_pk_bf16_f32 v87, v158, v159\n"
        "v_mfma_f32_16x16x32_bf16 v[176:179], v[208:211], v[188:191], v[176:179]\n"
        "v_mfma_f32_16x16x32_bf16 v[176:179], v[240:243], v[192:195], v[176:179]\n"
        "ds_read_b128 v[216:219], v22 offset:0\n"
        "v_cvt_pk_bf16_f32 v110, v172, v173\n"
        "v_mfma_f32_16x16x32_bf16 v[88:91], v[204:207], v[248:251], v[88:91]\n"
        "ds_read_b128 v[220:223], v23 offset:0\n"
        "v_cvt_pk_bf16_f32 v111, v174, v175\n"
        "v_mfma_f32_16x16x32_bf16 v[88:91], v[124:127], v[252:255], v[88:91]\n"
        "ds_read_b128 v[224:227], v24 offset:0\n"
        "v_cvt_pk_bf16_f32 v64, v64, v65\n"
        "v_mfma_f32_16x16x32_bf16 v[180:183], v[208:211], v[248:251], v[180:183]\n"
        "ds_read_b128 v[228:231], v25 offset:0\n"
        "v_cvt_pk_bf16_f32 v65, v66, v67\n"
        "v_mfma_f32_16x16x32_bf16 v[180:183], v[240:243], v[252:255], v[180:183]\n"
        "v_cvt_pk_bf16_f32 v66, v176, v177\n"
        "v_cvt_pk_bf16_f32 v67, v178, v179\n"
        "v_mfma_f32_16x16x32_bf16 v[112:115], v[204:207], v[232:235], v[112:115]\n"
        "ds_read_b128 v[200:203], v26 offset:0\n"
        "v_cvt_pk_bf16_f32 v88, v88, v89\n"
        "v_mfma_f32_16x16x32_bf16 v[112:115], v[124:127], v[236:239], v[112:115]\n"
        "ds_read_b128 v[204:207], v27 offset:0\n"
        "v_cvt_pk_bf16_f32 v89, v90, v91\n"
        "v_mfma_f32_16x16x32_bf16 v[184:187], v[208:211], v[232:235], v[184:187]\n"
        "v_cvt_pk_bf16_f32 v90, v180, v181\n"
        "v_cvt_pk_bf16_f32 v91, v182, v183\n"
        "v_mfma_f32_16x16x32_bf16 v[184:187], v[240:243], v[236:239], v[184:187]\n"
        "s_waitcnt lgkmcnt(5)\n"
        "ds_read_b128 v[208:211], v28 offset:0\n"
        "v_mfma_f32_16x16x32_bf16 v[116:119], v[216:219], v[44:47], 0\n"
        "s_waitcnt lgkmcnt(5)\n"
        "ds_read_b128 v[212:215], v29 offset:0\n"
        "v_cvt_pk_bf16_f32 v112, v112, v113\n"
        "v_mfma_f32_16x16x32_bf16 v[116:119], v[220:223], v[48:51], v[116:119]\n"
        "s_waitcnt lgkmcnt(5)\n"
        "v_cvt_pk_bf16_f32 v113, v114, v115\n"
        "v_cvt_pk_bf16_f32 v114, v184, v185\n"
        "v_mfma_f32_16x16x32_bf16 v[120:123], v[224:227], v[44:47], 0\n"
        "s_waitcnt lgkmcnt(4)\n"
        "v_cvt_pk_bf16_f32 v115, v186, v187\n"
        "v_mfma_f32_16x16x32_bf16 v[120:123], v[228:231], v[48:51], v[120:123]\n"
        "s_waitcnt lgkmcnt(3)\n"
        "ds_read_b128 v[232:235], v38 offset:0\n"
        "v_cndmask_b32 v116, v116, v8, s[50:51]\n"
        "v_mfma_f32_16x16x32_bf16 v[124:127], v[200:203], v[44:47], 0\n"
        "s_waitcnt lgkmcnt(3)\n"
        "ds_read_b128 v[236:239], v39 offset:0\n"
        "v_cndmask_b32 v118, v118, v8, s[52:53]\n"
        "v_mfma_f32_16x16x32_bf16 v[124:127], v[204:207], v[48:51], v[124:127]\n"
        "v_cndmask_b32 v120, v120, v8, s[50:51]\n"
        "v_cndmask_b32 v122, v122, v8, s[52:53]\n"
        "v_mfma_f32_16x16x32_bf16 v[144:147], v[200:203], v[68:71], 0\n"
        "v_mfma_f32_16x16x32_bf16 v[144:147], v[204:207], v[72:75], v[144:147]\n"
        "s_waitcnt lgkmcnt(3)\n"
        "v_mfma_f32_16x16x32_bf16 v[128:131], v[208:211], v[44:47], 0\n"
        "s_waitcnt lgkmcnt(2)\n"
        "ds_read_b32 v5, v2 offset:24\n"
        "v_mfma_f32_16x16x32_bf16 v[128:131], v[212:215], v[48:51], v[128:131]\n"
        "ds_read_b128 v[240:243], v40 offset:0\n"
        "v_cndmask_b32 v127, v127, v8, s[54:55]\n"
        "v_mfma_f32_16x16x32_bf16 v[148:151], v[208:211], v[68:71], 0\n"
        "ds_read_b128 v[244:247], v41 offset:0\n"
        "v_cndmask_b32 v144, v144, v8, s[58:59]\n"
        "v_mfma_f32_16x16x32_bf16 v[148:151], v[212:215], v[72:75], v[148:151]\n"
        "s_waitcnt lgkmcnt(4)\n"
        "v_cndmask_b32 v146, v146, v8, s[60:61]\n"
        "v_mfma_f32_16x16x32_bf16 v[132:135], v[232:235], v[44:47], 0\n"
        "s_waitcnt lgkmcnt(3)\n"
        "v_cndmask_b32 v131, v131, v8, s[54:55]\n"
        "v_mfma_f32_16x16x32_bf16 v[132:135], v[236:239], v[48:51], v[132:135]\n"
        "v_mfma_f32_16x16x32_bf16 v[160:163], v[232:235], v[68:71], 0\n"
        "s_waitcnt lgkmcnt(2)\n"
        "v_readfirstlane_b32 s86, v5\n"
        "v_cndmask_b32 v148, v148, v8, s[58:59]\n"
        "v_mfma_f32_16x16x32_bf16 v[160:163], v[236:239], v[72:75], v[160:163]\n"
        "ds_read_b128 v[216:219], v42 offset:0\n"
        "v_cndmask_b32 v150, v150, v8, s[60:61]\n"
        "v_mfma_f32_16x16x32_bf16 v[188:191], v[232:235], v[92:95], 0\n"
        "ds_read_b128 v[220:223], v43 offset:0\n"
        "v_cndmask_b32 v133, v133, v8, s[56:57]\n"
        "v_mfma_f32_16x16x32_bf16 v[188:191], v[236:239], v[96:99], v[188:191]\n"
        "s_waitcnt lgkmcnt(3)\n"
        "s_cmp_eq_u32 s86, 0x1010101\n"
        "v_cndmask_b32 v135, v135, v8, s[56:57]\n"
        "v_mfma_f32_16x16x32_bf16 v[136:139], v[240:243], v[44:47], 0\n"
        "s_waitcnt lgkmcnt(2)\n"
        "v_mfma_f32_16x16x32_bf16 v[136:139], v[244:247], v[48:51], v[136:139]\n"
        "s_cbranch_scc0 Lsl13_%=\n"
        "Lrt14_%=:\n"
        "v_mfma_f32_16x16x32_bf16 v[164:167], v[240:243], v[68:71], 0\n"
        "ds_read_b128 v[224:227], v22 offset:25600\n"
        "v_cndmask_b32 v188, v188, v8, s[76:77]\n"
        "v_mfma_f32_16x16x32_bf16 v[164:167], v[244:247], v[72:75], v[164:167]\n"
        "ds_read_b128 v[228:231], v23 offset:25600\n"
        "v_cndmask_b32 v190, v190, v8, s[76:77]\n"
        "v_mfma_f32_16x16x32_bf16 v[192:195], v[240:243], v[92:95], 0\n"
        "ds_read_b128 v[200:203], v24 offset:25600\n"
        "v_cndmask_b32 v137, v137, v8, s[56:57]\n"
        "v_mfma_f32_16x16x32_bf16 v[192:195], v[244:247], v[96:99], v[192:195]\n"
        "s_waitcnt lgkmcnt(4)\n"
        "ds_read_b128 v[204:207], v25 offset:25600\n"
        "v_cndmask_b32 v139, v139, v8, s[56:57]\n"
        "v_mfma_f32_16x16x32_bf16 v[140:143], v[216:219], v[44:47], 0\n"
        "s_waitcnt lgkmcnt(4)\n"
        "ds_read_b128 v[208:211], v26 offset:25600\n"
        "v_mfma_f32_16x16x32_bf16 v[140:143], v[220:223], v[48:51], v[140:143]\n"
        "ds_read_b128 v[212:215], v27 offset:25600\n"
        "v_cndmask_b32 v192, v192, v8, s[76:77]\n"
        "v_mfma_f32_16x16x32_bf16 v[168:171], v[216:219], v[68:71], 0\n"
        "v_cndmask_b32 v194, v194, v8, s[76:77]\n"
        "v_mfma_f32_16x16x32_bf16 v[168:171], v[220:223], v[72:75], v[168:171]\n"
        "v_mfma_f32_16x16x32_bf16 v[196:199], v[216:219], v[92:95], 0\n"
        "ds_read_b128 v[232:235], v28 offset:25600\n"
        "v_mfma_f32_16x16x32_bf16 v[196:199], v[220:223], v[96:99], v[196:199]\n"
        "s_waitcnt lgkmcnt(6)\n"
        "ds_read_b128 v[236:239], v29 offset:25600\n"
        "v_cndmask_b32 v141, v141, v8, s[56:57]\n"
        "v_mfma_f32_16x16x32_bf16 v[116:119], v[224:227], v[52:55], v[116:119]\n"
        "s_waitcnt lgkmcnt(6)\n"
        "v_cndmask_b32 v143, v143, v8, s[56:57]\n"
        "v_mov_b32 v152, 0\n"
        "v_mfma_f32_16x16x32_bf16 v[116:119], v[228:231], v[56:59], v[116:119]\n"
        "s_waitcnt lgkmcnt(5)\n"
        "v_cndmask_b32 v196, v196, v8, s[76:77]\n"
        "v_cndmask_b32 v198, v198, v8, s[76:77]\n"
        "v_mfma_f32_16x16x32_bf16 v[120:123], v[200:203], v[52:55], v[120:123]\n"
        "s_waitcnt lgkmcnt(4)\n"
        "ds_read_b128 v[240:243], v38 offset:25600\n"
        "v_cndmask_b32 v153, 0, v8, s[62:63]\n"
        "v_mfma_f32_16x16x32_bf16 v[120:123], v[204:207], v[56:59], v[120:123]\n"
        "s_waitcnt lgkmcnt(4)\n"
        "ds_read_b128 v[244:247], v39 offset:25600\n"
        "v_mov_b32 v154, 0\n"
        "v_mfma_f32_16x16x32_bf16 v[124:127], v[208:211], v[52:55], v[124:127]\n"
        "s_waitcnt lgkmcnt(4)\n"
        "v_cndmask_b32 v155, 0, v8, s[64:65]\n"
        "v_mov_b32 v156, 0\n"
        "v_mfma_f32_16x16x32_bf16 v[124:127], v[212:215], v[56:59], v[124:127]\n"
        "v_cndmask_b32 v157, 0, v8, s[62:63]\n"
        "v_mov_b32 v158, 0\n"
        "v_mfma_f32_16x16x32_bf16 v[144:147], v[208:211], v[76:79], v[144:147]\n"
        "v_cndmask_b32 v159, 0, v8, s[64:65]\n"
        "v_mov_b32 v172, 0\n"
        "v_mfma_f32_16x16x32_bf16 v[144:147], v[212:215], v[80:83], v[144:147]\n"
        "s_waitcnt lgkmcnt(3)\n"
        "v_cndmask_b32 v173, 0, v8, s[66:67]\n"
        "v_cndmask_b32 v174, 0, v8, s[68:69]\n"
        "v_mfma_f32_16x16x32_bf16 v[128:131], v[232:235], v[52:55], v[128:131]\n"
        "s_waitcnt lgkmcnt(2)\n"
        "ds_read_b32 v5, v2 offset:28\n"
        "v_cndmask_b32 v175, 0, v8, s[70:71]\n"
        "v_mfma_f32_16x16x32_bf16 v[128:131], v[236:239], v[56:59], v[128:131]\n"
        "ds_read_b128 v[216:219], v40 offset:25600\n"
        "v_mov_b32 v176, 0\n"
        "v_mfma_f32_16x16x32_bf16 v[148:151], v[232:235], v[76:79], v[148:151]\n"
        "ds_read_b128 v[220:223], v41 offset:25600\n"
        "v_cndmask_b32 v177, 0, v8, s[66:67]\n"
        "v_mfma_f32_16x16x32_bf16 v[148:151], v[236:239], v[80:83], v[148:151]\n"
        "s_waitcnt lgkmcnt(4)\n"
        "v_cndmask_b32 v178, 0, v8, s[68:69]\n"
        "v_cndmask_b32 v179, 0, v8, s[70:71]\n"
        "v_mfma_f32_16x16x32_bf16 v[132:135], v[240:243], v[52:55], v[132:135]\n"
        "s_waitcnt lgkmcnt(3)\n"
        "v_mov_b32 v180, 0\n"
        "v_cndmask_b32 v181, 0, v8, s[72:73]\n"
        "v_mfma_f32_16x16x32_bf16 v[132:135], v[244:247], v[56:59], v[132:135]\n"
        "v_mov_b32 v182, 0\n"
        "v_cndmask_b32 v183, 0, v8, s[74:75]\n"
        "v_mfma_f32_16x16x32_bf16 v[160:163], v[240:243], v[76:79], v[160:163]\n"
        "s_waitcnt lgkmcnt(2)\n"
        "ds_read_b128 v[224:227], v42 offset:25600\n"
        "v_readfirstlane_b32 s86, v5\n"
        "v_mfma_f32_16x16x32_bf16 v[160:163], v[244:247], v[80:83], v[160:163]\n"
        "ds_read_b128 v[228:231], v43 offset:25600\n"
        "v_mov_b32 v184, 0\n"
        "v_mfma_f32_16x16x32_bf16 v[188:191], v[240:243], v[100:103], v[188:191]\n"
        "v_cndmask_b32 v185, 0, v8, s[72:73]\n"
        "v_mov_b32 v186, 0\n"
        "v_mfma_f32_16x16x32_bf16 v[188:191], v[244:247], v[104:107], v[188:191]\n"
        "s_waitcnt lgkmcnt(3)\n"
        "s_cmp_eq_u32 s86, 0x1010101\n"
        "v_cndmask_b32 v187, 0, v8, s[74:75]\n"
        "v_mfma_f32_16x16x32_bf16 v[136:139], v[216:219], v[52:55], v[136:139]\n"
        "s_waitcnt lgkmcnt(2)\n"
        "v_xor_b32 v44, 32, v10\n"
        "v_xor_b32 v45, 32, v11\n"
        "v_mfma_f32_16x16x32_bf16 v[136:139], v[220:223], v[56:59], v[136:139]\n"
        "s_cbranch_scc0 Lsl16_%=\n"
        "Lrt17_%=:\n"
        "v_mfma_f32_16x16x32_bf16 v[164:167], v[216:219], v[76:79], v[164:167]\n"
        "ds_read_b128 v[200:203], v22 offset:51200\n"
        "v_xor_b32 v46, 32, v12\n"
        "v_mfma_f32_16x16x32_bf16 v[164:167], v[220:223], v[80:83], v[164:167]\n"
        "ds_read_b128 v[204:207], v23 offset:51200\n"
        "v_xor_b32 v47, 32, v13\n"
        "v_mfma_f32_16x16x32_bf16 v[192:195], v[216:219], v[100:103], v[192:195]\n"
        "ds_read_b128 v[208:211], v24 offset:51200\n"
        "v_xor_b32 v48, 32, v14\n"
        "v_mfma_f32_16x16x32_bf16 v[192:195], v[220:223], v[104:107], v[192:195]\n"
        "s_waitcnt lgkmcnt(4)\n"
        "ds_read_b128 v[212:215], v25 offset:51200\n"
        "v_xor_b32 v49, 32, v15\n"
        "v_mfma_f32_16x16x32_bf16 v[140:143], v[224:227], v[52:55], v[140:143]\n"
        "s_waitcnt lgkmcnt(4)\n"
        "ds_read_b128 v[232:235], v26 offset:51200\n"
        "v_xor_b32 v50, 32, v16\n"
        "v_mfma_f32_16x16x32_bf16 v[140:143], v[228:231], v[56:59], v[140:143]\n"
        "ds_read_b128 v[236:239], v27 offset:51200\n"
        "v_xor_b32 v51, 32, v17\n"
        "v_mfma_f32_16x16x32_bf16 v[168:171], v[224:227], v[76:79], v[168:171]\n"
        "ds_read_b128 v[240:243], v28 offset:51200\n"
        "v_xor_b32 v21, 32, v18\n"
        "v_mfma_f32_16x16x32_bf16 v[168:171], v[228:231], v[80:83], v[168:171]\n"
        "ds_read_b128 v[244:247], v29 offset:51200\n"
        "v_xor_b32 v252, 32, v19\n"
        "v_mfma_f32_16x16x32_bf16 v[196:199], v[224:227], v[100:103], v[196:199]\n"
        "ds_read_b128 v[216:219], v38 offset:51200\n"
        "v_xor_b32 v253, 32, v20\n"
        "v_mfma_f32_16x16x32_bf16 v[196:199], v[228:231], v[104:107], v[196:199]\n"
        "s_waitcnt lgkmcnt(8)\n"
        "ds_read_b128 v[220:223], v39 offset:51200\n"
        "v_mfma_f32_16x16x32_bf16 v[116:119], v[200:203], v[60:63], v[116:119]\n"
        "s_waitcnt lgkmcnt(8)\n"
        "ds_read_b128 v[224:227], v40 offset:51200\n"
        "v_mfma_f32_16x16x32_bf16 v[116:119], v[204:207], v[64:67], v[116:119]\n"
        "s_waitcnt lgkmcnt(8)\n"
        "ds_read_b128 v[228:231], v41 offset:51200\n"
        "v_mfma_f32_16x16x32_bf16 v[120:123], v[208:211], v[60:63], v[120:123]\n"
        "s_waitcnt lgkmcnt(8)\n"
        "ds_read_b128 v[200:203], v42 offset:51200\n"
        "v_mfma_f32_16x16x32_bf16 v[120:123], v[212:215], v[64:67], v[120:123]\n"
        "s_waitcnt lgkmcnt(8)\n"
        "ds_read_b128 v[204:207], v43 offset:51200\n"
        "v_mfma_f32_16x16x32_bf16 v[124:127], v[232:235], v[60:63], v[124:127]\n"
        "s_waitcnt lgkmcnt(8)\n"
        "v_mfma_f32_16x16x32_bf16 v[124:127], v[236:239], v[64:67], v[124:127]\n"
        "s_waitcnt lgkmcnt(7)\n"
        "v_mfma_f32_16x16x32_bf16 v[128:131], v[240:243], v[60:63], v[128:131]\n"
        "s_waitcnt lgkmcnt(6)\n"
        "v_mfma_f32_16x16x32_bf16 v[128:131], v[244:247], v[64:67], v[128:131]\n"
        "s_waitcnt lgkmcnt(5)\n"
        "v_cndmask_b32 v116, v120, v116, s[40:41]\n"
        "v_cndmask_b32 v117, v121, v117, s[40:41]\n"
        "v_mfma_f32_16x16x32_bf16 v[132:135], v[216:219], v[60:63], v[132:135]\n"
        "s_waitcnt lgkmcnt(4)\n"
        "v_cndmask_b32 v118, v122, v118, s[42:43]\n"
        "v_cndmask_b32 v119, v123, v119, s[42:43]\n"
        "v_mfma_f32_16x16x32_bf16 v[132:135], v[220:223], v[64:67], v[132:135]\n"
        "s_waitcnt lgkmcnt(3)\n"
        "v_cndmask_b32 v124, v128, v124, s[40:41]\n"
        "v_cndmask_b32 v125, v129, v125, s[40:41]\n"
        "v_mfma_f32_16x16x32_bf16 v[136:139], v[224:227], v[60:63], v[136:139]\n"
        "s_waitcnt lgkmcnt(2)\n"
        "v_cndmask_b32 v126, v130, v126, s[42:43]\n"
        "v_cndmask_b32 v127, v131, v127, s[42:43]\n"
        "v_mfma_f32_16x16x32_bf16 v[136:139], v[228:231], v[64:67], v[136:139]\n"
        "s_waitcnt lgkmcnt(1)\n"
        "v_max3_f32 v120, v116, v117, v118\n"
        "v_max3_f32 v120, v120, v119, v124\n"
        "v_mfma_f32_16x16x32_bf16 v[140:143], v[200:203], v[60:63], v[140:143]\n"
        "s_waitcnt lgkmcnt(0)\n"
        "v_max3_f32 v120, v120, v125, v126\n"
        "v_mfma_f32_16x16x32_bf16 v[140:143], v[204:207], v[64:67], v[140:143]\n"
        "v_mov_b64 v[66:67], 0\n"
        "v_mfma_f32_16x16x32_bf16 v[144:147], v[232:235], v[84:87], v[144:147]\n"
        "v_cndmask_b32 v132, v136, v132, s[44:45]\n"
        "v_cndmask_b32 v133, v137, v133, s[44:45]\n"
        "v_mfma_f32_16x16x32_bf16 v[144:147], v[236:239], v[88:91], v[144:147]\n"
        "v_cndmask_b32 v134, v138, v134, s[46:47]\n"
        "v_cndmask_b32 v135, v139, v135, s[46:47]\n"
        "v_mfma_f32_16x16x32_bf16 v[148:151], v[240:243], v[84:87], v[148:151]\n"
        "v_cndmask_b32 v140, v142, v140, s[48:49]\n"
        "v_cndmask_b32 v141, v143, v141, s[48:49]\n"
        "v_mfma_f32_16x16x32_bf16 v[148:151], v[244:247], v[88:91], v[148:151]\n"
        "v_max3_f32 v120, v120, v127, v132\n"
        "v_max3_f32 v120, v120, v133, v134\n"
        "v_mfma_f32_16x16x32_bf16 v[160:163], v[216:219], v[84:87], v[160:163]\n"
        "v_max3_f32 v120, v120, v135, v140\n"
        "v_max_f32 v120, v120, v141\n"
        "v_mfma_f32_16x16x32_bf16 v[160:163], v[220:223], v[88:91], v[160:163]\n"
        "v_mov_b32 v121, v120\n"
        "v_mov_b64 v[136:137], s[90:91]\n"
        "v_mfma_f32_16x16x32_bf16 v[188:191], v[216:219], v[108:111], v[188:191]\n"
        "ds_read_b128 v[208:211], v30 offset:0\n"
        "v_mov_b64 v[138:139], s[90:91]\n"
        "v_mfma_f32_16x16x32_bf16 v[188:191], v[220:223], v[112:115], v[188:191]\n"
        "ds_read_b128 v[212:215], v31 offset:0\n"
        "v_mfma_f32_16x16x32_bf16 v[164:167], v[224:227], v[84:87], v[164:167]\n"
        "v_permlane16_swap_b32 v120, v121\n"
        "v_mfma_f32_16x16x32_bf16 v[164:167], v[228:231], v[88:91], v[164:167]\n"
        "v_max_f32 v120, v120, v121\n"
        "v_mov_b32 v121, v120\n"
        "v_mfma_f32_16x16x32_bf16 v[192:195], v[224:227], v[108:111], v[192:195]\n"
        "ds_read_b128 v[232:235], v32 offset:0\n"
        "v_mfma_f32_16x16x32_bf16 v[192:195], v[228:231], v[112:115], v[192:195]\n"
        "ds_read_b128 v[236:239], v33 offset:0\n"
        "v_mfma_f32_16x16x32_bf16 v[168:171], v[200:203], v[84:87], v[168:171]\n"
        "v_permlane32_swap_b32 v120, v121\n"
        "v_mfma_f32_16x16x32_bf16 v[168:171], v[204:207], v[88:91], v[168:171]\n"
        "v_max_f32 v120, v120, v121\n"
        "v_sub_f32 v116, v116, v120\n"
        "v_mfma_f32_16x16x32_bf16 v[196:199], v[200:203], v[108:111], v[196:199]\n"
        "ds_read_b128 v[240:243], v34 offset:0\n"
        "v_sub_f32 v117, v117, v120\n"
        "v_mfma_f32_16x16x32_bf16 v[196:199], v[204:207], v[112:115], v[196:199]\n"
        "s_waitcnt lgkmcnt(4)\n"
        "ds_read_b128 v[244:247], v35 offset:0\n"
        "v_sub_f32 v118, v118, v120\n"
        "v_mfma_f32_16x16x32_bf16 v[152:155], v[208:211], v[68:71], v[152:155]\n"
        "s_waitcnt lgkmcnt(4)\n"
        "ds_read_b128 v[216:219], v36 offset:0\n"
        "v_sub_f32 v119, v119, v120\n"
        "v_mfma_f32_16x16x32_bf16 v[152:155], v[212:215], v[72:75], v[152:155]\n"
        "ds_read_b128 v[220:223], v37 offset:0\n"
        "v_sub_f32 v124, v124, v120\n"
        "v_mfma_f32_16x16x32_bf16 v[172:175], v[208:211], v[92:95], v[172:175]\n"
        "ds_read_b128 v[224:227], v30 offset:25600\n"
        "v_sub_f32 v125, v125, v120\n"
        "v_mfma_f32_16x16x32_bf16 v[172:175], v[212:215], v[96:99], v[172:175]\n"
        "s_waitcnt lgkmcnt(6)\n"
        "ds_read_b128 v[228:231], v31 offset:25600\n"
        "v_sub_f32 v126, v126, v120\n"
        "v_mfma_f32_16x16x32_bf16 v[156:159], v[232:235], v[68:71], v[156:159]\n"
        "s_waitcnt lgkmcnt(6)\n"
        "v_exp_f32 v116, v116\n"
        "v_mfma_f32_16x16x32_bf16 v[156:159], v[236:239], v[72:75], v[156:159]\n"
        "v_exp_f32 v117, v117\n"
        "v_mfma_f32_16x16x32_bf16 v[176:179], v[232:235], v[92:95], v[176:179]\n"
        "ds_read_b128 v[200:203], v32 offset:25600\n"
        "v_sub_f32 v127, v127, v120\n"
        "v_mfma_f32_16x16x32_bf16 v[176:179], v[236:239], v[96:99], v[176:179]\n"
        "s_waitcnt lgkmcnt(6)\n"
        "ds_read_b128 v[204:207], v33 offset:25600\n"
        "v_sub_f32 v132, v132, v120\n"
        "v_mfma_f32_16x16x32_bf16 v[180:183], v[240:243], v[92:95], v[180:183]\n"
        "s_waitcnt lgkmcnt(6)\n"
        "v_exp_f32 v118, v118\n"
        "v_mfma_f32_16x16x32_bf16 v[180:183], v[244:247], v[96:99], v[180:183]\n"
        "s_waitcnt lgkmcnt(5)\n"
        "v_exp_f32 v119, v119\n"
        "v_mfma_f32_16x16x32_bf16 v[184:187], v[216:219], v[92:95], v[184:187]\n"
        "s_waitcnt lgkmcnt(4)\n"
        "ds_read_b128 v[208:211], v34 offset:25600\n"
        "v_sub_f32 v133, v133, v120\n"
        "v_mfma_f32_16x16x32_bf16 v[184:187], v[220:223], v[96:99], v[184:187]\n"
        "s_waitcnt lgkmcnt(4)\n"
        "ds_read_b128 v[212:215], v35 offset:25600\n"
        "v_sub_f32 v134, v134, v120\n"
        "v_mfma_f32_16x16x32_bf16 v[152:155], v[224:227], v[76:79], v[152:155]\n"
        "s_waitcnt lgkmcnt(4)\n"
        "ds_read_b128 v[232:235], v36 offset:25600\n"
        "v_sub_f32 v135, v135, v120\n"
        "v_mfma_f32_16x16x32_bf16 v[152:155], v[228:231], v[80:83], v[152:155]\n"
        "ds_read_b128 v[236:239], v37 offset:25600\n"
        "v_sub_f32 v140, v140, v120\n"
        "v_mfma_f32_16x16x32_bf16 v[172:175], v[224:227], v[100:103], v[172:175]\n"
        "ds_read_b128 v[240:243], v30 offset:51200\n"
        "v_sub_f32 v141, v141, v120\n"
        "v_mfma_f32_16x16x32_bf16 v[172:175], v[228:231], v[104:107], v[172:175]\n"
        "s_waitcnt lgkmcnt(6)\n"
        "ds_read_b128 v[244:247], v31 offset:51200\n"
        "v_cvt_pk_bf16_f32 v54, v116, v117\n"
        "v_mfma_f32_16x16x32_bf16 v[156:159], v[200:203], v[76:79], v[156:159]\n"
        "s_waitcnt lgkmcnt(6)\n"
        "ds_read_b128 v[216:219], v32 offset:51200\n"
        "v_cndmask_b32 v52, 0, v54, s[40:41]\n"
        "v_mfma_f32_16x16x32_bf16 v[156:159], v[204:207], v[80:83], v[156:159]\n"
        "ds_read_b128 v[220:223], v33 offset:51200\n"
        "v_xor_b32 v54, v54, v52\n"
        "v_mfma_f32_16x16x32_bf16 v[176:179], v[200:203], v[100:103], v[176:179]\n"
        "v_exp_f32 v124, v124\n"
        "v_mfma_f32_16x16x32_bf16 v[176:179], v[204:207], v[104:107], v[176:179]\n"
        "s_waitcnt lgkmcnt(7)\n"
        "v_exp_f32 v125, v125\n"
        "v_mfma_f32_16x16x32_bf16 v[180:183], v[208:211], v[100:103], v[180:183]\n"
        "s_waitcnt lgkmcnt(6)\n"
        "v_exp_f32 v126, v126\n"
        "v_mfma_f32_16x16x32_bf16 v[180:183], v[212:215], v[104:107], v[180:183]\n"
        "s_waitcnt lgkmcnt(5)\n"
        "v_exp_f32 v127, v127\n"
        "v_mfma_f32_16x16x32_bf16 v[184:187], v[232:235], v[100:103], v[184:187]\n"
        "s_waitcnt lgkmcnt(4)\n"
        "ds_read_b128 v[224:227], v34 offset:51200\n"
        "v_cvt_pk_bf16_f32 v55, v118, v119\n"
        "v_mfma_f32_16x16x32_bf16 v[184:187], v[236:239], v[104:107], v[184:187]\n"
        "s_waitcnt lgkmcnt(4)\n"
        "ds_read_b128 v[228:231], v35 offset:51200\n"
        "v_cndmask_b32 v53, 0, v55, s[42:43]\n"
        "v_mfma_f32_16x16x32_bf16 v[152:155], v[240:243], v[84:87], v[152:155]\n"
        "s_waitcnt lgkmcnt(4)\n"
        "ds_read_b128 v[200:203], v36 offset:51200\n"
        "v_xor_b32 v55, v55, v53\n"
        "v_mfma_f32_16x16x32_bf16 v[152:155], v[244:247], v[88:91], v[152:155]\n"
        "s_waitcnt lgkmcnt(4)\n"
        "ds_read_b128 v[204:207], v37 offset:51200\n"
        "v_cvt_pk_bf16_f32 v58, v124, v125\n"
        "v_mfma_f32_16x16x32_bf16 v[156:159], v[216:219], v[84:87], v[156:159]\n"
        "s_waitcnt lgkmcnt(4)\n"
        "v_exp_f32 v132, v132\n"
        "v_mfma_f32_16x16x32_bf16 v[156:159], v[220:223], v[88:91], v[156:159]\n"
        "v_exp_f32 v133, v133\n"
        "v_mfma_f32_16x16x32_bf16 v[172:175], v[240:243], v[108:111], v[172:175]\n"
        "v_exp_f32 v134, v134\n"
        "v_mfma_f32_16x16x32_bf16 v[172:175], v[244:247], v[112:115], v[172:175]\n"
        "v_exp_f32 v135, v135\n"
        "v_mfma_f32_16x16x32_bf16 v[176:179], v[216:219], v[108:111], v[176:179]\n"
        "v_exp_f32 v140, v140\n"
        "v_mfma_f32_16x16x32_bf16 v[176:179], v[220:223], v[112:115], v[176:179]\n"
        "s_waitcnt lgkmcnt(3)\n"
        "v_exp_f32 v141, v141\n"
        "v_mfma_f32_16x16x32_bf16 v[180:183], v[224:227], v[108:111], v[180:183]\n"
        "s_waitcnt lgkmcnt(2)\n"
        "v_cndmask_b32 v56, 0, v58, s[40:41]\n"
        "v_xor_b32 v58, v58, v56\n"
        "v_mfma_f32_16x16x32_bf16 v[180:183], v[228:231], v[112:115], v[180:183]\n"
        "s_waitcnt lgkmcnt(1)\n"
        "v_cvt_pk_bf16_f32 v59, v126, v127\n"
        "v_cndmask_b32 v57, 0, v59, s[42:43]\n"
        "v_mfma_f32_16x16x32_bf16 v[184:187], v[200:203], v[108:111], v[184:187]\n"
        "s_waitcnt lgkmcnt(0)\n"
        "v_xor_b32 v59, v59, v57\n"
        "v_cvt_pk_bf16_f32 v62, v132, v133\n"
        "v_mfma_f32_16x16x32_bf16 v[184:187], v[204:207], v[112:115], v[184:187]\n"
        "ds_read_b64_tr_b16 v[200:201], v10 offset:0\n"
        "ds_read_b64_tr_b16 v[202:203], v11 offset:0\n"
        "ds_read_b64_tr_b16 v[204:205], v12 offset:0\n"
        "ds_read_b64_tr_b16 v[206:207], v13 offset:0\n"
        "ds_read_b64_tr_b16 v[216:217], v18 offset:0\n"
        "ds_read_b64_tr_b16 v[218:219], v19 offset:0\n"
        "ds_read_b64_tr_b16 v[220:221], v20 offset:0\n"
        "ds_read_b64_tr_b16 v[208:209], v14 offset:0\n"
        "ds_read_b64_tr_b16 v[210:211], v15 offset:0\n"
        "ds_read_b64_tr_b16 v[212:213], v16 offset:0\n"
        "ds_read_b64_tr_b16 v[214:215], v17 offset:0\n"
        "v_cndmask_b32 v60, 0, v62, s[44:45]\n"
        "v_xor_b32 v62, v62, v60\n"
        "v_cvt_pk_bf16_f32 v63, v134, v135\n"
        "v_cndmask_b32 v61, 0, v63, s[46:47]\n"
        "v_xor_b32 v63, v63, v61\n"
        "v_cvt_pk_bf16_f32 v65, v140, v141\n"
        "v_cndmask_b32 v64, 0, v65, s[48:49]\n"
        "v_xor_b32 v65, v65, v64\n"
        "s_not_b64 exec, s[44:45]\n"
        "v_mov_b64 v[160:161], v[164:165]\n"
        "v_mov_b64 v[188:189], v[192:193]\n"
        "s_not_b64 exec, s[46:47]\n"
        "v_mov_b64 v[162:163], v[166:167]\n"
        "v_mov_b64 v[190:191], v[194:195]\n"
        "s_not_b64 exec, s[48:49]\n"
        "v_mov_b64 v[168:169], v[170:171]\n"
        "v_mov_b64 v[196:197], v[198:199]\n"
        "s_not_b64 exec, s[40:41]\n"
        "v_mov_b64 v[144:145], v[148:149]\n"
        "v_mov_b64 v[152:153], v[156:157]\n"
        "v_mov_b64 v[172:173], v[176:177]\n"
        "v_mov_b64 v[180:181], v[184:185]\n"
        "s_not_b64 exec, s[42:43]\n"
        "v_mov_b64 v[146:147], v[150:151]\n"
        "v_mov_b64 v[154:155], v[158:159]\n"
        "v_mov_b64 v[174:175], v[178:179]\n"
        "v_mov_b64 v[182:183], v[186:187]\n"
        "s_mov_b64 exec, -1\n"
        "v_max3_f32 v148, v144, v145, v146\n"
        "v_max3_f32 v176, v172, v173, v174\n"
        "v_max3_f32 v148, v148, v147, v152\n"
        "v_max3_f32 v176, v176, v175, v180\n"
        "v_max3_f32 v148, v148, v153, v154\n"
        "v_max3_f32 v176, v176, v181, v182\n"
        "v_max3_f32 v148, v148, v155, v160\n"
        "v_max3_f32 v176, v176, v183, v188\n"
        "v_max3_f32 v148, v148, v161, v162\n"
        "v_max3_f32 v176, v176, v189, v190\n"
        "v_max3_f32 v148, v148, v163, v168\n"
        "v_max3_f32 v176, v176, v191, v196\n"
        "v_max_f32 v148, v148, v169\n"
        "v_max_f32 v176, v176, v197\n"
        "v_mov_b32 v149, v148\n"
        "v_mov_b32 v177, v176\n"
        "v_mov_b64 v[82:83], 0\n"
        "v_permlane16_swap_b32 v148, v149\n"
        "v_permlane16_swap_b32 v176, v177\n"
        "v_max_f32 v148, v148, v149\n"
        "v_max_f32 v176, v176, v177\n"
        "v_mov_b32 v149, v148\n"
        "v_mov_b32 v177, v176\n"
        "v_mov_b64 v[106:107], 0\n"
        "v_permlane32_swap_b32 v148, v149\n"
        "v_permlane32_swap_b32 v176, v177\n"
        "v_max_f32 v148, v148, v149\n"
        "v_max_f32 v176, v176, v177\n"
        "v_pk_add_f32 v[144:145], v[144:145], v[148:149] op_sel_hi:[1,0] neg_lo:[0,1] neg_hi:[0,1]\n"
        "v_pk_add_f32 v[146:147], v[146:147], v[148:149] op_sel_hi:[1,0] neg_lo:[0,1] neg_hi:[0,1]\n"
        "v_pk_add_f32 v[152:153], v[152:153], v[148:149] op_sel_hi:[1,0] neg_lo:[0,1] neg_hi:[0,1]\n"
        "v_pk_add_f32 v[154:155], v[154:155], v[148:149] op_sel_hi:[1,0] neg_lo:[0,1] neg_hi:[0,1]\n"
        "v_pk_add_f32 v[160:161], v[160:161], v[148:149] op_sel_hi:[1,0] neg_lo:[0,1] neg_hi:[0,1]\n"
        "v_pk_add_f32 v[162:163], v[162:163], v[148:149] op_sel_hi:[1,0] neg_lo:[0,1] neg_hi:[0,1]\n"
        "v_pk_add_f32 v[168:169], v[168:169], v[148:149] op_sel_hi:[1,0] neg_lo:[0,1] neg_hi:[0,1]\n"
        "v_pk_add_f32 v[172:173], v[172:173], v[176:177] op_sel_hi:[1,0] neg_lo:[0,1] neg_hi:[0,1]\n"
        "v_pk_add_f32 v[174:175], v[174:175], v[176:177] op_sel_hi:[1,0] neg_lo:[0,1] neg_hi:[0,1]\n"
        "v_pk_add_f32 v[180:181], v[180:181], v[176:177] op_sel_hi:[1,0] neg_lo:[0,1] neg_hi:[0,1]\n"
        "v_pk_add_f32 v[182:183], v[182:183], v[176:177] op_sel_hi:[1,0] neg_lo:[0,1] neg_hi:[0,1]\n"
        "v_pk_add_f32 v[188:189], v[188:189], v[176:177] op_sel_hi:[1,0] neg_lo:[0,1] neg_hi:[0,1]\n"
        "v_pk_add_f32 v[190:191], v[190:191], v[176:177] op_sel_hi:[1,0] neg_lo:[0,1] neg_hi:[0,1]\n"
        "v_pk_add_f32 v[196:197], v[196:197], v[176:177] op_sel_hi:[1,0] neg_lo:[0,1] neg_hi:[0,1]\n"
        "v_mfma_f32_16x16x32_bf16 v[108:111], v[52:55], v[136:139], 0\n"
        "ds_read_b64_tr_b16 v[224:225], v44 offset:0\n"
        "ds_read_b64_tr_b16 v[226:227], v45 offset:0\n"
        "ds_read_b64_tr_b16 v[228:229], v46 offset:0\n"
        "v_exp_f32 v144, v144\n"
        "v_mfma_f32_16x16x32_bf16 v[108:111], v[56:59], v[136:139], v[108:111]\n"
        "s_waitcnt lgkmcnt(12)\n"
        "ds_read_b64_tr_b16 v[230:231], v47 offset:0\n"
        "ds_read_b64_tr_b16 v[240:241], v21 offset:0\n"
        "ds_read_b64_tr_b16 v[242:243], v252 offset:0\n"
        "v_exp_f32 v145, v145\n"
        "v_mfma_f32_16x16x32_bf16 v[108:111], v[60:63], v[136:139], v[108:111]\n"
        "s_waitcnt lgkmcnt(12)\n"
        "ds_read_b64_tr_b16 v[244:245], v253 offset:0\n"
        "ds_read_b64_tr_b16 v[232:233], v48 offset:0\n"
        "ds_read_b64_tr_b16 v[234:235], v49 offset:0\n"
        "v_exp_f32 v146, v146\n"
        "v_mfma_f32_16x16x32_bf16 v[108:111], v[64:67], v[136:139], v[108:111]\n"
        "s_waitcnt lgkmcnt(13)\n"
        "ds_read_b64_tr_b16 v[236:237], v50 offset:0\n"
        "ds_read_b64_tr_b16 v[238:239], v51 offset:0\n"
        "v_exp_f32 v147, v147\n"
        "v_exp_f32 v152, v152\n"
        "v_mfma_f32_16x16x32_bf16 v[124:127], v[200:203], v[52:55], 0\n"
        "v_exp_f32 v153, v153\n"
        "v_exp_f32 v154, v154\n"
        "v_mfma_f32_16x16x32_bf16 v[124:127], v[204:207], v[56:59], v[124:127]\n"
        "v_exp_f32 v155, v155\n"
        "v_exp_f32 v160, v160\n"
        "v_exp_f32 v161, v161\n"
        "v_mfma_f32_16x16x32_bf16 v[124:127], v[216:219], v[60:63], v[124:127]\n"
        "v_exp_f32 v162, v162\n"
        "v_exp_f32 v163, v163\n"
        "v_mfma_f32_16x16x32_bf16 v[124:127], v[220:223], v[64:67], v[124:127]\n"
        "s_waitcnt lgkmcnt(9)\n"
        "v_exp_f32 v168, v168\n"
        "v_exp_f32 v169, v169\n"
        "v_cvt_pk_bf16_f32 v70, v144, v145\n"
        "v_mfma_f32_16x16x32_bf16 v[128:131], v[224:227], v[52:55], 0\n"
        "s_waitcnt lgkmcnt(7)\n"
        "v_cvt_pk_bf16_f32 v71, v146, v147\n"
        "v_cvt_pk_bf16_f32 v74, v152, v153\n"
        "v_cvt_pk_bf16_f32 v75, v154, v155\n"
        "v_cvt_pk_bf16_f32 v78, v160, v161\n"
        "v_cvt_pk_bf16_f32 v79, v162, v163\n"
        "v_mfma_f32_16x16x32_bf16 v[128:131], v[228:231], v[56:59], v[128:131]\n"
        "s_waitcnt lgkmcnt(5)\n"
        "v_cvt_pk_bf16_f32 v81, v168, v169\n"
        "v_cndmask_b32 v68, 0, v70, s[40:41]\n"
        "v_cndmask_b32 v69, 0, v71, s[42:43]\n"
        "v_cndmask_b32 v72, 0, v74, s[40:41]\n"
        "v_cndmask_b32 v73, 0, v75, s[42:43]\n"
        "v_mfma_f32_16x16x32_bf16 v[128:131], v[240:243], v[60:63], v[128:131]\n"
        "s_waitcnt lgkmcnt(4)\n"
        "v_cndmask_b32 v76, 0, v78, s[44:45]\n"
        "v_cndmask_b32 v77, 0, v79, s[46:47]\n"
        "v_cndmask_b32 v80, 0, v81, s[48:49]\n"
        "v_xor_b32 v70, v70, v68\n"
        "v_xor_b32 v71, v71, v69\n"
        "v_mfma_f32_16x16x32_bf16 v[128:131], v[244:247], v[64:67], v[128:131]\n"
        "v_xor_b32 v74, v74, v72\n"
        "v_xor_b32 v75, v75, v73\n"
        "v_xor_b32 v78, v78, v76\n"
        "v_xor_b32 v79, v79, v77\n"
        "v_xor_b32 v81, v81, v80\n"
        "v_mfma_f32_16x16x32_bf16 v[132:135], v[204:207], v[68:71], 0\n"
        "v_exp_f32 v172, v172\n"
        "v_exp_f32 v173, v173\n"
        "v_exp_f32 v174, v174\n"
        "v_mfma_f32_16x16x32_bf16 v[132:135], v[208:211], v[72:75], v[132:135]\n"
        "v_exp_f32 v175, v175\n"
        "v_exp_f32 v180, v180\n"
        "v_mfma_f32_16x16x32_bf16 v[132:135], v[216:219], v[76:79], v[132:135]\n"
        "v_exp_f32 v181, v181\n"
        "v_exp_f32 v182, v182\n"
        "v_mfma_f32_16x16x32_bf16 v[132:135], v[220:223], v[80:83], v[132:135]\n"
        "v_cvt_pk_bf16_f32 v116, v124, v125\n"
        "v_cvt_pk_bf16_f32 v117, v126, v127\n"
        "v_exp_f32 v183, v183\n"
        "v_exp_f32 v188, v188\n"
        "v_mfma_f32_16x16x32_bf16 v[124:127], v[228:231], v[68:71], 0\n"
        "s_waitcnt lgkmcnt(2)\n"
        "v_exp_f32 v189, v189\n"
        "v_exp_f32 v190, v190\n"
        "v_mfma_f32_16x16x32_bf16 v[124:127], v[232:235], v[72:75], v[124:127]\n"
        "v_exp_f32 v191, v191\n"
        "v_exp_f32 v196, v196\n"
        "v_mfma_f32_16x16x32_bf16 v[124:127], v[240:243], v[76:79], v[124:127]\n"
        "v_exp_f32 v197, v197\n"
        "v_cvt_pk_bf16_f32 v94, v172, v173\n"
        "v_mfma_f32_16x16x32_bf16 v[124:127], v[244:247], v[80:83], v[124:127]\n"
        "v_cvt_pk_bf16_f32 v118, v128, v129\n"
        "v_cvt_pk_bf16_f32 v119, v130, v131\n"
        "v_cvt_pk_bf16_f32 v95, v174, v175\n"
        "v_cvt_pk_bf16_f32 v98, v180, v181\n"
        "v_cvt_pk_bf16_f32 v99, v182, v183\n"
        "v_cvt_pk_bf16_f32 v102, v188, v189\n"
        "v_mfma_f32_16x16x32_bf16 v[112:115], v[68:71], v[136:139], 0\n"
        "v_cvt_pk_bf16_f32 v103, v190, v191\n"
        "v_cvt_pk_bf16_f32 v105, v196, v197\n"
        "v_cndmask_b32 v92, 0, v94, s[40:41]\n"
        "v_cndmask_b32 v93, 0, v95, s[42:43]\n"
        "v_mfma_f32_16x16x32_bf16 v[112:115], v[72:75], v[136:139], v[112:115]\n"
        "v_cndmask_b32 v96, 0, v98, s[40:41]\n"
        "v_cndmask_b32 v97, 0, v99, s[42:43]\n"
        "v_cndmask_b32 v100, 0, v102, s[44:45]\n"
        "v_cndmask_b32 v101, 0, v103, s[46:47]\n"
        "v_mfma_f32_16x16x32_bf16 v[112:115], v[76:79], v[136:139], v[112:115]\n"
        "v_cndmask_b32 v104, 0, v105, s[48:49]\n"
        "v_xor_b32 v94, v94, v92\n"
        "v_xor_b32 v95, v95, v93\n"
        "v_xor_b32 v98, v98, v96\n"
        "v_mfma_f32_16x16x32_bf16 v[112:115], v[80:83], v[136:139], v[112:115]\n"
        "v_cvt_pk_bf16_f32 v140, v132, v133\n"
        "v_cvt_pk_bf16_f32 v141, v134, v135\n"
        "v_xor_b32 v99, v99, v97\n"
        "v_xor_b32 v102, v102, v100\n"
        "v_xor_b32 v103, v103, v101\n"
        "v_xor_b32 v105, v105, v104\n"
        "v_xor_b32 v22, 64, v10\n"
        "v_xor_b32 v23, 64, v11\n"
        "v_mfma_f32_16x16x32_bf16 v[128:131], v[208:211], v[92:95], 0\n"
        "v_xor_b32 v24, 64, v12\n"
        "v_xor_b32 v25, 64, v13\n"
        "v_mfma_f32_16x16x32_bf16 v[128:131], v[212:215], v[96:99], v[128:131]\n"
        "v_xor_b32 v38, 64, v18\n"
        "v_xor_b32 v39, 64, v19\n"
        "v_mfma_f32_16x16x32_bf16 v[128:131], v[216:219], v[100:103], v[128:131]\n"
        "v_cvt_pk_bf16_f32 v142, v124, v125\n"
        "v_xor_b32 v40, 64, v20\n"
        "v_mfma_f32_16x16x32_bf16 v[128:131], v[220:223], v[104:107], v[128:131]\n"
        "v_cvt_pk_bf16_f32 v143, v126, v127\n"
        "v_xor_b32 v26, 64, v14\n"
        "v_mfma_f32_16x16x32_bf16 v[132:135], v[232:235], v[92:95], 0\n"
        "s_waitcnt lgkmcnt(0)\n"
        "ds_read_b64_tr_b16 v[200:201], v22 offset:0\n"
        "v_xor_b32 v27, 64, v15\n"
        "v_mfma_f32_16x16x32_bf16 v[132:135], v[236:239], v[96:99], v[132:135]\n"
        "ds_read_b64_tr_b16 v[202:203], v23 offset:0\n"
        "v_xor_b32 v28, 64, v16\n"
        "v_mfma_f32_16x16x32_bf16 v[132:135], v[240:243], v[100:103], v[132:135]\n"
        "ds_read_b64_tr_b16 v[204:205], v24 offset:0\n"
        "v_xor_b32 v29, 64, v17\n"
        "v_mfma_f32_16x16x32_bf16 v[132:135], v[244:247], v[104:107], v[132:135]\n"
        "ds_read_b64_tr_b16 v[206:207], v25 offset:0\n"
        "v_cvt_pk_bf16_f32 v164, v128, v129\n"
        "v_mfma_f32_16x16x32_bf16 v[248:251], v[92:95], v[136:139], 0\n"
        "ds_read_b64_tr_b16 v[216:217], v38 offset:0\n"
        "ds_read_b64_tr_b16 v[218:219], v39 offset:0\n"
        "v_mfma_f32_16x16x32_bf16 v[248:251], v[96:99], v[136:139], v[248:251]\n"
        "ds_read_b64_tr_b16 v[220:221], v40 offset:0\n"
        "v_cvt_pk_bf16_f32 v165, v130, v131\n"
        "v_mfma_f32_16x16x32_bf16 v[248:251], v[100:103], v[136:139], v[248:251]\n"
        "v_xor_b32 v41, 96, v10\n"
        "v_xor_b32 v42, 96, v11\n"
        "v_mfma_f32_16x16x32_bf16 v[248:251], v[104:107], v[136:139], v[248:251]\n"
        "s_waitcnt lgkmcnt(5)\n"
        "ds_read_b64_tr_b16 v[208:209], v26 offset:0\n"
        "ds_read_b64_tr_b16 v[210:211], v27 offset:0\n"
        "v_mfma_f32_16x16x32_bf16 v[188:191], v[200:203], v[52:55], 0\n"
        "s_waitcnt lgkmcnt(5)\n"
        "v_xor_b32 v43, 96, v12\n"
        "v_xor_b32 v30, 96, v13\n"
        "v_mfma_f32_16x16x32_bf16 v[188:191], v[204:207], v[56:59], v[188:191]\n"
        "s_waitcnt lgkmcnt(3)\n"
        "v_xor_b32 v35, 96, v18\n"
        "v_xor_b32 v36, 96, v19\n"
        "v_mfma_f32_16x16x32_bf16 v[188:191], v[216:219], v[60:63], v[188:191]\n"
        "s_waitcnt lgkmcnt(2)\n"
        "v_cvt_pk_bf16_f32 v166, v132, v133\n"
        "v_xor_b32 v37, 96, v20\n"
        "v_mfma_f32_16x16x32_bf16 v[188:191], v[220:223], v[64:67], v[188:191]\n"
        "ds_read_b64_tr_b16 v[212:213], v28 offset:0\n"
        "ds_read_b64_tr_b16 v[214:215], v29 offset:0\n"
        "v_cvt_pk_bf16_f32 v167, v134, v135\n"
        "v_mfma_f32_16x16x32_bf16 v[192:195], v[204:207], v[68:71], 0\n"
        "s_waitcnt lgkmcnt(2)\n"
        "v_xor_b32 v31, 96, v14\n"
        "v_xor_b32 v32, 96, v15\n"
        "v_mfma_f32_16x16x32_bf16 v[192:195], v[208:211], v[72:75], v[192:195]\n"
        "v_rcp_f32 v108, v108\n"
        "v_mfma_f32_16x16x32_bf16 v[192:195], v[216:219], v[76:79], v[192:195]\n"
        "ds_read_b64_tr_b16 v[224:225], v41 offset:0\n"
        "ds_read_b64_tr_b16 v[226:227], v42 offset:0\n"
        "v_mfma_f32_16x16x32_bf16 v[192:195], v[220:223], v[80:83], v[192:195]\n"
        "ds_read_b64_tr_b16 v[228:229], v43 offset:0\n"
        "ds_read_b64_tr_b16 v[230:231], v30 offset:0\n"
        "v_mfma_f32_16x16x32_bf16 v[196:199], v[208:211], v[92:95], 0\n"
        "s_waitcnt lgkmcnt(4)\n"
        "ds_read_b64_tr_b16 v[240:241], v35 offset:0\n"
        "ds_read_b64_tr_b16 v[242:243], v36 offset:0\n"
        "v_xor_b32 v33, 96, v16\n"
        "v_xor_b32 v34, 96, v17\n"
        "v_mfma_f32_16x16x32_bf16 v[196:199], v[212:215], v[96:99], v[196:199]\n"
        "ds_read_b64_tr_b16 v[244:245], v37 offset:0\n"
        "v_cvt_pk_bf16_f32 v120, v188, v189\n"
        "v_mfma_f32_16x16x32_bf16 v[196:199], v[216:219], v[100:103], v[196:199]\n"
        "v_rcp_f32 v109, v109\n"
        "v_mfma_f32_16x16x32_bf16 v[196:199], v[220:223], v[104:107], v[196:199]\n"
        "s_waitcnt lgkmcnt(5)\n"
        "ds_read_b64_tr_b16 v[232:233], v31 offset:0\n"
        "ds_read_b64_tr_b16 v[234:235], v32 offset:0\n"
        "v_cvt_pk_bf16_f32 v121, v190, v191\n"
        "v_mfma_f32_16x16x32_bf16 v[188:191], v[224:227], v[52:55], 0\n"
        "s_waitcnt lgkmcnt(5)\n"
        "v_rcp_f32 v110, v110\n"
        "v_mfma_f32_16x16x32_bf16 v[188:191], v[228:231], v[56:59], v[188:191]\n"
        "s_waitcnt lgkmcnt(3)\n"
        "v_rcp_f32 v111, v111\n"
        "v_mfma_f32_16x16x32_bf16 v[188:191], v[240:243], v[60:63], v[188:191]\n"
        "s_waitcnt lgkmcnt(2)\n"
        "v_cvt_pk_bf16_f32 v144, v192, v193\n"
        "v_cvt_pk_bf16_f32 v145, v194, v195\n"
        "v_mfma_f32_16x16x32_bf16 v[188:191], v[244:247], v[64:67], v[188:191]\n"
        "ds_read_b64_tr_b16 v[236:237], v33 offset:0\n"
        "ds_read_b64_tr_b16 v[238:239], v34 offset:0\n"
        "v_mfma_f32_16x16x32_bf16 v[192:195], v[228:231], v[68:71], 0\n"
        "s_waitcnt lgkmcnt(2)\n"
        "ds_write_b8 v3, v4 offset:32\n"
        "v_mfma_f32_16x16x32_bf16 v[192:195], v[232:235], v[72:75], v[192:195]\n"
        "ds_read_b64_tr_b16 v[200:201], v10 offset:25600\n"
        "v_cvt_pk_bf16_f32 v168, v196, v197\n"
        "v_mfma_f32_16x16x32_bf16 v[192:195], v[240:243], v[76:79], v[192:195]\n"
        "ds_read_b64_tr_b16 v[202:203], v11 offset:25600\n"
        "v_cvt_pk_bf16_f32 v169, v198, v199\n"
        "v_mfma_f32_16x16x32_bf16 v[192:195], v[244:247], v[80:83], v[192:195]\n"
        "ds_read_b64_tr_b16 v[204:205], v12 offset:25600\n"
        "ds_read_b64_tr_b16 v[206:207], v13 offset:25600\n"
        "v_mfma_f32_16x16x32_bf16 v[196:199], v[232:235], v[92:95], 0\n"
        "s_waitcnt lgkmcnt(5)\n"
        "ds_read_b64_tr_b16 v[216:217], v18 offset:25600\n"
        "ds_read_b64_tr_b16 v[218:219], v19 offset:25600\n"
        "v_mfma_f32_16x16x32_bf16 v[196:199], v[236:239], v[96:99], v[196:199]\n"
        "ds_read_b64_tr_b16 v[220:221], v20 offset:25600\n"
        "v_cvt_pk_bf16_f32 v122, v188, v189\n"
        "v_mfma_f32_16x16x32_bf16 v[196:199], v[240:243], v[100:103], v[196:199]\n"
        "ds_read_b64_tr_b16 v[208:209], v14 offset:25600\n"
        "v_cvt_pk_bf16_f32 v123, v190, v191\n"
        "v_mfma_f32_16x16x32_bf16 v[196:199], v[244:247], v[104:107], v[196:199]\n"
        "s_waitcnt lgkmcnt(6)\n"
        "ds_read_b64_tr_b16 v[210:211], v15 offset:25600\n"
        "v_cvt_pk_bf16_f32 v146, v192, v193\n"
        "v_mfma_f32_16x16x32_bf16 v[188:191], v[200:203], v[52:55], 0\n"
        "s_waitcnt lgkmcnt(5)\n"
        "v_rcp_f32 v112, v112\n"
        "v_mfma_f32_16x16x32_bf16 v[188:191], v[204:207], v[56:59], v[188:191]\n"
        "s_waitcnt lgkmcnt(3)\n"
        "v_rcp_f32 v113, v113\n"
        "v_mfma_f32_16x16x32_bf16 v[188:191], v[216:219], v[60:63], v[188:191]\n"
        "s_waitcnt lgkmcnt(2)\n"
        "v_rcp_f32 v114, v114\n"
        "v_mfma_f32_16x16x32_bf16 v[188:191], v[220:223], v[64:67], v[188:191]\n"
        "ds_read_b64_tr_b16 v[212:213], v16 offset:25600\n"
        "ds_read_b64_tr_b16 v[214:215], v17 offset:25600\n"
        "v_cvt_pk_bf16_f32 v147, v194, v195\n"
        "v_mfma_f32_16x16x32_bf16 v[192:195], v[204:207], v[68:71], 0\n"
        "s_waitcnt lgkmcnt(2)\n"
        "v_rcp_f32 v115, v115\n"
        "v_mfma_f32_16x16x32_bf16 v[192:195], v[208:211], v[72:75], v[192:195]\n"
        "ds_read_b64_tr_b16 v[224:225], v44 offset:25600\n"
        "v_cvt_pk_bf16_f32 v170, v196, v197\n"
        "v_mfma_f32_16x16x32_bf16 v[192:195], v[216:219], v[76:79], v[192:195]\n"
        "ds_read_b64_tr_b16 v[226:227], v45 offset:25600\n"
        "v_cvt_pk_bf16_f32 v171, v198, v199\n"
        "v_mfma_f32_16x16x32_bf16 v[192:195], v[220:223], v[80:83], v[192:195]\n"
        "ds_read_b64_tr_b16 v[228:229], v46 offset:25600\n"
        "ds_read_b64_tr_b16 v[230:231], v47 offset:25600\n"
        "v_mfma_f32_16x16x32_bf16 v[196:199], v[208:211], v[92:95], 0\n"
        "s_waitcnt lgkmcnt(4)\n"
        "ds_read_b64_tr_b16 v[240:241], v21 offset:25600\n"
        "ds_read_b64_tr_b16 v[242:243], v252 offset:25600\n"
        "v_mfma_f32_16x16x32_bf16 v[196:199], v[212:215], v[96:99], v[196:199]\n"
        "ds_read_b64_tr_b16 v[244:245], v253 offset:25600\n"
        "v_cvt_pk_bf16_f32 v124, v188, v189\n"
        "v_mfma_f32_16x16x32_bf16 v[196:199], v[216:219], v[100:103], v[196:199]\n"
        "ds_read_b64_tr_b16 v[232:233], v48 offset:25600\n"
        "v_cvt_pk_bf16_f32 v125, v190, v191\n"
        "v_mfma_f32_16x16x32_bf16 v[196:199], v[220:223], v[104:107], v[196:199]\n"
        "s_waitcnt lgkmcnt(6)\n"
        "ds_read_b64_tr_b16 v[234:235], v49 offset:25600\n"
        "v_cvt_pk_bf16_f32 v148, v192, v193\n"
        "v_mfma_f32_16x16x32_bf16 v[188:191], v[224:227], v[52:55], 0\n"
        "s_waitcnt lgkmcnt(5)\n"
        "v_rcp_f32 v248, v248\n"
        "v_mfma_f32_16x16x32_bf16 v[188:191], v[228:231], v[56:59], v[188:191]\n"
        "s_waitcnt lgkmcnt(3)\n"
        "v_rcp_f32 v249, v249\n"
        "v_mfma_f32_16x16x32_bf16 v[188:191], v[240:243], v[60:63], v[188:191]\n"
        "s_waitcnt lgkmcnt(2)\n"
        "v_rcp_f32 v250, v250\n"
        "v_mfma_f32_16x16x32_bf16 v[188:191], v[244:247], v[64:67], v[188:191]\n"
        "ds_read_b64_tr_b16 v[236:237], v50 offset:25600\n"
        "ds_read_b64_tr_b16 v[238:239], v51 offset:25600\n"
        "v_cvt_pk_bf16_f32 v149, v194, v195\n"
        "v_mfma_f32_16x16x32_bf16 v[192:195], v[228:231], v[68:71], 0\n"
        "s_waitcnt lgkmcnt(2)\n"
        "v_rcp_f32 v251, v251\n"
        "v_mfma_f32_16x16x32_bf16 v[192:195], v[232:235], v[72:75], v[192:195]\n"
        "ds_read_b64_tr_b16 v[200:201], v22 offset:25600\n"
        "v_cvt_pk_bf16_f32 v172, v196, v197\n"
        "v_mfma_f32_16x16x32_bf16 v[192:195], v[240:243], v[76:79], v[192:195]\n"
        "ds_read_b64_tr_b16 v[202:203], v23 offset:25600\n"
        "v_cvt_pk_bf16_f32 v173, v198, v199\n"
        "v_mfma_f32_16x16x32_bf16 v[192:195], v[244:247], v[80:83], v[192:195]\n"
        "ds_read_b64_tr_b16 v[204:205], v24 offset:25600\n"
        "ds_read_b64_tr_b16 v[206:207], v25 offset:25600\n"
        "v_mfma_f32_16x16x32_bf16 v[196:199], v[232:235], v[92:95], 0\n"
        "s_waitcnt lgkmcnt(4)\n"
        "ds_read_b64_tr_b16 v[216:217], v38 offset:25600\n"
        "ds_read_b64_tr_b16 v[218:219], v39 offset:25600\n"
        "v_mfma_f32_16x16x32_bf16 v[196:199], v[236:239], v[96:99], v[196:199]\n"
        "ds_read_b64_tr_b16 v[220:221], v40 offset:25600\n"
        "v_cvt_pk_bf16_f32 v126, v188, v189\n"
        "v_mfma_f32_16x16x32_bf16 v[196:199], v[240:243], v[100:103], v[196:199]\n"
        "ds_read_b64_tr_b16 v[208:209], v26 offset:25600\n"
        "v_cvt_pk_bf16_f32 v127, v190, v191\n"
        "v_mfma_f32_16x16x32_bf16 v[196:199], v[244:247], v[104:107], v[196:199]\n"
        "s_waitcnt lgkmcnt(6)\n"
        "ds_read_b64_tr_b16 v[210:211], v27 offset:25600\n"
        "v_cvt_pk_bf16_f32 v150, v192, v193\n"
        "v_mfma_f32_16x16x32_bf16 v[188:191], v[200:203], v[52:55], 0\n"
        "s_waitcnt lgkmcnt(5)\n"
        "ds_read_b64_tr_b16 v[212:213], v28 offset:25600\n"
        "v_cvt_pk_bf16_f32 v151, v194, v195\n"
        "v_mfma_f32_16x16x32_bf16 v[188:191], v[204:207], v[56:59], v[188:191]\n"
        "s_waitcnt lgkmcnt(4)\n"
        "ds_read_b64_tr_b16 v[214:215], v29 offset:25600\n"
        "v_cvt_pk_bf16_f32 v174, v196, v197\n"
        "v_mfma_f32_16x16x32_bf16 v[188:191], v[216:219], v[60:63], v[188:191]\n"
        "s_waitcnt lgkmcnt(4)\n"
        "ds_read_b64_tr_b16 v[224:225], v41 offset:25600\n"
        "v_cvt_pk_bf16_f32 v175, v198, v199\n"
        "v_mfma_f32_16x16x32_bf16 v[188:191], v[220:223], v[64:67], v[188:191]\n"
        "ds_read_b64_tr_b16 v[226:227], v42 offset:25600\n"
        "v_mfma_f32_16x16x32_bf16 v[192:195], v[204:207], v[68:71], 0\n"
        "s_waitcnt lgkmcnt(4)\n"
        "ds_read_b64_tr_b16 v[228:229], v43 offset:25600\n"
        "v_mfma_f32_16x16x32_bf16 v[192:195], v[208:211], v[72:75], v[192:195]\n"
        "ds_read_b64_tr_b16 v[230:231], v30 offset:25600\n"
        "v_mfma_f32_16x16x32_bf16 v[192:195], v[216:219], v[76:79], v[192:195]\n"
        "ds_read_b64_tr_b16 v[240:241], v35 offset:25600\n"
        "v_cvt_pk_bf16_f32 v128, v188, v189\n"
        "v_mfma_f32_16x16x32_bf16 v[192:195], v[220:223], v[80:83], v[192:195]\n"
        "ds_read_b64_tr_b16 v[242:243], v36 offset:25600\n"
        "v_cvt_pk_bf16_f32 v129, v190, v191\n"
        "v_mfma_f32_16x16x32_bf16 v[196:199], v[208:211], v[92:95], 0\n"
        "s_waitcnt lgkmcnt(6)\n"
        "ds_read_b64_tr_b16 v[244:245], v37 offset:25600\n"
        "v_mfma_f32_16x16x32_bf16 v[196:199], v[212:215], v[96:99], v[196:199]\n"
        "ds_read_b64_tr_b16 v[232:233], v31 offset:25600\n"
        "v_mfma_f32_16x16x32_bf16 v[196:199], v[216:219], v[100:103], v[196:199]\n"
        "ds_read_b64_tr_b16 v[234:235], v32 offset:25600\n"
        "v_cvt_pk_bf16_f32 v152, v192, v193\n"
        "v_mfma_f32_16x16x32_bf16 v[196:199], v[220:223], v[104:107], v[196:199]\n"
        "s_waitcnt lgkmcnt(7)\n"
        "ds_read_b64_tr_b16 v[236:237], v33 offset:25600\n"
        "v_cvt_pk_bf16_f32 v153, v194, v195\n"
        "v_mfma_f32_16x16x32_bf16 v[188:191], v[224:227], v[52:55], 0\n"
        "s_waitcnt lgkmcnt(6)\n"
        "ds_read_b64_tr_b16 v[238:239], v34 offset:25600\n"
        "v_mfma_f32_16x16x32_bf16 v[188:191], v[228:231], v[56:59], v[188:191]\n"
        "s_waitcnt lgkmcnt(5)\n"
        "ds_write_b8 v3, v4 offset:36\n"
        "v_mfma_f32_16x16x32_bf16 v[188:191], v[240:243], v[60:63], v[188:191]\n"
        "s_waitcnt lgkmcnt(5)\n"
        "ds_read_b64_tr_b16 v[200:201], v10 offset:51200\n"
        "v_cvt_pk_bf16_f32 v176, v196, v197\n"
        "v_mfma_f32_16x16x32_bf16 v[188:191], v[244:247], v[64:67], v[188:191]\n"
        "ds_read_b64_tr_b16 v[202:203], v11 offset:51200\n"
        "v_cvt_pk_bf16_f32 v177, v198, v199\n"
        "v_mfma_f32_16x16x32_bf16 v[192:195], v[228:231], v[68:71], 0\n"
        "s_waitcnt lgkmcnt(5)\n"
        "ds_read_b64_tr_b16 v[204:205], v12 offset:51200\n"
        "v_mfma_f32_16x16x32_bf16 v[192:195], v[232:235], v[72:75], v[192:195]\n"
        "ds_read_b64_tr_b16 v[206:207], v13 offset:51200\n"
        "v_mfma_f32_16x16x32_bf16 v[192:195], v[240:243], v[76:79], v[192:195]\n"
        "ds_read_b64_tr_b16 v[216:217], v18 offset:51200\n"
        "v_cvt_pk_bf16_f32 v130, v188, v189\n"
        "v_mfma_f32_16x16x32_bf16 v[192:195], v[244:247], v[80:83], v[192:195]\n"
        "ds_read_b64_tr_b16 v[218:219], v19 offset:51200\n"
        "v_cvt_pk_bf16_f32 v131, v190, v191\n"
        "v_mfma_f32_16x16x32_bf16 v[196:199], v[232:235], v[92:95], 0\n"
        "s_waitcnt lgkmcnt(7)\n"
        "ds_read_b64_tr_b16 v[220:221], v20 offset:51200\n"
        "v_mfma_f32_16x16x32_bf16 v[196:199], v[236:239], v[96:99], v[196:199]\n"
        "ds_read_b64_tr_b16 v[208:209], v14 offset:51200\n"
        "v_mfma_f32_16x16x32_bf16 v[196:199], v[240:243], v[100:103], v[196:199]\n"
        "ds_read_b64_tr_b16 v[210:211], v15 offset:51200\n"
        "v_cvt_pk_bf16_f32 v154, v192, v193\n"
        "v_mfma_f32_16x16x32_bf16 v[196:199], v[244:247], v[104:107], v[196:199]\n"
        "s_waitcnt lgkmcnt(7)\n"
        "ds_read_b64_tr_b16 v[212:213], v16 offset:51200\n"
        "v_cvt_pk_bf16_f32 v155, v194, v195\n"
        "v_mfma_f32_16x16x32_bf16 v[188:191], v[200:203], v[52:55], 0\n"
        "s_waitcnt lgkmcnt(6)\n"
        "ds_read_b64_tr_b16 v[214:215], v17 offset:51200\n"
        "v_mfma_f32_16x16x32_bf16 v[188:191], v[204:207], v[56:59], v[188:191]\n"
        "s_waitcnt lgkmcnt(5)\n"
        "ds_read_b64_tr_b16 v[224:225], v44 offset:51200\n"
        "v_cvt_pk_bf16_f32 v178, v196, v197\n"
        "v_mfma_f32_16x16x32_bf16 v[188:191], v[216:219], v[60:63], v[188:191]\n"
        "s_waitcnt lgkmcnt(5)\n"
        "ds_read_b64_tr_b16 v[226:227], v45 offset:51200\n"
        "v_cvt_pk_bf16_f32 v179, v198, v199\n"
        "v_mfma_f32_16x16x32_bf16 v[188:191], v[220:223], v[64:67], v[188:191]\n"
        "ds_read_b64_tr_b16 v[228:229], v46 offset:51200\n"
        "v_mfma_f32_16x16x32_bf16 v[192:195], v[204:207], v[68:71], 0\n"
        "s_waitcnt lgkmcnt(5)\n"
        "ds_read_b64_tr_b16 v[230:231], v47 offset:51200\n"
        "v_mfma_f32_16x16x32_bf16 v[192:195], v[208:211], v[72:75], v[192:195]\n"
        "ds_read_b64_tr_b16 v[240:241], v21 offset:51200\n"
        "v_mfma_f32_16x16x32_bf16 v[192:195], v[216:219], v[76:79], v[192:195]\n"
        "ds_read_b64_tr_b16 v[242:243], v252 offset:51200\n"
        "v_cvt_pk_bf16_f32 v132, v188, v189\n"
        "v_mfma_f32_16x16x32_bf16 v[192:195], v[220:223], v[80:83], v[192:195]\n"
        "ds_read_b64_tr_b16 v[244:245], v253 offset:51200\n"
        "v_cvt_pk_bf16_f32 v133, v190, v191\n"
        "v_mfma_f32_16x16x32_bf16 v[196:199], v[208:211], v[92:95], 0\n"
        "s_waitcnt lgkmcnt(7)\n"
        "ds_read_b32 v5, v2 offset:44\n"
        "v_mfma_f32_16x16x32_bf16 v[196:199], v[212:215], v[96:99], v[196:199]\n"
        "ds_read_b64_tr_b16 v[232:233], v48 offset:51200\n"
        "v_mfma_f32_16x16x32_bf16 v[196:199], v[216:219], v[100:103], v[196:199]\n"
        "ds_read_b64_tr_b16 v[234:235], v49 offset:51200\n"
        "v_cvt_pk_bf16_f32 v156, v192, v193\n"
        "v_mfma_f32_16x16x32_bf16 v[196:199], v[220:223], v[104:107], v[196:199]\n"
        "s_waitcnt lgkmcnt(8)\n"
        "ds_read_b64_tr_b16 v[236:237], v50 offset:51200\n"
        "v_cvt_pk_bf16_f32 v157, v194, v195\n"
        "v_mfma_f32_16x16x32_bf16 v[188:191], v[224:227], v[52:55], 0\n"
        "s_waitcnt lgkmcnt(3)\n"
        "ds_read_b64_tr_b16 v[238:239], v51 offset:51200\n"
        "v_readfirstlane_b32 s86, v5\n"
        "v_mfma_f32_16x16x32_bf16 v[188:191], v[228:231], v[56:59], v[188:191]\n"
        "ds_read_b64_tr_b16 v[200:201], v22 offset:51200\n"
        "v_cvt_pk_bf16_f32 v180, v196, v197\n"
        "v_mfma_f32_16x16x32_bf16 v[188:191], v[240:243], v[60:63], v[188:191]\n"
        "ds_read_b64_tr_b16 v[202:203], v23 offset:51200\n"
        "v_cvt_pk_bf16_f32 v181, v198, v199\n"
        "v_mfma_f32_16x16x32_bf16 v[188:191], v[244:247], v[64:67], v[188:191]\n"
        "s_cmp_eq_u32 s86, 0x1010100\n"
        "ds_read_b64_tr_b16 v[204:205], v24 offset:51200\n"
        "v_mfma_f32_16x16x32_bf16 v[192:195], v[228:231], v[68:71], 0\n"
        "s_waitcnt lgkmcnt(5)\n"
        "ds_read_b64_tr_b16 v[206:207], v25 offset:51200\n"
        "v_mfma_f32_16x16x32_bf16 v[192:195], v[232:235], v[72:75], v[192:195]\n"
        "s_cbranch_scc0 Lsl19_%=\n"
        "Lrt20_%=:\n"
        "v_mfma_f32_16x16x32_bf16 v[192:195], v[240:243], v[76:79], v[192:195]\n"
        "ds_read_b64_tr_b16 v[216:217], v38 offset:51200\n"
        "v_cvt_pk_bf16_f32 v134, v188, v189\n"
        "v_mfma_f32_16x16x32_bf16 v[192:195], v[244:247], v[80:83], v[192:195]\n"
        "ds_read_b64_tr_b16 v[218:219], v39 offset:51200\n"
        "v_cvt_pk_bf16_f32 v135, v190, v191\n"
        "v_mfma_f32_16x16x32_bf16 v[196:199], v[232:235], v[92:95], 0\n"
        "s_waitcnt lgkmcnt(6)\n"
        "ds_read_b64_tr_b16 v[220:221], v40 offset:51200\n"
        "v_mfma_f32_16x16x32_bf16 v[196:199], v[236:239], v[96:99], v[196:199]\n"
        "ds_read_b64_tr_b16 v[224:225], v41 offset:51200\n"
        "v_mfma_f32_16x16x32_bf16 v[196:199], v[240:243], v[100:103], v[196:199]\n"
        "ds_read_b64_tr_b16 v[226:227], v42 offset:51200\n"
        "v_cvt_pk_bf16_f32 v158, v192, v193\n"
        "v_mfma_f32_16x16x32_bf16 v[196:199], v[244:247], v[104:107], v[196:199]\n"
        "s_waitcnt lgkmcnt(7)\n"
        "ds_read_b64_tr_b16 v[228:229], v43 offset:51200\n"
        "ds_read_b64_tr_b16 v[230:231], v30 offset:51200\n"
        "v_mfma_f32_16x16x32_bf16 v[188:191], v[200:203], v[52:55], 0\n"
        "s_waitcnt lgkmcnt(7)\n"
        "ds_read_b64_tr_b16 v[84:85], v35 offset:51200\n"
        "ds_read_b64_tr_b16 v[86:87], v36 offset:51200\n"
        "v_mfma_f32_16x16x32_bf16 v[188:191], v[204:207], v[56:59], v[188:191]\n"
        "s_waitcnt lgkmcnt(7)\n"
        "ds_read_b64_tr_b16 v[88:89], v37 offset:51200\n"
        "v_cvt_pk_bf16_f32 v159, v194, v195\n"
        "v_mfma_f32_16x16x32_bf16 v[188:191], v[216:219], v[60:63], v[188:191]\n"
        "s_waitcnt lgkmcnt(7)\n"
        "ds_read_b128 v[44:47], v1 offset:0\n"
        "v_cvt_pk_bf16_f32 v182, v196, v197\n"
        "v_mfma_f32_16x16x32_bf16 v[188:191], v[220:223], v[64:67], v[188:191]\n"
        "s_waitcnt lgkmcnt(6)\n"
        "ds_read_b128 v[48:51], v1 offset:1024\n"
        "v_cvt_pk_bf16_f32 v183, v198, v199\n"
        "v_mfma_f32_16x16x32_bf16 v[192:195], v[224:227], v[52:55], 0\n"
        "s_waitcnt lgkmcnt(5)\n"
        "ds_read_b128 v[240:243], v1 offset:2048\n"
        "v_mfma_f32_16x16x32_bf16 v[192:195], v[228:231], v[56:59], v[192:195]\n"
        "s_waitcnt lgkmcnt(4)\n"
        "ds_read_b128 v[244:247], v1 offset:3072\n"
        "v_cvt_pk_bf16_f32 v136, v188, v189\n"
        "v_mfma_f32_16x16x32_bf16 v[192:195], v[84:87], v[60:63], v[192:195]\n"
        "s_waitcnt lgkmcnt(4)\n"
        "ds_read_b128 v[252:255], v1 offset:4096\n"
        "v_cvt_pk_bf16_f32 v137, v190, v191\n"
        "v_mfma_f32_16x16x32_bf16 v[192:195], v[88:91], v[64:67], v[192:195]\n"
        "s_waitcnt lgkmcnt(4)\n"
        "ds_read_b128 v[200:203], v1 offset:5120\n"
        "v_mfma_f32_16x16x32_bf16 v[12:15], v[116:119], v[44:47], 0\n"
        "s_waitcnt lgkmcnt(4)\n"
        "ds_read_b128 v[224:227], v1 offset:6144\n"
        "v_mfma_f32_16x16x32_bf16 v[12:15], v[120:123], v[48:51], v[12:15]\n"
        "s_waitcnt lgkmcnt(4)\n"
        "ds_read_b128 v[52:55], v1 offset:7168\n"
        "v_mfma_f32_16x16x32_bf16 v[12:15], v[124:127], v[240:243], v[12:15]\n"
        "s_waitcnt lgkmcnt(4)\n"
        "ds_read_b128 v[56:59], v1 offset:8192\n"
        "v_cvt_pk_bf16_f32 v138, v192, v193\n"
        "v_cvt_pk_bf16_f32 v139, v194, v195\n"
        "v_mfma_f32_16x16x32_bf16 v[12:15], v[128:131], v[244:247], v[12:15]\n"
        "s_waitcnt lgkmcnt(4)\n"
        "ds_read_b128 v[44:47], v1 offset:9216\n"
        "v_mfma_f32_16x16x32_bf16 v[12:15], v[132:135], v[252:255], v[12:15]\n"
        "s_waitcnt lgkmcnt(4)\n"
        "ds_read_b128 v[48:51], v1 offset:10240\n"
        "v_mfma_f32_16x16x32_bf16 v[12:15], v[136:139], v[200:203], v[12:15]\n"
        "s_waitcnt lgkmcnt(4)\n"
        "ds_read_b128 v[240:243], v1 offset:11264\n"
        "v_mfma_f32_16x16x32_bf16 v[16:19], v[116:119], v[224:227], 0\n"
        "s_waitcnt lgkmcnt(4)\n"
        "ds_read_b128 v[244:247], v1 offset:12288\n"
        "v_mfma_f32_16x16x32_bf16 v[16:19], v[120:123], v[52:55], v[16:19]\n"
        "s_waitcnt lgkmcnt(4)\n"
        "ds_read_b128 v[252:255], v1 offset:13312\n"
        "v_mfma_f32_16x16x32_bf16 v[16:19], v[124:127], v[56:59], v[16:19]\n"
        "s_waitcnt lgkmcnt(4)\n"
        "ds_read_b128 v[200:203], v1 offset:14336\n"
        "v_mul_f32 v12, v12, v108\n"
        "v_mfma_f32_16x16x32_bf16 v[16:19], v[128:131], v[44:47], v[16:19]\n"
        "s_waitcnt lgkmcnt(4)\n"
        "ds_read_b128 v[60:63], v1 offset:15360\n"
        "v_mul_f32 v13, v13, v109\n"
        "v_mfma_f32_16x16x32_bf16 v[16:19], v[132:135], v[48:51], v[16:19]\n"
        "s_waitcnt lgkmcnt(4)\n"
        "ds_read_b128 v[64:67], v1 offset:16384\n"
        "v_mul_f32 v14, v14, v110\n"
        "v_mfma_f32_16x16x32_bf16 v[16:19], v[136:139], v[240:243], v[16:19]\n"
        "s_waitcnt lgkmcnt(4)\n"
        "ds_read_b128 v[224:227], v1 offset:17408\n"
        "v_mul_f32 v15, v15, v111\n"
        "v_mfma_f32_16x16x32_bf16 v[20:23], v[116:119], v[244:247], 0\n"
        "s_waitcnt lgkmcnt(4)\n"
        "ds_read_b128 v[44:47], v1 offset:18432\n"
        "v_mfma_f32_16x16x32_bf16 v[20:23], v[120:123], v[252:255], v[20:23]\n"
        "s_waitcnt lgkmcnt(4)\n"
        "ds_read_b128 v[48:51], v1 offset:19456\n"
        "v_mfma_f32_16x16x32_bf16 v[20:23], v[124:127], v[200:203], v[20:23]\n"
        "s_waitcnt lgkmcnt(4)\n"
        "ds_read_b128 v[52:55], v1 offset:20480\n"
        "v_mul_f32 v16, v16, v108\n"
        "v_mfma_f32_16x16x32_bf16 v[20:23], v[128:131], v[60:63], v[20:23]\n"
        "s_waitcnt lgkmcnt(4)\n"
        "ds_read_b128 v[56:59], v1 offset:21504\n"
        "v_cvt_pk_bf16_f32 v36, v12, v16\n"
        "v_mfma_f32_16x16x32_bf16 v[20:23], v[132:135], v[64:67], v[20:23]\n"
        "s_waitcnt lgkmcnt(4)\n"
        "ds_read_b64_tr_b16 v[208:209], v26 offset:51200\n"
        "ds_read_b64_tr_b16 v[210:211], v27 offset:51200\n"
        "ds_read_b128 v[60:63], v1 offset:22528\n"
        "v_mfma_f32_16x16x32_bf16 v[20:23], v[136:139], v[224:227], v[20:23]\n"
        "s_waitcnt lgkmcnt(6)\n"
        "ds_read_b128 v[64:67], v1 offset:23552\n"
        "v_mul_f32 v17, v17, v109\n"
        "v_mfma_f32_16x16x32_bf16 v[24:27], v[116:119], v[44:47], 0\n"
        "s_waitcnt lgkmcnt(6)\n"
        "ds_read_b64_tr_b16 v[212:213], v28 offset:51200\n"
        "v_cvt_pk_bf16_f32 v38, v13, v17\n"
        "v_mfma_f32_16x16x32_bf16 v[24:27], v[120:123], v[48:51], v[24:27]\n"
        "s_waitcnt lgkmcnt(6)\n"
        "ds_read_b64_tr_b16 v[214:215], v29 offset:51200\n"
        "v_mul_f32 v18, v18, v110\n"
        "v_mfma_f32_16x16x32_bf16 v[24:27], v[124:127], v[52:55], v[24:27]\n"
        "s_waitcnt lgkmcnt(6)\n"
        "ds_read_b64_tr_b16 v[232:233], v31 offset:51200\n"
        "v_cvt_pk_bf16_f32 v40, v14, v18\n"
        "v_mfma_f32_16x16x32_bf16 v[24:27], v[128:131], v[56:59], v[24:27]\n"
        "s_waitcnt lgkmcnt(4)\n"
        "ds_read_b64_tr_b16 v[234:235], v32 offset:51200\n"
        "v_mul_f32 v19, v19, v111\n"
        "v_mfma_f32_16x16x32_bf16 v[24:27], v[132:135], v[60:63], v[24:27]\n"
        "s_waitcnt lgkmcnt(4)\n"
        "ds_read_b64_tr_b16 v[236:237], v33 offset:51200\n"
        "v_cvt_pk_bf16_f32 v42, v15, v19\n"
        "v_mfma_f32_16x16x32_bf16 v[24:27], v[136:139], v[64:67], v[24:27]\n"
        "ds_read_b64_tr_b16 v[238:239], v34 offset:51200\n"
        "v_mul_f32 v20, v20, v108\n"
        "v_mfma_f32_16x16x32_bf16 v[196:199], v[204:207], v[68:71], 0\n"
        "ds_write_b8 v3, v4 offset:40\n"
        "v_mfma_f32_16x16x32_bf16 v[196:199], v[208:211], v[72:75], v[196:199]\n"
        "v_mul_f32 v21, v21, v109\n"
        "v_mul_f32 v22, v22, v110\n"
        "v_mfma_f32_16x16x32_bf16 v[196:199], v[216:219], v[76:79], v[196:199]\n"
        "v_mul_f32 v23, v23, v111\n"
        "v_mul_f32 v24, v24, v108\n"
        "v_mfma_f32_16x16x32_bf16 v[196:199], v[220:223], v[80:83], v[196:199]\n"
        "v_mul_f32 v25, v25, v109\n"
        "v_cvt_pk_bf16_f32 v37, v20, v24\n"
        "v_mfma_f32_16x16x32_bf16 v[188:191], v[208:211], v[92:95], 0\n"
        "s_waitcnt lgkmcnt(5)\n"
        "buffer_store_dwordx2 v[36:37], v6, s[28:31], s80 offen offset:0 sc1\n"
        "v_cvt_pk_bf16_f32 v39, v21, v25\n"
        "v_mfma_f32_16x16x32_bf16 v[188:191], v[212:215], v[96:99], v[188:191]\n"
        "buffer_store_dwordx2 v[38:39], v6, s[28:31], s81 offen offset:0 sc1\n"
        "v_mul_f32 v26, v26, v110\n"
        "v_mfma_f32_16x16x32_bf16 v[188:191], v[216:219], v[100:103], v[188:191]\n"
        "v_cvt_pk_bf16_f32 v160, v196, v197\n"
        "v_cvt_pk_bf16_f32 v161, v198, v199\n"
        "v_mfma_f32_16x16x32_bf16 v[188:191], v[220:223], v[104:107], v[188:191]\n"
        "ds_read_b128 v[200:203], v1 offset:0\n"
        "v_cvt_pk_bf16_f32 v41, v22, v26\n"
        "v_mfma_f32_16x16x32_bf16 v[192:195], v[228:231], v[68:71], 0\n"
        "s_waitcnt lgkmcnt(4)\n"
        "buffer_store_dwordx2 v[40:41], v7, s[28:31], s80 offen offset:0 sc1\n"
        "ds_read_b128 v[204:207], v1 offset:1024\n"
        "v_mfma_f32_16x16x32_bf16 v[192:195], v[232:235], v[72:75], v[192:195]\n"
        "ds_read_b128 v[208:211], v1 offset:2048\n"
        "v_mul_f32 v27, v27, v111\n"
        "v_mfma_f32_16x16x32_bf16 v[192:195], v[84:87], v[76:79], v[192:195]\n"
        "v_cvt_pk_bf16_f32 v184, v188, v189\n"
        "v_cvt_pk_bf16_f32 v185, v190, v191\n"
        "v_mfma_f32_16x16x32_bf16 v[192:195], v[88:91], v[80:83], v[192:195]\n"
        "ds_read_b128 v[212:215], v1 offset:3072\n"
        "v_cvt_pk_bf16_f32 v43, v23, v27\n"
        "v_mfma_f32_16x16x32_bf16 v[196:199], v[232:235], v[92:95], 0\n"
        "s_waitcnt lgkmcnt(5)\n"
        "buffer_store_dwordx2 v[42:43], v7, s[28:31], s81 offen offset:0 sc1\n"
        "ds_read_b128 v[216:219], v1 offset:4096\n"
        "v_mfma_f32_16x16x32_bf16 v[196:199], v[236:239], v[96:99], v[196:199]\n"
        "ds_read_b128 v[220:223], v1 offset:5120\n"
        "v_mfma_f32_16x16x32_bf16 v[196:199], v[84:87], v[100:103], v[196:199]\n"
        "v_cvt_pk_bf16_f32 v162, v192, v193\n"
        "v_cvt_pk_bf16_f32 v163, v194, v195\n"
        "v_mfma_f32_16x16x32_bf16 v[196:199], v[88:91], v[104:107], v[196:199]\n"
        "ds_read_b128 v[68:71], v1 offset:6144\n"
        "v_mfma_f32_16x16x32_bf16 v[104:107], v[140:143], v[44:47], 0\n"
        "ds_read_b128 v[72:75], v1 offset:7168\n"
        "v_mfma_f32_16x16x32_bf16 v[104:107], v[144:147], v[48:51], v[104:107]\n"
        "ds_read_b128 v[76:79], v1 offset:8192\n"
        "v_mfma_f32_16x16x32_bf16 v[104:107], v[148:151], v[52:55], v[104:107]\n"
        "ds_read_b128 v[80:83], v1 offset:9216\n"
        "v_mfma_f32_16x16x32_bf16 v[104:107], v[152:155], v[56:59], v[104:107]\n"
        "v_cvt_pk_bf16_f32 v186, v196, v197\n"
        "v_cvt_pk_bf16_f32 v187, v198, v199\n"
        "v_mfma_f32_16x16x32_bf16 v[104:107], v[156:159], v[60:63], v[104:107]\n"
        "ds_read_b128 v[84:87], v1 offset:10240\n"
        "v_mfma_f32_16x16x32_bf16 v[104:107], v[160:163], v[64:67], v[104:107]\n"
        "s_waitcnt lgkmcnt(10)\n"
        "ds_read_b128 v[88:91], v1 offset:11264\n"
        "v_mfma_f32_16x16x32_bf16 v[92:95], v[140:143], v[200:203], 0\n"
        "s_waitcnt lgkmcnt(10)\n"
        "ds_read_b128 v[224:227], v1 offset:12288\n"
        "v_mfma_f32_16x16x32_bf16 v[92:95], v[144:147], v[204:207], v[92:95]\n"
        "s_waitcnt lgkmcnt(10)\n"
        "ds_read_b128 v[228:231], v1 offset:13312\n"
        "v_mfma_f32_16x16x32_bf16 v[92:95], v[148:151], v[208:211], v[92:95]\n"
        "s_waitcnt lgkmcnt(10)\n"
        "ds_read_b128 v[232:235], v1 offset:14336\n"
        "v_mul_f32 v104, v104, v112\n"
        "v_mfma_f32_16x16x32_bf16 v[92:95], v[152:155], v[212:215], v[92:95]\n"
        "s_waitcnt lgkmcnt(10)\n"
        "ds_read_b128 v[236:239], v1 offset:15360\n"
        "v_mul_f32 v105, v105, v113\n"
        "v_mfma_f32_16x16x32_bf16 v[92:95], v[156:159], v[216:219], v[92:95]\n"
        "s_waitcnt lgkmcnt(10)\n"
        "ds_read_b128 v[240:243], v1 offset:16384\n"
        "v_mul_f32 v106, v106, v114\n"
        "v_mfma_f32_16x16x32_bf16 v[92:95], v[160:163], v[220:223], v[92:95]\n"
        "s_waitcnt lgkmcnt(10)\n"
        "ds_read_b32 v5, v2 offset:48\n"
        "v_mul_f32 v107, v107, v115\n"
        "v_mfma_f32_16x16x32_bf16 v[96:99], v[140:143], v[68:71], 0\n"
        "s_waitcnt lgkmcnt(10)\n"
        "ds_read_b128 v[244:247], v1 offset:17408\n"
        "v_mfma_f32_16x16x32_bf16 v[96:99], v[144:147], v[72:75], v[96:99]\n"
        "s_waitcnt lgkmcnt(10)\n"
        "v_mfma_f32_16x16x32_bf16 v[96:99], v[148:151], v[76:79], v[96:99]\n"
        "s_waitcnt lgkmcnt(9)\n"
        "v_mul_f32 v92, v92, v112\n"
        "v_mul_f32 v93, v93, v113\n"
        "v_mfma_f32_16x16x32_bf16 v[96:99], v[152:155], v[80:83], v[96:99]\n"
        "s_waitcnt lgkmcnt(8)\n"
        "v_mul_f32 v94, v94, v114\n"
        "v_mul_f32 v95, v95, v115\n"
        "v_mfma_f32_16x16x32_bf16 v[96:99], v[156:159], v[84:87], v[96:99]\n"
        "s_waitcnt lgkmcnt(7)\n"
        "v_mfma_f32_16x16x32_bf16 v[96:99], v[160:163], v[88:91], v[96:99]\n"
        "s_waitcnt lgkmcnt(1)\n"
        "v_readfirstlane_b32 s86, v5\n"
        "v_mfma_f32_16x16x32_bf16 v[100:103], v[140:143], v[224:227], 0\n"
        "v_mfma_f32_16x16x32_bf16 v[100:103], v[144:147], v[228:231], v[100:103]\n"
        "v_mfma_f32_16x16x32_bf16 v[100:103], v[148:151], v[232:235], v[100:103]\n"
        "s_cmp_eq_u32 s86, 0x1010100\n"
        "v_mfma_f32_16x16x32_bf16 v[100:103], v[152:155], v[236:239], v[100:103]\n"
        "s_cbranch_scc0 Lsl22_%=\n"
        "Lrt23_%=:\n"
        "v_mfma_f32_16x16x32_bf16 v[100:103], v[156:159], v[240:243], v[100:103]\n"
        "s_waitcnt lgkmcnt(0)\n"
        "v_mul_f32 v96, v96, v112\n"
        "v_cvt_pk_bf16_f32 v28, v92, v96\n"
        "v_mfma_f32_16x16x32_bf16 v[100:103], v[160:163], v[244:247], v[100:103]\n"
        "v_mul_f32 v97, v97, v113\n"
        "v_cvt_pk_bf16_f32 v30, v93, v97\n"
        "v_mfma_f32_16x16x32_bf16 v[24:27], v[164:167], v[44:47], 0\n"
        "v_mul_f32 v98, v98, v114\n"
        "v_cvt_pk_bf16_f32 v32, v94, v98\n"
        "v_mfma_f32_16x16x32_bf16 v[24:27], v[168:171], v[48:51], v[24:27]\n"
        "v_mul_f32 v99, v99, v115\n"
        "v_cvt_pk_bf16_f32 v34, v95, v99\n"
        "v_mfma_f32_16x16x32_bf16 v[24:27], v[172:175], v[52:55], v[24:27]\n"
        "v_mul_f32 v100, v100, v112\n"
        "v_mul_f32 v101, v101, v113\n"
        "v_mfma_f32_16x16x32_bf16 v[24:27], v[176:179], v[56:59], v[24:27]\n"
        "v_cvt_pk_bf16_f32 v29, v100, v104\n"
        "v_cvt_pk_bf16_f32 v31, v101, v105\n"
        "v_mfma_f32_16x16x32_bf16 v[24:27], v[180:183], v[60:63], v[24:27]\n"
        "buffer_store_dwordx2 v[28:29], v6, s[28:31], s82 offen offset:0 sc1\n"
        "ds_read_b128 v[44:47], v1 offset:44032\n"
        "v_mfma_f32_16x16x32_bf16 v[24:27], v[184:187], v[64:67], v[24:27]\n"
        "buffer_store_dwordx2 v[30:31], v6, s[28:31], s83 offen offset:0 sc1\n"
        "v_mul_f32 v102, v102, v114\n"
        "v_mfma_f32_16x16x32_bf16 v[12:15], v[164:167], v[200:203], 0\n"
        "ds_read_b128 v[48:51], v1 offset:45056\n"
        "v_cvt_pk_bf16_f32 v33, v102, v106\n"
        "v_mfma_f32_16x16x32_bf16 v[12:15], v[168:171], v[204:207], v[12:15]\n"
        "buffer_store_dwordx2 v[32:33], v7, s[28:31], s82 offen offset:0 sc1\n"
        "v_mul_f32 v103, v103, v115\n"
        "v_mfma_f32_16x16x32_bf16 v[12:15], v[172:175], v[208:211], v[12:15]\n"
        "ds_read_b128 v[52:55], v1 offset:46080\n"
        "v_cvt_pk_bf16_f32 v35, v103, v107\n"
        "v_mfma_f32_16x16x32_bf16 v[12:15], v[176:179], v[212:215], v[12:15]\n"
        "buffer_store_dwordx2 v[34:35], v7, s[28:31], s83 offen offset:0 sc1\n"
        "ds_read_b128 v[56:59], v1 offset:47104\n"
        "v_mfma_f32_16x16x32_bf16 v[12:15], v[180:183], v[216:219], v[12:15]\n"
        "ds_read_b128 v[60:63], v1 offset:48128\n"
        "v_mul_f32 v24, v24, v248\n"
        "v_mfma_f32_16x16x32_bf16 v[12:15], v[184:187], v[220:223], v[12:15]\n"
        "ds_read_b128 v[64:67], v1 offset:49152\n"
        "v_mul_f32 v25, v25, v249\n"
        "v_mfma_f32_16x16x32_bf16 v[16:19], v[164:167], v[68:71], 0\n"
        "ds_read_b128 v[200:203], v1 offset:25600\n"
        "v_mul_f32 v26, v26, v250\n"
        "v_mfma_f32_16x16x32_bf16 v[16:19], v[168:171], v[72:75], v[16:19]\n"
        "ds_read_b128 v[204:207], v1 offset:26624\n"
        "v_mul_f32 v27, v27, v251\n"
        "v_mfma_f32_16x16x32_bf16 v[16:19], v[172:175], v[76:79], v[16:19]\n"
        "ds_read_b128 v[208:211], v1 offset:27648\n"
        "v_mul_f32 v12, v12, v248\n"
        "v_mfma_f32_16x16x32_bf16 v[16:19], v[176:179], v[80:83], v[16:19]\n"
        "ds_read_b128 v[212:215], v1 offset:28672\n"
        "v_mul_f32 v13, v13, v249\n"
        "v_mfma_f32_16x16x32_bf16 v[16:19], v[180:183], v[84:87], v[16:19]\n"
        "ds_read_b128 v[216:219], v1 offset:29696\n"
        "v_mul_f32 v14, v14, v250\n"
        "v_mfma_f32_16x16x32_bf16 v[16:19], v[184:187], v[88:91], v[16:19]\n"
        "ds_read_b128 v[220:223], v1 offset:30720\n"
        "v_mul_f32 v15, v15, v251\n"
        "v_mfma_f32_16x16x32_bf16 v[20:23], v[164:167], v[224:227], 0\n"
        "ds_read_b128 v[68:71], v1 offset:31744\n"
        "v_mfma_f32_16x16x32_bf16 v[20:23], v[168:171], v[228:231], v[20:23]\n"
        "ds_read_b128 v[72:75], v1 offset:32768\n"
        "v_mfma_f32_16x16x32_bf16 v[20:23], v[172:175], v[232:235], v[20:23]\n"
        "ds_read_b128 v[76:79], v1 offset:33792\n"
        "v_mul_f32 v16, v16, v248\n"
        "v_mfma_f32_16x16x32_bf16 v[20:23], v[176:179], v[236:239], v[20:23]\n"
        "s_waitcnt lgkmcnt(14)\n"
        "ds_read_b128 v[80:83], v1 offset:34816\n"
        "v_cvt_pk_bf16_f32 v36, v12, v16\n"
        "v_mfma_f32_16x16x32_bf16 v[20:23], v[180:183], v[240:243], v[20:23]\n"
        "s_waitcnt lgkmcnt(14)\n"
        "ds_read_b128 v[84:87], v1 offset:35840\n"
        "v_mul_f32 v17, v17, v249\n"
        "v_mfma_f32_16x16x32_bf16 v[20:23], v[184:187], v[244:247], v[20:23]\n"
        "s_waitcnt lgkmcnt(14)\n"
        "ds_read_b128 v[88:91], v1 offset:36864\n"
        "v_cvt_pk_bf16_f32 v38, v13, v17\n"
        "v_mfma_f32_16x16x32_bf16 v[104:107], v[116:119], v[44:47], 0\n"
        "s_waitcnt lgkmcnt(14)\n"
        "ds_read_b128 v[224:227], v1 offset:37888\n"
        "v_mul_f32 v18, v18, v250\n"
        "v_mfma_f32_16x16x32_bf16 v[104:107], v[120:123], v[48:51], v[104:107]\n"
        "s_waitcnt lgkmcnt(14)\n"
        "ds_read_b128 v[228:231], v1 offset:38912\n"
        "v_cvt_pk_bf16_f32 v40, v14, v18\n"
        "v_mfma_f32_16x16x32_bf16 v[104:107], v[124:127], v[52:55], v[104:107]\n"
        "v_mul_f32 v19, v19, v251\n"
        "v_cvt_pk_bf16_f32 v42, v15, v19\n"
        "v_mfma_f32_16x16x32_bf16 v[104:107], v[128:131], v[56:59], v[104:107]\n"
        "s_waitcnt lgkmcnt(14)\n"
        "ds_read_b128 v[232:235], v1 offset:39936\n"
        "v_mul_f32 v20, v20, v248\n"
        "v_mfma_f32_16x16x32_bf16 v[104:107], v[132:135], v[60:63], v[104:107]\n"
        "s_waitcnt lgkmcnt(14)\n"
        "ds_read_b128 v[236:239], v1 offset:40960\n"
        "v_cvt_pk_bf16_f32 v37, v20, v24\n"
        "v_mfma_f32_16x16x32_bf16 v[104:107], v[136:139], v[64:67], v[104:107]\n"
        "s_waitcnt lgkmcnt(14)\n"
        "buffer_store_dwordx2 v[36:37], v6, s[28:31], s84 offen offset:0 sc1\n"
        "ds_read_b128 v[240:243], v1 offset:41984\n"
        "v_mfma_f32_16x16x32_bf16 v[92:95], v[116:119], v[200:203], 0\n"
        "s_waitcnt lgkmcnt(14)\n"
        "ds_read_b128 v[244:247], v1 offset:43008\n"
        "v_mul_f32 v21, v21, v249\n"
        "v_mfma_f32_16x16x32_bf16 v[92:95], v[120:123], v[204:207], v[92:95]\n"
        "v_mul_f32 v22, v22, v250\n"
        "v_cvt_pk_bf16_f32 v39, v21, v25\n"
        "v_mfma_f32_16x16x32_bf16 v[92:95], v[124:127], v[208:211], v[92:95]\n"
        "s_waitcnt lgkmcnt(14)\n"
        "buffer_store_dwordx2 v[38:39], v6, s[28:31], s85 offen offset:0 sc1\n"
        "v_cvt_pk_bf16_f32 v41, v22, v26\n"
        "v_mfma_f32_16x16x32_bf16 v[92:95], v[128:131], v[212:215], v[92:95]\n"
        "s_waitcnt lgkmcnt(13)\n"
        "buffer_store_dwordx2 v[40:41], v7, s[28:31], s84 offen offset:0 sc1\n"
        "v_mul_f32 v23, v23, v251\n"
        "v_mfma_f32_16x16x32_bf16 v[92:95], v[132:135], v[216:219], v[92:95]\n"
        "s_waitcnt lgkmcnt(12)\n"
        "v_cvt_pk_bf16_f32 v43, v23, v27\n"
        "v_mul_f32 v104, v104, v108\n"
        "v_mfma_f32_16x16x32_bf16 v[92:95], v[136:139], v[220:223], v[92:95]\n"
        "s_waitcnt lgkmcnt(11)\n"
        "buffer_store_dwordx2 v[42:43], v7, s[28:31], s85 offen offset:0 sc1\n"
        "v_mul_f32 v105, v105, v109\n"
        "v_mfma_f32_16x16x32_bf16 v[96:99], v[116:119], v[68:71], 0\n"
        "s_waitcnt lgkmcnt(10)\n"
        "v_mul_f32 v106, v106, v110\n"
        "v_mul_f32 v107, v107, v111\n"
        "v_mfma_f32_16x16x32_bf16 v[96:99], v[120:123], v[72:75], v[96:99]\n"
        "s_waitcnt lgkmcnt(9)\n"
        "v_mfma_f32_16x16x32_bf16 v[96:99], v[124:127], v[76:79], v[96:99]\n"
        "s_waitcnt lgkmcnt(8)\n"
        "v_mul_f32 v92, v92, v108\n"
        "v_mul_f32 v93, v93, v109\n"
        "v_mfma_f32_16x16x32_bf16 v[96:99], v[128:131], v[80:83], v[96:99]\n"
        "s_waitcnt lgkmcnt(7)\n"
        "v_mul_f32 v94, v94, v110\n"
        "v_mul_f32 v95, v95, v111\n"
        "v_mfma_f32_16x16x32_bf16 v[96:99], v[132:135], v[84:87], v[96:99]\n"
        "s_waitcnt lgkmcnt(6)\n"
        "v_mfma_f32_16x16x32_bf16 v[96:99], v[136:139], v[88:91], v[96:99]\n"
        "s_waitcnt lgkmcnt(5)\n"
        "v_mfma_f32_16x16x32_bf16 v[100:103], v[116:119], v[224:227], 0\n"
        "s_waitcnt lgkmcnt(4)\n"
        "v_mfma_f32_16x16x32_bf16 v[100:103], v[120:123], v[228:231], v[100:103]\n"
        "s_waitcnt lgkmcnt(3)\n"
        "v_mfma_f32_16x16x32_bf16 v[100:103], v[124:127], v[232:235], v[100:103]\n"
        "s_waitcnt lgkmcnt(2)\n"
        "v_mfma_f32_16x16x32_bf16 v[100:103], v[128:131], v[236:239], v[100:103]\n"
        "s_waitcnt lgkmcnt(1)\n"
        "v_mul_f32 v96, v96, v108\n"
        "v_cvt_pk_bf16_f32 v28, v92, v96\n"
        "v_mfma_f32_16x16x32_bf16 v[100:103], v[132:135], v[240:243], v[100:103]\n"
        "s_waitcnt lgkmcnt(0)\n"
        "v_mul_f32 v97, v97, v109\n"
        "v_cvt_pk_bf16_f32 v30, v93, v97\n"
        "v_mfma_f32_16x16x32_bf16 v[100:103], v[136:139], v[244:247], v[100:103]\n"
        "v_mul_f32 v98, v98, v110\n"
        "v_cvt_pk_bf16_f32 v32, v94, v98\n"
        "v_mfma_f32_16x16x32_bf16 v[24:27], v[140:143], v[44:47], 0\n"
        "v_mul_f32 v99, v99, v111\n"
        "v_cvt_pk_bf16_f32 v34, v95, v99\n"
        "v_mfma_f32_16x16x32_bf16 v[24:27], v[144:147], v[48:51], v[24:27]\n"
        "v_mfma_f32_16x16x32_bf16 v[24:27], v[148:151], v[52:55], v[24:27]\n"
        "v_mfma_f32_16x16x32_bf16 v[24:27], v[152:155], v[56:59], v[24:27]\n"
        "v_mul_f32 v100, v100, v108\n"
        "v_cvt_pk_bf16_f32 v29, v100, v104\n"
        "v_mfma_f32_16x16x32_bf16 v[24:27], v[156:159], v[60:63], v[24:27]\n"
        "buffer_store_dwordx2 v[28:29], v6, s[28:31], s80 offen offset:128 sc1\n"
        "v_mul_f32 v101, v101, v109\n"
        "v_mfma_f32_16x16x32_bf16 v[24:27], v[160:163], v[64:67], v[24:27]\n"
        "v_mul_f32 v102, v102, v110\n"
        "v_cvt_pk_bf16_f32 v31, v101, v105\n"
        "v_mfma_f32_16x16x32_bf16 v[12:15], v[140:143], v[200:203], 0\n"
        "buffer_store_dwordx2 v[30:31], v6, s[28:31], s81 offen offset:128 sc1\n"
        "v_cvt_pk_bf16_f32 v33, v102, v106\n"
        "v_mfma_f32_16x16x32_bf16 v[12:15], v[144:147], v[204:207], v[12:15]\n"
        "buffer_store_dwordx2 v[32:33], v7, s[28:31], s80 offen offset:128 sc1\n"
        "v_mul_f32 v103, v103, v111\n"
        "v_mfma_f32_16x16x32_bf16 v[12:15], v[148:151], v[208:211], v[12:15]\n"
        "v_cvt_pk_bf16_f32 v35, v103, v107\n"
        "v_mul_f32 v24, v24, v112\n"
        "v_mfma_f32_16x16x32_bf16 v[12:15], v[152:155], v[212:215], v[12:15]\n"
        "buffer_store_dwordx2 v[34:35], v7, s[28:31], s81 offen offset:128 sc1\n"
        "v_mul_f32 v25, v25, v113\n"
        "v_mfma_f32_16x16x32_bf16 v[12:15], v[156:159], v[216:219], v[12:15]\n"
        "v_mul_f32 v26, v26, v114\n"
        "v_mul_f32 v27, v27, v115\n"
        "v_mfma_f32_16x16x32_bf16 v[12:15], v[160:163], v[220:223], v[12:15]\n"
        "ds_read_b32 v5, v2 offset:52\n"
        "v_mfma_f32_16x16x32_bf16 v[16:19], v[140:143], v[68:71], 0\n"
        "v_mfma_f32_16x16x32_bf16 v[16:19], v[144:147], v[72:75], v[16:19]\n"
        "v_mfma_f32_16x16x32_bf16 v[16:19], v[148:151], v[76:79], v[16:19]\n"
        "v_mfma_f32_16x16x32_bf16 v[16:19], v[152:155], v[80:83], v[16:19]\n"
        "v_mfma_f32_16x16x32_bf16 v[16:19], v[156:159], v[84:87], v[16:19]\n"
        "v_mfma_f32_16x16x32_bf16 v[16:19], v[160:163], v[88:91], v[16:19]\n"
        "s_waitcnt lgkmcnt(0)\n"
        "v_readfirstlane_b32 s86, v5\n"
        "v_mfma_f32_16x16x32_bf16 v[20:23], v[140:143], v[224:227], 0\n"
        "v_mul_f32 v12, v12, v112\n"
        "v_mul_f32 v13, v13, v113\n"
        "v_mfma_f32_16x16x32_bf16 v[20:23], v[144:147], v[228:231], v[20:23]\n"
        "v_mul_f32 v14, v14, v114\n"
        "v_mul_f32 v15, v15, v115\n"
        "v_mfma_f32_16x16x32_bf16 v[20:23], v[148:151], v[232:235], v[20:23]\n"
        "s_cmp_eq_u32 s86, 0x1010100\n"
        "v_mul_f32 v16, v16, v112\n"
        "v_mul_f32 v17, v17, v113\n"
        "v_mfma_f32_16x16x32_bf16 v[20:23], v[152:155], v[236:239], v[20:23]\n"
        "s_cbranch_scc0 Lsl25_%=\n"
        "Lrt26_%=:\n"
        "v_mfma_f32_16x16x32_bf16 v[20:23], v[156:159], v[240:243], v[20:23]\n"
        "v_cvt_pk_bf16_f32 v36, v12, v16\n"
        "v_cvt_pk_bf16_f32 v38, v13, v17\n"
        "v_mfma_f32_16x16x32_bf16 v[20:23], v[160:163], v[244:247], v[20:23]\n"
        "v_mul_f32 v18, v18, v114\n"
        "v_cvt_pk_bf16_f32 v40, v14, v18\n"
        "v_mfma_f32_16x16x32_bf16 v[104:107], v[164:167], v[44:47], 0\n"
        "v_mul_f32 v19, v19, v115\n"
        "v_cvt_pk_bf16_f32 v42, v15, v19\n"
        "v_mfma_f32_16x16x32_bf16 v[104:107], v[168:171], v[48:51], v[104:107]\n"
        "ds_read_b128 v[44:47], v9 offset:18432\n"
        "v_mfma_f32_16x16x32_bf16 v[104:107], v[172:175], v[52:55], v[104:107]\n"
        "v_mul_f32 v20, v20, v112\n"
        "v_mul_f32 v21, v21, v113\n"
        "v_mfma_f32_16x16x32_bf16 v[104:107], v[176:179], v[56:59], v[104:107]\n"
        "v_cvt_pk_bf16_f32 v37, v20, v24\n"
        "v_cvt_pk_bf16_f32 v39, v21, v25\n"
        "v_mfma_f32_16x16x32_bf16 v[104:107], v[180:183], v[60:63], v[104:107]\n"
        "buffer_store_dwordx2 v[36:37], v6, s[28:31], s82 offen offset:128 sc1\n"
        "v_mul_f32 v22, v22, v114\n"
        "v_mfma_f32_16x16x32_bf16 v[104:107], v[184:187], v[64:67], v[104:107]\n"
        "buffer_store_dwordx2 v[38:39], v6, s[28:31], s83 offen offset:128 sc1\n"
        "v_cvt_pk_bf16_f32 v41, v22, v26\n"
        "v_mfma_f32_16x16x32_bf16 v[92:95], v[164:167], v[200:203], 0\n"
        "buffer_store_dwordx2 v[40:41], v7, s[28:31], s82 offen offset:128 sc1\n"
        "ds_read_b128 v[48:51], v9 offset:19456\n"
        "v_mfma_f32_16x16x32_bf16 v[92:95], v[168:171], v[204:207], v[92:95]\n"
        "v_mul_f32 v23, v23, v115\n"
        "v_cvt_pk_bf16_f32 v43, v23, v27\n"
        "v_mfma_f32_16x16x32_bf16 v[92:95], v[172:175], v[208:211], v[92:95]\n"
        "buffer_store_dwordx2 v[42:43], v7, s[28:31], s83 offen offset:128 sc1\n"
        "ds_read_b128 v[52:55], v9 offset:20480\n"
        "v_mfma_f32_16x16x32_bf16 v[92:95], v[176:179], v[212:215], v[92:95]\n"
        "ds_read_b128 v[56:59], v9 offset:21504\n"
        "v_mul_f32 v104, v104, v248\n"
        "v_mfma_f32_16x16x32_bf16 v[92:95], v[180:183], v[216:219], v[92:95]\n"
        "ds_read_b128 v[60:63], v9 offset:22528\n"
        "v_mul_f32 v105, v105, v249\n"
        "v_mfma_f32_16x16x32_bf16 v[92:95], v[184:187], v[220:223], v[92:95]\n"
        "ds_read_b128 v[64:67], v9 offset:23552\n"
        "v_mul_f32 v106, v106, v250\n"
        "v_mfma_f32_16x16x32_bf16 v[96:99], v[164:167], v[68:71], 0\n"
        "ds_read_b128 v[200:203], v9 offset:0\n"
        "v_mul_f32 v107, v107, v251\n"
        "v_mfma_f32_16x16x32_bf16 v[96:99], v[168:171], v[72:75], v[96:99]\n"
        "ds_read_b128 v[204:207], v9 offset:1024\n"
        "v_mfma_f32_16x16x32_bf16 v[96:99], v[172:175], v[76:79], v[96:99]\n"
        "ds_read_b128 v[208:211], v9 offset:2048\n"
        "v_mul_f32 v92, v92, v248\n"
        "v_mfma_f32_16x16x32_bf16 v[96:99], v[176:179], v[80:83], v[96:99]\n"
        "ds_read_b128 v[212:215], v9 offset:3072\n"
        "v_mul_f32 v93, v93, v249\n"
        "v_mfma_f32_16x16x32_bf16 v[96:99], v[180:183], v[84:87], v[96:99]\n"
        "ds_read_b128 v[216:219], v9 offset:4096\n"
        "v_mul_f32 v94, v94, v250\n"
        "v_mfma_f32_16x16x32_bf16 v[96:99], v[184:187], v[88:91], v[96:99]\n"
        "ds_read_b128 v[220:223], v9 offset:5120\n"
        "v_mul_f32 v95, v95, v251\n"
        "v_mfma_f32_16x16x32_bf16 v[100:103], v[164:167], v[224:227], 0\n"
        "ds_read_b128 v[68:71], v9 offset:6144\n"
        "v_mfma_f32_16x16x32_bf16 v[100:103], v[168:171], v[228:231], v[100:103]\n"
        "ds_read_b128 v[72:75], v9 offset:7168\n"
        "v_mfma_f32_16x16x32_bf16 v[100:103], v[172:175], v[232:235], v[100:103]\n"
        "ds_read_b128 v[76:79], v9 offset:8192\n"
        "v_mul_f32 v96, v96, v248\n"
        "v_mfma_f32_16x16x32_bf16 v[100:103], v[176:179], v[236:239], v[100:103]\n"
        "s_waitcnt lgkmcnt(14)\n"
        "ds_read_b128 v[80:83], v9 offset:9216\n"
        "v_cvt_pk_bf16_f32 v28, v92, v96\n"
        "v_mfma_f32_16x16x32_bf16 v[100:103], v[180:183], v[240:243], v[100:103]\n"
        "s_waitcnt lgkmcnt(14)\n"
        "ds_read_b128 v[84:87], v9 offset:10240\n"
        "v_mul_f32 v97, v97, v249\n"
        "v_mfma_f32_16x16x32_bf16 v[100:103], v[184:187], v[244:247], v[100:103]\n"
        "s_waitcnt lgkmcnt(14)\n"
        "ds_read_b128 v[88:91], v9 offset:11264\n"
        "v_cvt_pk_bf16_f32 v30, v93, v97\n"
        "v_mfma_f32_16x16x32_bf16 v[24:27], v[116:119], v[44:47], 0\n"
        "s_waitcnt lgkmcnt(14)\n"
        "ds_read_b128 v[224:227], v9 offset:12288\n"
        "v_mul_f32 v98, v98, v250\n"
        "v_mfma_f32_16x16x32_bf16 v[24:27], v[120:123], v[48:51], v[24:27]\n"
        "s_waitcnt lgkmcnt(14)\n"
        "ds_read_b128 v[228:231], v9 offset:13312\n"
        "v_cvt_pk_bf16_f32 v32, v94, v98\n"
        "v_mfma_f32_16x16x32_bf16 v[24:27], v[124:127], v[52:55], v[24:27]\n"
        "v_mul_f32 v99, v99, v251\n"
        "v_cvt_pk_bf16_f32 v34, v95, v99\n"
        "v_mfma_f32_16x16x32_bf16 v[24:27], v[128:131], v[56:59], v[24:27]\n"
        "s_waitcnt lgkmcnt(14)\n"
        "ds_read_b128 v[232:235], v9 offset:14336\n"
        "v_mul_f32 v100, v100, v248\n"
        "v_mfma_f32_16x16x32_bf16 v[24:27], v[132:135], v[60:63], v[24:27]\n"
        "s_waitcnt lgkmcnt(14)\n"
        "ds_read_b128 v[236:239], v9 offset:15360\n"
        "v_cvt_pk_bf16_f32 v29, v100, v104\n"
        "v_mfma_f32_16x16x32_bf16 v[24:27], v[136:139], v[64:67], v[24:27]\n"
        "s_waitcnt lgkmcnt(14)\n"
        "buffer_store_dwordx2 v[28:29], v6, s[28:31], s84 offen offset:128 sc1\n"
        "ds_read_b128 v[240:243], v9 offset:16384\n"
        "v_mfma_f32_16x16x32_bf16 v[12:15], v[116:119], v[200:203], 0\n"
        "s_waitcnt lgkmcnt(14)\n"
        "ds_read_b128 v[244:247], v9 offset:17408\n"
        "v_mul_f32 v101, v101, v249\n"
        "v_mfma_f32_16x16x32_bf16 v[12:15], v[120:123], v[204:207], v[12:15]\n"
        "v_mul_f32 v102, v102, v250\n"
        "v_cvt_pk_bf16_f32 v31, v101, v105\n"
        "v_mfma_f32_16x16x32_bf16 v[12:15], v[124:127], v[208:211], v[12:15]\n"
        "s_waitcnt lgkmcnt(14)\n"
        "buffer_store_dwordx2 v[30:31], v6, s[28:31], s85 offen offset:128 sc1\n"
        "v_cvt_pk_bf16_f32 v33, v102, v106\n"
        "v_mfma_f32_16x16x32_bf16 v[12:15], v[128:131], v[212:215], v[12:15]\n"
        "s_waitcnt lgkmcnt(13)\n"
        "buffer_store_dwordx2 v[32:33], v7, s[28:31], s84 offen offset:128 sc1\n"
        "v_mul_f32 v103, v103, v251\n"
        "v_mfma_f32_16x16x32_bf16 v[12:15], v[132:135], v[216:219], v[12:15]\n"
        "s_waitcnt lgkmcnt(12)\n"
        "v_cvt_pk_bf16_f32 v35, v103, v107\n"
        "v_mul_f32 v24, v24, v108\n"
        "v_mfma_f32_16x16x32_bf16 v[12:15], v[136:139], v[220:223], v[12:15]\n"
        "s_waitcnt lgkmcnt(11)\n"
        "buffer_store_dwordx2 v[34:35], v7, s[28:31], s85 offen offset:128 sc1\n"
        "v_mul_f32 v25, v25, v109\n"
        "v_mfma_f32_16x16x32_bf16 v[16:19], v[116:119], v[68:71], 0\n"
        "s_waitcnt lgkmcnt(10)\n"
        "v_mul_f32 v26, v26, v110\n"
        "v_mul_f32 v27, v27, v111\n"
        "v_mfma_f32_16x16x32_bf16 v[16:19], v[120:123], v[72:75], v[16:19]\n"
        "s_waitcnt lgkmcnt(9)\n"
        "v_mfma_f32_16x16x32_bf16 v[16:19], v[124:127], v[76:79], v[16:19]\n"
        "s_waitcnt lgkmcnt(8)\n"
        "v_mul_f32 v12, v12, v108\n"
        "v_mul_f32 v13, v13, v109\n"
        "v_mfma_f32_16x16x32_bf16 v[16:19], v[128:131], v[80:83], v[16:19]\n"
        "s_waitcnt lgkmcnt(7)\n"
        "v_mul_f32 v14, v14, v110\n"
        "v_mul_f32 v15, v15, v111\n"
        "v_mfma_f32_16x16x32_bf16 v[16:19], v[132:135], v[84:87], v[16:19]\n"
        "s_waitcnt lgkmcnt(6)\n"
        "v_mfma_f32_16x16x32_bf16 v[16:19], v[136:139], v[88:91], v[16:19]\n"
        "s_waitcnt lgkmcnt(5)\n"
        "v_mfma_f32_16x16x32_bf16 v[20:23], v[116:119], v[224:227], 0\n"
        "s_waitcnt lgkmcnt(4)\n"
        "v_mfma_f32_16x16x32_bf16 v[20:23], v[120:123], v[228:231], v[20:23]\n"
        "s_waitcnt lgkmcnt(3)\n"
        "v_mfma_f32_16x16x32_bf16 v[20:23], v[124:127], v[232:235], v[20:23]\n"
        "s_waitcnt lgkmcnt(2)\n"
        "v_mfma_f32_16x16x32_bf16 v[20:23], v[128:131], v[236:239], v[20:23]\n"
        "s_waitcnt lgkmcnt(1)\n"
        "v_mul_f32 v16, v16, v108\n"
        "v_cvt_pk_bf16_f32 v36, v12, v16\n"
        "v_mfma_f32_16x16x32_bf16 v[20:23], v[132:135], v[240:243], v[20:23]\n"
        "s_waitcnt lgkmcnt(0)\n"
        "v_mul_f32 v17, v17, v109\n"
        "v_cvt_pk_bf16_f32 v38, v13, v17\n"
        "v_mfma_f32_16x16x32_bf16 v[20:23], v[136:139], v[244:247], v[20:23]\n"
        "v_mul_f32 v18, v18, v110\n"
        "v_cvt_pk_bf16_f32 v40, v14, v18\n"
        "v_mfma_f32_16x16x32_bf16 v[104:107], v[140:143], v[44:47], 0\n"
        "v_mul_f32 v19, v19, v111\n"
        "v_cvt_pk_bf16_f32 v42, v15, v19\n"
        "v_mfma_f32_16x16x32_bf16 v[104:107], v[144:147], v[48:51], v[104:107]\n"
        "v_mfma_f32_16x16x32_bf16 v[104:107], v[148:151], v[52:55], v[104:107]\n"
        "v_mfma_f32_16x16x32_bf16 v[104:107], v[152:155], v[56:59], v[104:107]\n"
        "v_mul_f32 v20, v20, v108\n"
        "v_cvt_pk_bf16_f32 v37, v20, v24\n"
        "v_mfma_f32_16x16x32_bf16 v[104:107], v[156:159], v[60:63], v[104:107]\n"
        "buffer_store_dwordx2 v[36:37], v6, s[28:31], s80 offen offset:256 sc1\n"
        "v_mul_f32 v21, v21, v109\n"
        "v_mfma_f32_16x16x32_bf16 v[104:107], v[160:163], v[64:67], v[104:107]\n"
        "v_mul_f32 v22, v22, v110\n"
        "v_cvt_pk_bf16_f32 v39, v21, v25\n"
        "v_mfma_f32_16x16x32_bf16 v[92:95], v[140:143], v[200:203], 0\n"
        "buffer_store_dwordx2 v[38:39], v6, s[28:31], s81 offen offset:256 sc1\n"
        "v_cvt_pk_bf16_f32 v41, v22, v26\n"
        "v_mfma_f32_16x16x32_bf16 v[92:95], v[144:147], v[204:207], v[92:95]\n"
        "buffer_store_dwordx2 v[40:41], v7, s[28:31], s80 offen offset:256 sc1\n"
        "v_mul_f32 v23, v23, v111\n"
        "v_mfma_f32_16x16x32_bf16 v[92:95], v[148:151], v[208:211], v[92:95]\n"
        "v_cvt_pk_bf16_f32 v43, v23, v27\n"
        "v_mul_f32 v104, v104, v112\n"
        "v_mfma_f32_16x16x32_bf16 v[92:95], v[152:155], v[212:215], v[92:95]\n"
        "buffer_store_dwordx2 v[42:43], v7, s[28:31], s81 offen offset:256 sc1\n"
        "v_mul_f32 v105, v105, v113\n"
        "v_mfma_f32_16x16x32_bf16 v[92:95], v[156:159], v[216:219], v[92:95]\n"
        "v_mul_f32 v106, v106, v114\n"
        "v_mul_f32 v107, v107, v115\n"
        "v_mfma_f32_16x16x32_bf16 v[92:95], v[160:163], v[220:223], v[92:95]\n"
        "v_mfma_f32_16x16x32_bf16 v[96:99], v[140:143], v[68:71], 0\n"
        "v_mfma_f32_16x16x32_bf16 v[96:99], v[144:147], v[72:75], v[96:99]\n"
        "v_mfma_f32_16x16x32_bf16 v[96:99], v[148:151], v[76:79], v[96:99]\n"
        "v_mfma_f32_16x16x32_bf16 v[96:99], v[152:155], v[80:83], v[96:99]\n"
        "v_mfma_f32_16x16x32_bf16 v[96:99], v[156:159], v[84:87], v[96:99]\n"
        "v_mfma_f32_16x16x32_bf16 v[96:99], v[160:163], v[88:91], v[96:99]\n"
        "v_mfma_f32_16x16x32_bf16 v[100:103], v[140:143], v[224:227], 0\n"
        "v_mfma_f32_16x16x32_bf16 v[100:103], v[144:147], v[228:231], v[100:103]\n"
        "v_mul_f32 v92, v92, v112\n"
        "v_mul_f32 v93, v93, v113\n"
        "v_mfma_f32_16x16x32_bf16 v[100:103], v[148:151], v[232:235], v[100:103]\n"
        "v_mul_f32 v94, v94, v114\n"
        "v_mul_f32 v95, v95, v115\n"
        "v_mfma_f32_16x16x32_bf16 v[100:103], v[152:155], v[236:239], v[100:103]\n"
        "v_mul_f32 v96, v96, v112\n"
        "v_cvt_pk_bf16_f32 v28, v92, v96\n"
        "v_mfma_f32_16x16x32_bf16 v[100:103], v[156:159], v[240:243], v[100:103]\n"
        "v_mul_f32 v97, v97, v113\n"
        "v_cvt_pk_bf16_f32 v30, v93, v97\n"
        "v_mfma_f32_16x16x32_bf16 v[100:103], v[160:163], v[244:247], v[100:103]\n"
        "v_mul_f32 v98, v98, v114\n"
        "v_cvt_pk_bf16_f32 v32, v94, v98\n"
        "v_mfma_f32_16x16x32_bf16 v[24:27], v[164:167], v[44:47], 0\n"
        "v_mul_f32 v99, v99, v115\n"
        "v_cvt_pk_bf16_f32 v34, v95, v99\n"
        "v_mfma_f32_16x16x32_bf16 v[24:27], v[168:171], v[48:51], v[24:27]\n"
        "v_mfma_f32_16x16x32_bf16 v[24:27], v[172:175], v[52:55], v[24:27]\n"
        "v_mfma_f32_16x16x32_bf16 v[24:27], v[176:179], v[56:59], v[24:27]\n"
        "v_mul_f32 v100, v100, v112\n"
        "v_cvt_pk_bf16_f32 v29, v100, v104\n"
        "v_mfma_f32_16x16x32_bf16 v[24:27], v[180:183], v[60:63], v[24:27]\n"
        "buffer_store_dwordx2 v[28:29], v6, s[28:31], s82 offen offset:256 sc1\n"
        "v_mul_f32 v101, v101, v113\n"
        "v_mfma_f32_16x16x32_bf16 v[24:27], v[184:187], v[64:67], v[24:27]\n"
        "v_mul_f32 v102, v102, v114\n"
        "v_cvt_pk_bf16_f32 v31, v101, v105\n"
        "v_mfma_f32_16x16x32_bf16 v[12:15], v[164:167], v[200:203], 0\n"
        "buffer_store_dwordx2 v[30:31], v6, s[28:31], s83 offen offset:256 sc1\n"
        "v_cvt_pk_bf16_f32 v33, v102, v106\n"
        "v_mfma_f32_16x16x32_bf16 v[12:15], v[168:171], v[204:207], v[12:15]\n"
        "buffer_store_dwordx2 v[32:33], v7, s[28:31], s82 offen offset:256 sc1\n"
        "v_mul_f32 v103, v103, v115\n"
        "v_mfma_f32_16x16x32_bf16 v[12:15], v[172:175], v[208:211], v[12:15]\n"
        "v_cvt_pk_bf16_f32 v35, v103, v107\n"
        "v_mul_f32 v24, v24, v248\n"
        "v_mfma_f32_16x16x32_bf16 v[12:15], v[176:179], v[212:215], v[12:15]\n"
        "buffer_store_dwordx2 v[34:35], v7, s[28:31], s83 offen offset:256 sc1\n"
        "v_mul_f32 v25, v25, v249\n"
        "v_mfma_f32_16x16x32_bf16 v[12:15], v[180:183], v[216:219], v[12:15]\n"
        "v_mul_f32 v26, v26, v250\n"
        "v_mul_f32 v27, v27, v251\n"
        "v_mfma_f32_16x16x32_bf16 v[12:15], v[184:187], v[220:223], v[12:15]\n"
        "v_mfma_f32_16x16x32_bf16 v[16:19], v[164:167], v[68:71], 0\n"
        "v_mfma_f32_16x16x32_bf16 v[16:19], v[168:171], v[72:75], v[16:19]\n"
        "v_mfma_f32_16x16x32_bf16 v[16:19], v[172:175], v[76:79], v[16:19]\n"
        "v_mfma_f32_16x16x32_bf16 v[16:19], v[176:179], v[80:83], v[16:19]\n"
        "v_mfma_f32_16x16x32_bf16 v[16:19], v[180:183], v[84:87], v[16:19]\n"
        "v_mfma_f32_16x16x32_bf16 v[16:19], v[184:187], v[88:91], v[16:19]\n"
        "v_mfma_f32_16x16x32_bf16 v[20:23], v[164:167], v[224:227], 0\n"
        "v_mfma_f32_16x16x32_bf16 v[20:23], v[168:171], v[228:231], v[20:23]\n"
        "v_mul_f32 v12, v12, v248\n"
        "v_mul_f32 v13, v13, v249\n"
        "v_mfma_f32_16x16x32_bf16 v[20:23], v[172:175], v[232:235], v[20:23]\n"
        "v_mul_f32 v14, v14, v250\n"
        "v_mul_f32 v15, v15, v251\n"
        "v_mfma_f32_16x16x32_bf16 v[20:23], v[176:179], v[236:239], v[20:23]\n"
        "v_mul_f32 v16, v16, v248\n"
        "v_cvt_pk_bf16_f32 v36, v12, v16\n"
        "v_mfma_f32_16x16x32_bf16 v[20:23], v[180:183], v[240:243], v[20:23]\n"
        "v_mul_f32 v17, v17, v249\n"
        "v_cvt_pk_bf16_f32 v38, v13, v17\n"
        "v_mfma_f32_16x16x32_bf16 v[20:23], v[184:187], v[244:247], v[20:23]\n"
        "v_mul_f32 v18, v18, v250\n"
        "v_mul_f32 v19, v19, v251\n"
        "s_nop 5\n"
        "v_mul_f32 v20, v20, v248\n"
        "v_mul_f32 v21, v21, v249\n"
        "v_mul_f32 v22, v22, v250\n"
        "v_mul_f32 v23, v23, v251\n"
        "v_cvt_pk_bf16_f32 v37, v20, v24\n"
        "buffer_store_dwordx2 v[36:37], v6, s[28:31], s84 offen offset:256 nt\n"
        "v_cvt_pk_bf16_f32 v39, v21, v25\n"
        "buffer_store_dwordx2 v[38:39], v6, s[28:31], s85 offen offset:256 nt\n"
        "v_cvt_pk_bf16_f32 v40, v14, v18\n"
        "v_cvt_pk_bf16_f32 v41, v22, v26\n"
        "buffer_store_dwordx2 v[40:41], v7, s[28:31], s84 offen offset:256 nt\n"
        "v_cvt_pk_bf16_f32 v42, v15, v19\n"
        "v_cvt_pk_bf16_f32 v43, v23, v27\n"
        "buffer_store_dwordx2 v[42:43], v7, s[28:31], s85 offen offset:256 nt\n"
        "s_endpgm\n"
        "Lwave4_%=:\n"
        "s_waitcnt lgkmcnt(0)\n"
        "s_bitcmp1_b32 s18, 31\n"
        "s_cbranch_scc1 Ltg28_%=\n"
        "s_bitset1_b32 s18, 31\n"
        "s_mul_i32 s7, s7, 0x4c957f2d\n"
        "s_mul_i32 s86, s6, 0x5851f42d\n"
        "s_add_u32 s7, s7, s86\n"
        "s_mul_hi_u32 s86, s6, 0x4c957f2d\n"
        "s_add_u32 s7, s7, s86\n"
        "s_mul_i32 s6, s6, 0x4c957f2d\n"
        "s_add_u32 s6, s6, 0xf767814f\n"
        "s_addc_u32 s7, s7, 0x14057b7e\n"
        "Ltg28_%=:\n"
        "s_mov_b32 s88, s46\n"
        "s_mov_b32 s89, s47\n"
        "v_mov_b32 v2, 0x27000\n"
        "s_and_b32 s86, s3, 3\n"
        "v_mov_b32 v4, 1\n"
        "v_add_u32 v3, s86, v2\n"
        "s_mov_b32 s30, 0x1260000\n"
        "s_mov_b32 s31, 0x20000\n"
        "s_branch Lhjoin_%=\n"
        "Lhelper_%=:\n"
        "s_add_u32 m0, s91, 0x7f80\n"
        "s_nop 0\n"
        "buffer_load_dwordx4 v249, s[20:23], 0 offen offset:128 lds\n"
        "s_add_u32 m0, s91, 0x9b80\n"
        "s_nop 0\n"
        "buffer_load_dwordx4 v250, s[20:23], 0 offen offset:128 lds\n"
        "v_bfe_u32 v10, v0, 5, 1\n"
        "v_bfe_u32 v11, v0, 4, 1\n"
        "s_add_u32 s86, s12, 18\n"
        "s_add_u32 s87, s13, 18\n"
        "v_lshl_add_u32 v10, v10, 1, s86\n"
        "v_lshl_add_u32 v11, v11, 1, s87\n"
        "v_mad_i32_i24 v29, v10, s14, v9\n"
        "v_mad_i32_i24 v29, v11, s15, v29\n"
        "v_and_b32 v10, 15, v0\n"
        "v_lshl_add_u32 v29, v10, 3, v29\n"
        "s_cmp_eq_u32 s80, 0x1000\n"
        "s_cbranch_scc1 Lhjoin_%=\n"
        "s_waitcnt vmcnt(5)\n"
        "Lhjoin_%=:\n"
        "v_bfe_u32 v145, v212, 0, 8\n"
        "v_bfe_u32 v144, v212, 8, 8\n"
        "v_lshl_add_u32 v146, v145, 1, s12\n"
        "v_add_u32 v147, 7, v146\n"
        "v_min_u32 v146, v146, v147\n"
        "v_mad_i32_i24 v22, v146, s14, v9\n"
        "v_lshl_add_u32 v146, v144, 1, s13\n"
        "v_add_u32 v147, 7, v146\n"
        "v_min_u32 v146, v146, v147\n"
        "v_mad_i32_i24 v22, v146, s15, v22\n"
        "v_xor_b32 v146, v145, v144\n"
        "v_and_b32 v146, 2, v146\n"
        "v_and_b32 v147, 1, v145\n"
        "v_lshl_or_b32 v146, v147, 2, v146\n"
        "v_xor_b32 v146, v146, v7\n"
        "v_lshl_add_u32 v22, v146, 4, v22\n"
        "v_bfe_u32 v145, v212, 16, 8\n"
        "v_bfe_u32 v144, v212, 24, 8\n"
        "v_lshl_add_u32 v146, v145, 1, s12\n"
        "v_add_u32 v147, 7, v146\n"
        "v_min_u32 v146, v146, v147\n"
        "v_mad_i32_i24 v23, v146, s14, v9\n"
        "v_lshl_add_u32 v146, v144, 1, s13\n"
        "v_add_u32 v147, 7, v146\n"
        "v_min_u32 v146, v146, v147\n"
        "v_mad_i32_i24 v23, v146, s15, v23\n"
        "v_xor_b32 v146, v145, v144\n"
        "v_and_b32 v146, 2, v146\n"
        "v_and_b32 v147, 1, v145\n"
        "v_lshl_or_b32 v146, v147, 2, v146\n"
        "v_xor_b32 v146, v146, v7\n"
        "v_lshl_add_u32 v23, v146, 4, v23\n"
        "v_bfe_u32 v145, v213, 0, 8\n"
        "v_bfe_u32 v144, v213, 8, 8\n"
        "v_lshl_add_u32 v146, v145, 1, s12\n"
        "v_add_u32 v147, 7, v146\n"
        "v_min_u32 v146, v146, v147\n"
        "v_mad_i32_i24 v24, v146, s14, v9\n"
        "v_lshl_add_u32 v146, v144, 1, s13\n"
        "v_add_u32 v147, 7, v146\n"
        "v_min_u32 v146, v146, v147\n"
        "v_mad_i32_i24 v24, v146, s15, v24\n"
        "v_xor_b32 v146, v145, v144\n"
        "v_and_b32 v146, 2, v146\n"
        "v_and_b32 v147, 1, v145\n"
        "v_lshl_or_b32 v146, v147, 2, v146\n"
        "v_xor_b32 v146, v146, v7\n"
        "v_lshl_add_u32 v24, v146, 4, v24\n"
        "v_bfe_u32 v145, v213, 16, 8\n"
        "v_bfe_u32 v144, v213, 24, 8\n"
        "v_lshl_add_u32 v146, v145, 1, s12\n"
        "v_add_u32 v147, 7, v146\n"
        "v_min_u32 v146, v146, v147\n"
        "v_mad_i32_i24 v25, v146, s14, v9\n"
        "v_lshl_add_u32 v146, v144, 1, s13\n"
        "v_add_u32 v147, 7, v146\n"
        "v_min_u32 v146, v146, v147\n"
        "v_mad_i32_i24 v25, v146, s15, v25\n"
        "v_xor_b32 v146, v145, v144\n"
        "v_and_b32 v146, 2, v146\n"
        "v_and_b32 v147, 1, v145\n"
        "v_lshl_or_b32 v146, v147, 2, v146\n"
        "v_xor_b32 v146, v146, v7\n"
        "v_lshl_add_u32 v25, v146, 4, v25\n"
        "v_bfe_u32 v145, v214, 0, 8\n"
        "v_bfe_u32 v144, v214, 8, 8\n"
        "v_lshl_add_u32 v146, v145, 1, s12\n"
        "v_add_u32 v147, 7, v146\n"
        "v_min_u32 v146, v146, v147\n"
        "v_mad_i32_i24 v26, v146, s14, v9\n"
        "v_lshl_add_u32 v146, v144, 1, s13\n"
        "v_add_u32 v147, 7, v146\n"
        "v_min_u32 v146, v146, v147\n"
        "v_mad_i32_i24 v26, v146, s15, v26\n"
        "v_xor_b32 v146, v145, v144\n"
        "v_and_b32 v146, 2, v146\n"
        "v_and_b32 v147, 1, v145\n"
        "v_lshl_or_b32 v146, v147, 2, v146\n"
        "v_xor_b32 v146, v146, v7\n"
        "v_lshl_add_u32 v26, v146, 4, v26\n"
        "v_bfe_u32 v145, v214, 16, 8\n"
        "v_bfe_u32 v144, v214, 24, 8\n"
        "v_lshl_add_u32 v146, v145, 1, s12\n"
        "v_add_u32 v147, 7, v146\n"
        "v_min_u32 v146, v146, v147\n"
        "v_mad_i32_i24 v27, v146, s14, v9\n"
        "v_lshl_add_u32 v146, v144, 1, s13\n"
        "v_add_u32 v147, 7, v146\n"
        "v_min_u32 v146, v146, v147\n"
        "v_mad_i32_i24 v27, v146, s15, v27\n"
        "v_xor_b32 v146, v145, v144\n"
        "v_and_b32 v146, 2, v146\n"
        "v_and_b32 v147, 1, v145\n"
        "v_lshl_or_b32 v146, v147, 2, v146\n"
        "v_xor_b32 v146, v146, v7\n"
        "v_lshl_add_u32 v27, v146, 4, v27\n"
        "v_bfe_u32 v145, v215, 0, 8\n"
        "v_bfe_u32 v144, v215, 8, 8\n"
        "v_lshl_add_u32 v146, v145, 1, s12\n"
        "v_add_u32 v147, 7, v146\n"
        "v_min_u32 v146, v146, v147\n"
        "v_mad_i32_i24 v28, v146, s14, v9\n"
        "v_lshl_add_u32 v146, v144, 1, s13\n"
        "v_add_u32 v147, 7, v146\n"
        "v_min_u32 v146, v146, v147\n"
        "v_mad_i32_i24 v28, v146, s15, v28\n"
        "v_xor_b32 v146, v145, v144\n"
        "v_and_b32 v146, 2, v146\n"
        "v_and_b32 v147, 1, v145\n"
        "v_lshl_or_b32 v146, v147, 2, v146\n"
        "v_xor_b32 v146, v146, v7\n"
        "v_lshl_add_u32 v28, v146, 4, v28\n"
        "v_mov_b32 v18, 5\n"
        "v_and_b32 v19, 48, v0\n"
        "v_lshlrev_b32_sdwa v16, v18, v202 dst_sel:DWORD dst_unused:UNUSED_PAD src0_sel:DWORD src1_sel:WORD_0\n"
        "v_lshlrev_b32 v144, 1, v19\n"
        "v_xor_b32 v16, v16, v144\n"
        "v_and_b32 v144, 1, v0\n"
        "v_lshl_add_u32 v16, v144, 2, v16\n"
        "s_mov_b32 s89, 0x9e3779b1\n"
        "v_add_u32 v33, 0x51ed27, v8\n"
        "v_mul_lo_u32 v30, v33, s89\n"
        "v_mul_lo_u32 v31, v30, s89\n"
        "v_mul_lo_u32 v32, v31, s89\n"
        "v_mul_lo_u32 v33, v32, s89\n"
        "v_or_b32 v30, 1, v30\n"
        "v_or_b32 v31, 1, v31\n"
        "v_or_b32 v32, 1, v32\n"
        "v_or_b32 v33, 1, v33\n"
        "v_lshlrev_b32 v1, 4, v8\n"
        "s_and_b32 s88, s3, 3\n"
        "v_lshrrev_b32 v14, 3, v8\n"
        "v_lshl_add_u32 v14, s88, 3, v14\n"
        "v_add_u32 v10, 42, v14\n"
        "v_mul_u32_u24 v11, 293, v10\n"
        "v_lshrrev_b32 v11, 12, v11\n"
        "v_mad_i32_i24 v10, v11, -14, v10\n"
        "v_add_u32 v10, 3, v10\n"
        "v_lshl_add_u32 v12, v11, 1, s12\n"
        "v_add_u32 v13, 7, v12\n"
        "v_min_u32 v12, v12, v13\n"
        "v_mad_i32_i24 v218, v12, s14, v9\n"
        "v_lshl_add_u32 v12, v10, 1, s13\n"
        "v_add_u32 v13, 7, v12\n"
        "v_min_u32 v12, v12, v13\n"
        "v_mad_i32_i24 v218, v12, s15, v218\n"
        "v_xor_b32 v12, v11, v10\n"
        "v_and_b32 v12, 2, v12\n"
        "v_and_b32 v13, 1, v11\n"
        "v_lshl_or_b32 v12, v13, 2, v12\n"
        "v_xor_b32 v12, v12, v7\n"
        "v_lshl_add_u32 v218, v12, 4, v218\n"
        "v_add_u32 v10, 74, v14\n"
        "v_mul_u32_u24 v11, 293, v10\n"
        "v_lshrrev_b32 v11, 12, v11\n"
        "v_mad_i32_i24 v10, v11, -14, v10\n"
        "v_add_u32 v10, 3, v10\n"
        "v_lshl_add_u32 v12, v11, 1, s12\n"
        "v_add_u32 v13, 7, v12\n"
        "v_min_u32 v12, v12, v13\n"
        "v_mad_i32_i24 v219, v12, s14, v9\n"
        "v_lshl_add_u32 v12, v10, 1, s13\n"
        "v_add_u32 v13, 7, v12\n"
        "v_min_u32 v12, v12, v13\n"
        "v_mad_i32_i24 v219, v12, s15, v219\n"
        "v_xor_b32 v12, v11, v10\n"
        "v_and_b32 v12, 2, v12\n"
        "v_and_b32 v13, 1, v11\n"
        "v_lshl_or_b32 v12, v13, 2, v12\n"
        "v_xor_b32 v12, v12, v7\n"
        "v_lshl_add_u32 v219, v12, 4, v219\n"
        "v_add_u32 v10, 106, v14\n"
        "v_mul_u32_u24 v11, 293, v10\n"
        "v_lshrrev_b32 v11, 12, v11\n"
        "v_mad_i32_i24 v10, v11, -14, v10\n"
        "v_add_u32 v10, 3, v10\n"
        "v_lshl_add_u32 v12, v11, 1, s12\n"
        "v_add_u32 v13, 7, v12\n"
        "v_min_u32 v12, v12, v13\n"
        "v_mad_i32_i24 v220, v12, s14, v9\n"
        "v_lshl_add_u32 v12, v10, 1, s13\n"
        "v_add_u32 v13, 7, v12\n"
        "v_min_u32 v12, v12, v13\n"
        "v_mad_i32_i24 v220, v12, s15, v220\n"
        "v_xor_b32 v12, v11, v10\n"
        "v_and_b32 v12, 2, v12\n"
        "v_and_b32 v13, 1, v11\n"
        "v_lshl_or_b32 v12, v13, 2, v12\n"
        "v_xor_b32 v12, v12, v7\n"
        "v_lshl_add_u32 v220, v12, 4, v220\n"
        "v_add_u32 v10, 138, v14\n"
        "v_mul_u32_u24 v11, 293, v10\n"
        "v_lshrrev_b32 v11, 12, v11\n"
        "v_mad_i32_i24 v10, v11, -14, v10\n"
        "v_add_u32 v10, 3, v10\n"
        "v_lshl_add_u32 v12, v11, 1, s12\n"
        "v_add_u32 v13, 7, v12\n"
        "v_min_u32 v12, v12, v13\n"
        "v_mad_i32_i24 v221, v12, s14, v9\n"
        "v_lshl_add_u32 v12, v10, 1, s13\n"
        "v_add_u32 v13, 7, v12\n"
        "v_min_u32 v12, v12, v13\n"
        "v_mad_i32_i24 v221, v12, s15, v221\n"
        "v_xor_b32 v12, v11, v10\n"
        "v_and_b32 v12, 2, v12\n"
        "v_and_b32 v13, 1, v11\n"
        "v_lshl_or_b32 v12, v13, 2, v12\n"
        "v_xor_b32 v12, v12, v7\n"
        "v_lshl_add_u32 v221, v12, 4, v221\n"
        "v_add_u32 v10, 170, v14\n"
        "v_mul_u32_u24 v11, 293, v10\n"
        "v_lshrrev_b32 v11, 12, v11\n"
        "v_mad_i32_i24 v10, v11, -14, v10\n"
        "v_add_u32 v10, 3, v10\n"
        "v_lshl_add_u32 v12, v11, 1, s12\n"
        "v_add_u32 v13, 7, v12\n"
        "v_min_u32 v12, v12, v13\n"
        "v_mad_i32_i24 v222, v12, s14, v9\n"
        "v_lshl_add_u32 v12, v10, 1, s13\n"
        "v_add_u32 v13, 7, v12\n"
        "v_min_u32 v12, v12, v13\n"
        "v_mad_i32_i24 v222, v12, s15, v222\n"
        "v_xor_b32 v12, v11, v10\n"
        "v_and_b32 v12, 2, v12\n"
        "v_and_b32 v13, 1, v11\n"
        "v_lshl_or_b32 v12, v13, 2, v12\n"
        "v_xor_b32 v12, v12, v7\n"
        "v_lshl_add_u32 v222, v12, 4, v222\n"
        "v_add_u32 v10, 202, v14\n"
        "v_mul_u32_u24 v11, 293, v10\n"
        "v_lshrrev_b32 v11, 12, v11\n"
        "v_mad_i32_i24 v10, v11, -14, v10\n"
        "v_add_u32 v10, 3, v10\n"
        "v_lshl_add_u32 v12, v11, 1, s12\n"
        "v_add_u32 v13, 7, v12\n"
        "v_min_u32 v12, v12, v13\n"
        "v_mad_i32_i24 v223, v12, s14, v9\n"
        "v_lshl_add_u32 v12, v10, 1, s13\n"
        "v_add_u32 v13, 7, v12\n"
        "v_min_u32 v12, v12, v13\n"
        "v_mad_i32_i24 v223, v12, s15, v223\n"
        "v_xor_b32 v12, v11, v10\n"
        "v_and_b32 v12, 2, v12\n"
        "v_and_b32 v13, 1, v11\n"
        "v_lshl_or_b32 v12, v13, 2, v12\n"
        "v_xor_b32 v12, v12, v7\n"
        "v_lshl_add_u32 v223, v12, 4, v223\n"
        "v_add_u32 v10, 234, v14\n"
        "v_mul_u32_u24 v11, 293, v10\n"
        "v_lshrrev_b32 v11, 12, v11\n"
        "v_mad_i32_i24 v10, v11, -14, v10\n"
        "v_add_u32 v10, 3, v10\n"
        "v_lshl_add_u32 v12, v11, 1, s12\n"
        "v_add_u32 v13, 7, v12\n"
        "v_min_u32 v12, v12, v13\n"
        "v_mad_i32_i24 v224, v12, s14, v9\n"
        "v_lshl_add_u32 v12, v10, 1, s13\n"
        "v_add_u32 v13, 7, v12\n"
        "v_min_u32 v12, v12, v13\n"
        "v_mad_i32_i24 v224, v12, s15, v224\n"
        "v_xor_b32 v12, v11, v10\n"
        "v_and_b32 v12, 2, v12\n"
        "v_and_b32 v13, 1, v11\n"
        "v_lshl_or_b32 v12, v13, 2, v12\n"
        "v_xor_b32 v12, v12, v7\n"
        "v_lshl_add_u32 v224, v12, 4, v224\n"
        "s_lshl_b32 s86, s88, 10\n"
        "s_add_u32 m0, s86, 0xc700\n"
        "s_nop 0\n"
        "buffer_load_dwordx4 v218, s[20:23], 0 offen offset:256 lds\n"
        "v_and_b32 v15, 63, v0\n"
        "v_min_u32 v15, 17, v15\n"
        "v_mul_u32_u24 v11, 43, v15\n"
        "v_lshrrev_b32 v11, 7, v11\n"
        "v_mul_u32_u24 v11, 12752, v11\n"
        "v_lshl_add_u32 v15, v15, 4, v11\n"
        "s_and_b32 s11, s2, 0\n"
        "s_mul_i32 s11, s11, 76800\n"
        "s_cmp_lg_u32 s19, 0\n"
        "s_cselect_b32 s11, s19, s11\n"
        "s_add_u32 s11, s11, 81920\n"
        "s_mul_i32 s10, s88, 3200\n"
        "s_add_u32 s10, s10, s11\n"
        "s_mul_i32 s11, s88, 6144\n"
        "v_add_u32 v14, s11, v1\n"
        "v_add_u32 v14, 0x12c00, v14\n"
        "v_add_u32 v13, s10, v1\n"
        "v_mov_b32 v10, s6\n"
        "v_mov_b32 v11, s7\n"
        "s_waitcnt lgkmcnt(0)\n"
        "Lt129_%=:\n"
        "s_memtime s[86:87]\n"
        "s_waitcnt lgkmcnt(0)\n"
        "s_sub_u32 s86, s86, s8\n"
        "s_cmp_lt_u32 s86, 5700\n"
        "s_cbranch_scc1 Lt129_%=\n"
        "Lhre_%=:\n"
        "s_lshl_b32 s86, s88, 10\n"
        "buffer_load_dwordx2 v[36:37], v15, s[24:27], s10 offen offset:3072 sc1\n"
        "s_mov_b32 s87, 0\n"
        "s_add_u32 m0, s11, 0x12c00\n"
        "s_nop 0\n"
        "buffer_load_dwordx4 v13, s[24:27], s87 offen sc1 lds\n"
        "s_mov_b32 s87, 12800\n"
        "s_add_u32 m0, s11, 0x13000\n"
        "s_nop 0\n"
        "buffer_load_dwordx4 v13, s[24:27], s87 offen sc1 lds\n"
        "s_mov_b32 s87, 1024\n"
        "s_add_u32 m0, s11, 0x13400\n"
        "s_nop 0\n"
        "buffer_load_dwordx4 v13, s[24:27], s87 offen sc1 lds\n"
        "s_mov_b32 s87, 13824\n"
        "s_add_u32 m0, s11, 0x13800\n"
        "s_nop 0\n"
        "buffer_load_dwordx4 v13, s[24:27], s87 offen sc1 lds\n"
        "s_mov_b32 s87, 2048\n"
        "s_add_u32 m0, s11, 0x13c00\n"
        "s_nop 0\n"
        "buffer_load_dwordx4 v13, s[24:27], s87 offen sc1 lds\n"
        "s_mov_b32 s87, 14848\n"
        "s_add_u32 m0, s11, 0x14000\n"
        "s_nop 0\n"
        "buffer_load_dwordx4 v13, s[24:27], s87 offen sc1 lds\n"
        "s_sleep 2\n"
        "s_add_u32 m0, s86, 0xd700\n"
        "s_nop 0\n"
        "buffer_load_dwordx4 v219, s[20:23], 0 offen offset:256 lds\n"
        "s_add_u32 m0, s86, 0xe700\n"
        "s_nop 0\n"
        "buffer_load_dwordx4 v220, s[20:23], 0 offen offset:256 lds\n"
        "s_mov_b32 s87, 25600\n"
        "s_add_u32 m0, s11, 0x19000\n"
        "s_nop 0\n"
        "buffer_load_dwordx4 v13, s[24:27], s87 offen sc1 lds\n"
        "s_mov_b32 s87, 38400\n"
        "s_add_u32 m0, s11, 0x19400\n"
        "s_nop 0\n"
        "buffer_load_dwordx4 v13, s[24:27], s87 offen sc1 lds\n"
        "s_mov_b32 s87, 26624\n"
        "s_add_u32 m0, s11, 0x19800\n"
        "s_nop 0\n"
        "buffer_load_dwordx4 v13, s[24:27], s87 offen sc1 lds\n"
        "s_mov_b32 s87, 39424\n"
        "s_add_u32 m0, s11, 0x19c00\n"
        "s_nop 0\n"
        "buffer_load_dwordx4 v13, s[24:27], s87 offen sc1 lds\n"
        "s_mov_b32 s87, 27648\n"
        "s_add_u32 m0, s11, 0x1a000\n"
        "s_nop 0\n"
        "buffer_load_dwordx4 v13, s[24:27], s87 offen sc1 lds\n"
        "s_mov_b32 s87, 40448\n"
        "s_add_u32 m0, s11, 0x1a400\n"
        "s_nop 0\n"
        "buffer_load_dwordx4 v13, s[24:27], s87 offen sc1 lds\n"
        "s_add_u32 m0, s86, 0xf700\n"
        "s_nop 0\n"
        "buffer_load_dwordx4 v221, s[20:23], 0 offen offset:256 lds\n"
        "s_add_u32 m0, s86, 0x10700\n"
        "s_nop 0\n"
        "buffer_load_dwordx4 v222, s[20:23], 0 offen offset:256 lds\n"
        "s_mov_b32 s90, 40\n"
        "s_waitcnt vmcnt(16)\n"
        "v_cmp_ne_u32 vcc, s7, v37\n"
        "s_nop 4\n"
        "s_cbranch_vccnz Lgret_%=\n"
        "Lgok_%=:\n"
        "s_waitcnt vmcnt(10)\n"
        "s_barrier\n"
        "s_load_dwordx8 s[40:47], s[4:5], 512\n"
        "s_add_u32 m0, s86, 0x11700\n"
        "s_nop 0\n"
        "buffer_load_dwordx4 v223, s[20:23], 0 offen offset:256 lds\n"
        "s_waitcnt vmcnt(3)\n"
        "ds_write_b8 v3, v4 offset:0\n"
        "s_cmp_lg_u32 s3, 4\n"
        "s_cbranch_scc1 Lw4x_%=\n"
        "s_add_u32 m0, s86, 0x12700\n"
        "s_nop 0\n"
        "buffer_load_dwordx4 v224, s[20:23], 0 offen offset:256 lds\n"
        "Lw4x_%=:\n"
        "s_mov_b32 s87, 51200\n"
        "s_add_u32 m0, s11, 0x1f400\n"
        "s_nop 0\n"
        "buffer_load_dwordx4 v13, s[24:27], s87 offen sc1 lds\n"
        "s_mov_b32 s87, 64000\n"
        "s_add_u32 m0, s11, 0x1f800\n"
        "s_nop 0\n"
        "buffer_load_dwordx4 v13, s[24:27], s87 offen sc1 lds\n"
        "s_mov_b32 s87, 52224\n"
        "s_add_u32 m0, s11, 0x1fc00\n"
        "s_nop 0\n"
        "buffer_load_dwordx4 v13, s[24:27], s87 offen sc1 lds\n"
        "s_mov_b32 s87, 65024\n"
        "s_add_u32 m0, s11, 0x20000\n"
        "s_nop 0\n"
        "buffer_load_dwordx4 v13, s[24:27], s87 offen sc1 lds\n"
        "s_mov_b32 s87, 53248\n"
        "s_add_u32 m0, s11, 0x20400\n"
        "s_nop 0\n"
        "buffer_load_dwordx4 v13, s[24:27], s87 offen sc1 lds\n"
        "s_mov_b32 s87, 66048\n"
        "s_add_u32 m0, s11, 0x20800\n"
        "s_nop 0\n"
        "buffer_load_dwordx4 v13, s[24:27], s87 offen sc1 lds\n"
        "ds_read_b128 v[40:43], v14 offset:0\n"
        "ds_read_b128 v[44:47], v14 offset:1024\n"
        "ds_read_b128 v[48:51], v14 offset:2048\n"
        "ds_read_b128 v[52:55], v14 offset:3072\n"
        "ds_read_b128 v[56:59], v14 offset:4096\n"
        "ds_read_b128 v[60:63], v14 offset:5120\n"
        "ds_read_b128 v[64:67], v14 offset:25600\n"
        "ds_read_b128 v[68:71], v14 offset:26624\n"
        "ds_read_b128 v[72:75], v14 offset:27648\n"
        "ds_read_b128 v[76:79], v14 offset:28672\n"
        "ds_read_b128 v[80:83], v14 offset:29696\n"
        "ds_read_b128 v[84:87], v14 offset:30720\n"
        "v_cmp_gt_u32 vcc, 18, v8\n"
        "v_subrev_u32 v35, s6, v36\n"
        "s_nop 0\n"
        "v_cndmask_b32 v35, 0, v35, vcc\n"
        "v_cmp_gt_u32 vcc, 12, v8\n"
        "s_nop 1\n"
        "v_cndmask_b32 v12, 0, v35, vcc\n"
        "v_sub_u32 v35, v35, v12\n"
        "v_cmp_ne_u32 vcc, s7, v37\n"
        "s_waitcnt lgkmcnt(10)\n"
        "v_add_u32 v124, v40, v44\n"
        "v_add_u32 v125, v41, v45\n"
        "v_add_u32 v126, v42, v46\n"
        "v_add_u32 v127, v43, v47\n"
        "s_waitcnt lgkmcnt(8)\n"
        "v_add3_u32 v124, v124, v48, v52\n"
        "v_add3_u32 v125, v125, v49, v53\n"
        "v_add3_u32 v126, v126, v50, v54\n"
        "v_add3_u32 v127, v127, v51, v55\n"
        "s_waitcnt lgkmcnt(6)\n"
        "v_add3_u32 v124, v124, v56, v60\n"
        "v_add3_u32 v125, v125, v57, v61\n"
        "v_add3_u32 v126, v126, v58, v62\n"
        "v_add3_u32 v127, v127, v59, v63\n"
        "s_waitcnt lgkmcnt(4)\n"
        "v_add3_u32 v124, v124, v64, v68\n"
        "v_add3_u32 v125, v125, v65, v69\n"
        "v_add3_u32 v126, v126, v66, v70\n"
        "v_add3_u32 v127, v127, v67, v71\n"
        "s_waitcnt lgkmcnt(2)\n"
        "v_add3_u32 v124, v124, v72, v76\n"
        "v_add3_u32 v125, v125, v73, v77\n"
        "v_add3_u32 v126, v126, v74, v78\n"
        "v_add3_u32 v127, v127, v75, v79\n"
        "s_waitcnt lgkmcnt(0)\n"
        "v_add3_u32 v124, v124, v80, v84\n"
        "v_add3_u32 v125, v125, v81, v85\n"
        "v_add3_u32 v126, v126, v82, v86\n"
        "v_add3_u32 v127, v127, v83, v87\n"
        "v_mul_lo_u32 v124, v124, v30\n"
        "v_mul_lo_u32 v125, v125, v31\n"
        "v_mul_lo_u32 v126, v126, v32\n"
        "v_mul_lo_u32 v127, v127, v33\n"
        "v_add3_u32 v34, v124, v125, v126\n"
        "v_add_u32 v34, v34, v127\n"
        "v_sub_u32 v34, v34, v12\n"
        "s_nop 1\n"
        "v_add_u32_dpp v34, v34, v34 row_shr:1 row_mask:0xf bank_mask:0xf\n"
        "s_nop 1\n"
        "v_add_u32_dpp v34, v34, v34 row_shr:2 row_mask:0xf bank_mask:0xf\n"
        "s_nop 1\n"
        "v_add_u32_dpp v34, v34, v34 row_shr:4 row_mask:0xf bank_mask:0xf\n"
        "s_nop 1\n"
        "v_add_u32_dpp v34, v34, v34 row_shr:8 row_mask:0xf bank_mask:0xf\n"
        "s_nop 1\n"
        "v_add_u32_dpp v34, v34, v34 row_bcast:15 row_mask:0xa bank_mask:0xf\n"
        "s_nop 1\n"
        "v_add_u32_dpp v34, v34, v34 row_bcast:31 row_mask:0xc bank_mask:0xf\n"
        "s_nop 0\n"
        "v_readlane_b32 s89, v34, 63\n"
        "s_or_b32 s89, s89, vcc_lo\n"
        "s_or_b32 s89, s89, vcc_hi\n"
        "s_cmp_eq_u32 s89, 0\n"
        "s_cselect_b32 s89, 1, 2\n"
        "v_mov_b32 v12, s89\n"
        "s_mov_b32 s87, 0x200000\n"
        "Lpl31_%=:\n"
        "ds_read_b32 v5, v2 offset:8\n"
        "s_waitcnt lgkmcnt(0)\n"
        "v_readfirstlane_b32 s86, v5\n"
        "s_cmp_eq_u32 s86, 0x1010101\n"
        "s_cbranch_scc1 Lrt30_%=\n"
        "s_sub_u32 s87, s87, 1\n"
        "s_cbranch_scc1 Lrt30_%=\n"
        "s_sleep 1\n"
        "s_branch Lpl31_%=\n"
        "Lrt30_%=:\n"
        "s_add_u32 m0, s11, 0x12c00\n"
        "s_nop 0\n"
        "buffer_load_dwordx4 v22, s[20:23], 0 offen lds\n"
        "s_add_u32 m0, s11, 0x13000\n"
        "s_nop 0\n"
        "buffer_load_dwordx4 v23, s[20:23], 0 offen lds\n"
        "s_add_u32 m0, s11, 0x13400\n"
        "s_nop 0\n"
        "buffer_load_dwordx4 v24, s[20:23], 0 offen lds\n"
        "s_add_u32 m0, s11, 0x13800\n"
        "s_nop 0\n"
        "buffer_load_dwordx4 v25, s[20:23], 0 offen lds\n"
        "s_add_u32 m0, s11, 0x13c00\n"
        "s_nop 0\n"
        "buffer_load_dwordx4 v26, s[20:23], 0 offen lds\n"
        "s_add_u32 m0, s11, 0x14000\n"
        "s_nop 0\n"
        "buffer_load_dwordx4 v27, s[20:23], 0 offen lds\n"
        "s_waitcnt vmcnt(6)\n"
        "ds_write_b8 v3, v12 offset:4\n"
        "s_cmp_lg_u32 s88, 1\n"
        "s_cbranch_scc1 Lhk32_%=\n"
        "s_mov_b32 m0, 0x18c00\n"
        "s_nop 0\n"
        "buffer_load_dwordx4 v28, s[20:23], 0 offen lds\n"
        "Lhk32_%=:\n"
        "ds_read_b128 v[88:91], v14 offset:51200\n"
        "ds_read_b128 v[92:95], v14 offset:52224\n"
        "ds_read_b128 v[96:99], v14 offset:53248\n"
        "ds_read_b128 v[100:103], v14 offset:54272\n"
        "ds_read_b128 v[104:107], v14 offset:55296\n"
        "ds_read_b128 v[108:111], v14 offset:56320\n"
        "s_mov_b32 s87, 0x200000\n"
        "Lpl34_%=:\n"
        "ds_read_b32 v5, v2 offset:4\n"
        "s_waitcnt lgkmcnt(0)\n"
        "v_readfirstlane_b32 s86, v5\n"
        "s_cmp_eq_u32 s86, 0x1010101\n"
        "s_cbranch_scc1 Lrt33_%=\n"
        "s_and_b32 s86, s86, 0x02020202\n"
        "s_cbranch_scc1 Lrelight_%=\n"
        "s_sub_u32 s87, s87, 1\n"
        "s_cbranch_scc1 Lrt33_%=\n"
        "s_sleep 1\n"
        "s_branch Lpl34_%=\n"
        "Lrt33_%=:\n"
        "s_waitcnt vmcnt(0)\n"
        "ds_write_b8 v3, v4 offset:20\n"
        "s_cmp_lg_u32 s3, 4\n"
        "s_cbranch_scc1 Lh0_%=\n"
        "s_sub_u32 s86, s2, 24\n"
        "s_cmp_ge_u32 s86, 36\n"
        "s_cbranch_scc1 Lh0_%=\n"
        "s_cmp_lg_u32 s19, 0\n"
        "s_cbranch_scc1 Lh0_%=\n"
        "buffer_store_dwordx4 v[232:235], v140, s[24:27], s19 offen offset:0 sc1\n"
        "buffer_store_dwordx4 v[236:239], v140, s[24:27], s19 offen offset:64 sc1\n"
        "Lh0_%=:\n"
        "s_cmp_lg_u32 s3, 7\n"
        "s_cbranch_scc1 Lh2_%=\n"
        "s_lshl_b32 s86, s2, 3\n"
        "s_add_u32 s86, s86, 0xc4000\n"
        "v_mov_b32 v12, 0\n"
        "s_mov_b64 exec, 1\n"
        "buffer_store_dwordx2 v[10:11], v12, s[24:27], s86 offen sc1\n"
        "s_mov_b64 exec, -1\n"
        "Lh2_%=:\n"
        "s_mov_b32 s87, 0x200000\n"
        "Lpl36_%=:\n"
        "ds_read_b32 v5, v2 offset:12\n"
        "s_waitcnt lgkmcnt(0)\n"
        "v_readfirstlane_b32 s86, v5\n"
        "s_cmp_eq_u32 s86, 0x1010101\n"
        "s_cbranch_scc1 Lrt35_%=\n"
        "s_sub_u32 s87, s87, 1\n"
        "s_cbranch_scc1 Lrt35_%=\n"
        "s_sleep 1\n"
        "s_branch Lpl36_%=\n"
        "Lrt35_%=:\n"
        "s_add_u32 m0, s11, 0x18f80\n"
        "s_nop 0\n"
        "buffer_load_dwordx4 v22, s[20:23], 0 offen offset:128 lds\n"
        "s_add_u32 m0, s11, 0x19380\n"
        "s_nop 0\n"
        "buffer_load_dwordx4 v23, s[20:23], 0 offen offset:128 lds\n"
        "s_add_u32 m0, s11, 0x19780\n"
        "s_nop 0\n"
        "buffer_load_dwordx4 v24, s[20:23], 0 offen offset:128 lds\n"
        "s_add_u32 m0, s11, 0x19b80\n"
        "s_nop 0\n"
        "buffer_load_dwordx4 v25, s[20:23], 0 offen offset:128 lds\n"
        "s_add_u32 m0, s11, 0x19f80\n"
        "s_nop 0\n"
        "buffer_load_dwordx4 v26, s[20:23], 0 offen offset:128 lds\n"
        "s_add_u32 m0, s11, 0x1a380\n"
        "s_nop 0\n"
        "buffer_load_dwordx4 v27, s[20:23], 0 offen offset:128 lds\n"
        "s_cmp_lg_u32 s88, 2\n"
        "s_cbranch_scc1 Lhk37_%=\n"
        "s_mov_b32 m0, 0x1ef80\n"
        "s_nop 0\n"
        "buffer_load_dwordx4 v28, s[20:23], 0 offen offset:128 lds\n"
        "Lhk37_%=:\n"
        "v_add_u32 v124, v88, v92\n"
        "v_add_u32 v125, v89, v93\n"
        "v_add_u32 v126, v90, v94\n"
        "v_add_u32 v127, v91, v95\n"
        "v_add3_u32 v124, v124, v96, v100\n"
        "v_add3_u32 v125, v125, v97, v101\n"
        "v_add3_u32 v126, v126, v98, v102\n"
        "v_add3_u32 v127, v127, v99, v103\n"
        "v_add3_u32 v124, v124, v104, v108\n"
        "v_add3_u32 v125, v125, v105, v109\n"
        "v_add3_u32 v126, v126, v106, v110\n"
        "v_add3_u32 v127, v127, v107, v111\n"
        "v_mul_lo_u32 v124, v124, v30\n"
        "v_mul_lo_u32 v125, v125, v31\n"
        "v_mul_lo_u32 v126, v126, v32\n"
        "v_mul_lo_u32 v127, v127, v33\n"
        "v_add3_u32 v34, v124, v125, v126\n"
        "v_add_u32 v34, v34, v127\n"
        "v_sub_u32 v34, v34, v35\n"
        "s_nop 1\n"
        "v_add_u32_dpp v34, v34, v34 row_shr:1 row_mask:0xf bank_mask:0xf\n"
        "s_nop 1\n"
        "v_add_u32_dpp v34, v34, v34 row_shr:2 row_mask:0xf bank_mask:0xf\n"
        "s_nop 1\n"
        "v_add_u32_dpp v34, v34, v34 row_shr:4 row_mask:0xf bank_mask:0xf\n"
        "s_nop 1\n"
        "v_add_u32_dpp v34, v34, v34 row_shr:8 row_mask:0xf bank_mask:0xf\n"
        "s_nop 1\n"
        "v_add_u32_dpp v34, v34, v34 row_bcast:15 row_mask:0xa bank_mask:0xf\n"
        "s_nop 1\n"
        "v_add_u32_dpp v34, v34, v34 row_bcast:31 row_mask:0xc bank_mask:0xf\n"
        "s_nop 0\n"
        "v_readlane_b32 s91, v34, 63\n"
        "s_waitcnt vmcnt(0)\n"
        "ds_write_b8 v3, v4 offset:24\n"
        "s_cmp_lg_u32 s3, 4\n"
        "s_cbranch_scc1 Lh1_%=\n"
        "s_sub_u32 s86, s2, 24\n"
        "s_cmp_ge_u32 s86, 36\n"
        "s_cbranch_scc1 Lh1_%=\n"
        "s_cmp_lg_u32 s19, 0\n"
        "s_cbranch_scc1 Lh1_%=\n"
        "s_lshl_b32 s86, s86, 3\n"
        "v_lshlrev_b32 v12, 10, v8\n"
        "s_add_u32 s86, s86, 0x12000\n"
        "s_mov_b64 exec, 0xff\n"
        "buffer_store_dwordx2 v[10:11], v12, s[24:27], s86 offen sc1\n"
        "s_mov_b64 exec, -1\n"
        "Lh1_%=:\n"
        "s_mov_b32 s87, 0x200000\n"
        "Lpl39_%=:\n"
        "ds_read_b32 v5, v2 offset:16\n"
        "s_waitcnt lgkmcnt(0)\n"
        "v_readfirstlane_b32 s86, v5\n"
        "s_cmp_eq_u32 s86, 0x1010101\n"
        "s_cbranch_scc1 Lrt38_%=\n"
        "s_sub_u32 s87, s87, 1\n"
        "s_cbranch_scc1 Lrt38_%=\n"
        "s_sleep 1\n"
        "s_branch Lpl39_%=\n"
        "Lrt38_%=:\n"
        "s_add_u32 m0, s11, 0x1f300\n"
        "s_nop 0\n"
        "buffer_load_dwordx4 v22, s[20:23], 0 offen offset:256 lds\n"
        "s_add_u32 m0, s11, 0x1f700\n"
        "s_nop 0\n"
        "buffer_load_dwordx4 v23, s[20:23], 0 offen offset:256 lds\n"
        "s_add_u32 m0, s11, 0x1fb00\n"
        "s_nop 0\n"
        "buffer_load_dwordx4 v24, s[20:23], 0 offen offset:256 lds\n"
        "s_add_u32 m0, s11, 0x1ff00\n"
        "s_nop 0\n"
        "buffer_load_dwordx4 v25, s[20:23], 0 offen offset:256 lds\n"
        "s_add_u32 m0, s11, 0x20300\n"
        "s_nop 0\n"
        "buffer_load_dwordx4 v26, s[20:23], 0 offen offset:256 lds\n"
        "s_add_u32 m0, s11, 0x20700\n"
        "s_nop 0\n"
        "buffer_load_dwordx4 v27, s[20:23], 0 offen offset:256 lds\n"
        "s_cmp_lg_u32 s88, 1\n"
        "s_cbranch_scc1 Lhk40_%=\n"
        "s_mov_b32 m0, 0x25300\n"
        "s_nop 0\n"
        "buffer_load_dwordx4 v28, s[20:23], 0 offen offset:256 lds\n"
        "Lhk40_%=:\n"
        "v_mov_b32 v12, 0xbc000\n"
        "buffer_load_dwordx2 v[38:39], v12, s[24:27], 0 offen sc1\n"
        "ds_read2_b32 v[112:113], v16 offset0:0 offset1:2\n"
        "ds_read2_b32 v[114:115], v16 offset0:4 offset1:6\n"
        "v_add_u32 v16, 0x6400, v16\n"
        "ds_read2_b32 v[116:117], v16 offset0:0 offset1:2\n"
        "ds_read2_b32 v[118:119], v16 offset0:4 offset1:6\n"
        "v_add_u32 v16, 0x6400, v16\n"
        "ds_read2_b32 v[120:121], v16 offset0:0 offset1:2\n"
        "ds_read2_b32 v[122:123], v16 offset0:4 offset1:6\n"
        "s_setprio 3\n"
        "s_waitcnt vmcnt(0)\n"
        "ds_write_b8 v3, v4 offset:28\n"
        "s_cmp_lg_u32 s3, 7\n"
        "s_cbranch_scc1 Lh3_%=\n"
        "s_cmp_lg_u32 s2, 0\n"
        "s_cbranch_scc1 Lh3_%=\n"
        "s_mov_b64 exec, 1\n"
        "buffer_store_dwordx2 v[10:11], v12, s[24:27], 0 offen sc1\n"
        "s_mov_b64 exec, -1\n"
        "Lh3_%=:\n"
        "v_min_u32 v12, 17, v8\n"
        "v_lshlrev_b32 v12, 4, v12\n"
        "s_and_b32 s89, s2, 7\n"
        "s_lshl_b32 s89, s89, 10\n"
        "s_add_u32 s89, s89, 0x12000\n"
        "s_cmp_lg_u32 s3, 5\n"
        "s_cbranch_scc1 Lh7_%=\n"
        "s_sleep 12\n"
        "buffer_load_dwordx4 v[160:163], v12, s[24:27], s89 offen sc1\n"
        "Lh7_%=:\n"
        "s_mov_b32 s86, s91\n"
        "v_mov_b32 v15, 1\n"
        "s_cmp_lg_u32 s86, 0\n"
        "s_cbranch_scc1 Lh6_%=\n"
        "s_cmp_lg_u32 s3, 5\n"
        "s_cbranch_scc1 Lh5_%=\n"
        "s_cmp_lg_u32 s19, 0\n"
        "s_cbranch_scc1 Lh5_%=\n"
        "s_mov_b32 s87, 300\n"
        "Lm2p41_%=:\n"
        "s_waitcnt vmcnt(0)\n"
        "v_cmp_ne_u64 vcc, s[6:7], v[160:161]\n"
        "v_cmp_ne_u64 s[10:11], s[6:7], v[162:163]\n"
        "s_nop 1\n"
        "s_or_b64 vcc, vcc, s[10:11]\n"
        "s_cbranch_scc0 Lh5_%=\n"
        "s_sleep 8\n"
        "buffer_load_dwordx4 v[160:163], v12, s[24:27], s89 offen sc1\n"
        "s_sub_u32 s87, s87, 1\n"
        "s_cbranch_scc0 Lm2p41_%=\n"
        "Lh6_%=:\n"
        "v_mov_b32 v15, 2\n"
        "ds_write_b8 v3, v15 offset:44\n"
        "Lh5_%=:\n"
        "ds_write_b8 v3, v15 offset:56\n"
        "s_waitcnt vmcnt(0)\n"
        "s_setprio 0\n"
        "v_and_b32 v12, 1, v0\n"
        "v_mul_u32_u24 v17, 0xaaaa, v12\n"
        "v_add_u32 v17, 0x4444, v17\n"
        "v_bfe_u32 v15, v0, 2, 2\n"
        "v_mul_u32_u24 v16, 384, v15\n"
        "v_lshl_add_u32 v16, v12, 6, v16\n"
        "v_add_u32 v16, v16, v19\n"
        "v_add_u32 v16, 0x25800, v16\n"
        "v_mul_u32_u24 v13, 24, v19\n"
        "v_lshl_add_u32 v13, v15, 3, v13\n"
        "v_lshl_add_u32 v13, v12, 1, v13\n"
        "v_and_b32 v12, 2, v0\n"
        "v_lshl_add_u32 v13, v12, 5, v13\n"
        "v_add_u32 v13, 0x25800, v13\n"
        "s_and_b32 s86, s3, 2\n"
        "s_lshl_b32 s86, s86, 4\n"
        "s_and_b32 s87, s3, 1\n"
        "s_lshl2_add_u32 s86, s87, s86\n"
        "v_add_u32 v15, s86, v13\n"
        "v_mov_b64 v[124:125], 0\n"
        "v_mov_b64 v[128:129], 0\n"
        "v_mov_b64 v[132:133], 0\n"
        "s_cmp_lg_u32 s3, 4\n"
        "s_cbranch_scc1 Lhm2_%=\n"
        "v_and_b32 v20, 3, v0\n"
        "v_lshlrev_b32 v20, 3, v20\n"
        "v_lshlrev_b32_sdwa v152, v18, v200 dst_sel:DWORD dst_unused:UNUSED_PAD src0_sel:DWORD src1_sel:WORD_0\n"
        "v_xor_b32 v152, v152, v19\n"
        "v_xor_b32 v153, 64, v152\n"
        "v_lshlrev_b32_sdwa v154, v18, v200 dst_sel:DWORD dst_unused:UNUSED_PAD src0_sel:DWORD src1_sel:WORD_1\n"
        "v_xor_b32 v154, v154, v19\n"
        "v_xor_b32 v155, 64, v154\n"
        "v_lshlrev_b32_sdwa v156, v18, v201 dst_sel:DWORD dst_unused:UNUSED_PAD src0_sel:DWORD src1_sel:WORD_0\n"
        "v_xor_b32 v156, v156, v19\n"
        "v_xor_b32 v157, 64, v156\n"
        "v_lshlrev_b32_sdwa v158, v18, v201 dst_sel:DWORD dst_unused:UNUSED_PAD src0_sel:DWORD src1_sel:WORD_1\n"
        "v_xor_b32 v158, v158, v19\n"
        "v_xor_b32 v159, 64, v158\n"
        "v_lshlrev_b32_sdwa v136, v18, v208 dst_sel:DWORD dst_unused:UNUSED_PAD src0_sel:DWORD src1_sel:WORD_0\n"
        "v_or_b32 v136, v136, v20\n"
        "v_xor_b32 v140, 32, v136\n"
        "v_xor_b32 v144, 64, v136\n"
        "v_xor_b32 v148, 96, v136\n"
        "v_lshlrev_b32_sdwa v137, v18, v208 dst_sel:DWORD dst_unused:UNUSED_PAD src0_sel:DWORD src1_sel:WORD_1\n"
        "v_or_b32 v137, v137, v20\n"
        "v_xor_b32 v141, 32, v137\n"
        "v_xor_b32 v145, 64, v137\n"
        "v_xor_b32 v149, 96, v137\n"
        "v_lshlrev_b32_sdwa v138, v18, v209 dst_sel:DWORD dst_unused:UNUSED_PAD src0_sel:DWORD src1_sel:WORD_0\n"
        "v_or_b32 v138, v138, v20\n"
        "v_xor_b32 v142, 32, v138\n"
        "v_xor_b32 v146, 64, v138\n"
        "v_xor_b32 v150, 96, v138\n"
        "v_lshlrev_b32_sdwa v139, v18, v209 dst_sel:DWORD dst_unused:UNUSED_PAD src0_sel:DWORD src1_sel:WORD_1\n"
        "v_or_b32 v139, v139, v20\n"
        "v_xor_b32 v143, 32, v139\n"
        "v_xor_b32 v147, 64, v139\n"
        "v_xor_b32 v151, 96, v139\n"
        "s_setprio 0\n"
        "ds_read_b64_tr_b16 v[160:161], v136 offset:0\n"
        "ds_read_b64_tr_b16 v[162:163], v137 offset:0\n"
        "ds_read_b64_tr_b16 v[164:165], v138 offset:0\n"
        "ds_read_b64_tr_b16 v[166:167], v139 offset:0\n"
        "ds_read_b64_tr_b16 v[168:169], v140 offset:0\n"
        "ds_read_b64_tr_b16 v[170:171], v141 offset:0\n"
        "s_waitcnt lgkmcnt(12)\n"
        "ds_read_b64_tr_b16 v[172:173], v142 offset:0\n"
        "ds_read_b64_tr_b16 v[174:175], v143 offset:0\n"
        "ds_read_b64_tr_b16 v[176:177], v144 offset:0\n"
        "s_waitcnt lgkmcnt(12)\n"
        "ds_read_b64_tr_b16 v[178:179], v145 offset:0\n"
        "ds_read_b64_tr_b16 v[180:181], v146 offset:0\n"
        "ds_read_b64_tr_b16 v[182:183], v147 offset:0\n"
        "s_waitcnt lgkmcnt(12)\n"
        "ds_read_b64_tr_b16 v[184:185], v148 offset:0\n"
        "ds_read_b64_tr_b16 v[186:187], v149 offset:0\n"
        "ds_read_b64_tr_b16 v[188:189], v150 offset:0\n"
        "s_waitcnt lgkmcnt(12)\n"
        "ds_read_b64_tr_b16 v[190:191], v151 offset:0\n"
        "ds_read_b64_tr_b16 v[192:193], v136 offset:25600\n"
        "ds_read_b64_tr_b16 v[194:195], v137 offset:25600\n"
        "s_waitcnt lgkmcnt(12)\n"
        "ds_read_b64_tr_b16 v[196:197], v138 offset:25600\n"
        "ds_read_b64_tr_b16 v[198:199], v139 offset:25600\n"
        "ds_read_b64_tr_b16 v[200:201], v140 offset:25600\n"
        "s_waitcnt lgkmcnt(12)\n"
        "ds_read_b64_tr_b16 v[202:203], v141 offset:25600\n"
        "ds_read_b64_tr_b16 v[204:205], v142 offset:25600\n"
        "ds_read_b64_tr_b16 v[206:207], v143 offset:25600\n"
        "s_waitcnt lgkmcnt(12)\n"
        "ds_read_b64_tr_b16 v[208:209], v144 offset:25600\n"
        "ds_read_b64_tr_b16 v[210:211], v145 offset:25600\n"
        "ds_read_b64_tr_b16 v[212:213], v146 offset:25600\n"
        "s_waitcnt lgkmcnt(12)\n"
        "ds_read_b64_tr_b16 v[214:215], v147 offset:25600\n"
        "ds_read_b64_tr_b16 v[216:217], v148 offset:25600\n"
        "ds_read_b64_tr_b16 v[218:219], v149 offset:25600\n"
        "s_waitcnt lgkmcnt(12)\n"
        "ds_read_b64_tr_b16 v[220:221], v150 offset:25600\n"
        "ds_read_b64_tr_b16 v[222:223], v151 offset:25600\n"
        "ds_read_b64_tr_b16 v[224:225], v136 offset:51200\n"
        "s_waitcnt lgkmcnt(12)\n"
        "ds_read_b64_tr_b16 v[226:227], v137 offset:51200\n"
        "ds_read_b64_tr_b16 v[228:229], v138 offset:51200\n"
        "ds_read_b64_tr_b16 v[230:231], v139 offset:51200\n"
        "s_waitcnt lgkmcnt(12)\n"
        "ds_read_b64_tr_b16 v[232:233], v140 offset:51200\n"
        "ds_read_b64_tr_b16 v[234:235], v141 offset:51200\n"
        "ds_read_b64_tr_b16 v[236:237], v142 offset:51200\n"
        "s_waitcnt lgkmcnt(12)\n"
        "ds_read_b64_tr_b16 v[238:239], v143 offset:51200\n"
        "ds_read_b64_tr_b16 v[240:241], v144 offset:51200\n"
        "ds_read_b64_tr_b16 v[242:243], v145 offset:51200\n"
        "s_waitcnt lgkmcnt(12)\n"
        "ds_read_b64_tr_b16 v[244:245], v146 offset:51200\n"
        "ds_read_b64_tr_b16 v[246:247], v147 offset:51200\n"
        "ds_read_b64_tr_b16 v[248:249], v148 offset:51200\n"
        "s_waitcnt lgkmcnt(12)\n"
        "ds_read_b64_tr_b16 v[250:251], v149 offset:51200\n"
        "ds_read_b64_tr_b16 v[252:253], v150 offset:51200\n"
        "ds_read_b64_tr_b16 v[254:255], v151 offset:51200\n"
        "v_mov_b32 v12, 0xf149f2ca\n"
        "v_cndmask_b32 v136, 0, v12, s[40:41]\n"
        "v_mov_b32 v137, 0\n"
        "v_cndmask_b32 v140, 0, v12, s[42:43]\n"
        "v_mov_b32 v141, 0\n"
        "v_cndmask_b32 v144, 0, v12, s[44:45]\n"
        "v_mov_b32 v145, 0\n"
        "v_cndmask_b32 v148, 0, v12, s[46:47]\n"
        "v_mov_b32 v149, 0\n"
        "s_setprio 3\n"
        "s_sleep 36\n"
        "s_mov_b32 s87, 0x200000\n"
        "Lpl43_%=:\n"
        "ds_read_b32 v5, v2 offset:56\n"
        "s_waitcnt lgkmcnt(0)\n"
        "v_readfirstlane_b32 s86, v5\n"
        "s_cmp_eq_u32 s86, 0x1010101\n"
        "s_cbranch_scc1 Lrt42_%=\n"
        "s_and_b32 s86, s86, 0x02020202\n"
        "s_cbranch_scc1 Lfallback_%=\n"
        "s_sub_u32 s87, s87, 1\n"
        "s_cbranch_scc1 Lrt42_%=\n"
        "s_sleep 4\n"
        "s_branch Lpl43_%=\n"
        "Lrt42_%=:\n"
        "s_mov_b32 s87, 0x200000\n"
        "Lpl45_%=:\n"
        "ds_read_b32 v5, v2 offset:40\n"
        "s_waitcnt lgkmcnt(0)\n"
        "v_readfirstlane_b32 s86, v5\n"
        "s_cmp_eq_u32 s86, 0x1010101\n"
        "s_cbranch_scc1 Lrt44_%=\n"
        "s_sub_u32 s87, s87, 1\n"
        "s_cbranch_scc1 Lrt44_%=\n"
        "s_sleep 3\n"
        "s_branch Lpl45_%=\n"
        "Lrt44_%=:\n"
        "v_smfmac_f32_16x16x64_bf16 v[124:127], v[112:115], v[40:47], v17\n"
        "v_smfmac_f32_16x16x64_bf16 v[124:127], v[116:119], v[64:71], v17\n"
        "v_smfmac_f32_16x16x64_bf16 v[124:127], v[120:123], v[88:95], v17\n"
        "v_smfmac_f32_16x16x64_bf16 v[128:131], v[112:115], v[48:55], v17\n"
        "v_smfmac_f32_16x16x64_bf16 v[128:131], v[116:119], v[72:79], v17\n"
        "v_smfmac_f32_16x16x64_bf16 v[128:131], v[120:123], v[96:103], v17\n"
        "v_smfmac_f32_16x16x64_bf16 v[132:135], v[112:115], v[56:63], v17\n"
        "v_smfmac_f32_16x16x64_bf16 v[132:135], v[116:119], v[80:87], v17\n"
        "v_smfmac_f32_16x16x64_bf16 v[132:135], v[120:123], v[104:111], v17\n"
        "s_nop 1\n"
        "v_add_f32 v124, v124, v125\n"
        "s_nop 1\n"
        "v_add_f32 v128, v128, v129\n"
        "s_nop 1\n"
        "v_add_f32 v132, v132, v133\n"
        "v_cvt_pk_bf16_f32 v124, v124, v128\n"
        "v_cvt_pk_bf16_f32 v132, v132, v132\n"
        "ds_write_b16 v15, v124\n"
        "ds_write_b16_d16_hi v15, v124 offset:128\n"
        "ds_write_b16 v15, v132 offset:256\n"
        "ds_write_b8 v3, v4 offset:60\n"
        "ds_read_b128 v[40:43], v152 offset:0\n"
        "ds_read_b128 v[44:47], v153 offset:0\n"
        "ds_read_b128 v[48:51], v152 offset:25600\n"
        "ds_read_b128 v[52:55], v153 offset:25600\n"
        "ds_read_b128 v[56:59], v152 offset:51200\n"
        "ds_read_b128 v[60:63], v153 offset:51200\n"
        "ds_read_b128 v[64:67], v154 offset:0\n"
        "ds_read_b128 v[68:71], v155 offset:0\n"
        "ds_read_b128 v[72:75], v154 offset:25600\n"
        "ds_read_b128 v[76:79], v155 offset:25600\n"
        "ds_read_b128 v[80:83], v154 offset:51200\n"
        "s_waitcnt lgkmcnt(12)\n"
        "ds_read_b128 v[84:87], v155 offset:51200\n"
        "ds_read_b128 v[88:91], v156 offset:0\n"
        "ds_read_b128 v[92:95], v157 offset:0\n"
        "s_waitcnt lgkmcnt(12)\n"
        "ds_read_b128 v[96:99], v156 offset:25600\n"
        "ds_read_b128 v[100:103], v157 offset:25600\n"
        "ds_read_b128 v[104:107], v156 offset:51200\n"
        "s_waitcnt lgkmcnt(12)\n"
        "ds_read_b128 v[108:111], v157 offset:51200\n"
        "ds_read_b128 v[112:115], v158 offset:0\n"
        "ds_read_b128 v[116:119], v159 offset:0\n"
        "s_waitcnt lgkmcnt(12)\n"
        "ds_read_b128 v[120:123], v158 offset:25600\n"
        "ds_read_b128 v[124:127], v159 offset:25600\n"
        "ds_read_b128 v[128:131], v158 offset:51200\n"
        "s_waitcnt lgkmcnt(12)\n"
        "ds_read_b128 v[132:135], v159 offset:51200\n"
        "s_mov_b32 s87, 0x200000\n"
        "Lpl47_%=:\n"
        "ds_read_b32 v5, v2 offset:60\n"
        "s_waitcnt lgkmcnt(0)\n"
        "v_readfirstlane_b32 s86, v5\n"
        "s_cmp_eq_u32 s86, 0x1010101\n"
        "s_cbranch_scc1 Lrt46_%=\n"
        "s_sub_u32 s87, s87, 1\n"
        "s_cbranch_scc1 Lrt46_%=\n"
        "s_sleep 1\n"
        "s_branch Lpl47_%=\n"
        "Lrt46_%=:\n"
        "ds_read_b128 v[22:25], v16 offset:0\n"
        "ds_read_b128 v[26:29], v16 offset:128\n"
        "ds_read_b128 v[30:33], v16 offset:256\n"
        "s_waitcnt lgkmcnt(2)\n"
        "v_smfmac_f32_16x16x64_bf16 v[136:139], v[22:25], v[40:47], v17\n"
        "s_waitcnt lgkmcnt(1)\n"
        "v_smfmac_f32_16x16x64_bf16 v[136:139], v[26:29], v[48:55], v17\n"
        "s_waitcnt lgkmcnt(0)\n"
        "v_smfmac_f32_16x16x64_bf16 v[136:139], v[30:33], v[56:63], v17\n"
        "v_smfmac_f32_16x16x64_bf16 v[140:143], v[22:25], v[64:71], v17\n"
        "v_smfmac_f32_16x16x64_bf16 v[140:143], v[26:29], v[72:79], v17\n"
        "v_smfmac_f32_16x16x64_bf16 v[140:143], v[30:33], v[80:87], v17\n"
        "v_smfmac_f32_16x16x64_bf16 v[144:147], v[22:25], v[88:95], v17\n"
        "v_smfmac_f32_16x16x64_bf16 v[144:147], v[26:29], v[96:103], v17\n"
        "v_smfmac_f32_16x16x64_bf16 v[144:147], v[30:33], v[104:111], v17\n"
        "v_smfmac_f32_16x16x64_bf16 v[148:151], v[22:25], v[112:119], v17\n"
        "v_smfmac_f32_16x16x64_bf16 v[148:151], v[26:29], v[120:127], v17\n"
        "v_smfmac_f32_16x16x64_bf16 v[148:151], v[30:33], v[128:135], v17\n"
        "s_setprio 0\n"
        "v_mov_b64 v[40:41], 0\n"
        "v_mov_b64 v[44:45], 0\n"
        "v_mov_b64 v[48:49], 0\n"
        "v_mov_b64 v[52:53], 0\n"
        "v_mov_b64 v[56:57], 0\n"
        "v_mov_b64 v[60:61], 0\n"
        "v_mov_b64 v[64:65], 0\n"
        "v_mov_b64 v[68:69], 0\n"
        "v_add_f32 v136, v136, v137\n"
        "v_add_f32 v140, v140, v141\n"
        "v_add_f32 v144, v144, v145\n"
        "v_add_f32 v148, v148, v149\n"
        "v_max3_f32 v10, v136, v140, v144\n"
        "v_max_f32 v10, v10, v148\n"
        "v_mov_b64 v[72:73], 0\n"
        "v_mov_b64 v[76:77], 0\n"
        "v_max_f32_dpp v10, v10, v10 row_ror:8 row_mask:0xf bank_mask:0xf\n"
        "v_mov_b64 v[80:81], 0\n"
        "v_mov_b64 v[84:85], 0\n"
        "v_max_f32_dpp v10, v10, v10 row_ror:4 row_mask:0xf bank_mask:0xf\n"
        "s_nop 1\n"
        "v_max_f32_dpp v10, v10, v10 row_ror:2 row_mask:0xf bank_mask:0xf\n"
        "s_nop 1\n"
        "v_max_f32_dpp v10, v10, v10 row_ror:1 row_mask:0xf bank_mask:0xf\n"
        "v_sub_f32 v136, v136, v10\n"
        "v_sub_f32 v140, v140, v10\n"
        "v_sub_f32 v144, v144, v10\n"
        "v_sub_f32 v148, v148, v10\n"
        "v_exp_f32 v136, v136\n"
        "v_exp_f32 v140, v140\n"
        "v_exp_f32 v144, v144\n"
        "v_exp_f32 v148, v148\n"
        "v_add_f32 v10, v136, v140\n"
        "v_add_f32 v11, v144, v148\n"
        "v_add_f32 v10, v10, v11\n"
        "s_nop 1\n"
        "v_add_f32_dpp v10, v10, v10 row_ror:8 row_mask:0xf bank_mask:0xf\n"
        "s_nop 1\n"
        "v_add_f32_dpp v10, v10, v10 row_ror:4 row_mask:0xf bank_mask:0xf\n"
        "s_nop 1\n"
        "v_add_f32_dpp v10, v10, v10 row_ror:2 row_mask:0xf bank_mask:0xf\n"
        "s_nop 1\n"
        "v_add_f32_dpp v10, v10, v10 row_ror:1 row_mask:0xf bank_mask:0xf\n"
        "v_rcp_f32 v10, v10\n"
        "s_nop 0\n"
        "v_mul_f32 v136, v136, v10\n"
        "v_mul_f32 v140, v140, v10\n"
        "v_mul_f32 v144, v144, v10\n"
        "v_mul_f32 v148, v148, v10\n"
        "v_cvt_pk_bf16_f32 v10, v136, v140\n"
        "v_cvt_pk_bf16_f32 v11, v144, v148\n"
        "ds_write_b16 v13, v10 offset:2048\n"
        "ds_write_b16_d16_hi v13, v10 offset:2052\n"
        "ds_write_b16 v13, v11 offset:2080\n"
        "ds_write_b16_d16_hi v13, v11 offset:2084\n"
        "ds_read_b128 v[22:25], v16 offset:2048\n"
        "s_setprio 3\n"
        "s_waitcnt lgkmcnt(0)\n"
        "v_smfmac_f32_16x16x64_bf16 v[40:43], v[22:25], v[160:167], v17\n"
        "v_smfmac_f32_16x16x64_bf16 v[44:47], v[22:25], v[168:175], v17\n"
        "v_smfmac_f32_16x16x64_bf16 v[48:51], v[22:25], v[176:183], v17\n"
        "v_smfmac_f32_16x16x64_bf16 v[52:55], v[22:25], v[184:191], v17\n"
        "v_smfmac_f32_16x16x64_bf16 v[56:59], v[22:25], v[192:199], v17\n"
        "v_smfmac_f32_16x16x64_bf16 v[60:63], v[22:25], v[200:207], v17\n"
        "v_smfmac_f32_16x16x64_bf16 v[64:67], v[22:25], v[208:215], v17\n"
        "v_smfmac_f32_16x16x64_bf16 v[68:71], v[22:25], v[216:223], v17\n"
        "v_smfmac_f32_16x16x64_bf16 v[72:75], v[22:25], v[224:231], v17\n"
        "v_smfmac_f32_16x16x64_bf16 v[76:79], v[22:25], v[232:239], v17\n"
        "v_smfmac_f32_16x16x64_bf16 v[80:83], v[22:25], v[240:247], v17\n"
        "v_smfmac_f32_16x16x64_bf16 v[84:87], v[22:25], v[248:255], v17\n"
        "s_setprio 0\n"
        "v_add_f32 v40, v40, v41\n"
        "v_add_f32 v44, v44, v45\n"
        "v_add_f32 v48, v48, v49\n"
        "v_add_f32 v52, v52, v53\n"
        "v_add_f32 v56, v56, v57\n"
        "v_add_f32 v60, v60, v61\n"
        "v_add_f32 v64, v64, v65\n"
        "v_add_f32 v68, v68, v69\n"
        "v_add_f32 v72, v72, v73\n"
        "v_add_f32 v76, v76, v77\n"
        "v_add_f32 v80, v80, v81\n"
        "v_add_f32 v84, v84, v85\n"
        "v_cvt_pk_bf16_f32 v40, v40, v44\n"
        "v_cvt_pk_bf16_f32 v48, v48, v52\n"
        "v_cvt_pk_bf16_f32 v56, v56, v60\n"
        "v_cvt_pk_bf16_f32 v64, v64, v68\n"
        "v_cvt_pk_bf16_f32 v72, v72, v76\n"
        "v_cvt_pk_bf16_f32 v80, v80, v84\n"
        "ds_write_b16 v13, v40 offset:4096\n"
        "ds_write_b16_d16_hi v13, v40 offset:4100\n"
        "ds_write_b16 v13, v48 offset:4128\n"
        "ds_write_b16_d16_hi v13, v48 offset:4132\n"
        "ds_write_b16 v13, v56 offset:4224\n"
        "ds_write_b16_d16_hi v13, v56 offset:4228\n"
        "ds_write_b16 v13, v64 offset:4256\n"
        "ds_write_b16_d16_hi v13, v64 offset:4260\n"
        "ds_write_b16 v13, v72 offset:4352\n"
        "ds_write_b16_d16_hi v13, v72 offset:4356\n"
        "ds_write_b16 v13, v80 offset:4384\n"
        "ds_write_b16_d16_hi v13, v80 offset:4388\n"
        "ds_write_b8 v2, v4 offset:64\n"
        "s_branch Lhend_%=\n"
        "Lhm2_%=:\n"
        "v_mov_b64 v[136:137], 0\n"
        "v_mov_b64 v[140:141], 0\n"
        "v_mov_b64 v[144:145], 0\n"
        "v_mov_b64 v[148:149], 0\n"
        "s_setprio 3\n"
        "s_sleep 10\n"
        "s_mov_b32 s87, 0x200000\n"
        "Lpl49_%=:\n"
        "ds_read_b32 v5, v2 offset:56\n"
        "s_waitcnt lgkmcnt(0)\n"
        "v_readfirstlane_b32 s86, v5\n"
        "s_cmp_eq_u32 s86, 0x1010101\n"
        "s_cbranch_scc1 Lrt48_%=\n"
        "s_and_b32 s86, s86, 0x02020202\n"
        "s_cbranch_scc1 Lfallback_%=\n"
        "s_sub_u32 s87, s87, 1\n"
        "s_cbranch_scc1 Lrt48_%=\n"
        "s_sleep 2\n"
        "s_branch Lpl49_%=\n"
        "Lrt48_%=:\n"
        "s_sub_u32 s88, s88, 1\n"
        "s_lshl_b32 s11, s88, 13\n"
        "s_add_u32 s10, s11, s19\n"
        "v_add_u32 v14, s10, v1\n"
        "v_lshlrev_b32 v12, 3, v1\n"
        "s_add_u32 s87, s10, 0\n"
        "buffer_load_dword v244, v12, s[24:27], s87 offen sc1\n"
        "s_mov_b32 s87, 0x200000\n"
        "Lpl51_%=:\n"
        "ds_read_b32 v5, v2 offset:32\n"
        "s_waitcnt lgkmcnt(0)\n"
        "v_readfirstlane_b32 s86, v5\n"
        "s_cmp_eq_u32 s86, 0x1010101\n"
        "s_cbranch_scc1 Lrt50_%=\n"
        "s_sub_u32 s87, s87, 1\n"
        "s_cbranch_scc1 Lrt50_%=\n"
        "s_sleep 2\n"
        "s_branch Lpl51_%=\n"
        "Lrt50_%=:\n"
        "s_mov_b32 s87, 0x0\n"
        "s_add_u32 m0, s11, 0x12c00\n"
        "s_nop 0\n"
        "buffer_load_dwordx4 v14, s[24:27], s87 offen offset:0 sc1 lds\n"
        "s_add_u32 m0, s11, 0x12c00\n"
        "s_nop 0\n"
        "buffer_load_dwordx4 v14, s[24:27], s87 offen offset:1024 sc1 lds\n"
        "s_add_u32 m0, s11, 0x12c00\n"
        "s_nop 0\n"
        "buffer_load_dwordx4 v14, s[24:27], s87 offen offset:2048 sc1 lds\n"
        "s_add_u32 m0, s11, 0x12c00\n"
        "s_nop 0\n"
        "buffer_load_dwordx4 v14, s[24:27], s87 offen offset:3072 sc1 lds\n"
        "s_mov_b32 s87, 0x1000\n"
        "s_add_u32 m0, s11, 0x13c00\n"
        "s_nop 0\n"
        "buffer_load_dwordx4 v14, s[24:27], s87 offen offset:0 sc1 lds\n"
        "s_add_u32 m0, s11, 0x13c00\n"
        "s_nop 0\n"
        "buffer_load_dwordx4 v14, s[24:27], s87 offen offset:1024 sc1 lds\n"
        "s_add_u32 m0, s11, 0x13c00\n"
        "s_nop 0\n"
        "buffer_load_dwordx4 v14, s[24:27], s87 offen offset:2048 sc1 lds\n"
        "s_add_u32 m0, s11, 0x13c00\n"
        "s_nop 0\n"
        "buffer_load_dwordx4 v14, s[24:27], s87 offen offset:3072 sc1 lds\n"
        "s_waitcnt vmcnt(0)\n"
        "ds_write_b8 v3, v4 offset:44\n"
        "s_mov_b32 s87, 0x200000\n"
        "Lpl53_%=:\n"
        "ds_read_b32 v5, v2 offset:36\n"
        "s_waitcnt lgkmcnt(0)\n"
        "v_readfirstlane_b32 s86, v5\n"
        "s_cmp_eq_u32 s86, 0x1010101\n"
        "s_cbranch_scc1 Lrt52_%=\n"
        "s_sub_u32 s87, s87, 1\n"
        "s_cbranch_scc1 Lrt52_%=\n"
        "s_sleep 2\n"
        "s_branch Lpl53_%=\n"
        "Lrt52_%=:\n"
        "s_mov_b32 s87, 0x6000\n"
        "s_add_u32 m0, s11, 0x19000\n"
        "s_nop 0\n"
        "buffer_load_dwordx4 v14, s[24:27], s87 offen offset:0 sc1 lds\n"
        "s_add_u32 m0, s11, 0x19000\n"
        "s_nop 0\n"
        "buffer_load_dwordx4 v14, s[24:27], s87 offen offset:1024 sc1 lds\n"
        "s_add_u32 m0, s11, 0x19000\n"
        "s_nop 0\n"
        "buffer_load_dwordx4 v14, s[24:27], s87 offen offset:2048 sc1 lds\n"
        "s_add_u32 m0, s11, 0x19000\n"
        "s_nop 0\n"
        "buffer_load_dwordx4 v14, s[24:27], s87 offen offset:3072 sc1 lds\n"
        "s_mov_b32 s87, 0x7000\n"
        "s_add_u32 m0, s11, 0x1a000\n"
        "s_nop 0\n"
        "buffer_load_dwordx4 v14, s[24:27], s87 offen offset:0 sc1 lds\n"
        "s_add_u32 m0, s11, 0x1a000\n"
        "s_nop 0\n"
        "buffer_load_dwordx4 v14, s[24:27], s87 offen offset:1024 sc1 lds\n"
        "s_add_u32 m0, s11, 0x1a000\n"
        "s_nop 0\n"
        "buffer_load_dwordx4 v14, s[24:27], s87 offen offset:2048 sc1 lds\n"
        "s_add_u32 m0, s11, 0x1a000\n"
        "s_nop 0\n"
        "buffer_load_dwordx4 v14, s[24:27], s87 offen offset:3072 sc1 lds\n"
        "s_waitcnt vmcnt(0)\n"
        "ds_write_b8 v3, v4 offset:48\n"
        "s_mov_b32 s87, 0x200000\n"
        "Lpl55_%=:\n"
        "ds_read_b32 v5, v2 offset:40\n"
        "s_waitcnt lgkmcnt(0)\n"
        "v_readfirstlane_b32 s86, v5\n"
        "s_cmp_eq_u32 s86, 0x1010101\n"
        "s_cbranch_scc1 Lrt54_%=\n"
        "s_sub_u32 s87, s87, 1\n"
        "s_cbranch_scc1 Lrt54_%=\n"
        "s_sleep 2\n"
        "s_branch Lpl55_%=\n"
        "Lrt54_%=:\n"
        "s_mov_b32 s87, 0xc000\n"
        "s_add_u32 m0, s11, 0x1f400\n"
        "s_nop 0\n"
        "buffer_load_dwordx4 v14, s[24:27], s87 offen offset:0 sc1 lds\n"
        "s_add_u32 m0, s11, 0x1f400\n"
        "s_nop 0\n"
        "buffer_load_dwordx4 v14, s[24:27], s87 offen offset:1024 sc1 lds\n"
        "s_add_u32 m0, s11, 0x1f400\n"
        "s_nop 0\n"
        "buffer_load_dwordx4 v14, s[24:27], s87 offen offset:2048 sc1 lds\n"
        "s_add_u32 m0, s11, 0x1f400\n"
        "s_nop 0\n"
        "buffer_load_dwordx4 v14, s[24:27], s87 offen offset:3072 sc1 lds\n"
        "s_mov_b32 s87, 0xd000\n"
        "s_add_u32 m0, s11, 0x20400\n"
        "s_nop 0\n"
        "buffer_load_dwordx4 v14, s[24:27], s87 offen offset:0 sc1 lds\n"
        "s_add_u32 m0, s11, 0x20400\n"
        "s_nop 0\n"
        "buffer_load_dwordx4 v14, s[24:27], s87 offen offset:1024 sc1 lds\n"
        "s_add_u32 m0, s11, 0x20400\n"
        "s_nop 0\n"
        "buffer_load_dwordx4 v14, s[24:27], s87 offen offset:2048 sc1 lds\n"
        "s_add_u32 m0, s11, 0x20400\n"
        "s_nop 0\n"
        "buffer_load_dwordx4 v14, s[24:27], s87 offen offset:3072 sc1 lds\n"
        "v_smfmac_f32_16x16x64_bf16 v[124:127], v[112:115], v[40:47], v17\n"
        "v_smfmac_f32_16x16x64_bf16 v[124:127], v[116:119], v[64:71], v17\n"
        "v_smfmac_f32_16x16x64_bf16 v[124:127], v[120:123], v[88:95], v17\n"
        "v_smfmac_f32_16x16x64_bf16 v[128:131], v[112:115], v[48:55], v17\n"
        "v_smfmac_f32_16x16x64_bf16 v[128:131], v[116:119], v[72:79], v17\n"
        "v_smfmac_f32_16x16x64_bf16 v[128:131], v[120:123], v[96:103], v17\n"
        "v_smfmac_f32_16x16x64_bf16 v[132:135], v[112:115], v[56:63], v17\n"
        "v_smfmac_f32_16x16x64_bf16 v[132:135], v[116:119], v[80:87], v17\n"
        "v_smfmac_f32_16x16x64_bf16 v[132:135], v[120:123], v[104:111], v17\n"
        "s_nop 1\n"
        "v_add_f32 v124, v124, v125\n"
        "s_nop 1\n"
        "v_add_f32 v128, v128, v129\n"
        "s_nop 1\n"
        "v_add_f32 v132, v132, v133\n"
        "v_cvt_pk_bf16_f32 v124, v124, v128\n"
        "v_cvt_pk_bf16_f32 v132, v132, v132\n"
        "ds_write_b16 v15, v124\n"
        "ds_write_b16_d16_hi v15, v124 offset:128\n"
        "ds_write_b16 v15, v132 offset:256\n"
        "ds_write_b8 v3, v4 offset:60\n"
        "s_waitcnt vmcnt(0)\n"
        "ds_write_b8 v3, v4 offset:52\n"
        "s_cmp_lg_u32 s3, 7\n"
        "s_cbranch_scc1 Lhcx_%=\n"
        "v_cmp_eq_u64 vcc, s[6:7], v[38:39]\n"
        "s_cmp_eq_u32 s2, 0\n"
        "s_cbranch_scc1 Lhcl_%=\n"
        "s_nop 4\n"
        "s_cbranch_vccz Lhcx_%=\n"
        "Lhcl_%=:\n"
        "s_mov_b32 s86, 0xc4000\n"
        "buffer_load_dwordx4 v[160:163], v1, s[24:27], s86 offen sc1\n"
        "buffer_load_dwordx4 v[164:167], v1, s[24:27], s86 offen offset:1024 sc1\n"
        "s_waitcnt vmcnt(0)\n"
        "v_cmp_ne_u64 vcc, s[6:7], v[160:161]\n"
        "v_cmp_ne_u64 s[10:11], s[6:7], v[162:163]\n"
        "v_cmp_ne_u64 s[40:41], s[6:7], v[164:165]\n"
        "v_cmp_ne_u64 s[42:43], s[6:7], v[166:167]\n"
        "s_nop 1\n"
        "s_or_b64 vcc, vcc, s[10:11]\n"
        "s_or_b64 vcc, vcc, s[40:41]\n"
        "s_or_b64 vcc, vcc, s[42:43]\n"
        "s_cbranch_scc1 Lhcx_%=\n"
        "v_mov_b32 v12, 0xb4000\n"
        "s_mov_b64 exec, 1\n"
        "buffer_store_dwordx2 v[10:11], v12, s[24:27], 0 offen sc1\n"
        "s_mov_b64 exec, -1\n"
        "Lhcx_%=:\n"
        "s_mul_i32 s89, s88, 25600\n"
        "v_add_u32 v14, s89, v1\n"
        "v_add_u32 v14, 0x12c00, v14\n"
        "s_lshl_b32 s89, s88, 7\n"
        "s_setprio 0\n"
        "s_mov_b32 s87, 0x200000\n"
        "Lpl57_%=:\n"
        "ds_read_b32 v5, v2 offset:52\n"
        "s_waitcnt lgkmcnt(0)\n"
        "v_readfirstlane_b32 s86, v5\n"
        "s_cmp_eq_u32 s86, 0x1010100\n"
        "s_cbranch_scc1 Lrt56_%=\n"
        "s_sub_u32 s87, s87, 1\n"
        "s_cbranch_scc1 Lrt56_%=\n"
        "s_sleep 1\n"
        "s_branch Lpl57_%=\n"
        "Lrt56_%=:\n"
        "ds_read_b128 v[40:43], v14 offset:0\n"
        "ds_read_b128 v[44:47], v14 offset:1024\n"
        "ds_read_b128 v[48:51], v14 offset:2048\n"
        "ds_read_b128 v[52:55], v14 offset:3072\n"
        "ds_read_b128 v[56:59], v14 offset:4096\n"
        "ds_read_b128 v[60:63], v14 offset:5120\n"
        "ds_read_b128 v[64:67], v14 offset:6144\n"
        "ds_read_b128 v[68:71], v14 offset:7168\n"
        "ds_read_b128 v[72:75], v14 offset:8192\n"
        "ds_read_b128 v[76:79], v14 offset:9216\n"
        "ds_read_b128 v[80:83], v14 offset:10240\n"
        "ds_read_b128 v[84:87], v14 offset:11264\n"
        "ds_read_b128 v[88:91], v14 offset:12288\n"
        "ds_read_b128 v[92:95], v14 offset:13312\n"
        "ds_read_b128 v[96:99], v14 offset:14336\n"
        "s_waitcnt lgkmcnt(12)\n"
        "ds_read_b128 v[100:103], v14 offset:15360\n"
        "ds_read_b128 v[104:107], v14 offset:16384\n"
        "ds_read_b128 v[108:111], v14 offset:17408\n"
        "s_waitcnt lgkmcnt(12)\n"
        "ds_read_b128 v[112:115], v14 offset:18432\n"
        "ds_read_b128 v[116:119], v14 offset:19456\n"
        "ds_read_b128 v[120:123], v14 offset:20480\n"
        "s_waitcnt lgkmcnt(12)\n"
        "ds_read_b128 v[124:127], v14 offset:21504\n"
        "ds_read_b128 v[128:131], v14 offset:22528\n"
        "ds_read_b128 v[132:135], v14 offset:23552\n"
        "s_mov_b32 s87, 0x200000\n"
        "Lpl59_%=:\n"
        "ds_read_b32 v5, v2 offset:64\n"
        "s_waitcnt lgkmcnt(0)\n"
        "v_readfirstlane_b32 s86, v5\n"
        "s_cmp_eq_u32 s86, 0x1\n"
        "s_cbranch_scc1 Lrt58_%=\n"
        "s_sub_u32 s87, s87, 1\n"
        "s_cbranch_scc1 Lrt58_%=\n"
        "s_sleep 2\n"
        "s_branch Lpl59_%=\n"
        "Lrt58_%=:\n"
        "s_setprio 3\n"
        "ds_read_b128 v[160:163], v16 offset:4096\n"
        "ds_read_b128 v[164:167], v16 offset:4224\n"
        "ds_read_b128 v[168:171], v16 offset:4352\n"
        "s_waitcnt lgkmcnt(2)\n"
        "v_smfmac_f32_16x16x64_bf16 v[136:139], v[160:163], v[40:47], v17\n"
        "s_waitcnt lgkmcnt(1)\n"
        "v_smfmac_f32_16x16x64_bf16 v[136:139], v[164:167], v[48:55], v17\n"
        "s_waitcnt lgkmcnt(0)\n"
        "v_smfmac_f32_16x16x64_bf16 v[136:139], v[168:171], v[56:63], v17\n"
        "v_smfmac_f32_16x16x64_bf16 v[140:143], v[160:163], v[64:71], v17\n"
        "v_smfmac_f32_16x16x64_bf16 v[140:143], v[164:167], v[72:79], v17\n"
        "v_smfmac_f32_16x16x64_bf16 v[140:143], v[168:171], v[80:87], v17\n"
        "v_smfmac_f32_16x16x64_bf16 v[144:147], v[160:163], v[88:95], v17\n"
        "v_smfmac_f32_16x16x64_bf16 v[144:147], v[164:167], v[96:103], v17\n"
        "v_smfmac_f32_16x16x64_bf16 v[144:147], v[168:171], v[104:111], v17\n"
        "v_smfmac_f32_16x16x64_bf16 v[148:151], v[160:163], v[112:119], v17\n"
        "v_smfmac_f32_16x16x64_bf16 v[148:151], v[164:167], v[120:127], v17\n"
        "v_smfmac_f32_16x16x64_bf16 v[148:151], v[168:171], v[128:135], v17\n"
        "v_add_f32 v136, v136, v137\n"
        "s_nop 0\n"
        "v_add_f32 v140, v140, v141\n"
        "s_nop 1\n"
        "v_add_f32 v144, v144, v145\n"
        "s_nop 1\n"
        "v_add_f32 v148, v148, v149\n"
        "v_cvt_pk_bf16_f32 v16, v136, v140\n"
        "v_cvt_pk_bf16_f32 v17, v144, v148\n"
        "buffer_store_dwordx2 v[16:17], v29, s[28:31], s89 offen sc1\n"
        "Lhend_%=:\n"
        "s_endpgm\n"
        "Lpriv_%=:\n"
        "s_mov_b32 s40, -1\n"
        "s_cmp_lg_u32 s3, 4\n"
        "s_cbranch_scc1 Lp2e_%=\n"
        "s_branch Lpnx_%=\n"
        "Lprn_%=:\n"
        "s_waitcnt vmcnt(0)\n"
        "s_add_u32 s80, s80, 1\n"
        "s_cmp_lt_u32 s80, 3\n"
        "s_cbranch_scc1 Lprl_%=\n"
        "s_branch Lpnx_%=\n"
        "Lp2_%=:\n"
        "buffer_store_dwordx4 v[232:235], v140, s[24:27], s19 offen offset:0 sc1\n"
        "buffer_store_dwordx4 v[236:239], v140, s[24:27], s19 offen offset:64 sc1\n"
        "s_waitcnt vmcnt(0)\n"
        "Lpnx_%=:\n"
        "s_add_u32 s40, s40, 1\n"
        "s_cmp_lt_u32 s40, 24\n"
        "s_cselect_b32 s47, 3, 2\n"
        "v_bfe_u32 v10, v0, 3, 2\n"
        "v_bfe_u32 v11, v0, 5, 1\n"
        "v_lshl_add_u32 v10, v11, s47, v10\n"
        "s_lshl_b32 s47, 2304, s47\n"
        "s_lshr_b32 s42, s40, 2\n"
        "v_lshrrev_b32 v11, 3, v0\n"
        "s_mul_i32 s42, s42, 36864\n"
        "v_and_b32 v11, 6, v11\n"
        "s_bfe_u32 s44, s40, 0x10001\n"
        "v_xor_b32 v11, v11, v0\n"
        "s_mul_i32 s44, s44, 36864\n"
        "v_and_b32 v11, 7, v11\n"
        "s_bitcmp1_b32 s40, 0\n"
        "v_mul_u32_u24 v10, 1152, v10\n"
        "s_cselect_b32 s45, 12, 0\n"
        "v_lshl_add_u32 v10, v11, 4, v10\n"
        "s_add_u32 s45, s45, 1\n"
        "v_add_u32 v22, s47, v10\n"
        "s_mul_i32 s45, s45, 384\n"
        "v_mov_b32 v12, v10\n"
        "s_add_u32 s44, s44, s45\n"
        "v_mov_b32 v13, v22\n"
        "s_add_u32 s43, s42, 4608\n"
        "s_add_u32 s45, s44, 73728\n"
        "s_add_u32 s46, s44, 147456\n"
        "s_mov_b32 s47, 0\n"
        "s_cmp_lt_u32 s40, 24\n"
        "s_cbranch_scc1 Lpa60_%=\n"
        "s_mul_i32 s48, s40, 43\n"
        "v_bfe_u32 v12, v0, 2, 4\n"
        "s_lshr_b32 s48, s48, 8\n"
        "v_and_b32 v14, 3, v0\n"
        "s_mul_i32 s49, s48, 6\n"
        "v_mul_u32_u24 v12, 384, v12\n"
        "s_sub_u32 s49, s40, s49\n"
        "v_lshl_add_u32 v12, v14, 4, v12\n"
        "s_sub_u32 s48, s48, 4\n"
        "v_add_u32 v13, 6144, v12\n"
        "s_mul_i32 s42, s49, 36864\n"
        "s_lshl_b32 s44, s48, 6\n"
        "s_add_u32 s42, s42, 768\n"
        "s_add_u32 s45, s44, 12288\n"
        "s_add_u32 s43, s42, 18432\n"
        "s_mov_b32 s47, 24448\n"
        "Lpa60_%=:\n"
        "s_cmp_lt_u32 s40, 60\n"
        "s_cbranch_scc1 Lp1_%=\n"
        "Lp2e_%=:\n"
        "s_waitcnt vmcnt(0)\n"
        "s_barrier\n"
        "s_branch Lpdone_%=\n"
        "Lrelight_%=:\n"
        "s_nop 7\n"
        "s_waitcnt vmcnt(0) lgkmcnt(0)\n"
        "s_barrier\n"
        "s_add_u32 s18, s18, 0x100\n"
        "s_bitcmp1_b32 s18, 10\n"
        "s_cbranch_scc1 Lfallback_%=\n"
        "s_cmp_lg_u32 s3, 1\n"
        "s_cbranch_scc1 Lrl1_%=\n"
        "v_mov_b32 v200, 0\n"
        "v_and_b32 v201, 63, v0\n"
        "v_lshl_add_u32 v201, v201, 2, v2\n"
        "ds_write_b32 v201, v200\n"
        "s_waitcnt lgkmcnt(0)\n"
        "Lrl1_%=:\n"
        "s_barrier\n"
        "s_cmp_ge_u32 s3, 4\n"
        "s_cbranch_scc1 Lhre_%=\n"
        "s_branch Lcre_%=\n"
        "Lfallback_%=:\n"
        "s_nop 7\n"
        "s_setprio 0\n"
        "s_waitcnt vmcnt(0) lgkmcnt(0)\n"
        "s_barrier\n"
        "s_andn2_b32 s18, s18, 0xff00\n"
        "s_add_u32 s18, s18, 1\n"
        "s_and_b32 s86, s18, 0xff\n"
        "s_cmp_lt_u32 s86, 3\n"
        "s_cbranch_scc1 Ltop_%=\n"
        "s_mul_i32 s19, s2, 196608\n"
        "s_add_u32 s19, s19, 0x100000\n"
        "s_cmp_lt_u32 s86, 100\n"
        "s_cbranch_scc1 Ltop_%=\n"
        "s_endpgm\n"
        "Lsl4_%=:\n"
        "s_mov_b32 s87, 0x200000\n"
        "Lpl6_%=:\n"
        "ds_read_b32 v5, v2 offset:0\n"
        "s_waitcnt lgkmcnt(0)\n"
        "v_readfirstlane_b32 s86, v5\n"
        "s_cmp_eq_u32 s86, 0x1010101\n"
        "s_cbranch_scc1 Lrt5_%=\n"
        "s_sub_u32 s87, s87, 1\n"
        "s_cbranch_scc1 Lrt5_%=\n"
        "s_branch Lpl6_%=\n"
        "Lsl7_%=:\n"
        "s_mov_b32 s87, 0x200000\n"
        "Lpl9_%=:\n"
        "ds_read_b32 v5, v2 offset:4\n"
        "s_waitcnt lgkmcnt(0)\n"
        "v_readfirstlane_b32 s86, v5\n"
        "s_cmp_eq_u32 s86, 0x1010101\n"
        "s_cbranch_scc1 Lrt8_%=\n"
        "s_and_b32 s86, s86, 0x02020202\n"
        "s_cbranch_scc1 Lrelight_%=\n"
        "s_sub_u32 s87, s87, 1\n"
        "s_cbranch_scc1 Lrt8_%=\n"
        "s_branch Lpl9_%=\n"
        "Lsl10_%=:\n"
        "s_mov_b32 s87, 0x200000\n"
        "Lpl12_%=:\n"
        "ds_read_b32 v5, v2 offset:20\n"
        "s_waitcnt lgkmcnt(0)\n"
        "v_readfirstlane_b32 s86, v5\n"
        "s_cmp_eq_u32 s86, 0x1010101\n"
        "s_cbranch_scc1 Lrt11_%=\n"
        "s_sub_u32 s87, s87, 1\n"
        "s_cbranch_scc1 Lrt11_%=\n"
        "s_branch Lpl12_%=\n"
        "Lsl13_%=:\n"
        "s_mov_b32 s87, 0x200000\n"
        "Lpl15_%=:\n"
        "ds_read_b32 v5, v2 offset:24\n"
        "s_waitcnt lgkmcnt(0)\n"
        "v_readfirstlane_b32 s86, v5\n"
        "s_cmp_eq_u32 s86, 0x1010101\n"
        "s_cbranch_scc1 Lrt14_%=\n"
        "s_sub_u32 s87, s87, 1\n"
        "s_cbranch_scc1 Lrt14_%=\n"
        "s_branch Lpl15_%=\n"
        "Lsl16_%=:\n"
        "s_mov_b32 s87, 0x200000\n"
        "Lpl18_%=:\n"
        "ds_read_b32 v5, v2 offset:28\n"
        "s_waitcnt lgkmcnt(0)\n"
        "v_readfirstlane_b32 s86, v5\n"
        "s_cmp_eq_u32 s86, 0x1010101\n"
        "s_cbranch_scc1 Lrt17_%=\n"
        "s_sub_u32 s87, s87, 1\n"
        "s_cbranch_scc1 Lrt17_%=\n"
        "s_branch Lpl18_%=\n"
        "Lsl19_%=:\n"
        "s_mov_b32 s87, 0x200000\n"
        "Lpl21_%=:\n"
        "ds_read_b32 v5, v2 offset:44\n"
        "s_waitcnt lgkmcnt(0)\n"
        "v_readfirstlane_b32 s86, v5\n"
        "s_cmp_eq_u32 s86, 0x1010100\n"
        "s_cbranch_scc1 Lrt20_%=\n"
        "s_and_b32 s86, s86, 0x02020202\n"
        "s_cbranch_scc1 Lfallback_%=\n"
        "s_sub_u32 s87, s87, 1\n"
        "s_cbranch_scc1 Lrt20_%=\n"
        "s_branch Lpl21_%=\n"
        "Lsl22_%=:\n"
        "s_mov_b32 s87, 0x200000\n"
        "Lpl24_%=:\n"
        "ds_read_b32 v5, v2 offset:48\n"
        "s_waitcnt lgkmcnt(0)\n"
        "v_readfirstlane_b32 s86, v5\n"
        "s_cmp_eq_u32 s86, 0x1010100\n"
        "s_cbranch_scc1 Lrt23_%=\n"
        "s_sub_u32 s87, s87, 1\n"
        "s_cbranch_scc1 Lrt23_%=\n"
        "s_branch Lpl24_%=\n"
        "Lsl25_%=:\n"
        "s_mov_b32 s87, 0x200000\n"
        "Lpl27_%=:\n"
        "ds_read_b32 v5, v2 offset:52\n"
        "s_waitcnt lgkmcnt(0)\n"
        "v_readfirstlane_b32 s86, v5\n"
        "s_cmp_eq_u32 s86, 0x1010100\n"
        "s_cbranch_scc1 Lrt26_%=\n"
        "s_sub_u32 s87, s87, 1\n"
        "s_cbranch_scc1 Lrt26_%=\n"
        "s_branch Lpl27_%=\n"
        "Lgret_%=:\n"
        "s_sub_u32 s90, s90, 1\n"
        "s_cbranch_scc1 Lgok_%=\n"
        "s_sleep 4\n"
        "buffer_load_dwordx2 v[36:37], v15, s[24:27], s10 offen offset:3072 sc1\n"
        "s_waitcnt vmcnt(0)\n"
        "v_cmp_ne_u32 vcc, s7, v37\n"
        "s_nop 4\n"
        "s_cbranch_vccnz Lgret_%=\n"
        "s_mov_b32 s87, 0\n"
        "s_add_u32 m0, s11, 0x12c00\n"
        "s_nop 0\n"
        "buffer_load_dwordx4 v13, s[24:27], s87 offen sc1 lds\n"
        "s_mov_b32 s87, 12800\n"
        "s_add_u32 m0, s11, 0x13000\n"
        "s_nop 0\n"
        "buffer_load_dwordx4 v13, s[24:27], s87 offen sc1 lds\n"
        "s_mov_b32 s87, 1024\n"
        "s_add_u32 m0, s11, 0x13400\n"
        "s_nop 0\n"
        "buffer_load_dwordx4 v13, s[24:27], s87 offen sc1 lds\n"
        "s_mov_b32 s87, 13824\n"
        "s_add_u32 m0, s11, 0x13800\n"
        "s_nop 0\n"
        "buffer_load_dwordx4 v13, s[24:27], s87 offen sc1 lds\n"
        "s_mov_b32 s87, 2048\n"
        "s_add_u32 m0, s11, 0x13c00\n"
        "s_nop 0\n"
        "buffer_load_dwordx4 v13, s[24:27], s87 offen sc1 lds\n"
        "s_mov_b32 s87, 14848\n"
        "s_add_u32 m0, s11, 0x14000\n"
        "s_nop 0\n"
        "buffer_load_dwordx4 v13, s[24:27], s87 offen sc1 lds\n"
        "s_mov_b32 s87, 25600\n"
        "s_add_u32 m0, s11, 0x19000\n"
        "s_nop 0\n"
        "buffer_load_dwordx4 v13, s[24:27], s87 offen sc1 lds\n"
        "s_mov_b32 s87, 38400\n"
        "s_add_u32 m0, s11, 0x19400\n"
        "s_nop 0\n"
        "buffer_load_dwordx4 v13, s[24:27], s87 offen sc1 lds\n"
        "s_mov_b32 s87, 26624\n"
        "s_add_u32 m0, s11, 0x19800\n"
        "s_nop 0\n"
        "buffer_load_dwordx4 v13, s[24:27], s87 offen sc1 lds\n"
        "s_mov_b32 s87, 39424\n"
        "s_add_u32 m0, s11, 0x19c00\n"
        "s_nop 0\n"
        "buffer_load_dwordx4 v13, s[24:27], s87 offen sc1 lds\n"
        "s_mov_b32 s87, 27648\n"
        "s_add_u32 m0, s11, 0x1a000\n"
        "s_nop 0\n"
        "buffer_load_dwordx4 v13, s[24:27], s87 offen sc1 lds\n"
        "s_mov_b32 s87, 40448\n"
        "s_add_u32 m0, s11, 0x1a400\n"
        "s_nop 0\n"
        "buffer_load_dwordx4 v13, s[24:27], s87 offen sc1 lds\n"
        "s_waitcnt vmcnt(0)\n"
        "s_branch Lgok_%=\n"
        :
        : "{s[0:1]}"(__builtin_amdgcn_kernarg_segment_ptr()), "{s2}"(blockIdx.x), "{v0}"(threadIdx.x), "{s[4:5]}"(na_tab), "{m0}"((unsigned)(size_t)smem)
        : "memory", "scc", "vcc", "v1", "v2", "v3", "v4", "v5", "v6", "v7", "v8", "v9", "v10", "v11", "v12", "v13", "v14", "v15", "v16", "v17", "v18", "v19", "v20", "v21", "v22", "v23", "v24", "v25", "v26", "v27", "v28", "v29", "v30", "v31", "v32", "v33", "v34", "v35", "v36", "v37", "v38", "v39", "v40", "v41", "v42", "v43", "v44", "v45", "v46", "v47", "v48", "v49", "v50", "v51", "v52", "v53", "v54", "v55", "v56", "v57", "v58", "v59", "v60", "v61", "v62", "v63", "v64", "v65", "v66", "v67", "v68", "v69", "v70", "v71", "v72", "v73", "v74", "v75", "v76", "v77", "v78", "v79", "v80", "v81", "v82", "v83", "v84", "v85", "v86", "v87", "v88", "v89", "v90", "v91", "v92", "v93", "v94", "v95", "v96", "v97", "v98", "v99", "v100", "v101", "v102", "v103", "v104", "v105", "v106", "v107", "v108", "v109", "v110", "v111", "v112", "v113", "v114", "v115", "v116", "v117", "v118", "v119", "v120", "v121", "v122", "v123", "v124", "v125", "v126", "v127", "v128", "v129", "v130", "v131", "v132", "v133", "v134", "v135", "v136", "v137", "v138", "v139", "v140", "v141", "v142", "v143", "v144", "v145", "v146", "v147", "v148", "v149", "v150", "v151", "v152", "v153", "v154", "v155", "v156", "v157", "v158", "v159", "v160", "v161", "v162", "v163", "v164", "v165", "v166", "v167", "v168", "v169", "v170", "v171", "v172", "v173", "v174", "v175", "v176", "v177", "v178", "v179", "v180", "v181", "v182", "v183", "v184", "v185", "v186", "v187", "v188", "v189", "v190", "v191", "v192", "v193", "v194", "v195", "v196", "v197", "v198", "v199", "v200", "v201", "v202", "v203", "v204", "v205", "v206", "v207", "v208", "v209", "v210", "v211", "v212", "v213", "v214", "v215", "v216", "v217", "v218", "v219", "v220", "v221", "v222", "v223", "v224", "v225", "v226", "v227", "v228", "v229", "v230", "v231", "v232", "v233", "v234", "v235", "v236", "v237", "v238", "v239", "v240", "v241", "v242", "v243", "v244", "v245", "v246", "v247", "v248", "v249", "v250", "v251", "v252", "v253", "v254", "v255", "s3", "s6", "s7", "s8", "s9", "s10", "s11", "s12", "s13", "s14", "s15", "s16", "s17", "s18", "s19", "s20", "s21", "s22", "s23", "s24", "s25", "s26", "s27", "s28", "s29", "s30", "s31", "s32", "s33", "s34", "s35", "s36", "s37", "s38", "s39", "s40", "s41", "s42", "s43", "s44", "s45", "s46", "s47", "s48", "s49", "s50", "s51", "s52", "s53", "s54", "s55", "s56", "s57", "s58", "s59", "s60", "s61", "s62", "s63", "s64", "s65", "s66", "s67", "s68", "s69", "s70", "s71", "s72", "s73", "s74", "s75", "s76", "s77", "s78", "s79", "s80", "s81", "s82", "s83", "s84", "s85", "s86", "s87", "s88", "s89", "s90", "s91", "s92", "s93", "s94", "s95", "s96", "s97", "s98", "s99", "s100", "s101");
}

extern "C" void kernel_launch(void* const* d_in, const int* in_sizes, int n_in,
                              void* d_out, int out_size, void* d_ws, size_t ws_size,
                              hipStream_t stream) {
    na_main<<<256, 512, 0, stream>>>(d_in[0], d_in[1], d_in[2], d_ws, d_out);
}
```

```cpp
#include <hip/hip_runtime.h>

__device__ const unsigned na_tab[3840] = {
    0x12500b4, 0x2050194, 0x16a, 0x0, 0xf110711, 0x9120112, 0x5131112, 0x10f010f, 0x15f00ee, 0x23f01ce, 0x16a, 0x0,
    0xf110711, 0x9120112, 0x5131112, 0x10f010f, 0x12900b8, 0x2090198, 0x16a, 0x0, 0x10110811, 0xa120212, 0x6131212, 0x20f020f,
    0x16300f2, 0x24301d2, 0x16a, 0x0, 0x10110811, 0xa120212, 0x6131212, 0x20f020f, 0x12c00bd, 0x20c019d, 0x16f, 0x0,
    0x11110911, 0xb120312, 0x7131312, 0x11101110, 0x16600f7, 0x24601d7, 0x16f, 0x0, 0x11110911, 0xb120312, 0x7131312, 0x11101110,
    0x13000c1, 0x21001a1, 0x16f, 0x0, 0x12110a11, 0xc120412, 0x8130012, 0x12101210, 0x16a00fb, 0x24a01db, 0x16f, 0x0,
    0x12110a11, 0xc120412, 0x8130012, 0x12101210, 0x13500c4, 0x21501a4, 0x1a1, 0x0, 0x13110b11, 0xd120512, 0x9130113, 0x13101310,
    0x16f00fe, 0x24f01de, 0x1a1, 0x0, 0x13110b11, 0xd120512, 0x9130113, 0x13101310, 0x13900c8, 0x21901a8, 0x1a1, 0x0,
    0x110c11, 0xe120612, 0xa130213, 0x100010, 0x1730102, 0x25301e2, 0x1a1, 0x0, 0x110c11, 0xe120612, 0xa130213, 0x100010,
    0x13c00cd, 0x21c01ad, 0x1a4, 0x0, 0x1110d11, 0xf120712, 0xb130313, 0x1100110, 0x1760107, 0x25601e7, 0x1a4, 0x0,
    0x1110d11, 0xf120712, 0xb130313, 0x1100110, 0x14000d1, 0x22001b1, 0x1a4, 0x0, 0x2110e11, 0x10120812, 0xc130413, 0x2100210,
    0x17a010b, 0x25a01eb, 0x1a4, 0x0, 0x2110e11, 0x10120812, 0xc130413, 0x2100210, 0x3c000, 0x5557d555, 0x3c000, 0x3c000,
    0x3c000, 0x3c000, 0xaaabeaaa, 0x3c000, 0x0, 0x0, 0x0, 0x0, 0x0, 0x0, 0x0, 0x0,
    0x0, 0x0, 0x0, 0x0, 0x0, 0x0, 0x0, 0x0, 0x0, 0x0, 0x0, 0x0,
    0x0, 0x0, 0x0, 0x0, 0x0, 0x0, 0x0, 0x0, 0x0, 0x0, 0x0, 0x0,
    0x0, 0x0, 0x0, 0x0, 0x0, 0x0, 0x0, 0x0, 0x0, 0x0, 0x0, 0x0,
    0x0, 0x0, 0x0, 0x0, 0x0, 0x0, 0x0, 0x0, 0x0, 0x0, 0x0, 0x0,
    0x12500b4, 0x2050194, 0x16a, 0x0, 0x1000d13, 0x11000900, 0xd010501, 0x10f010f, 0x15f00ee, 0x23f01ce, 0x16a, 0x0,
    0x1000d13, 0x11000900, 0xd010501, 0x10f010f, 0x12900b8, 0x2090198, 0x16a, 0x0, 0x2000e13, 0x12000a00, 0xe010601, 0x20f020f,
    0x16300f2, 0x24301d2, 0x16a, 0x0, 0x2000e13, 0x12000a00, 0xe010601, 0x20f020f, 0x12c00bd, 0x20c019d, 0x16f, 0x0,
    0x3000f13, 0x13000b00, 0xf010701, 0x11101110, 0x16600f7, 0x24601d7, 0x16f, 0x0, 0x3000f13, 0x13000b00, 0xf010701, 0x11101110,
    0x13000c1, 0x21001a1, 0x16f, 0x0, 0x4001013, 0xc00, 0x10010801, 0x12101210, 0x16a00fb, 0x24a01db, 0x16f, 0x0,
    0x4001013, 0xc00, 0x10010801, 0x12101210, 0x13500c4, 0x21501a4, 0x1a1, 0x0, 0x5001113, 0x1010d00, 0x11010901, 0x13101310,
    0x16f00fe, 0x24f01de, 0x1a1, 0x0, 0x5001113, 0x1010d00, 0x11010901, 0x13101310, 0x13900c8, 0x21901a8, 0x1a1, 0x0,
    0x6001213, 0x2010e00, 0x12010a01, 0x100010, 0x1730102, 0x25301e2, 0x1a1, 0x0, 0x6001213, 0x2010e00, 0x12010a01, 0x100010,
    0x13c00cd, 0x21c01ad, 0x1a4, 0x0, 0x7001313, 0x3010f00, 0x13010b01, 0x1100110, 0x1760107, 0x25601e7, 0x1a4, 0x0,
    0x7001313, 0x3010f00, 0x13010b01, 0x1100110, 0x14000d1, 0x22001b1, 0x1a4, 0x0, 0x8000013, 0x4011000, 0x10c01, 0x2100210,
    0x17a010b, 0x25a01eb, 0x1a4, 0x0, 0x8000013, 0x4011000, 0x10c01, 0x2100210, 0x3c000, 0x5557d555, 0x3c000, 0x3c000,
    0x3c000, 0x3c000, 0xaaabeaaa, 0x3c000, 0x0, 0x0, 0x0, 0x0, 0x0, 0x0, 0x0, 0x0,
    0x0, 0x0, 0x0, 0x0, 0x0, 0x0, 0x0, 0x0, 0x0, 0x0, 0x0, 0x0,
    0x0, 0x0, 0x0, 0x0, 0x0, 0x0, 0x0, 0x0, 0x0, 0x0, 0x0, 0x0,
    0x0, 0x0, 0x0, 0x0, 0x0, 0x0, 0x0, 0x0, 0x0, 0x0, 0x0, 0x0,
    0x0, 0x0, 0x0, 0x0, 0x0, 0x0, 0x0, 0x0, 0x0, 0x0, 0x0, 0x0,
    0x12500b4, 0x2050194, 0x16a, 0x0, 0x9020102, 0x1031102, 0x13061105, 0x10f010f, 0x15f00ee, 0x23f01ce, 0x16a, 0x0,
    0x9020102, 0x1031102, 0x13061105, 0x10f010f, 0x12900b8, 0x2090198, 0x16a, 0x0, 0xa020202, 0x2031202, 0x61205, 0x20f020f,
    0x16300f2, 0x24301d2, 0x16a, 0x0, 0xa020202, 0x2031202, 0x61205, 0x20f020f, 0x12c00bd, 0x20c019d, 0x16f, 0x0,
    0xb020302, 0x11041302, 0x1061305, 0x11101110, 0x16600f7, 0x24601d7, 0x16f, 0x0, 0xb020302, 0x11041302, 0x1061305, 0x11101110,
    0x13000c1, 0x21001a1, 0x16f, 0x0, 0xc020402, 0x12040002, 0x2060005, 0x12101210, 0x16a00fb, 0x24a01db, 0x16f, 0x0,
    0xc020402, 0x12040002, 0x2060005, 0x12101210, 0x13500c4, 0x21501a4, 0x1a1, 0x0, 0xd020502, 0x13041103, 0x11070105, 0x13101310,
    0x16f00fe, 0x24f01de, 0x1a1, 0x0, 0xd020502, 0x13041103, 0x11070105, 0x13101310, 0x13900c8, 0x21901a8, 0x1a1, 0x0,
    0xe020602, 0x41203, 0x12070205, 0x100010, 0x1730102, 0x25301e2, 0x1a1, 0x0, 0xe020602, 0x41203, 0x12070205, 0x100010,
    0x13c00cd, 0x21c01ad, 0x1a4, 0x0, 0xf020702, 0x1041303, 0x13071106, 0x1100110, 0x1760107, 0x25601e7, 0x1a4, 0x0,
    0xf020702, 0x1041303, 0x13071106, 0x1100110, 0x14000d1, 0x22001b1, 0x1a4, 0x0, 0x10020802, 0x2040003, 0x71206, 0x2100210,
    0x17a010b, 0x25a01eb, 0x1a4, 0x0, 0x10020802, 0x2040003, 0x71206, 0x2100210, 0x3c000, 0x5557d555, 0x3c000, 0x3c000,
    0x3c000, 0x3c000, 0xaaabeaaa, 0x3c000, 0x0, 0x0, 0x0, 0x0, 0x0, 0x0, 0x0, 0x0,
    0x0, 0x0, 0x0, 0x0, 0x0, 0x0, 0x0, 0x0, 0x0, 0x0, 0x0, 0x0,
    0x0, 0x0, 0x0, 0x0, 0x0, 0x0, 0x0, 0x0, 0x0, 0x0, 0x0, 0x0,
    0x0, 0x0, 0x0, 0x0, 0x0, 0x0, 0x0, 0x0, 0x0, 0x0, 0x0, 0x0,
    0x0, 0x0, 0x0, 0x0, 0x0, 0x0, 0x0, 0x0, 0x0, 0x0, 0x0, 0x0,
    0x12500b4, 0x2050194, 0x16a, 0x0, 0x11090107, 0x10b130a, 0x130e110d, 0x10f010f, 0x15f00ee, 0x23f01ce, 0x16a, 0x0,
    0x11090107, 0x10b130a, 0x130e110d, 0x10f010f, 0x12900b8, 0x2090198, 0x16a, 0x0, 0x12090207, 0x20b000a, 0xe120d, 0x20f020f,
    0x16300f2, 0x24301d2, 0x16a, 0x0, 0x12090207, 0x20b000a, 0xe120d, 0x20f020f, 0x12c00bd, 0x20c019d, 0x16f, 0x0,
    0x13091108, 0x110c010a, 0x10e130d, 0x11101110, 0x16600f7, 0x24601d7, 0x16f, 0x0, 0x13091108, 0x110c010a, 0x10e130d, 0x11101110,
    0x13000c1, 0x21001a1, 0x16f, 0x0, 0x91208, 0x120c020a, 0x20e000d, 0x12101210, 0x16a00fb, 0x24a01db, 0x16f, 0x0,
    0x91208, 0x120c020a, 0x20e000d, 0x12101210, 0x13500c4, 0x21501a4, 0x1a1, 0x0, 0x1091308, 0x130c110b, 0x110f010d, 0x13101310,
    0x16f00fe, 0x24f01de, 0x1a1, 0x0, 0x1091308, 0x130c110b, 0x110f010d, 0x13101310, 0x13900c8, 0x21901a8, 0x1a1, 0x0,
    0x2090008, 0xc120b, 0x120f020d, 0x100010, 0x1730102, 0x25301e2, 0x1a1, 0x0, 0x2090008, 0xc120b, 0x120f020d, 0x100010,
    0x13c00cd, 0x21c01ad, 0x1a4, 0x0, 0x110a0108, 0x10c130b, 0x130f110e, 0x1100110, 0x1760107, 0x25601e7, 0x1a4, 0x0,
    0x110a0108, 0x10c130b, 0x130f110e, 0x1100110, 0x14000d1, 0x22001b1, 0x1a4, 0x0, 0x120a0208, 0x20c000b, 0xf120e, 0x2100210,
    0x17a010b, 0x25a01eb, 0x1a4, 0x0, 0x120a0208, 0x20c000b, 0xf120e, 0x2100210, 0x3c000, 0x5557d555, 0x3c000, 0x3c000,
    0x3c000, 0x3c000, 0xaaabeaaa, 0x3c000, 0x0, 0x0, 0x0, 0x0, 0x0, 0x0, 0x0, 0x0,
    0x0, 0x0, 0x0, 0x0, 0x0, 0x0, 0x0, 0x0, 0x0, 0x0, 0x0, 0x0,
    0x0, 0x0, 0x0, 0x0, 0x0, 0x0, 0x0, 0x0, 0x0, 0x0, 0x0, 0x0,
    0x0, 0x0, 0x0, 0x0, 0x0, 0x0, 0x0, 0x0, 0x0, 0x0, 0x0, 0x0,
    0x0, 0x0, 0x0, 0x0, 0x0, 0x0, 0x0, 0x0, 0x0, 0x0, 0x0, 0x0,
    0xa590a8c, 0xaf80b2d, 0x1290bb4, 0x1980be5, 0x490b54, 0x20b5d, 0xe20073, 0x0, 0xaab0ade, 0x120b3f, 0x1630bce, 0x1d20bff,
    0x830b6e, 0x390b77, 0x11900a8, 0x0, 0xa5c0a40, 0xafd0ae1, 0x12c0bb8, 0x19d0be9, 0x4c0b58, 0x70050, 0xe70076, 0x0,
    0xaae0a92, 0x170b43, 0x1660bd2, 0x1d70c03, 0x860b72, 0x3c008a, 0x11c00ad, 0x0, 0xa600a45, 0xb010ae4, 0x1300bbd, 0x1a10bec,
    0xb80b85, 0xb0b8c, 0xeb007a, 0x0, 0xab20a97, 0x1b0b46, 0x16a0bd7, 0x1db0c06, 0xf20b9f, 0x400ba6, 0x12000b1, 0x0,
    0xa650a49, 0xb040ae8, 0x1350119, 0x1a40188, 0xbd0b89, 0xe00c1, 0xee007f, 0x0, 0xab70a9b, 0x1e0002, 0x16f0153, 0x1de01c2,
    0xf70ba3, 0x4500fb, 0x12500b4, 0x0, 0xa690a4c, 0xb080aed, 0x139011c, 0x1a8018d, 0x59003c, 0x120045, 0xf20083, 0x0,
    0xabb0a9e, 0x220007, 0x1730156, 0x1e201c7, 0x930076, 0x49007f, 0x12900b8, 0x0, 0xa6c0a50, 0xb0d0af1, 0x13c0120, 0x1ad0191,
    0x5c0040, 0x170060, 0xf70086, 0x0, 0xabe0aa2, 0x27000b, 0x176015a, 0x1e701cb, 0x96007a, 0x4c009a, 0x12c00bd, 0x0,
    0xa700a55, 0xb110af4, 0x1400125, 0x1b10194, 0xc800ad, 0x1b00b4, 0xfb008a, 0x0, 0xac20aa7, 0x2b000e, 0x17a015f, 0x1eb01ce,
    0x10200e7, 0x5000ee, 0x13000c1, 0x0, 0x550039, 0xc400a8, 0x550039, 0xc400a8, 0xcd00b1, 0x1e00d1, 0xfe008f, 0x0,
    0x8f0073, 0xfe00e2, 0x8f0073, 0xfe00e2, 0x10700eb, 0x55010b, 0x13500c4, 0x0, 0x3f0003, 0x3fff03ff, 0xff000f, 0xff0fff,
    0x30003, 0x3ff03ff, 0xf000f, 0xfff0fff, 0x3f003f, 0x3fff3fff, 0xaaaaaaaa, 0xaaaaaaaa, 0xaaaaaaaa, 0xaaaa, 0x0, 0x55550000,
    0x55550000, 0x55550000, 0xaaaaaaaa, 0xaaaaaaaa, 0xaaaaaaaa, 0xaaaa, 0x55555555, 0x55555555, 0x55555555, 0x5555, 0x0, 0x0,
    0x0, 0xaaaa0000, 0x0, 0x0, 0x55555555, 0x55555555, 0x55555555, 0x5555, 0xaaaa, 0xaaaa, 0x0, 0x0,
    0x0, 0x0, 0x0, 0x0, 0x0, 0x0, 0x0, 0x0, 0x0, 0x0, 0x0, 0x0,
    0x0, 0x0, 0x0, 0x0, 0x0, 0x0, 0x0, 0x0, 0x0, 0x0, 0x0, 0x0,
    0xf70a5c, 0xfe0a65, 0x1170a7c, 0xb960a85, 0x1070a6c, 0x220b0d, 0x32002b, 0x0, 0xfb0a60, 0x1020a69, 0xb930a80, 0xb9a0a89,
    0x10b0a70, 0x270b11, 0x37002e, 0x0, 0x12c0aae, 0x1350ab7, 0x14c0ace, 0xbad0ad7, 0x13c0abe, 0x590176, 0x690060, 0x0,
    0x1300ab2, 0x1390abb, 0xba80ad2, 0xbb10adb, 0x1400ac2, 0x5c017a, 0x6c0065, 0x0, 0x1660afd, 0x16f0b04, 0x1860b1d, 0xbc70b24,
    0x10e0a75, 0x930b14, 0xa3009a, 0x0, 0x16a0b01, 0x1730b08, 0xbc20b21, 0xbcb0b28, 0x1120a79, 0x960b18, 0xa6009f, 0x0,
    0x19d0017, 0x1a4001e, 0x1bd0037, 0xbdc0b36, 0x1450ac7, 0xc8017f, 0xd800d1, 0x0, 0x1a1001b, 0x1a80022, 0xbd90b33, 0xbe00b3a,
    0x1490acb, 0xcd0183, 0xdd00d4, 0x0, 0x1d7004c, 0x1de0055, 0x1f7006c, 0xbf60b4d, 0x1e7005c, 0x10200cd, 0x112010b, 0x0,
    0x1db0050, 0x1e20059, 0xbf30b48, 0xbfa0b51, 0x1eb0060, 0x10700d1, 0x117010e, 0x0, 0x20c0086, 0x215008f, 0x22c00a6, 0xc0d0b67,
    0x21c0096, 0x1390256, 0x1490140, 0x0, 0x210008a, 0x2190093, 0xc080b62, 0xc110b6b, 0x220009a, 0x13c025a, 0x14c0145, 0x0,
    0x24600bd, 0x24f00c4, 0x26600dd, 0xc270b7c, 0x1ee0065, 0x17300d4, 0x183017a, 0x0, 0x24a00c1, 0x25300c8, 0xc220b79, 0xc2b0b80,
    0x1f20069, 0x17600d8, 0x186017f, 0x0, 0x1ad0027, 0x1b4002e, 0x1ad0027, 0x1b4002e, 0x225009f, 0x1a8025f, 0x1b801b1, 0x0,
    0x1b1002b, 0x1b80032, 0x1b1002b, 0x1b80032, 0x22900a3, 0x1ad0263, 0x1bd01b4, 0x0, 0x3f0003, 0x3fff03ff, 0xff000f, 0xff0fff,
    0x30003, 0x3ff03ff, 0xf000f, 0xfff0fff, 0x3f003f, 0x3fff3fff, 0xaaaaaaaa, 0xaaaaaaaa, 0xaaaaaaaa, 0xaaaa, 0x0, 0x55550000,
    0x55550000, 0x55550000, 0xaaaaaaaa, 0xaaaaaaaa, 0xaaaaaaaa, 0xaaaa, 0x55555555, 0x55555555, 0x55555555, 0x5555, 0x0, 0x0,
    0x0, 0xaaaa0000, 0x0, 0x0, 0x55555555, 0x55555555, 0x55555555, 0x5555, 0xaaaa, 0xaaaa, 0x0, 0x0,
    0x0, 0x0, 0x0, 0x0, 0x0, 0x0, 0x0, 0x0, 0x0, 0x0, 0x0, 0x0,
    0x0, 0x0, 0x0, 0x0, 0x0, 0x0, 0x0, 0x0, 0x0, 0x0, 0x0, 0x0,
    0x1400125, 0x1b10194, 0x30002e5, 0x9d109b4, 0x2200205, 0x1db020c, 0x2bb024a, 0x0, 0x17a015f, 0x1eb01ce, 0x97a031f, 0xa230a06,
    0x25a023f, 0x2100246, 0x2f00281, 0x0, 0x1450129, 0x1b40198, 0x30502e9, 0x9d409b8, 0x2250209, 0x1de0229, 0x2be024f, 0x0,
    0x17f0163, 0x1ee01d2, 0x97f0963, 0xa260a0a, 0x25f0243, 0x2150263, 0x2f50284, 0x0, 0x149012c, 0x1b8019d, 0x30902ec, 0x9d809bd,
    0x2910274, 0x1e2027d, 0x2c20253, 0x0, 0x1830166, 0x1f201d7, 0x9830966, 0xa2a0a0f, 0x2cb02ae, 0x21902b7, 0x2f90288, 0x0,
    0x14c0130, 0x1bd01a1, 0x30c02f0, 0x9dd09c1, 0x2940278, 0x1e70298, 0x2c70256, 0x0, 0x186016a, 0x1f701db, 0x986096a, 0xa2f0a13,
    0x2ce02b2, 0x21c02d2, 0x2fc028d, 0x0, 0xba80135, 0xbd901a4, 0xc6802f5, 0x9e109c4, 0xc080215, 0x1eb021c, 0x2cb025a, 0x0,
    0xbc2016f, 0xbf301de, 0x98a096f, 0xa330a16, 0xc22024f, 0x2200256, 0x3000291, 0x0, 0xbad0139, 0xbdc01a8, 0xc6d02f9, 0x9e409c8,
    0xc0d0219, 0x1ee0c11, 0x2ce025f, 0x0, 0xbc70173, 0xbf601e2, 0x98f0973, 0xa360a1a, 0xc270253, 0x2250c2b, 0x3050294, 0x0,
    0xbb1013c, 0xbe001ad, 0xc7102fc, 0x9e809cd, 0xc390284, 0x1f2028d, 0x2d20263, 0x0, 0xbcb0176, 0xbfa01e7, 0x9930976, 0xa3a0a1f,
    0xc5302be, 0x22902c7, 0x3090298, 0x0, 0x22c0210, 0x29d0281, 0x22c0210, 0x29d0281, 0xc3c0288, 0x1f70c40, 0x2d70266, 0x0,
    0x266024a, 0x2d702bb, 0x266024a, 0x2d702bb, 0xc5602c2, 0x22c0c5a, 0x30c029d, 0x0, 0x3f0003, 0x3fff03ff, 0xff000f, 0xff0fff,
    0x30003, 0x3ff03ff, 0xf000f, 0xfff0fff, 0x3f003f, 0x3fff3fff, 0xaaaaaaaa, 0xaaaaaaaa, 0xaaaaaaaa, 0xaaaa, 0x0, 0x55550000,
    0x55550000, 0x55550000, 0xaaaaaaaa, 0xaaaaaaaa, 0xaaaaaaaa, 0xaaaa, 0x55555555, 0x55555555, 0x55555555, 0x5555, 0x0, 0x0,
    0x0, 0xaaaa0000, 0x0, 0x0, 0x55555555, 0x55555555, 0x55555555, 0x5555, 0xaaaa, 0xaaaa, 0x0, 0x0,
    0x0, 0x0, 0x0, 0x0, 0x0, 0x0, 0x0, 0x0, 0x0, 0x0, 0x0, 0x0,
    0x0, 0x0, 0x0, 0x0, 0x0, 0x0, 0x0, 0x0, 0x0, 0x0, 0x0, 0x0,
    0xc2e0b85, 0xc370b8c, 0x24600bd, 0x24f00c4, 0x23600ad, 0x153011c, 0x163015a, 0x0, 0xc320b89, 0x23300a8, 0x24a00c1, 0x25300c8,
    0x23a00b1, 0x1560120, 0x166015f, 0x0, 0xc450b9f, 0xc4c0ba6, 0x27d00f7, 0x28400fe, 0x26d00e7, 0x18802a7, 0x1980191, 0x0,
    0xc490ba3, 0x26800e2, 0x28100fb, 0x2880102, 0x27100eb, 0x18d02ab, 0x19d0194, 0x0, 0xc5f0bb4, 0xc660bbd, 0x2b7012c, 0x2be0135,
    0x23f00b4, 0x1c20125, 0x1d201cb, 0x0, 0xc630bb8, 0x2a20119, 0x2bb0130, 0x2c20139, 0x24300b8, 0x1c70129, 0x1d701ce, 0x0,
    0xc740bce, 0xc7d0bd7, 0x2ec0166, 0x2f5016f, 0x27400ee, 0x1f902ae, 0x2090200, 0x0, 0xc780bd2, 0x2d90153, 0x2f0016a, 0x2f90173,
    0x27800f2, 0x1fc02b2, 0x20c0205, 0x0, 0x9960be5, 0x99f0bec, 0x966019d, 0x96f01a4, 0x316018d, 0x23301fc, 0x243023a, 0x0,
    0x99a0be9, 0x3130188, 0x96a01a1, 0x97301a8, 0x31a0191, 0x2360200, 0x246023f, 0x0, 0x9ed0bff, 0x9a40c06, 0x9bd01d7, 0x9c401de,
    0x9ad01c7, 0x26809ff, 0x2780271, 0x0, 0x9a10c03, 0x9a801c2, 0x9c101db, 0x9c801e2, 0x9b101cb, 0x26d0a03, 0x27d0274, 0x0,
    0xa3f0c14, 0x9f60c1d, 0xa0f020c, 0xa160215, 0x31f0194, 0x2a20205, 0x2b202ab, 0x0, 0x9f30c18, 0x9fa01f9, 0xa130210, 0xa1a0219,
    0x9630198, 0x2a70209, 0x2b702ae, 0x0, 0x2dc0156, 0x2e5015f, 0x2dc0156, 0x2e5015f, 0x9b401ce, 0x2d90a06, 0x2e902e0, 0x0,
    0x2e0015a, 0x2e90163, 0x2e0015a, 0x2e90163, 0x9b801d2, 0x2dc0a0a, 0x2ec02e5, 0x0, 0x3f0003, 0x3fff03ff, 0xff000f, 0xff0fff,
    0x30003, 0x3ff03ff, 0xf000f, 0xfff0fff, 0x3f003f, 0x3fff3fff, 0xaaaaaaaa, 0xaaaaaaaa, 0xaaaaaaaa, 0xaaaa, 0x0, 0x55550000,
    0x55550000, 0x55550000, 0xaaaaaaaa, 0xaaaaaaaa, 0xaaaaaaaa, 0xaaaa, 0x55555555, 0x55555555, 0x55555555, 0x5555, 0x0, 0x0,
    0x0, 0xaaaa0000, 0x0, 0x0, 0x55555555, 0x55555555, 0x55555555, 0x5555, 0xaaaa, 0xaaaa, 0x0, 0x0,
    0x0, 0x0, 0x0, 0x0, 0x0, 0x0, 0x0, 0x0, 0x0, 0x0, 0x0, 0x0,
    0x0, 0x0, 0x0, 0x0, 0x0, 0x0, 0x0, 0x0, 0x0, 0x0, 0x0, 0x0,
    0xa590a8c, 0xaf80b2d, 0x1290bb4, 0x1980be5, 0x490b54, 0xe0b5d, 0xee007f, 0x0, 0xaab0ade, 0x120b3f, 0x1630bce, 0x1d20bff,
    0x830b6e, 0x450b77, 0x12500b4, 0x0, 0xa5c0a40, 0xafd0ae1, 0x12c0bb8, 0x19d0be9, 0x4c0b58, 0x20050, 0xe20073, 0x0,
    0xaae0a92, 0x170b43, 0x1660bd2, 0x1d70c03, 0x860b72, 0x39008a, 0x11900a8, 0x0, 0xa600a45, 0xb010ae4, 0x1300bbd, 0x1a10bec,
    0xb80b85, 0x120b8c, 0xf20083, 0x0, 0xab20a97, 0x1b0b46, 0x16a0bd7, 0x1db0c06, 0xf20b9f, 0x490ba6, 0x12900b8, 0x0,
    0xa650a49, 0xb040ae8, 0x1350119, 0x1a40188, 0xbd0b89, 0x700c1, 0xe70076, 0x0, 0xab70a9b, 0x1e0002, 0x16f0153, 0x1de01c2,
    0xf70ba3, 0x3c00fb, 0x11c00ad, 0x0, 0xa690a4c, 0xb080aed, 0x139011c, 0x1a8018d, 0x59003c, 0x170045, 0xf70086, 0x0,
    0xabb0a9e, 0x220007, 0x1730156, 0x1e201c7, 0x930076, 0x4c007f, 0x12c00bd, 0x0, 0xa6c0a50, 0xb0d0af1, 0x13c0120, 0x1ad0191,
    0x5c0040, 0xb0060, 0xeb007a, 0x0, 0xabe0aa2, 0x27000b, 0x176015a, 0x1e701cb, 0x96007a, 0x40009a, 0x12000b1, 0x0,
    0xa700a55, 0xb110af4, 0x1400125, 0x1b10194, 0xc800ad, 0x1b00b4, 0xfb008a, 0x0, 0xac20aa7, 0x2b000e, 0x17a015f, 0x1eb01ce,
    0x10200e7, 0x5000ee, 0x13000c1, 0x0, 0x550039, 0xc400a8, 0x550039, 0xc400a8, 0xcd00b1, 0x1e00d1, 0xfe008f, 0x0,
    0x8f0073, 0xfe00e2, 0x8f0073, 0xfe00e2, 0x10700eb, 0x55010b, 0x13500c4, 0x0, 0x0, 0x3fff0000, 0x0, 0x0,
    0x0, 0x0, 0x0, 0x0, 0x0, 0x3fff3fff, 0xaaaaaaaa, 0xaaaaaaaa, 0xaaaaaaaa, 0xaaaa, 0x0, 0x55550000,
    0x55550000, 0x55550000, 0xaaaaaaaa, 0xaaaaaaaa, 0xaaaaaaaa, 0xaaaa, 0x55555555, 0x55555555, 0x55555555, 0x5555, 0x0, 0x0,
    0x0, 0xaaaa0000, 0x0, 0x0, 0x55555555, 0x55555555, 0x55555555, 0x5555, 0xaaaa, 0xaaaa, 0x0, 0x0,
    0x0, 0x0, 0x0, 0x0, 0x0, 0x0, 0x0, 0x0, 0x0, 0x0, 0x0, 0x0,
    0x0, 0x0, 0x0, 0x0, 0x0, 0x0, 0x0, 0x0, 0x0, 0x0, 0x0, 0x0,
    0xf70a5c, 0xfe0a65, 0x1170a7c, 0xb960a85, 0x1070a6c, 0x220b0d, 0x32002b, 0x0, 0xfb0a60, 0x1020a69, 0xb930a80, 0xb9a0a89,
    0x10b0a70, 0x270b11, 0x37002e, 0x0, 0x12c0aae, 0x1350ab7, 0x14c0ace, 0xbad0ad7, 0x13c0abe, 0x590176, 0x690060, 0x0,
    0x1300ab2, 0x1390abb, 0xba80ad2, 0xbb10adb, 0x1400ac2, 0x5c017a, 0x6c0065, 0x0, 0x1660afd, 0x16f0b04, 0x1860b1d, 0xbc70b24,
    0x10e0a75, 0x930b14, 0xa3009a, 0x0, 0x16a0b01, 0x1730b08, 0xbc20b21, 0xbcb0b28, 0x1120a79, 0x960b18, 0xa6009f, 0x0,
    0x19d0017, 0x1a4001e, 0x1bd0037, 0xbdc0b36, 0x1450ac7, 0xc8017f, 0xd800d1, 0x0, 0x1a1001b, 0x1a80022, 0xbd90b33, 0xbe00b3a,
    0x1490acb, 0xcd0183, 0xdd00d4, 0x0, 0x1d7004c, 0x1de0055, 0x1f7006c, 0xbf60b4d, 0x1e7005c, 0x10200cd, 0x112010b, 0x0,
    0x1db0050, 0x1e20059, 0xbf30b48, 0xbfa0b51, 0x1eb0060, 0x10700d1, 0x117010e, 0x0, 0x20c0086, 0x215008f, 0x22c00a6, 0xc0d0b67,
    0x21c0096, 0x1390256, 0x1490140, 0x0, 0x210008a, 0x2190093, 0xc080b62, 0xc110b6b, 0x220009a, 0x13c025a, 0x14c0145, 0x0,
    0x24600bd, 0x24f00c4, 0x26600dd, 0xc270b7c, 0x1ee0065, 0x17300d4, 0x183017a, 0x0, 0x24a00c1, 0x25300c8, 0xc220b79, 0xc2b0b80,
    0x1f20069, 0x17600d8, 0x186017f, 0x0, 0x1ad0027, 0x1b4002e, 0x1ad0027, 0x1b4002e, 0x225009f, 0x1a8025f, 0x1b801b1, 0x0,
    0x1b1002b, 0x1b80032, 0x1b1002b, 0x1b80032, 0x22900a3, 0x1ad0263, 0x1bd01b4, 0x0, 0x3f0003, 0x3fff03ff, 0xff000f, 0xff0fff,
    0x30003, 0x3ff03ff, 0xf000f, 0xfff0fff, 0x3f003f, 0x3fff3fff, 0xaaaaaaaa, 0xaaaaaaaa, 0xaaaaaaaa, 0xaaaa, 0x0, 0x55550000,
    0x55550000, 0x55550000, 0xaaaaaaaa, 0xaaaaaaaa, 0xaaaaaaaa, 0xaaaa, 0x55555555, 0x55555555, 0x55555555, 0x5555, 0x0, 0x0,
    0x0, 0xaaaa0000, 0x0, 0x0, 0x55555555, 0x55555555, 0x55555555, 0x5555, 0xaaaa, 0xaaaa, 0x0, 0x0,
    0x0, 0x0, 0x0, 0x0, 0x0, 0x0, 0x0, 0x0, 0x0, 0x0, 0x0, 0x0,
    0x0, 0x0, 0x0, 0x0, 0x0, 0x0, 0x0, 0x0, 0x0, 0x0, 0x0, 0x0,
    0x1400125, 0x1b10194, 0x30002e5, 0x9d109b4, 0x2200205, 0x1db020c, 0x2bb024a, 0x0, 0x17a015f, 0x1eb01ce, 0x97a031f, 0xa230a06,
    0x25a023f, 0x2100246, 0x2f00281, 0x0, 0x1450129, 0x1b40198, 0x30502e9, 0x9d409b8, 0x2250209, 0x1de0229, 0x2be024f, 0x0,
    0x17f0163, 0x1ee01d2, 0x97f0963, 0xa260a0a, 0x25f0243, 0x2150263, 0x2f50284, 0x0, 0x149012c, 0x1b8019d, 0x30902ec, 0x9d809bd,
    0x2910274, 0x1e2027d, 0x2c20253, 0x0, 0x1830166, 0x1f201d7, 0x9830966, 0xa2a0a0f, 0x2cb02ae, 0x21902b7, 0x2f90288, 0x0,
    0x14c0130, 0x1bd01a1, 0x30c02f0, 0x9dd09c1, 0x2940278, 0x1e70298, 0x2c70256, 0x0, 0x186016a, 0x1f701db, 0x986096a, 0xa2f0a13,
    0x2ce02b2, 0x21c02d2, 0x2fc028d, 0x0, 0xba80135, 0xbd901a4, 0xc6802f5, 0x9e109c4, 0xc080215, 0x1eb021c, 0x2cb025a, 0x0,
    0xbc2016f, 0xbf301de, 0x98a096f, 0xa330a16, 0xc22024f, 0x2200256, 0x3000291, 0x0, 0xbad0139, 0xbdc01a8, 0xc6d02f9, 0x9e409c8,
    0xc0d0219, 0x1ee0c11, 0x2ce025f, 0x0, 0xbc70173, 0xbf601e2, 0x98f0973, 0xa360a1a, 0xc270253, 0x2250c2b, 0x3050294, 0x0,
    0xbb1013c, 0xbe001ad, 0xc7102fc, 0x9e809cd, 0xc390284, 0x1f2028d, 0x2d20263, 0x0, 0xbcb0176, 0xbfa01e7, 0x9930976, 0xa3a0a1f,
    0xc5302be, 0x22902c7, 0x3090298, 0x0, 0x22c0210, 0x29d0281, 0x22c0210, 0x29d0281, 0xc3c0288, 0x1f70c40, 0x2d70266, 0x0,
    0x266024a, 0x2d702bb, 0x266024a, 0x2d702bb, 0xc5602c2, 0x22c0c5a, 0x30c029d, 0x0, 0x3f0003, 0x3fff03ff, 0xff000f, 0xff0fff,
    0x30003, 0x3ff03ff, 0xf000f, 0xfff0fff, 0x3f003f, 0x3fff3fff, 0xaaaaaaaa, 0xaaaaaaaa, 0xaaaaaaaa, 0xaaaa, 0x0, 0x55550000,
    0x55550000, 0x55550000, 0xaaaaaaaa, 0xaaaaaaaa, 0xaaaaaaaa, 0xaaaa, 0x55555555, 0x55555555, 0x55555555, 0x5555, 0x0, 0x0,
    0x0, 0xaaaa0000, 0x0, 0x0, 0x55555555, 0x55555555, 0x55555555, 0x5555, 0xaaaa, 0xaaaa, 0x0, 0x0,
    0x0, 0x0, 0x0, 0x0, 0x0, 0x0, 0x0, 0x0, 0x0, 0x0, 0x0, 0x0,
    0x0, 0x0, 0x0, 0x0, 0x0, 0x0, 0x0, 0x0, 0x0, 0x0, 0x0, 0x0,
    0x25a00d1, 0x23f00b4, 0x25600cd, 0x23f00b4, 0x24600bd, 0x15f012c, 0x1660163, 0x0, 0x25600cd, 0x24300b8, 0x25a00d1, 0x24300b8,
    0x24a00c1, 0x1530130, 0x15a0156, 0x0, 0x291010b, 0x27400ee, 0x28d0107, 0x27400ee, 0x27d00f7, 0x19402b7, 0x19d0198, 0x0,
    0x28d0107, 0x27800f2, 0x291010b, 0x27800f2, 0x28100fb, 0x18802bb, 0x191018d, 0x0, 0x2cb0140, 0x2ae0125, 0x2c7013c, 0x2ae0125,
    0x24f00c4, 0x1ce0135, 0x1d701d2, 0x0, 0x2c7013c, 0x2b20129, 0x2cb0140, 0x2b20129, 0x25300c8, 0x1c20139, 0x1cb01c7, 0x0,
    0x300017a, 0x2e5015f, 0x2fc0176, 0x2e5015f, 0x28400fe, 0x20502be, 0x20c0209, 0x0, 0x2fc0176, 0x2e90163, 0x300017a, 0x2e90163,
    0x2880102, 0x1f902c2, 0x20001fc, 0x0, 0x97a01b1, 0x31f0194, 0x97601ad, 0x31f0194, 0x966019d, 0x23f020c, 0x2460243, 0x0,
    0x97601ad, 0x9630198, 0x97a01b1, 0x9630198, 0x96a01a1, 0x2330210, 0x23a0236, 0x0, 0x9d101eb, 0x9b401ce, 0x9cd01e7, 0x9b401ce,
    0x9bd01d7, 0x2740a0f, 0x27d0278, 0x0, 0x9cd01e7, 0x9b801d2, 0x9d101eb, 0x9b801d2, 0x9c101db, 0x2680a13, 0x271026d, 0x0,
    0xa230220, 0xa060205, 0xa1f021c, 0xa060205, 0x96f01a4, 0x2ae0215, 0x2b702b2, 0x0, 0xa1f021c, 0xa0a0209, 0xa230220, 0xa0a0209,
    0x97301a8, 0x2a20219, 0x2ab02a7, 0x0, 0x2ec0166, 0x2f5016f, 0x2ec0166, 0x2f5016f, 0x9c401de, 0x2e50a16, 0x2ec02e9, 0x0,
    0x2f0016a, 0x2f90173, 0x2f0016a, 0x2f90173, 0x9c801e2, 0x2d90a1a, 0x2e002dc, 0x0, 0x3f0003, 0x3fff03ff, 0xff000f, 0xff0fff,
    0x30003, 0x3ff03ff, 0xf000f, 0xfff0fff, 0x3f003f, 0x3fff3fff, 0xffffffff, 0xffffffff, 0xffffffff, 0xffff, 0x0, 0x0,
    0x0, 0x0, 0x0, 0x0, 0x0, 0x0, 0xffffffff, 0xffffffff, 0xffffffff, 0xffff, 0xffffffff, 0xffffffff,
    0x0, 0x0, 0xffffffff, 0xffff, 0x0, 0x0, 0x0, 0x0, 0x0, 0x0, 0x0, 0x0,
    0x0, 0x0, 0x0, 0x0, 0x0, 0x0, 0x0, 0x0, 0x0, 0x0, 0x0, 0x0,
    0x0, 0x0, 0x0, 0x0, 0x0, 0x0, 0x0, 0x0, 0x0, 0x0, 0x0, 0x0,
    0x2430c2e, 0xb80b85, 0x2090c14, 0xb80b85, 0x1290bb4, 0xa80bbd, 0x11900e2, 0x0, 0x2090c14, 0xf20b9f, 0x2430c2e, 0xf20b9f,
    0x1630bce, 0x20bd7, 0x730039, 0x0, 0x2460c32, 0xbd0b89, 0x20c0c18, 0xbd0b89, 0x12c0bb8, 0xad0130, 0x11c00e7, 0x0,
    0x20c0c18, 0xf70ba3, 0x2460c32, 0xf70ba3, 0x1660bd2, 0x7016a, 0x76003c, 0x0, 0x24a0c37, 0xc10b8c, 0x2100c1d, 0xc10b8c,
    0x1980be5, 0xb10bec, 0x12000eb, 0x0, 0x2100c1d, 0xfb0ba6, 0x24a0c37, 0xfb0ba6, 0x1d20bff, 0xb0c06, 0x7a0040, 0x0,
    0x24f0233, 0xc400a8, 0x21501f9, 0xc400a8, 0x19d0be9, 0xb401a1, 0x12500ee, 0x0, 0x21501f9, 0xfe00e2, 0x24f0233, 0xfe00e2,
    0x1d70c03, 0xe01db, 0x7f0045, 0x0, 0x2530236, 0xc800ad, 0x21901fc, 0xc800ad, 0x139011c, 0xb80125, 0x12900f2, 0x0,
    0x21901fc, 0x10200e7, 0x2530236, 0x10200e7, 0x1730156, 0x12015f, 0x830049, 0x0, 0x256023a, 0xcd00b1, 0x21c0200, 0xcd00b1,
    0x13c0120, 0xbd0140, 0x12c00f7, 0x0, 0x21c0200, 0x10700eb, 0x256023a, 0x10700eb, 0x176015a, 0x17017a, 0x86004c, 0x0,
    0x25a023f, 0xd100b4, 0x2200205, 0xd100b4, 0x1a8018d, 0xc10194, 0x13000fb, 0x0, 0x2200205, 0x10b00ee, 0x25a023f, 0x10b00ee,
    0x1e201c7, 0x1b01ce, 0x8a0050, 0x0, 0x1350119, 0x1a40188, 0x1350119, 0x1a40188, 0x1ad0191, 0xc401b1, 0x13500fe, 0x0,
    0x16f0153, 0x1de01c2, 0x16f0153, 0x1de01c2, 0x1e701cb, 0x1e01eb, 0x8f0055, 0x0, 0x3f0003, 0x3fff03ff, 0xff000f, 0xff0fff,
    0x30003, 0x3ff03ff, 0xf000f, 0xfff0fff, 0x3f003f, 0x3fff3fff, 0xffffffff, 0xffffffff, 0xffffffff, 0xffff, 0x0, 0x0,
    0x0, 0x0, 0x0, 0x0, 0x0, 0x0, 0xffffffff, 0xffffffff, 0xffffffff, 0xffff, 0xffffffff, 0xffffffff,
    0x0, 0x0, 0xffffffff, 0xffff, 0x0, 0x0, 0x0, 0x0, 0x0, 0x0, 0x0, 0x0,
    0x0, 0x0, 0x0, 0x0, 0x0, 0x0, 0x0, 0x0, 0x0, 0x0, 0x0, 0x0,
    0x0, 0x0, 0x0, 0x0, 0x0, 0x0, 0x0, 0x0, 0x0, 0x0, 0x0, 0x0,
    0xf70a5c, 0xfe0a65, 0x1170a7c, 0xb960a85, 0x1070a6c, 0xc80b0d, 0xd800d1, 0x0, 0xfb0a60, 0x1020a69, 0xb930a80, 0xb9a0a89,
    0x10b0a70, 0xcd0b11, 0xdd00d4, 0x0, 0x12c0aae, 0x1350ab7, 0x14c0ace, 0xbad0ad7, 0x13c0abe, 0x220176, 0x32002b, 0x0,
    0x1300ab2, 0x1390abb, 0xba80ad2, 0xbb10adb, 0x1400ac2, 0x27017a, 0x37002e, 0x0, 0x1660afd, 0x16f0b04, 0x1860b1d, 0xbc70b24,
    0x10e0a75, 0x1020b14, 0x112010b, 0x0, 0x16a0b01, 0x1730b08, 0xbc20b21, 0xbcb0b28, 0x1120a79, 0x1070b18, 0x117010e, 0x0,
    0x19d0017, 0x1a4001e, 0x1bd0037, 0xbdc0b36, 0x1450ac7, 0x59017f, 0x690060, 0x0, 0x1a1001b, 0x1a80022, 0xbd90b33, 0xbe00b3a,
    0x1490acb, 0x5c0183, 0x6c0065, 0x0, 0x1d7004c, 0x1de0055, 0x1f7006c, 0xbf60b4d, 0x1e7005c, 0x13900cd, 0x1490140, 0x0,
    0x1db0050, 0x1e20059, 0xbf30b48, 0xbfa0b51, 0x1eb0060, 0x13c00d1, 0x14c0145, 0x0, 0x20c0086, 0x215008f, 0x22c00a6, 0xc0d0b67,
    0x21c0096, 0x930256, 0xa3009a, 0x0, 0x210008a, 0x2190093, 0xc080b62, 0xc110b6b, 0x220009a, 0x96025a, 0xa6009f, 0x0,
    0x24600bd, 0x24f00c4, 0x26600dd, 0xc270b7c, 0x1ee0065, 0x17300d4, 0x183017a, 0x0, 0x24a00c1, 0x25300c8, 0xc220b79, 0xc2b0b80,
    0x1f20069, 0x17600d8, 0x186017f, 0x0, 0x1ad0027, 0x1b4002e, 0x1ad0027, 0x1b4002e, 0x225009f, 0x1a8025f, 0x1b801b1, 0x0,
    0x1b1002b, 0x1b80032, 0x1b1002b, 0x1b80032, 0x22900a3, 0x1ad0263, 0x1bd01b4, 0x0, 0x0, 0x3fff0000, 0x0, 0x0,
    0x0, 0x0, 0x0, 0x0, 0x0, 0x3fff3fff, 0xaaaaaaaa, 0xaaaaaaaa, 0xaaaaaaaa, 0xaaaa, 0x0, 0x55550000,
    0x55550000, 0x55550000, 0xaaaaaaaa, 0xaaaaaaaa, 0xaaaaaaaa, 0xaaaa, 0x55555555, 0x55555555, 0x55555555, 0x5555, 0x0, 0x0,
    0x0, 0xaaaa0000, 0x0, 0x0, 0x55555555, 0x55555555, 0x55555555, 0x5555, 0xaaaa, 0xaaaa, 0x0, 0x0,
    0x0, 0x0, 0x0, 0x0, 0x0, 0x0, 0x0, 0x0, 0x0, 0x0, 0x0, 0x0,
    0x0, 0x0, 0x0, 0x0, 0x0, 0x0, 0x0, 0x0, 0x0, 0x0, 0x0, 0x0,
    0x1400125, 0x1b10194, 0x30002e5, 0x9d109b4, 0x2200205, 0x1db020c, 0x2bb024a, 0x0, 0x17a015f, 0x1eb01ce, 0x97a031f, 0xa230a06,
    0x25a023f, 0x2100246, 0x2f00281, 0x0, 0x1450129, 0x1b40198, 0x30502e9, 0x9d409b8, 0x2250209, 0x1de0229, 0x2be024f, 0x0,
    0x17f0163, 0x1ee01d2, 0x97f0963, 0xa260a0a, 0x25f0243, 0x2150263, 0x2f50284, 0x0, 0x149012c, 0x1b8019d, 0x30902ec, 0x9d809bd,
    0x2910274, 0x1e2027d, 0x2c20253, 0x0, 0x1830166, 0x1f201d7, 0x9830966, 0xa2a0a0f, 0x2cb02ae, 0x21902b7, 0x2f90288, 0x0,
    0x14c0130, 0x1bd01a1, 0x30c02f0, 0x9dd09c1, 0x2940278, 0x1e70298, 0x2c70256, 0x0, 0x186016a, 0x1f701db, 0x986096a, 0xa2f0a13,
    0x2ce02b2, 0x21c02d2, 0x2fc028d, 0x0, 0xba80135, 0xbd901a4, 0xc6802f5, 0x9e109c4, 0xc080215, 0x1eb021c, 0x2cb025a, 0x0,
    0xbc2016f, 0xbf301de, 0x98a096f, 0xa330a16, 0xc22024f, 0x2200256, 0x3000291, 0x0, 0xbad0139, 0xbdc01a8, 0xc6d02f9, 0x9e409c8,
    0xc0d0219, 0x1ee0c11, 0x2ce025f, 0x0, 0xbc70173, 0xbf601e2, 0x98f0973, 0xa360a1a, 0xc270253, 0x2250c2b, 0x3050294, 0x0,
    0xbb1013c, 0xbe001ad, 0xc7102fc, 0x9e809cd, 0xc390284, 0x1f2028d, 0x2d20263, 0x0, 0xbcb0176, 0xbfa01e7, 0x9930976, 0xa3a0a1f,
    0xc5302be, 0x22902c7, 0x3090298, 0x0, 0x22c0210, 0x29d0281, 0x22c0210, 0x29d0281, 0xc3c0288, 0x1f70c40, 0x2d70266, 0x0,
    0x266024a, 0x2d702bb, 0x266024a, 0x2d702bb, 0xc5602c2, 0x22c0c5a, 0x30c029d, 0x0, 0x3f0003, 0x3fff03ff, 0xff000f, 0xff0fff,
    0x30003, 0x3ff03ff, 0xf000f, 0xfff0fff, 0x3f003f, 0x3fff3fff, 0xaaaaaaaa, 0xaaaaaaaa, 0xaaaaaaaa, 0xaaaa, 0x0, 0x55550000,
    0x55550000, 0x55550000, 0xaaaaaaaa, 0xaaaaaaaa, 0xaaaaaaaa, 0xaaaa, 0x55555555, 0x55555555, 0x55555555, 0x5555, 0x0, 0x0,
    0x0, 0xaaaa0000, 0x0, 0x0, 0x55555555, 0x55555555, 0x55555555, 0x5555, 0xaaaa, 0xaaaa, 0x0, 0x0,
    0x0, 0x0, 0x0, 0x0, 0x0, 0x0, 0x0, 0x0, 0x0, 0x0, 0x0, 0x0,
    0x0, 0x0, 0x0, 0x0, 0x0, 0x0, 0x0, 0x0, 0x0, 0x0, 0x0, 0x0,
    0xc2e0b85, 0xc370b8c, 0x24600bd, 0x24f00c4, 0x23600ad, 0x153011c, 0x163015a, 0x0, 0xc320b89, 0x23300a8, 0x24a00c1, 0x25300c8,
    0x23a00b1, 0x1560120, 0x166015f, 0x0, 0xc450b9f, 0xc4c0ba6, 0x27d00f7, 0x28400fe, 0x26d00e7, 0x18802a7, 0x1980191, 0x0,
    0xc490ba3, 0x26800e2, 0x28100fb, 0x2880102, 0x27100eb, 0x18d02ab, 0x19d0194, 0x0, 0xc5f0bb4, 0xc660bbd, 0x2b7012c, 0x2be0135,
    0x23f00b4, 0x1c20125, 0x1d201cb, 0x0, 0xc630bb8, 0x2a20119, 0x2bb0130, 0x2c20139, 0x24300b8, 0x1c70129, 0x1d701ce, 0x0,
    0xc740bce, 0xc7d0bd7, 0x2ec0166, 0x2f5016f, 0x27400ee, 0x1f902ae, 0x2090200, 0x0, 0xc780bd2, 0x2d90153, 0x2f0016a, 0x2f90173,
    0x27800f2, 0x1fc02b2, 0x20c0205, 0x0, 0x9960be5, 0x99f0bec, 0x966019d, 0x96f01a4, 0x316018d, 0x23301fc, 0x243023a, 0x0,
    0x99a0be9, 0x3130188, 0x96a01a1, 0x97301a8, 0x31a0191, 0x2360200, 0x246023f, 0x0, 0x9ed0bff, 0x9a40c06, 0x9bd01d7, 0x9c401de,
    0x9ad01c7, 0x26809ff, 0x2780271, 0x0, 0x9a10c03, 0x9a801c2, 0x9c101db, 0x9c801e2, 0x9b101cb, 0x26d0a03, 0x27d0274, 0x0,
    0xa3f0c14, 0x9f60c1d, 0xa0f020c, 0xa160215, 0x31f0194, 0x2a20205, 0x2b202ab, 0x0, 0x9f30c18, 0x9fa01f9, 0xa130210, 0xa1a0219,
    0x9630198, 0x2a70209, 0x2b702ae, 0x0, 0x2dc0156, 0x2e5015f, 0x2dc0156, 0x2e5015f, 0x9b401ce, 0x2d90a06, 0x2e902e0, 0x0,
    0x2e0015a, 0x2e90163, 0x2e0015a, 0x2e90163, 0x9b801d2, 0x2dc0a0a, 0x2ec02e5, 0x0, 0x3f0003, 0x3fff03ff, 0xff000f, 0xff0fff,
    0x30003, 0x3ff03ff, 0xf000f, 0xfff0fff, 0x3f003f, 0x3fff3fff, 0xaaaaaaaa, 0xaaaaaaaa, 0xaaaaaaaa, 0xaaaa, 0x0, 0x55550000,
    0x55550000, 0x55550000, 0xaaaaaaaa, 0xaaaaaaaa, 0xaaaaaaaa, 0xaaaa, 0x55555555, 0x55555555, 0x55555555, 0x5555, 0x0, 0x0,
    0x0, 0xaaaa0000, 0x0, 0x0, 0x55555555, 0x55555555, 0x55555555, 0x5555, 0xaaaa, 0xaaaa, 0x0, 0x0,
    0x0, 0x0, 0x0, 0x0, 0x0, 0x0, 0x0, 0x0, 0x0, 0x0, 0x0, 0x0,
    0x0, 0x0, 0x0, 0x0, 0x0, 0x0, 0x0, 0x0, 0x0, 0x0, 0x0, 0x0,
    0x2430c2e, 0xb80b85, 0x2090c14, 0xb80b85, 0x1290bb4, 0xb40bbd, 0x12500ee, 0x0, 0x2090c14, 0xf20b9f, 0x2430c2e, 0xf20b9f,
    0x1630bce, 0xe0bd7, 0x7f0045, 0x0, 0x2460c32, 0xbd0b89, 0x20c0c18, 0xbd0b89, 0x12c0bb8, 0xa80130, 0x11900e2, 0x0,
    0x20c0c18, 0xf70ba3, 0x2460c32, 0xf70ba3, 0x1660bd2, 0x2016a, 0x730039, 0x0, 0x24a0c37, 0xc10b8c, 0x2100c1d, 0xc10b8c,
    0x1980be5, 0xb80bec, 0x12900f2, 0x0, 0x2100c1d, 0xfb0ba6, 0x24a0c37, 0xfb0ba6, 0x1d20bff, 0x120c06, 0x830049, 0x0,
    0x24f0233, 0xc400a8, 0x21501f9, 0xc400a8, 0x19d0be9, 0xad01a1, 0x11c00e7, 0x0, 0x21501f9, 0xfe00e2, 0x24f0233, 0xfe00e2,
    0x1d70c03, 0x701db, 0x76003c, 0x0, 0x2530236, 0xc800ad, 0x21901fc, 0xc800ad, 0x139011c, 0xbd0125, 0x12c00f7, 0x0,
    0x21901fc, 0x10200e7, 0x2530236, 0x10200e7, 0x1730156, 0x17015f, 0x86004c, 0x0, 0x256023a, 0xcd00b1, 0x21c0200, 0xcd00b1,
    0x13c0120, 0xb10140, 0x12000eb, 0x0, 0x21c0200, 0x10700eb, 0x256023a, 0x10700eb, 0x176015a, 0xb017a, 0x7a0040, 0x0,
    0x25a023f, 0xd100b4, 0x2200205, 0xd100b4, 0x1a8018d, 0xc10194, 0x13000fb, 0x0, 0x2200205, 0x10b00ee, 0x25a023f, 0x10b00ee,
    0x1e201c7, 0x1b01ce, 0x8a0050, 0x0, 0x1350119, 0x1a40188, 0x1350119, 0x1a40188, 0x1ad0191, 0xc401b1, 0x13500fe, 0x0,
    0x16f0153, 0x1de01c2, 0x16f0153, 0x1de01c2, 0x1e701cb, 0x1e01eb, 0x8f0055, 0x0, 0x0, 0x3fff0000, 0x0, 0x0,
    0x0, 0x0, 0x0, 0x0, 0x0, 0x3fff3fff, 0xffffffff, 0xffffffff, 0xffffffff, 0xffff, 0x0, 0x0,
    0x0, 0x0, 0x0, 0x0, 0x0, 0x0, 0xffffffff, 0xffffffff, 0xffffffff, 0xffff, 0xffffffff, 0xffffffff,
    0x0, 0x0, 0xffffffff, 0xffff, 0x0, 0x0, 0x0, 0x0, 0x0, 0x0, 0x0, 0x0,
    0x0, 0x0, 0x0, 0x0, 0x0, 0x0, 0x0, 0x0, 0x0, 0x0, 0x0, 0x0,
    0x0, 0x0, 0x0, 0x0, 0x0, 0x0, 0x0, 0x0, 0x0, 0x0, 0x0, 0x0,
    0xf70a5c, 0xfe0a65, 0x1170a7c, 0xb960a85, 0x1070a6c, 0xc80b0d, 0xd800d1, 0x0, 0xfb0a60, 0x1020a69, 0xb930a80, 0xb9a0a89,
    0x10b0a70, 0xcd0b11, 0xdd00d4, 0x0, 0x12c0aae, 0x1350ab7, 0x14c0ace, 0xbad0ad7, 0x13c0abe, 0x220176, 0x32002b, 0x0,
    0x1300ab2, 0x1390abb, 0xba80ad2, 0xbb10adb, 0x1400ac2, 0x27017a, 0x37002e, 0x0, 0x1660afd, 0x16f0b04, 0x1860b1d, 0xbc70b24,
    0x10e0a75, 0x1020b14, 0x112010b, 0x0, 0x16a0b01, 0x1730b08, 0xbc20b21, 0xbcb0b28, 0x1120a79, 0x1070b18, 0x117010e, 0x0,
    0x19d0017, 0x1a4001e, 0x1bd0037, 0xbdc0b36, 0x1450ac7, 0x59017f, 0x690060, 0x0, 0x1a1001b, 0x1a80022, 0xbd90b33, 0xbe00b3a,
    0x1490acb, 0x5c0183, 0x6c0065, 0x0, 0x1d7004c, 0x1de0055, 0x1f7006c, 0xbf60b4d, 0x1e7005c, 0x13900cd, 0x1490140, 0x0,
    0x1db0050, 0x1e20059, 0xbf30b48, 0xbfa0b51, 0x1eb0060, 0x13c00d1, 0x14c0145, 0x0, 0x20c0086, 0x215008f, 0x22c00a6, 0xc0d0b67,
    0x21c0096, 0x930256, 0xa3009a, 0x0, 0x210008a, 0x2190093, 0xc080b62, 0xc110b6b, 0x220009a, 0x96025a, 0xa6009f, 0x0,
    0x24600bd, 0x24f00c4, 0x26600dd, 0xc270b7c, 0x1ee0065, 0x17300d4, 0x183017a, 0x0, 0x24a00c1, 0x25300c8, 0xc220b79, 0xc2b0b80,
    0x1f20069, 0x17600d8, 0x186017f, 0x0, 0x1ad0027, 0x1b4002e, 0x1ad0027, 0x1b4002e, 0x225009f, 0x1a8025f, 0x1b801b1, 0x0,
    0x1b1002b, 0x1b80032, 0x1b1002b, 0x1b80032, 0x22900a3, 0x1ad0263, 0x1bd01b4, 0x0, 0x0, 0x3fff0000, 0x0, 0x0,
    0x0, 0x0, 0x0, 0x0, 0x0, 0x3fff3fff, 0xaaaaaaaa, 0xaaaaaaaa, 0xaaaaaaaa, 0xaaaa, 0x0, 0x55550000,
    0x55550000, 0x55550000, 0xaaaaaaaa, 0xaaaaaaaa, 0xaaaaaaaa, 0xaaaa, 0x55555555, 0x55555555, 0x55555555, 0x5555, 0x0, 0x0,
    0x0, 0xaaaa0000, 0x0, 0x0, 0x55555555, 0x55555555, 0x55555555, 0x5555, 0xaaaa, 0xaaaa, 0x0, 0x0,
    0x0, 0x0, 0x0, 0x0, 0x0, 0x0, 0x0, 0x0, 0x0, 0x0, 0x0, 0x0,
    0x0, 0x0, 0x0, 0x0, 0x0, 0x0, 0x0, 0x0, 0x0, 0x0, 0x0, 0x0,
    0x1400125, 0x1b10194, 0x30002e5, 0x9d109b4, 0x2200205, 0x1db020c, 0x2bb024a, 0x0, 0x17a015f, 0x1eb01ce, 0x97a031f, 0xa230a06,
    0x25a023f, 0x2100246, 0x2f00281, 0x0, 0x1450129, 0x1b40198, 0x30502e9, 0x9d409b8, 0x2250209, 0x1de0229, 0x2be024f, 0x0,
    0x17f0163, 0x1ee01d2, 0x97f0963, 0xa260a0a, 0x25f0243, 0x2150263, 0x2f50284, 0x0, 0x149012c, 0x1b8019d, 0x30902ec, 0x9d809bd,
    0x2910274, 0x1e2027d, 0x2c20253, 0x0, 0x1830166, 0x1f201d7, 0x9830966, 0xa2a0a0f, 0x2cb02ae, 0x21902b7, 0x2f90288, 0x0,
    0x14c0130, 0x1bd01a1, 0x30c02f0, 0x9dd09c1, 0x2940278, 0x1e70298, 0x2c70256, 0x0, 0x186016a, 0x1f701db, 0x986096a, 0xa2f0a13,
    0x2ce02b2, 0x21c02d2, 0x2fc028d, 0x0, 0xba80135, 0xbd901a4, 0xc6802f5, 0x9e109c4, 0xc080215, 0x1eb021c, 0x2cb025a, 0x0,
    0xbc2016f, 0xbf301de, 0x98a096f, 0xa330a16, 0xc22024f, 0x2200256, 0x3000291, 0x0, 0xbad0139, 0xbdc01a8, 0xc6d02f9, 0x9e409c8,
    0xc0d0219, 0x1ee0c11, 0x2ce025f, 0x0, 0xbc70173, 0xbf601e2, 0x98f0973, 0xa360a1a, 0xc270253, 0x2250c2b, 0x3050294, 0x0,
    0xbb1013c, 0xbe001ad, 0xc7102fc, 0x9e809cd, 0xc390284, 0x1f2028d, 0x2d20263, 0x0, 0xbcb0176, 0xbfa01e7, 0x9930976, 0xa3a0a1f,
    0xc5302be, 0x22902c7, 0x3090298, 0x0, 0x22c0210, 0x29d0281, 0x22c0210, 0x29d0281, 0xc3c0288, 0x1f70c40, 0x2d70266, 0x0,
    0x266024a, 0x2d702bb, 0x266024a, 0x2d702bb, 0xc5602c2, 0x22c0c5a, 0x30c029d, 0x0, 0x3f0003, 0x3fff03ff, 0xff000f, 0xff0fff,
    0x30003, 0x3ff03ff, 0xf000f, 0xfff0fff, 0x3f003f, 0x3fff3fff, 0xaaaaaaaa, 0xaaaaaaaa, 0xaaaaaaaa, 0xaaaa, 0x0, 0x55550000,
    0x55550000, 0x55550000, 0xaaaaaaaa, 0xaaaaaaaa, 0xaaaaaaaa, 0xaaaa, 0x55555555, 0x55555555, 0x55555555, 0x5555, 0x0, 0x0,
    0x0, 0xaaaa0000, 0x0, 0x0, 0x55555555, 0x55555555, 0x55555555, 0x5555, 0xaaaa, 0xaaaa, 0x0, 0x0,
    0x0, 0x0, 0x0, 0x0, 0x0, 0x0, 0x0, 0x0, 0x0, 0x0, 0x0, 0x0,
    0x0, 0x0, 0x0, 0x0, 0x0, 0x0, 0x0, 0x0, 0x0, 0x0, 0x0, 0x0,
    0x25a00d1, 0x23f00b4, 0x25600cd, 0x23f00b4, 0x24600bd, 0x15f012c, 0x1660163, 0x0, 0x25600cd, 0x24300b8, 0x25a00d1, 0x24300b8,
    0x24a00c1, 0x1530130, 0x15a0156, 0x0, 0x291010b, 0x27400ee, 0x28d0107, 0x27400ee, 0x27d00f7, 0x19402b7, 0x19d0198, 0x0,
    0x28d0107, 0x27800f2, 0x291010b, 0x27800f2, 0x28100fb, 0x18802bb, 0x191018d, 0x0, 0x2cb0140, 0x2ae0125, 0x2c7013c, 0x2ae0125,
    0x24f00c4, 0x1ce0135, 0x1d701d2, 0x0, 0x2c7013c, 0x2b20129, 0x2cb0140, 0x2b20129, 0x25300c8, 0x1c20139, 0x1cb01c7, 0x0,
    0x300017a, 0x2e5015f, 0x2fc0176, 0x2e5015f, 0x28400fe, 0x20502be, 0x20c0209, 0x0, 0x2fc0176, 0x2e90163, 0x300017a, 0x2e90163,
    0x2880102, 0x1f902c2, 0x20001fc, 0x0, 0x97a01b1, 0x31f0194, 0x97601ad, 0x31f0194, 0x966019d, 0x23f020c, 0x2460243, 0x0,
    0x97601ad, 0x9630198, 0x97a01b1, 0x9630198, 0x96a01a1, 0x2330210, 0x23a0236, 0x0, 0x9d101eb, 0x9b401ce, 0x9cd01e7, 0x9b401ce,
    0x9bd01d7, 0x2740a0f, 0x27d0278, 0x0, 0x9cd01e7, 0x9b801d2, 0x9d101eb, 0x9b801d2, 0x9c101db, 0x2680a13, 0x271026d, 0x0,
    0xa230220, 0xa060205, 0xa1f021c, 0xa060205, 0x96f01a4, 0x2ae0215, 0x2b702b2, 0x0, 0xa1f021c, 0xa0a0209, 0xa230220, 0xa0a0209,
    0x97301a8, 0x2a20219, 0x2ab02a7, 0x0, 0x2ec0166, 0x2f5016f, 0x2ec0166, 0x2f5016f, 0x9c401de, 0x2e50a16, 0x2ec02e9, 0x0,
    0x2f0016a, 0x2f90173, 0x2f0016a, 0x2f90173, 0x9c801e2, 0x2d90a1a, 0x2e002dc, 0x0, 0x3f0003, 0x3fff03ff, 0xff000f, 0xff0fff,
    0x30003, 0x3ff03ff, 0xf000f, 0xfff0fff, 0x3f003f, 0x3fff3fff, 0xffffffff, 0xffffffff, 0xffffffff, 0xffff, 0x0, 0x0,
    0x0, 0x0, 0x0, 0x0, 0x0, 0x0, 0xffffffff, 0xffffffff, 0xffffffff, 0xffff, 0xffffffff, 0xffffffff,
    0x0, 0x0, 0xffffffff, 0xffff, 0x0, 0x0, 0x0, 0x0, 0x0, 0x0, 0x0, 0x0,
    0x0, 0x0, 0x0, 0x0, 0x0, 0x0, 0x0, 0x0, 0x0, 0x0, 0x0, 0x0,
    0x0, 0x0, 0x0, 0x0, 0x0, 0x0, 0x0, 0x0, 0x0, 0x0, 0x0, 0x0
};

__global__ __launch_bounds__(512, 2) void na_main(const void* x, const void* wqkv, const void* wo, void* ws, void* out) {
    __shared__ __attribute__((aligned(1024))) char smem[163840];
    asm volatile(
        "s_mov_b32 s18, 0\n"
        "s_mov_b32 s19, 0\n"
        "s_memtime s[8:9]\n"
        "Ltop_%=:\n"
        "s_load_dwordx2 s[24:25], s[0:1], 24\n"
        "s_load_dwordx2 s[32:33], s[0:1], 8\n"
        "s_load_dwordx2 s[20:21], s[0:1], 0\n"
        "s_load_dwordx2 s[36:37], s[0:1], 16\n"
        "s_load_dwordx2 s[28:29], s[0:1], 32\n"
        "s_getpc_b64 s[10:11]\n"
        "v_and_b32 v9, 0x1c0, v0\n"
        "v_bfe_u32 v1, v0, 5, 1\n"
        "s_lshr_b32 s88, s2, 3\n"
        "v_lshl_add_u32 v9, v1, 14, v9\n"
        "s_lshl_b32 s86, s88, 9\n"
        "v_readfirstlane_b32 s3, v0\n"
        "v_add_u32 v9, s86, v9\n"
        "v_min_u32 v9, 28664, v9\n"
        "global_load_dword v21, v9, s[10:11]\n"
        "s_lshr_b32 s3, s3, 6\n"
        "s_xor_b32 s3, s3, 4\n"
        "s_mov_b32 s34, 0x36000\n"
        "s_mov_b32 s35, 0x20000\n"
        "s_mov_b32 s26, 0x3100000\n"
        "s_mov_b32 s27, 0x20000\n"
        "v_and_b32 v8, 63, v0\n"
        "s_lshr_b32 s40, s2, 0\n"
        "s_mov_b32 s41, 0\n"
        "s_cmp_lg_u32 s3, 4\n"
        "s_cbranch_scc1 Lnoad_%=\n"
        "v_mov_b32 v10, 0\n"
        "v_lshlrev_b32 v11, 2, v8\n"
        "v_add_u32 v11, 0x27000, v11\n"
        "ds_write_b32 v11, v10\n"
        "s_cmp_lt_u32 s40, 24\n"
        "s_cselect_b32 s47, 3, 2\n"
        "v_bfe_u32 v10, v0, 3, 2\n"
        "v_bfe_u32 v11, v0, 5, 1\n"
        "v_lshl_add_u32 v10, v11, s47, v10\n"
        "s_lshl_b32 s47, 2304, s47\n"
        "s_lshr_b32 s42, s40, 2\n"
        "v_lshrrev_b32 v11, 3, v0\n"
        "s_mul_i32 s42, s42, 36864\n"
        "v_and_b32 v11, 6, v11\n"
        "s_bfe_u32 s44, s40, 0x10001\n"
        "v_xor_b32 v11, v11, v0\n"
        "s_mul_i32 s44, s44, 36864\n"
        "v_and_b32 v11, 7, v11\n"
        "s_bitcmp1_b32 s40, 0\n"
        "v_mul_u32_u24 v10, 1152, v10\n"
        "s_cselect_b32 s45, 12, 0\n"
        "v_lshl_add_u32 v10, v11, 4, v10\n"
        "s_add_u32 s45, s45, 1\n"
        "v_add_u32 v22, s47, v10\n"
        "s_mul_i32 s45, s45, 384\n"
        "v_mov_b32 v12, v10\n"
        "s_add_u32 s44, s44, s45\n"
        "v_mov_b32 v13, v22\n"
        "s_add_u32 s43, s42, 4608\n"
        "s_add_u32 s45, s44, 73728\n"
        "s_add_u32 s46, s44, 147456\n"
        "s_mov_b32 s47, 0\n"
        "s_cmp_lt_u32 s40, 24\n"
        "s_cbranch_scc1 Lpa1_%=\n"
        "s_mul_i32 s48, s40, 43\n"
        "v_bfe_u32 v12, v0, 2, 4\n"
        "s_lshr_b32 s48, s48, 8\n"
        "v_and_b32 v14, 3, v0\n"
        "s_mul_i32 s49, s48, 6\n"
        "v_mul_u32_u24 v12, 384, v12\n"
        "s_sub_u32 s49, s40, s49\n"
        "v_lshl_add_u32 v12, v14, 4, v12\n"
        "s_sub_u32 s48, s48, 4\n"
        "v_add_u32 v13, 6144, v12\n"
        "s_mul_i32 s42, s49, 36864\n"
        "s_lshl_b32 s44, s48, 6\n"
        "s_add_u32 s42, s42, 768\n"
        "s_add_u32 s45, s44, 12288\n"
        "s_add_u32 s43, s42, 18432\n"
        "s_mov_b32 s47, 24448\n"
        "Lpa1_%=:\n"
        "Lnoad_%=:\n"
        "s_waitcnt lgkmcnt(0)\n"
        "s_cmp_lg_u32 s18, 0\n"
        "s_cbranch_scc1 Lnoep_%=\n"
        "s_load_dwordx2 s[6:7], s[24:25], 0xb4000\n"
        "Lnoep_%=:\n"
        "s_cmp_lg_u32 s19, 0\n"
        "s_cbranch_scc1 Lpriv_%=\n"
        "s_cmp_lg_u32 s3, 4\n"
        "s_cbranch_scc1 Lpdone_%=\n"
        "s_cmp_ge_u32 s2, 60\n"
        "s_cbranch_scc1 Lp2n_%=\n"
        "Lp1_%=:\n"
        "s_mov_b32 m0, 0x1f400\n"
        "s_nop 0\n"
        "buffer_load_dwordx4 v10, s[32:35], s42 offen lds\n"
        "s_mov_b32 m0, 0x1f800\n"
        "s_nop 0\n"
        "buffer_load_dwordx4 v22, s[32:35], s42 offen lds\n"
        "s_cmp_lt_u32 s40, 24\n"
        "s_cselect_b64 s[52:53], s[32:33], s[36:37]\n"
        "s_cselect_b32 s54, s34, 0x12000\n"
        "s_mov_b32 s55, 0x20000\n"
        "s_mov_b32 m0, 0x20400\n"
        "s_nop 0\n"
        "buffer_load_dwordx4 v12, s[52:55], s44 offen lds\n"
        "s_mov_b32 m0, 0x20800\n"
        "s_nop 0\n"
        "buffer_load_dwordx4 v13, s[52:55], s44 offen lds\n"
        "s_mov_b32 m0, 0x1fc00\n"
        "s_nop 0\n"
        "buffer_load_dwordx4 v10, s[32:35], s43 offen lds\n"
        "s_mov_b32 m0, 0x20000\n"
        "s_nop 0\n"
        "buffer_load_dwordx4 v22, s[32:35], s43 offen lds\n"
        "s_mov_b32 m0, 0x20c00\n"
        "s_nop 0\n"
        "buffer_load_dwordx4 v12, s[52:55], s45 offen lds\n"
        "s_mov_b32 m0, 0x21000\n"
        "s_nop 0\n"
        "buffer_load_dwordx4 v13, s[52:55], s45 offen lds\n"
        "s_add_u32 s44, s44, s47\n"
        "s_add_u32 s45, s45, s47\n"
        "s_mov_b32 m0, 0x21b80\n"
        "s_nop 0\n"
        "buffer_load_dwordx4 v10, s[32:35], s42 offen offset:128 lds\n"
        "s_mov_b32 m0, 0x21f80\n"
        "s_nop 0\n"
        "buffer_load_dwordx4 v22, s[32:35], s42 offen offset:128 lds\n"
        "s_mov_b32 m0, 0x22b80\n"
        "s_nop 0\n"
        "buffer_load_dwordx4 v12, s[52:55], s44 offen offset:128 lds\n"
        "s_mov_b32 m0, 0x22f80\n"
        "s_nop 0\n"
        "buffer_load_dwordx4 v13, s[52:55], s44 offen offset:128 lds\n"
        "s_mov_b32 m0, 0x22380\n"
        "s_nop 0\n"
        "buffer_load_dwordx4 v10, s[32:35], s43 offen offset:128 lds\n"
        "s_mov_b32 m0, 0x22780\n"
        "s_nop 0\n"
        "buffer_load_dwordx4 v22, s[32:35], s43 offen offset:128 lds\n"
        "s_mov_b32 m0, 0x23380\n"
        "s_nop 0\n"
        "buffer_load_dwordx4 v12, s[52:55], s45 offen offset:128 lds\n"
        "s_mov_b32 m0, 0x23780\n"
        "s_nop 0\n"
        "buffer_load_dwordx4 v13, s[52:55], s45 offen offset:128 lds\n"
        "s_add_u32 s44, s44, s47\n"
        "s_add_u32 s45, s45, s47\n"
        "s_mov_b32 m0, 0x24300\n"
        "s_nop 0\n"
        "buffer_load_dwordx4 v10, s[32:35], s42 offen offset:256 lds\n"
        "s_mov_b32 m0, 0x24700\n"
        "s_nop 0\n"
        "buffer_load_dwordx4 v22, s[32:35], s42 offen offset:256 lds\n"
        "s_mov_b32 m0, 0x25300\n"
        "s_nop 0\n"
        "buffer_load_dwordx4 v12, s[52:55], s44 offen offset:256 lds\n"
        "s_mov_b32 m0, 0x25700\n"
        "s_nop 0\n"
        "buffer_load_dwordx4 v13, s[52:55], s44 offen offset:256 lds\n"
        "s_mov_b32 m0, 0x24b00\n"
        "s_nop 0\n"
        "buffer_load_dwordx4 v10, s[32:35], s43 offen offset:256 lds\n"
        "s_mov_b32 m0, 0x24f00\n"
        "s_nop 0\n"
        "buffer_load_dwordx4 v22, s[32:35], s43 offen offset:256 lds\n"
        "s_mov_b32 m0, 0x25b00\n"
        "s_nop 0\n"
        "buffer_load_dwordx4 v12, s[52:55], s45 offen offset:256 lds\n"
        "s_mov_b32 m0, 0x25f00\n"
        "s_nop 0\n"
        "buffer_load_dwordx4 v13, s[52:55], s45 offen offset:256 lds\n"
        "s_cmp_ge_u32 s40, 24\n"
        "s_cbranch_scc1 Lp2g_%=\n"
        "s_mov_b32 m0, 0x21400\n"
        "s_nop 0\n"
        "buffer_load_dwordx4 v10, s[32:35], s46 offen lds\n"
        "s_mov_b32 m0, 0x21800\n"
        "s_nop 0\n"
        "buffer_load_dwordx4 v22, s[32:35], s46 offen lds\n"
        "s_mov_b32 m0, 0x23b80\n"
        "s_nop 0\n"
        "buffer_load_dwordx4 v10, s[32:35], s46 offen offset:128 lds\n"
        "s_mov_b32 m0, 0x23f80\n"
        "s_nop 0\n"
        "buffer_load_dwordx4 v22, s[32:35], s46 offen offset:128 lds\n"
        "s_mov_b32 m0, 0x26300\n"
        "s_nop 0\n"
        "buffer_load_dwordx4 v10, s[32:35], s46 offen offset:256 lds\n"
        "s_mov_b32 m0, 0x26700\n"
        "s_nop 0\n"
        "buffer_load_dwordx4 v22, s[32:35], s46 offen offset:256 lds\n"
        "s_mov_b64 s[10:11], s[4:5]\n"
        "Lprod_%=:\n"
        "v_and_b32 v216, 15, v0\n"
        "v_bfe_u32 v217, v0, 2, 4\n"
        "v_lshlrev_b32 v216, 5, v216\n"
        "v_lshlrev_b32 v217, 5, v217\n"
        "global_load_dwordx4 v[200:203], v216, s[10:11]\n"
        "global_load_dwordx4 v[204:207], v216, s[10:11] offset:16\n"
        "global_load_dwordx4 v[208:211], v217, s[10:11]\n"
        "global_load_dwordx4 v[212:215], v217, s[10:11] offset:16\n"
        "s_setprio 3\n"
        "s_sub_u32 s80, s3, 4\n"
        "v_and_b32 v23, 15, v0\n"
        "v_bfe_u32 v18, v0, 4, 2\n"
        "v_and_b32 v19, 6, v0\n"
        "v_xor_b32 v18, v18, v19\n"
        "v_lshlrev_b32 v23, 7, v23\n"
        "v_lshl_add_u32 v18, v18, 4, v23\n"
        "v_add_u32 v18, 0x1f400, v18\n"
        "v_xor_b32 v19, 64, v18\n"
        "s_and_b32 s81, s2, 0\n"
        "s_mul_i32 s81, s81, 76800\n"
        "s_cmp_lg_u32 s19, 0\n"
        "s_cselect_b32 s81, s19, s81\n"
        "s_add_u32 s81, s81, 81920\n"
        "s_mul_i32 s82, s40, 3200\n"
        "s_add_u32 s81, s81, s82\n"
        "s_mov_b32 s84, 0x3dd53b94\n"
        "s_mov_b32 s85, 0x3dd53b94\n"
        "s_mov_b32 s87, 0x9e3779b1\n"
        "v_add_u32 v15, 0x51ed27, v8\n"
        "v_mul_lo_u32 v12, v15, s87\n"
        "v_mul_lo_u32 v13, v12, s87\n"
        "v_mul_lo_u32 v14, v13, s87\n"
        "v_mul_lo_u32 v15, v14, s87\n"
        "v_or_b32 v12, 1, v12\n"
        "v_or_b32 v13, 1, v13\n"
        "v_or_b32 v14, 1, v14\n"
        "v_or_b32 v15, 1, v15\n"
        "v_lshlrev_b32 v11, 4, v8\n"
        "s_waitcnt lgkmcnt(0)\n"
        "s_bitcmp1_b32 s18, 31\n"
        "s_cbranch_scc1 Ltg2_%=\n"
        "s_bitset1_b32 s18, 31\n"
        "s_mul_i32 s7, s7, 0x4c957f2d\n"
        "s_mul_i32 s86, s6, 0x5851f42d\n"
        "s_add_u32 s7, s7, s86\n"
        "s_mul_hi_u32 s86, s6, 0x4c957f2d\n"
        "s_add_u32 s7, s7, s86\n"
        "s_mul_i32 s6, s6, 0x4c957f2d\n"
        "s_add_u32 s6, s6, 0xf767814f\n"
        "s_addc_u32 s7, s7, 0x14057b7e\n"
        "Ltg2_%=:\n"
        "v_mov_b32 v17, s7\n"
        "Lprl_%=:\n"
        "s_lshl_b32 s82, s80, 11\n"
        "v_add_u32 v22, s82, v18\n"
        "v_add_u32 v23, s82, v19\n"
        "s_lshl_b32 s82, s80, 10\n"
        "s_add_u32 s82, s82, s81\n"
        "s_lshl4_add_u32 s83, s80, s81\n"
        "s_cmp_lg_u32 s3, 4\n"
        "s_cbranch_scc1 Lprw_%=\n"
        "s_waitcnt vmcnt(4)\n"
        "s_cmp_lg_u32 s19, 0\n"
        "s_cbranch_scc1 Lprb_%=\n"
        "Lprw_%=:\n"
        "s_barrier\n"
        "Lprb_%=:\n"
        "ds_read_b128 v[48:51], v18 offset:0\n"
        "ds_read_b128 v[56:59], v22 offset:4096\n"
        "ds_read_b128 v[52:55], v18 offset:2048\n"
        "ds_read_b128 v[60:63], v19 offset:0\n"
        "ds_read_b128 v[68:71], v23 offset:4096\n"
        "ds_read_b128 v[64:67], v19 offset:2048\n"
        "ds_read_b128 v[72:75], v18 offset:10240\n"
        "ds_read_b128 v[80:83], v22 offset:14336\n"
        "ds_read_b128 v[76:79], v18 offset:12288\n"
        "ds_read_b128 v[84:87], v19 offset:10240\n"
        "ds_read_b128 v[92:95], v23 offset:14336\n"
        "ds_read_b128 v[88:91], v19 offset:12288\n"
        "ds_read_b128 v[96:99], v18 offset:20480\n"
        "ds_read_b128 v[104:107], v22 offset:24576\n"
        "ds_read_b128 v[100:103], v18 offset:22528\n"
        "s_waitcnt lgkmcnt(13)\n"
        "v_mfma_f32_16x16x32_bf16 v[24:27], v[48:51], v[56:59], 0\n"
        "s_waitcnt lgkmcnt(12)\n"
        "v_mfma_f32_16x16x32_bf16 v[28:31], v[52:55], v[56:59], 0\n"
        "ds_read_b128 v[108:111], v19 offset:20480\n"
        "ds_read_b128 v[116:119], v23 offset:24576\n"
        "ds_read_b128 v[112:115], v19 offset:22528\n"
        "s_waitcnt lgkmcnt(13)\n"
        "v_mfma_f32_16x16x32_bf16 v[24:27], v[60:63], v[68:71], v[24:27]\n"
        "s_waitcnt lgkmcnt(12)\n"
        "v_mfma_f32_16x16x32_bf16 v[28:31], v[64:67], v[68:71], v[28:31]\n"
        "s_waitcnt lgkmcnt(10)\n"
        "v_mfma_f32_16x16x32_bf16 v[24:27], v[72:75], v[80:83], v[24:27]\n"
        "s_waitcnt lgkmcnt(9)\n"
        "v_mfma_f32_16x16x32_bf16 v[28:31], v[76:79], v[80:83], v[28:31]\n"
        "s_waitcnt lgkmcnt(7)\n"
        "v_mfma_f32_16x16x32_bf16 v[24:27], v[84:87], v[92:95], v[24:27]\n"
        "s_waitcnt lgkmcnt(6)\n"
        "v_mfma_f32_16x16x32_bf16 v[28:31], v[88:91], v[92:95], v[28:31]\n"
        "s_waitcnt lgkmcnt(4)\n"
        "v_mfma_f32_16x16x32_bf16 v[24:27], v[96:99], v[104:107], v[24:27]\n"
        "s_waitcnt lgkmcnt(3)\n"
        "v_mfma_f32_16x16x32_bf16 v[28:31], v[100:103], v[104:107], v[28:31]\n"
        "s_waitcnt lgkmcnt(1)\n"
        "v_mfma_f32_16x16x32_bf16 v[24:27], v[108:111], v[116:119], v[24:27]\n"
        "s_waitcnt lgkmcnt(0)\n"
        "v_mfma_f32_16x16x32_bf16 v[28:31], v[112:115], v[116:119], v[28:31]\n"
        "s_waitcnt vmcnt(0)\n"
        "s_nop 4\n"
        "v_pk_mul_f32 v[24:25], v[24:25], s[84:85]\n"
        "v_pk_mul_f32 v[26:27], v[26:27], s[84:85]\n"
        "v_pk_mul_f32 v[28:29], v[28:29], s[84:85]\n"
        "v_pk_mul_f32 v[30:31], v[30:31], s[84:85]\n"
        "v_cvt_pk_bf16_f32 v32, v24, v25\n"
        "v_cvt_pk_bf16_f32 v33, v26, v27\n"
        "v_cvt_pk_bf16_f32 v34, v28, v29\n"
        "v_cvt_pk_bf16_f32 v35, v30, v31\n"
        "buffer_store_dwordx4 v[32:35], v11, s[24:27], s82 offen sc1\n"
        "v_mul_lo_u32 v36, v32, v12\n"
        "v_mul_lo_u32 v37, v33, v13\n"
        "v_mul_lo_u32 v38, v34, v14\n"
        "v_mul_lo_u32 v39, v35, v15\n"
        "v_add3_u32 v16, v36, v37, v38\n"
        "v_add_u32 v16, v16, v39\n"
        "s_nop 1\n"
        "v_add_u32_dpp v16, v16, v16 row_shr:1 row_mask:0xf bank_mask:0xf\n"
        "s_nop 1\n"
        "v_add_u32_dpp v16, v16, v16 row_shr:2 row_mask:0xf bank_mask:0xf\n"
        "s_nop 1\n"
        "v_add_u32_dpp v16, v16, v16 row_shr:4 row_mask:0xf bank_mask:0xf\n"
        "s_nop 1\n"
        "v_add_u32_dpp v16, v16, v16 row_shr:8 row_mask:0xf bank_mask:0xf\n"
        "s_nop 1\n"
        "v_add_u32_dpp v16, v16, v16 row_bcast:15 row_mask:0xa bank_mask:0xf\n"
        "s_nop 1\n"
        "v_add_u32_dpp v16, v16, v16 row_bcast:31 row_mask:0xc bank_mask:0xf\n"
        "s_mov_b64 exec, 1\n"
        "s_lshl_b64 exec, exec, 63\n"
        "v_add_u32 v16, s6, v16\n"
        "buffer_store_dwordx2 v[16:17], v11, s[24:27], s83 offen offset:2064 sc1\n"
        "s_mov_b64 exec, -1\n"
        "s_setprio 0\n"
        "s_cmp_lg_u32 s19, 0\n"
        "s_cbranch_scc1 Lprn_%=\n"
        "s_barrier\n"
        "s_mov_b32 s80, 0x1000\n"
        "s_cmp_lg_u32 s3, 4\n"
        "s_cbranch_scc1 Lnopr_%=\n"
        "s_branch Lpdone_%=\n"
        "Lp2n_%=:\n"
        "v_and_b32 v216, 15, v0\n"
        "v_bfe_u32 v217, v0, 2, 4\n"
        "v_lshlrev_b32 v216, 5, v216\n"
        "v_lshlrev_b32 v217, 5, v217\n"
        "global_load_dwordx4 v[200:203], v216, s[4:5]\n"
        "global_load_dwordx4 v[204:207], v216, s[4:5] offset:16\n"
        "global_load_dwordx4 v[208:211], v217, s[4:5]\n"
        "global_load_dwordx4 v[212:215], v217, s[4:5] offset:16\n"
        "s_waitcnt vmcnt(0)\n"
        "s_branch Lpdone_%=\n"
        "Lp2g_%=:\n"
        "v_and_b32 v216, 15, v0\n"
        "v_bfe_u32 v217, v0, 2, 4\n"
        "v_lshlrev_b32 v216, 5, v216\n"
        "v_lshlrev_b32 v217, 5, v217\n"
        "global_load_dwordx4 v[200:203], v216, s[4:5]\n"
        "global_load_dwordx4 v[204:207], v216, s[4:5] offset:16\n"
        "global_load_dwordx4 v[208:211], v217, s[4:5]\n"
        "global_load_dwordx4 v[212:215], v217, s[4:5] offset:16\n"
        "v_and_b32 v23, 15, v0\n"
        "v_bfe_u32 v18, v0, 4, 2\n"
        "v_and_b32 v19, 6, v0\n"
        "v_xor_b32 v18, v18, v19\n"
        "v_lshlrev_b32 v23, 7, v23\n"
        "v_lshl_add_u32 v18, v18, 4, v23\n"
        "v_add_u32 v240, 0x1f400, v18\n"
        "v_xor_b32 v241, 64, v240\n"
        "v_and_b32 v12, 48, v0\n"
        "v_and_b32 v15, 12, v0\n"
        "v_lshlrev_b32 v22, 3, v14\n"
        "v_lshl_add_u32 v22, v15, 4, v22\n"
        "v_lshl_add_u32 v22, v12, 5, v22\n"
        "v_add_u32 v242, 0x20400, v22\n"
        "v_mul_u32_u24 v14, 6144, v14\n"
        "v_lshl_add_u32 v14, v12, 4, v14\n"
        "v_lshl_add_u32 v14, v15, 2, v14\n"
        "s_lshr_b32 s44, s48, 1\n"
        "s_mul_i32 s44, s44, 24\n"
        "s_add_u32 s44, s44, s49\n"
        "s_lshl_b32 s44, s44, 10\n"
        "s_and_b32 s45, s48, 1\n"
        "s_lshl_b32 s45, s45, 7\n"
        "s_add_u32 s44, s44, s45\n"
        "v_add_u32 v140, s44, v14\n"
        "s_waitcnt vmcnt(4) lgkmcnt(0)\n"
        "ds_read_b128 v[40:43], v240 offset:0\n"
        "ds_read_b128 v[44:47], v240 offset:2048\n"
        "ds_read_b64_tr_b16 v[48:49], v242 offset:0\n"
        "ds_read_b64_tr_b16 v[50:51], v242 offset:256\n"
        "ds_read_b64_tr_b16 v[52:53], v242 offset:32\n"
        "ds_read_b64_tr_b16 v[54:55], v242 offset:288\n"
        "ds_read_b128 v[56:59], v241 offset:0\n"
        "ds_read_b128 v[60:63], v241 offset:2048\n"
        "ds_read_b64_tr_b16 v[64:65], v242 offset:2048\n"
        "ds_read_b64_tr_b16 v[66:67], v242 offset:2304\n"
        "ds_read_b64_tr_b16 v[68:69], v242 offset:2080\n"
        "ds_read_b64_tr_b16 v[70:71], v242 offset:2336\n"
        "s_waitcnt lgkmcnt(8)\n"
        "v_mfma_f32_16x16x32_bf16 v[148:151], v[40:43], v[48:51], 0\n"
        "v_mfma_f32_16x16x32_bf16 v[152:155], v[44:47], v[48:51], 0\n"
        "s_waitcnt lgkmcnt(6)\n"
        "v_mfma_f32_16x16x32_bf16 v[156:159], v[40:43], v[52:55], 0\n"
        "v_mfma_f32_16x16x32_bf16 v[160:163], v[44:47], v[52:55], 0\n"
        "ds_read_b128 v[72:75], v240 offset:10240\n"
        "ds_read_b128 v[76:79], v240 offset:12288\n"
        "ds_read_b64_tr_b16 v[80:81], v242 offset:10240\n"
        "ds_read_b64_tr_b16 v[82:83], v242 offset:10496\n"
        "ds_read_b64_tr_b16 v[84:85], v242 offset:10272\n"
        "ds_read_b64_tr_b16 v[86:87], v242 offset:10528\n"
        "s_waitcnt lgkmcnt(8)\n"
        "v_mfma_f32_16x16x32_bf16 v[148:151], v[56:59], v[64:67], v[148:151]\n"
        "v_mfma_f32_16x16x32_bf16 v[152:155], v[60:63], v[64:67], v[152:155]\n"
        "s_waitcnt lgkmcnt(6)\n"
        "v_mfma_f32_16x16x32_bf16 v[156:159], v[56:59], v[68:71], v[156:159]\n"
        "v_mfma_f32_16x16x32_bf16 v[160:163], v[60:63], v[68:71], v[160:163]\n"
        "ds_read_b128 v[88:91], v241 offset:10240\n"
        "ds_read_b128 v[92:95], v241 offset:12288\n"
        "ds_read_b64_tr_b16 v[96:97], v242 offset:12288\n"
        "ds_read_b64_tr_b16 v[98:99], v242 offset:12544\n"
        "ds_read_b64_tr_b16 v[100:101], v242 offset:12320\n"
        "ds_read_b64_tr_b16 v[102:103], v242 offset:12576\n"
        "s_waitcnt lgkmcnt(8)\n"
        "v_mfma_f32_16x16x32_bf16 v[148:151], v[72:75], v[80:83], v[148:151]\n"
        "v_mfma_f32_16x16x32_bf16 v[152:155], v[76:79], v[80:83], v[152:155]\n"
        "s_waitcnt lgkmcnt(6)\n"
        "v_mfma_f32_16x16x32_bf16 v[156:159], v[72:75], v[84:87], v[156:159]\n"
        "v_mfma_f32_16x16x32_bf16 v[160:163], v[76:79], v[84:87], v[160:163]\n"
        "ds_read_b128 v[104:107], v240 offset:20480\n"
        "ds_read_b128 v[108:111], v240 offset:22528\n"
        "ds_read_b64_tr_b16 v[112:113], v242 offset:20480\n"
        "ds_read_b64_tr_b16 v[114:115], v242 offset:20736\n"
        "ds_read_b64_tr_b16 v[116:117], v242 offset:20512\n"
        "ds_read_b64_tr_b16 v[118:119], v242 offset:20768\n"
        "s_waitcnt lgkmcnt(8)\n"
        "v_mfma_f32_16x16x32_bf16 v[148:151], v[88:91], v[96:99], v[148:151]\n"
        "v_mfma_f32_16x16x32_bf16 v[152:155], v[92:95], v[96:99], v[152:155]\n"
        "s_waitcnt lgkmcnt(6)\n"
        "v_mfma_f32_16x16x32_bf16 v[156:159], v[88:91], v[100:103], v[156:159]\n"
        "v_mfma_f32_16x16x32_bf16 v[160:163], v[92:95], v[100:103], v[160:163]\n"
        "ds_read_b128 v[120:123], v241 offset:20480\n"
        "ds_read_b128 v[124:127], v241 offset:22528\n"
        "ds_read_b64_tr_b16 v[128:129], v242 offset:22528\n"
        "ds_read_b64_tr_b16 v[130:131], v242 offset:22784\n"
        "ds_read_b64_tr_b16 v[132:133], v242 offset:22560\n"
        "ds_read_b64_tr_b16 v[134:135], v242 offset:22816\n"
        "s_waitcnt lgkmcnt(8)\n"
        "v_mfma_f32_16x16x32_bf16 v[148:151], v[104:107], v[112:115], v[148:151]\n"
        "v_mfma_f32_16x16x32_bf16 v[152:155], v[108:111], v[112:115], v[152:155]\n"
        "s_waitcnt lgkmcnt(6)\n"
        "v_mfma_f32_16x16x32_bf16 v[156:159], v[104:107], v[116:119], v[156:159]\n"
        "v_mfma_f32_16x16x32_bf16 v[160:163], v[108:111], v[116:119], v[160:163]\n"
        "s_waitcnt lgkmcnt(2)\n"
        "v_mfma_f32_16x16x32_bf16 v[148:151], v[120:123], v[128:131], v[148:151]\n"
        "v_mfma_f32_16x16x32_bf16 v[152:155], v[124:127], v[128:131], v[152:155]\n"
        "s_waitcnt lgkmcnt(0)\n"
        "v_mfma_f32_16x16x32_bf16 v[156:159], v[120:123], v[132:135], v[156:159]\n"
        "v_mfma_f32_16x16x32_bf16 v[160:163], v[124:127], v[132:135], v[160:163]\n"
        "s_nop 3\n"
        "v_cvt_pk_bf16_f32 v232, v148, v149\n"
        "v_cvt_pk_bf16_f32 v233, v150, v151\n"
        "v_cvt_pk_bf16_f32 v234, v152, v153\n"
        "v_cvt_pk_bf16_f32 v235, v154, v155\n"
        "v_cvt_pk_bf16_f32 v236, v156, v157\n"
        "v_cvt_pk_bf16_f32 v237, v158, v159\n"
        "v_cvt_pk_bf16_f32 v238, v160, v161\n"
        "v_cvt_pk_bf16_f32 v239, v162, v163\n"
        "s_waitcnt vmcnt(0)\n"
        "s_cmp_lg_u32 s19, 0\n"
        "s_cbranch_scc1 Lp2_%=\n"
        "Lpdone_%=:\n"
        "s_and_b32 s86, s2, 1\n"
        "s_lshr_b32 s87, s2, 6\n"
        "s_lshl_b32 s86, s86, 2\n"
        "s_add_u32 s46, s86, s87\n"
        "s_bfe_u32 s47, s2, 0x30003\n"
        "s_mul_i32 s12, s46, 28\n"
        "s_mul_i32 s13, s47, 28\n"
        "s_sub_u32 s12, s12, 12\n"
        "s_sub_u32 s13, s13, 12\n"
        "s_bitcmp1_b32 s2, 2\n"
        "s_cselect_b32 s14, -1, 1\n"
        "s_cselect_b32 s16, 0x124b000, 0\n"
        "s_mul_i32 s14, s14, 86016\n"
        "s_bitcmp1_b32 s2, 1\n"
        "s_cselect_b32 s15, -1, 1\n"
        "s_cselect_b32 s17, 0x14e80, 0\n"
        "s_mul_i32 s15, s15, 384\n"
        "s_add_u32 s86, s16, s17\n"
        "s_cmp_gt_u32 s3, 4\n"
        "s_subb_u32 s91, s3, 0\n"
        "v_mov_b32 v9, s86\n"
        "v_and_b32 v7, 7, v0\n"
        "v_lshrrev_b32 v6, 3, v8\n"
        "v_lshl_add_u32 v6, s91, 3, v6\n"
        "s_mov_b32 s22, 0x1260000\n"
        "s_mov_b32 s23, 0x20000\n"
        "s_lshl_b32 s91, s91, 10\n"
        "s_cmp_eq_u32 s46, 0\n"
        "s_cselect_b32 s86, 12, 4\n"
        "s_cmp_eq_u32 s47, 0\n"
        "s_cselect_b32 s87, 4, 0\n"
        "s_add_u32 s86, s86, s87\n"
        "s_cmp_ge_u32 s3, 4\n"
        "s_cselect_b32 s86, -4, s86\n"
        "s_add_u32 s86, s86, s3\n"
        "s_mul_i32 s86, s86, 768\n"
        "s_add_u32 s10, s4, s86\n"
        "s_addc_u32 s11, s5, 0\n"
        "s_cmp_eq_u32 s3, 4\n"
        "s_cbranch_scc1 Lwave4_%=\n"
        "v_add_u32 v10, 42, v6\n"
        "v_mul_u32_u24 v11, 293, v10\n"
        "v_lshrrev_b32 v11, 12, v11\n"
        "v_mad_i32_i24 v10, v11, -14, v10\n"
        "v_add_u32 v10, 3, v10\n"
        "v_lshl_add_u32 v12, v11, 1, s12\n"
        "v_add_u32 v13, 7, v12\n"
        "v_min_u32 v12, v12, v13\n"
        "v_mad_i32_i24 v248, v12, s14, v9\n"
        "v_lshl_add_u32 v12, v10, 1, s13\n"
        "v_add_u32 v13, 7, v12\n"
        "v_min_u32 v12, v12, v13\n"
        "v_mad_i32_i24 v248, v12, s15, v248\n"
        "v_xor_b32 v12, v11, v10\n"
        "v_and_b32 v12, 2, v12\n"
        "v_and_b32 v13, 1, v11\n"
        "v_lshl_or_b32 v12, v13, 2, v12\n"
        "v_xor_b32 v12, v12, v7\n"
        "v_lshl_add_u32 v248, v12, 4, v248\n"
        "s_cmp_lg_u32 s19, 0\n"
        "s_cselect_b32 s80, 24, s2\n"
        "s_cmp_ge_u32 s80, 24\n"
        "s_cbranch_scc1 Lnopr_%=\n"
        "s_sub_u32 s86, s3, 5\n"
        "s_cmp_lt_u32 s86, 2\n"
        "s_cbranch_scc1 Lprod_%=\n"
        "s_barrier\n"
        "Lnopr_%=:\n"
        "s_add_u32 m0, s91, 0x0\n"
        "s_nop 0\n"
        "buffer_load_dwordx4 v248, s[20:23], 0 offen lds\n"
        "s_cmp_eq_u32 s80, 0x1000\n"
        "s_cbranch_scc1 Lprx_%=\n"
        "v_and_b32 v216, 15, v0\n"
        "v_bfe_u32 v217, v0, 2, 4\n"
        "v_lshlrev_b32 v216, 5, v216\n"
        "v_lshlrev_b32 v217, 5, v217\n"
        "global_load_dwordx4 v[200:203], v216, s[10:11]\n"
        "global_load_dwordx4 v[204:207], v216, s[10:11] offset:16\n"
        "global_load_dwordx4 v[208:211], v217, s[10:11]\n"
        "global_load_dwordx4 v[212:215], v217, s[10:11] offset:16\n"
        "Lprx_%=:\n"
        "v_add_u32 v10, 98, v6\n"
        "v_mul_u32_u24 v11, 293, v10\n"
        "v_lshrrev_b32 v11, 12, v11\n"
        "v_mad_i32_i24 v10, v11, -14, v10\n"
        "v_add_u32 v10, 3, v10\n"
        "v_lshl_add_u32 v12, v11, 1, s12\n"
        "v_add_u32 v13, 7, v12\n"
        "v_min_u32 v12, v12, v13\n"
        "v_mad_i32_i24 v249, v12, s14, v9\n"
        "v_lshl_add_u32 v12, v10, 1, s13\n"
        "v_add_u32 v13, 7, v12\n"
        "v_min_u32 v12, v12, v13\n"
        "v_mad_i32_i24 v249, v12, s15, v249\n"
        "v_xor_b32 v12, v11, v10\n"
        "v_and_b32 v12, 2, v12\n"
        "v_and_b32 v13, 1, v11\n"
        "v_lshl_or_b32 v12, v13, 2, v12\n"
        "v_xor_b32 v12, v12, v7\n"
        "v_lshl_add_u32 v249, v12, 4, v249\n"
        "s_add_u32 m0, s91, 0x1c00\n"
        "s_nop 0\n"
        "buffer_load_dwordx4 v249, s[20:23], 0 offen lds\n"
        "v_add_u32 v10, 154, v6\n"
        "v_mul_u32_u24 v11, 293, v10\n"
        "v_lshrrev_b32 v11, 12, v11\n"
        "v_mad_i32_i24 v10, v11, -14, v10\n"
        "v_add_u32 v10, 3, v10\n"
        "v_lshl_add_u32 v12, v11, 1, s12\n"
        "v_add_u32 v13, 7, v12\n"
        "v_min_u32 v12, v12, v13\n"
        "v_mad_i32_i24 v250, v12, s14, v9\n"
        "v_lshl_add_u32 v12, v10, 1, s13\n"
        "v_add_u32 v13, 7, v12\n"
        "v_min_u32 v12, v12, v13\n"
        "v_mad_i32_i24 v250, v12, s15, v250\n"
        "v_xor_b32 v12, v11, v10\n"
        "v_and_b32 v12, 2, v12\n"
        "v_and_b32 v13, 1, v11\n"
        "v_lshl_or_b32 v12, v13, 2, v12\n"
        "v_xor_b32 v12, v12, v7\n"
        "v_lshl_add_u32 v250, v12, 4, v250\n"
        "s_add_u32 m0, s91, 0x3800\n"
        "s_nop 0\n"
        "buffer_load_dwordx4 v250, s[20:23], 0 offen lds\n"
        "s_add_u32 m0, s91, 0x6380\n"
        "s_nop 0\n"
        "buffer_load_dwordx4 v248, s[20:23], 0 offen offset:128 lds\n"
        "s_waitcnt lgkmcnt(0)\n"
        "s_bitcmp1_b32 s18, 31\n"
        "s_cbranch_scc1 Ltg3_%=\n"
        "s_bitset1_b32 s18, 31\n"
        "s_mul_i32 s7, s7, 0x4c957f2d\n"
        "s_mul_i32 s86, s6, 0x5851f42d\n"
        "s_add_u32 s7, s7, s86\n"
        "s_mul_hi_u32 s86, s6, 0x4c957f2d\n"
        "s_add_u32 s7, s7, s86\n"
        "s_mul_i32 s6, s6, 0x4c957f2d\n"
        "s_add_u32 s6, s6, 0xf767814f\n"
        "s_addc_u32 s7, s7, 0x14057b7e\n"
        "Ltg3_%=:\n"
        "s_mov_b32 s88, s46\n"
        "s_mov_b32 s89, s47\n"
        "s_cmp_ge_u32 s3, 4\n"
        "s_cbranch_scc1 Lnomk_%=\n"
        "s_load_dwordx16 s[40:55], s[10:11], 512\n"
        "s_load_dwordx16 s[56:71], s[10:11], 576\n"
        "s_load_dwordx4 s[72:75], s[10:11], 640\n"
        "s_load_dwordx2 s[76:77], s[10:11], 656\n"
        "Lnomk_%=:\n"
        "v_mov_b32 v2, 0x27000\n"
        "s_and_b32 s86, s3, 3\n"
        "v_mov_b32 v4, 1\n"
        "v_add_u32 v3, s86, v2\n"
        "s_mov_b32 s30, 0x1260000\n"
        "s_mov_b32 s31, 0x20000\n"
        "s_cmp_ge_u32 s80, 24\n"
        "s_cbranch_scc1 Lnob0_%=\n"
        "s_barrier\n"
        "Lnob0_%=:\n"
        "s_cmp_ge_u32 s3, 4\n"
        "s_cbranch_scc1 Lhelper_%=\n"
        "s_add_u32 m0, s91, 0x7f80\n"
        "s_nop 0\n"
        "buffer_load_dwordx4 v249, s[20:23], 0 offen offset:128 lds\n"
        "v_add_u32 v10, 210, v6\n"
        "v_mul_u32_u24 v11, 293, v10\n"
        "v_lshrrev_b32 v11, 12, v11\n"
        "v_mad_i32_i24 v10, v11, -14, v10\n"
        "v_add_u32 v10, 3, v10\n"
        "v_lshl_add_u32 v12, v11, 1, s12\n"
        "v_add_u32 v13, 7, v12\n"
        "v_min_u32 v12, v12, v13\n"
        "v_mad_i32_i24 v251, v12, s14, v9\n"
        "v_lshl_add_u32 v12, v10, 1, s13\n"
        "v_add_u32 v13, 7, v12\n"
        "v_min_u32 v12, v12, v13\n"
        "v_mad_i32_i24 v251, v12, s15, v251\n"
        "v_xor_b32 v12, v11, v10\n"
        "v_and_b32 v12, 2, v12\n"
        "v_and_b32 v13, 1, v11\n"
        "v_lshl_or_b32 v12, v13, 2, v12\n"
        "v_xor_b32 v12, v12, v7\n"
        "v_lshl_add_u32 v251, v12, 4, v251\n"
        "s_add_u32 m0, s91, 0x5400\n"
        "s_nop 0\n"
        "buffer_load_dwordx4 v251, s[20:23], 0 offen lds\n"
        "s_add_u32 m0, s91, 0x9b80\n"
        "s_nop 0\n"
        "buffer_load_dwordx4 v250, s[20:23], 0 offen offset:128 lds\n"
        "s_add_u32 m0, s91, 0xb780\n"
        "s_nop 0\n"
        "buffer_load_dwordx4 v251, s[20:23], 0 offen offset:128 lds\n"
        "s_bitcmp1_b32 s3, 0\n"
        "s_cselect_b32 s80, s89, s88\n"
        "s_cselect_b32 s81, s88, s89\n"
        "s_cselect_b32 s88, s15, s14\n"
        "s_cselect_b32 s89, s17, s16\n"
        "s_cselect_b32 s82, s14, s15\n"
        "s_cselect_b32 s83, s16, s17\n"
        "s_mul_i32 s81, s81, 14\n"
        "s_cmp_ge_u32 s3, 2\n"
        "s_cselect_b32 s86, 6, 0\n"
        "s_add_u32 s81, s81, s86\n"
        "v_bfe_u32 v10, v0, 4, 2\n"
        "v_lshl_add_u32 v10, v10, 1, s81\n"
        "v_and_b32 v12, 15, v0\n"
        "v_lshl_add_u32 v12, v12, 3, s83\n"
        "v_lshl_add_u32 v11, v10, 1, -6\n"
        "v_max_i32 v11, v11, v10\n"
        "v_mad_i32_i24 v6, v11, s82, v12\n"
        "v_add_u32 v10, 1, v10\n"
        "v_lshl_add_u32 v11, v10, 1, -6\n"
        "v_max_i32 v11, v11, v10\n"
        "v_mad_i32_i24 v7, v11, s82, v12\n"
        "s_mul_i32 s86, s80, 14\n"
        "s_add_u32 s87, s3, 1\n"
        "s_and_b32 s87, s87, 2\n"
        "s_lshl_b32 s87, s87, 2\n"
        "s_add_u32 s86, s86, s87\n"
        "s_lshl_b32 s87, s86, 1\n"
        "s_sub_u32 s87, s87, 6\n"
        "s_max_i32 s87, s87, s86\n"
        "s_mul_i32 s87, s87, s88\n"
        "s_add_u32 s80, s87, s89\n"
        "s_add_u32 s86, s86, 1\n"
        "s_lshl_b32 s87, s86, 1\n"
        "s_sub_u32 s87, s87, 6\n"
        "s_max_i32 s87, s87, s86\n"
        "s_mul_i32 s87, s87, s88\n"
        "s_add_u32 s81, s87, s89\n"
        "s_add_u32 s86, s86, 1\n"
        "s_lshl_b32 s87, s86, 1\n"
        "s_sub_u32 s87, s87, 6\n"
        "s_max_i32 s87, s87, s86\n"
        "s_mul_i32 s87, s87, s88\n"
        "s_add_u32 s82, s87, s89\n"
        "s_add_u32 s86, s86, 1\n"
        "s_lshl_b32 s87, s86, 1\n"
        "s_sub_u32 s87, s87, 6\n"
        "s_max_i32 s87, s87, s86\n"
        "s_mul_i32 s87, s87, s88\n"
        "s_add_u32 s83, s87, s89\n"
        "s_add_u32 s86, s86, 1\n"
        "s_lshl_b32 s87, s86, 1\n"
        "s_sub_u32 s87, s87, 6\n"
        "s_max_i32 s87, s87, s86\n"
        "s_mul_i32 s87, s87, s88\n"
        "s_add_u32 s84, s87, s89\n"
        "s_add_u32 s86, s86, 1\n"
        "s_lshl_b32 s87, s86, 1\n"
        "s_sub_u32 s87, s87, 6\n"
        "s_max_i32 s87, s87, s86\n"
        "s_mul_i32 s87, s87, s88\n"
        "s_add_u32 s85, s87, s89\n"
        "s_add_u32 s86, s86, 1\n"
        "s_waitcnt vmcnt(7)\n"
        "v_mov_b32 v218, 5\n"
        "v_and_b32 v219, 48, v0\n"
        "v_and_b32 v220, 3, v0\n"
        "v_lshlrev_b32 v220, 3, v220\n"
        "v_lshlrev_b32_sdwa v22, v218, v200 dst_sel:DWORD dst_unused:UNUSED_PAD src0_sel:DWORD src1_sel:WORD_0\n"
        "v_xor_b32 v22, v22, v219\n"
        "v_xor_b32 v23, 64, v22\n"
        "v_lshlrev_b32_sdwa v24, v218, v200 dst_sel:DWORD dst_unused:UNUSED_PAD src0_sel:DWORD src1_sel:WORD_1\n"
        "v_xor_b32 v24, v24, v219\n"
        "v_xor_b32 v25, 64, v24\n"
        "v_lshlrev_b32_sdwa v26, v218, v201 dst_sel:DWORD dst_unused:UNUSED_PAD src0_sel:DWORD src1_sel:WORD_0\n"
        "v_xor_b32 v26, v26, v219\n"
        "v_xor_b32 v27, 64, v26\n"
        "v_lshlrev_b32_sdwa v28, v218, v201 dst_sel:DWORD dst_unused:UNUSED_PAD src0_sel:DWORD src1_sel:WORD_1\n"
        "v_xor_b32 v28, v28, v219\n"
        "v_xor_b32 v29, 64, v28\n"
        "v_lshlrev_b32_sdwa v30, v218, v202 dst_sel:DWORD dst_unused:UNUSED_PAD src0_sel:DWORD src1_sel:WORD_0\n"
        "v_xor_b32 v30, v30, v219\n"
        "v_lshlrev_b32_sdwa v32, v218, v202 dst_sel:DWORD dst_unused:UNUSED_PAD src0_sel:DWORD src1_sel:WORD_1\n"
        "v_xor_b32 v32, v32, v219\n"
        "v_lshlrev_b32_sdwa v34, v218, v203 dst_sel:DWORD dst_unused:UNUSED_PAD src0_sel:DWORD src1_sel:WORD_0\n"
        "v_xor_b32 v34, v34, v219\n"
        "v_lshlrev_b32_sdwa v36, v218, v203 dst_sel:DWORD dst_unused:UNUSED_PAD src0_sel:DWORD src1_sel:WORD_1\n"
        "v_xor_b32 v36, v36, v219\n"
        "v_lshlrev_b32_sdwa v38, v218, v204 dst_sel:DWORD dst_unused:UNUSED_PAD src0_sel:DWORD src1_sel:WORD_0\n"
        "v_xor_b32 v38, v38, v219\n"
        "v_xor_b32 v39, 64, v38\n"
        "v_lshlrev_b32_sdwa v40, v218, v204 dst_sel:DWORD dst_unused:UNUSED_PAD src0_sel:DWORD src1_sel:WORD_1\n"
        "v_xor_b32 v40, v40, v219\n"
        "v_lshlrev_b32_sdwa v42, v218, v205 dst_sel:DWORD dst_unused:UNUSED_PAD src0_sel:DWORD src1_sel:WORD_0\n"
        "v_xor_b32 v42, v42, v219\n"
        "v_lshlrev_b32_sdwa v31, v218, v205 dst_sel:DWORD dst_unused:UNUSED_PAD src0_sel:DWORD src1_sel:WORD_1\n"
        "v_xor_b32 v31, v31, v219\n"
        "v_xor_b32 v33, 64, v31\n"
        "v_lshlrev_b32_sdwa v35, v218, v206 dst_sel:DWORD dst_unused:UNUSED_PAD src0_sel:DWORD src1_sel:WORD_0\n"
        "v_xor_b32 v35, v35, v219\n"
        "v_xor_b32 v37, 64, v35\n"
        "v_lshlrev_b32_sdwa v41, v218, v206 dst_sel:DWORD dst_unused:UNUSED_PAD src0_sel:DWORD src1_sel:WORD_1\n"
        "v_xor_b32 v41, v41, v219\n"
        "v_xor_b32 v43, 64, v41\n"
        "v_lshlrev_b32_sdwa v10, v218, v208 dst_sel:DWORD dst_unused:UNUSED_PAD src0_sel:DWORD src1_sel:WORD_0\n"
        "v_or_b32 v10, v10, v220\n"
        "v_lshlrev_b32_sdwa v11, v218, v208 dst_sel:DWORD dst_unused:UNUSED_PAD src0_sel:DWORD src1_sel:WORD_1\n"
        "v_or_b32 v11, v11, v220\n"
        "v_lshlrev_b32_sdwa v12, v218, v209 dst_sel:DWORD dst_unused:UNUSED_PAD src0_sel:DWORD src1_sel:WORD_0\n"
        "v_or_b32 v12, v12, v220\n"
        "v_lshlrev_b32_sdwa v13, v218, v209 dst_sel:DWORD dst_unused:UNUSED_PAD src0_sel:DWORD src1_sel:WORD_1\n"
        "v_or_b32 v13, v13, v220\n"
        "v_lshlrev_b32_sdwa v14, v218, v210 dst_sel:DWORD dst_unused:UNUSED_PAD src0_sel:DWORD src1_sel:WORD_0\n"
        "v_or_b32 v14, v14, v220\n"
        "v_lshlrev_b32_sdwa v15, v218, v210 dst_sel:DWORD dst_unused:UNUSED_PAD src0_sel:DWORD src1_sel:WORD_1\n"
        "v_or_b32 v15, v15, v220\n"
        "v_lshlrev_b32_sdwa v16, v218, v211 dst_sel:DWORD dst_unused:UNUSED_PAD src0_sel:DWORD src1_sel:WORD_0\n"
        "v_or_b32 v16, v16, v220\n"
        "v_lshlrev_b32_sdwa v17, v218, v211 dst_sel:DWORD dst_unused:UNUSED_PAD src0_sel:DWORD src1_sel:WORD_1\n"
        "v_or_b32 v17, v17, v220\n"
        "v_lshlrev_b32_sdwa v18, v218, v212 dst_sel:DWORD dst_unused:UNUSED_PAD src0_sel:DWORD src1_sel:WORD_0\n"
        "v_or_b32 v18, v18, v220\n"
        "v_lshlrev_b32_sdwa v19, v218, v212 dst_sel:DWORD dst_unused:UNUSED_PAD src0_sel:DWORD src1_sel:WORD_1\n"
        "v_or_b32 v19, v19, v220\n"
        "v_lshlrev_b32_sdwa v20, v218, v213 dst_sel:DWORD dst_unused:UNUSED_PAD src0_sel:DWORD src1_sel:WORD_0\n"
        "v_or_b32 v20, v20, v220\n"
        "v_and_b32 v1, 63, v0\n"
        "v_lshlrev_b32 v1, 4, v1\n"
        "v_add_u32 v1, 0x12c00, v1\n"
        "v_add_u32 v9, 0xc800, v1\n"
        "v_mov_b32 v8, 0xf149f2ca\n"
        "s_mov_b32 s90, 0x3f803f80\n"
        "s_mov_b32 s91, 0x3f803f80\n"
        "s_setprio 2\n"
        "s_waitcnt vmcnt(0)\n"
        "Lcre_%=:\n"
        "s_waitcnt lgkmcnt(0)\n"
        "s_barrier\n"
        "ds_read_b128 v[232:235], v31 offset:0\n"
        "ds_read_b128 v[236:239], v33 offset:0\n"
        "ds_read_b128 v[200:203], v1 offset:0\n"
        "ds_read_b128 v[204:207], v1 offset:1024\n"
        "ds_read_b128 v[208:211], v1 offset:6144\n"
        "ds_read_b128 v[212:215], v1 offset:7168\n"
        "ds_read_b128 v[240:243], v35 offset:0\n"
        "ds_read_b128 v[244:247], v37 offset:0\n"
        "ds_read_b128 v[188:191], v41 offset:0\n"
        "ds_read_b128 v[192:195], v43 offset:0\n"
        "s_waitcnt lgkmcnt(7)\n"
        "ds_read_b128 v[216:219], v1 offset:12288\n"
        "v_mfma_f32_16x16x32_bf16 v[44:47], v[200:203], v[232:235], 0\n"
        "s_waitcnt lgkmcnt(7)\n"
        "ds_read_b128 v[220:223], v1 offset:13312\n"
        "v_mfma_f32_16x16x32_bf16 v[44:47], v[204:207], v[236:239], v[44:47]\n"
        "s_waitcnt lgkmcnt(7)\n"
        "ds_read_b128 v[224:227], v1 offset:18432\n"
        "v_mfma_f32_16x16x32_bf16 v[116:119], v[208:211], v[232:235], 0\n"
        "s_waitcnt lgkmcnt(7)\n"
        "ds_read_b128 v[228:231], v1 offset:19456\n"
        "v_mfma_f32_16x16x32_bf16 v[116:119], v[212:215], v[236:239], v[116:119]\n"
        "s_waitcnt lgkmcnt(7)\n"
        "ds_read_b128 v[196:199], v1 offset:2048\n"
        "v_mfma_f32_16x16x32_bf16 v[68:71], v[200:203], v[240:243], 0\n"
        "s_waitcnt lgkmcnt(7)\n"
        "v_mfma_f32_16x16x32_bf16 v[68:71], v[204:207], v[244:247], v[68:71]\n"
        "v_mfma_f32_16x16x32_bf16 v[120:123], v[208:211], v[240:243], 0\n"
        "v_mfma_f32_16x16x32_bf16 v[120:123], v[212:215], v[244:247], v[120:123]\n"
        "s_waitcnt lgkmcnt(6)\n"
        "ds_read_b128 v[248:251], v31 offset:25600\n"
        "v_mfma_f32_16x16x32_bf16 v[92:95], v[200:203], v[188:191], 0\n"
        "s_waitcnt lgkmcnt(6)\n"
        "ds_read_b128 v[200:203], v1 offset:3072\n"
        "v_mfma_f32_16x16x32_bf16 v[92:95], v[204:207], v[192:195], v[92:95]\n"
        "ds_read_b128 v[204:207], v1 offset:8192\n"
        "v_mfma_f32_16x16x32_bf16 v[124:127], v[208:211], v[188:191], 0\n"
        "ds_read_b128 v[208:211], v1 offset:9216\n"
        "v_mfma_f32_16x16x32_bf16 v[124:127], v[212:215], v[192:195], v[124:127]\n"
        "s_waitcnt lgkmcnt(8)\n"
        "ds_read_b128 v[212:215], v1 offset:14336\n"
        "v_mfma_f32_16x16x32_bf16 v[48:51], v[216:219], v[232:235], 0\n"
        "s_waitcnt lgkmcnt(8)\n"
        "ds_read_b128 v[252:255], v33 offset:25600\n"
        "v_mfma_f32_16x16x32_bf16 v[48:51], v[220:223], v[236:239], v[48:51]\n"
        "v_mfma_f32_16x16x32_bf16 v[72:75], v[216:219], v[240:243], 0\n"
        "v_mfma_f32_16x16x32_bf16 v[72:75], v[220:223], v[244:247], v[72:75]\n"
        "v_mfma_f32_16x16x32_bf16 v[96:99], v[216:219], v[188:191], 0\n"
        "ds_read_b128 v[216:219], v1 offset:15360\n"
        "v_mfma_f32_16x16x32_bf16 v[96:99], v[220:223], v[192:195], v[96:99]\n"
        "s_waitcnt lgkmcnt(9)\n"
        "ds_read_b128 v[220:223], v1 offset:20480\n"
        "v_mfma_f32_16x16x32_bf16 v[144:147], v[224:227], v[232:235], 0\n"
        "s_waitcnt lgkmcnt(9)\n"
        "v_mfma_f32_16x16x32_bf16 v[144:147], v[228:231], v[236:239], v[144:147]\n"
        "v_mfma_f32_16x16x32_bf16 v[128:131], v[224:227], v[240:243], 0\n"
        "v_mfma_f32_16x16x32_bf16 v[128:131], v[228:231], v[244:247], v[128:131]\n"
        "v_mfma_f32_16x16x32_bf16 v[148:151], v[224:227], v[188:191], 0\n"
        "ds_read_b128 v[224:227], v1 offset:21504\n"
        "v_mfma_f32_16x16x32_bf16 v[148:151], v[228:231], v[192:195], v[148:151]\n"
        "s_waitcnt lgkmcnt(9)\n"
        "ds_read_b128 v[228:231], v1 offset:4096\n"
        "v_mfma_f32_16x16x32_bf16 v[52:55], v[196:199], v[232:235], 0\n"
        "s_waitcnt lgkmcnt(8)\n"
        "v_mfma_f32_16x16x32_bf16 v[52:55], v[200:203], v[236:239], v[52:55]\n"
        "v_mfma_f32_16x16x32_bf16 v[76:79], v[196:199], v[240:243], 0\n"
        "v_mfma_f32_16x16x32_bf16 v[76:79], v[200:203], v[244:247], v[76:79]\n"
        "v_mfma_f32_16x16x32_bf16 v[100:103], v[196:199], v[188:191], 0\n"
        "ds_read_b128 v[196:199], v1 offset:5120\n"
        "v_mfma_f32_16x16x32_bf16 v[100:103], v[200:203], v[192:195], v[100:103]\n"
        "s_waitcnt lgkmcnt(8)\n"
        "ds_read_b128 v[200:203], v1 offset:10240\n"
        "v_mfma_f32_16x16x32_bf16 v[132:135], v[204:207], v[232:235], 0\n"
        "s_waitcnt lgkmcnt(8)\n"
        "v_mfma_f32_16x16x32_bf16 v[132:135], v[208:211], v[236:239], v[132:135]\n"
        "v_mfma_f32_16x16x32_bf16 v[160:163], v[204:207], v[240:243], 0\n"
        "v_mfma_f32_16x16x32_bf16 v[160:163], v[208:211], v[244:247], v[160:163]\n"
        "v_mfma_f32_16x16x32_bf16 v[136:139], v[204:207], v[188:191], 0\n"
        "ds_read_b128 v[204:207], v1 offset:11264\n"
        "v_mfma_f32_16x16x32_bf16 v[136:139], v[208:211], v[192:195], v[136:139]\n"
        "s_waitcnt lgkmcnt(8)\n"
        "ds_read_b128 v[208:211], v1 offset:16384\n"
        "v_mfma_f32_16x16x32_bf16 v[56:59], v[212:215], v[232:235], 0\n"
        "s_waitcnt lgkmcnt(7)\n"
        "v_mfma_f32_16x16x32_bf16 v[56:59], v[216:219], v[236:239], v[56:59]\n"
        "v_mfma_f32_16x16x32_bf16 v[80:83], v[212:215], v[240:243], 0\n"
        "v_mfma_f32_16x16x32_bf16 v[80:83], v[216:219], v[244:247], v[80:83]\n"
        "v_mfma_f32_16x16x32_bf16 v[104:107], v[212:215], v[188:191], 0\n"
        "ds_read_b128 v[212:215], v1 offset:17408\n"
        "v_mfma_f32_16x16x32_bf16 v[104:107], v[216:219], v[192:195], v[104:107]\n"
        "s_waitcnt lgkmcnt(7)\n"
        "ds_read_b128 v[216:219], v1 offset:22528\n"
        "v_mfma_f32_16x16x32_bf16 v[164:167], v[220:223], v[232:235], 0\n"
        "s_waitcnt lgkmcnt(7)\n"
        "v_mfma_f32_16x16x32_bf16 v[164:167], v[224:227], v[236:239], v[164:167]\n"
        "v_mfma_f32_16x16x32_bf16 v[140:143], v[220:223], v[240:243], 0\n"
        "ds_read_b32 v5, v2 offset:0\n"
        "v_mfma_f32_16x16x32_bf16 v[140:143], v[224:227], v[244:247], v[140:143]\n"
        "v_mfma_f32_16x16x32_bf16 v[168:171], v[220:223], v[188:191], 0\n"
        "ds_read_b128 v[220:223], v1 offset:23552\n"
        "v_mfma_f32_16x16x32_bf16 v[168:171], v[224:227], v[192:195], v[168:171]\n"
        "s_waitcnt lgkmcnt(8)\n"
        "ds_write_b8 v3, v4 offset:8\n"
        "v_mfma_f32_16x16x32_bf16 v[60:63], v[228:231], v[232:235], 0\n"
        "s_waitcnt lgkmcnt(8)\n"
        "v_mfma_f32_16x16x32_bf16 v[60:63], v[196:199], v[236:239], v[60:63]\n"
        "v_mfma_f32_16x16x32_bf16 v[84:87], v[228:231], v[240:243], 0\n"
        "s_waitcnt lgkmcnt(2)\n"
        "v_readfirstlane_b32 s86, v5\n"
        "v_mfma_f32_16x16x32_bf16 v[84:87], v[196:199], v[244:247], v[84:87]\n"
        "v_mfma_f32_16x16x32_bf16 v[108:111], v[228:231], v[188:191], 0\n"
        "v_mfma_f32_16x16x32_bf16 v[108:111], v[196:199], v[192:195], v[108:111]\n"
        "s_cmp_eq_u32 s86, 0x1010101\n"
        "v_mfma_f32_16x16x32_bf16 v[152:155], v[200:203], v[232:235], 0\n"
        "s_cbranch_scc0 Lsl4_%=\n"
        "Lrt5_%=:\n"
        "v_mfma_f32_16x16x32_bf16 v[152:155], v[204:207], v[236:239], v[152:155]\n"
        "ds_read_b128 v[224:227], v1 offset:25600\n"
        "v_mfma_f32_16x16x32_bf16 v[156:159], v[200:203], v[240:243], 0\n"
        "ds_read_b128 v[228:231], v1 offset:26624\n"
        "v_mfma_f32_16x16x32_bf16 v[156:159], v[204:207], v[244:247], v[156:159]\n"
        "ds_read_b128 v[196:199], v1 offset:31744\n"
        "v_mfma_f32_16x16x32_bf16 v[172:175], v[200:203], v[188:191], 0\n"
        "ds_read_b128 v[200:203], v1 offset:32768\n"
        "v_mfma_f32_16x16x32_bf16 v[172:175], v[204:207], v[192:195], v[172:175]\n"
        "ds_read_b128 v[204:207], v1 offset:37888\n"
        "v_mfma_f32_16x16x32_bf16 v[64:67], v[208:211], v[232:235], 0\n"
        "v_mfma_f32_16x16x32_bf16 v[64:67], v[212:215], v[236:239], v[64:67]\n"
        "v_mfma_f32_16x16x32_bf16 v[176:179], v[216:219], v[232:235], 0\n"
        "s_waitcnt lgkmcnt(6)\n"
        "v_mfma_f32_16x16x32_bf16 v[176:179], v[220:223], v[236:239], v[176:179]\n"
        "ds_read_b128 v[232:235], v35 offset:25600\n"
        "v_mfma_f32_16x16x32_bf16 v[88:91], v[208:211], v[240:243], 0\n"
        "ds_read_b128 v[236:239], v37 offset:25600\n"
        "v_mfma_f32_16x16x32_bf16 v[88:91], v[212:215], v[244:247], v[88:91]\n"
        "v_mfma_f32_16x16x32_bf16 v[180:183], v[216:219], v[240:243], 0\n"
        "v_mfma_f32_16x16x32_bf16 v[180:183], v[220:223], v[244:247], v[180:183]\n"
        "ds_read_b128 v[240:243], v41 offset:25600\n"
        "v_mfma_f32_16x16x32_bf16 v[112:115], v[208:211], v[188:191], 0\n"
        "ds_read_b128 v[244:247], v43 offset:25600\n"
        "v_mfma_f32_16x16x32_bf16 v[112:115], v[212:215], v[192:195], v[112:115]\n"
        "ds_read_b128 v[208:211], v1 offset:38912\n"
        "v_mfma_f32_16x16x32_bf16 v[184:187], v[216:219], v[188:191], 0\n"
        "ds_read_b128 v[212:215], v1 offset:44032\n"
        "v_mfma_f32_16x16x32_bf16 v[184:187], v[220:223], v[192:195], v[184:187]\n"
        "s_waitcnt lgkmcnt(10)\n"
        "ds_read_b128 v[216:219], v1 offset:45056\n"
        "v_mfma_f32_16x16x32_bf16 v[44:47], v[224:227], v[248:251], v[44:47]\n"
        "s_waitcnt lgkmcnt(10)\n"
        "ds_read_b128 v[220:223], v1 offset:27648\n"
        "v_mfma_f32_16x16x32_bf16 v[44:47], v[228:231], v[252:255], v[44:47]\n"
        "s_waitcnt lgkmcnt(10)\n"
        "v_mfma_f32_16x16x32_bf16 v[116:119], v[196:199], v[248:251], v[116:119]\n"
        "s_waitcnt lgkmcnt(9)\n"
        "v_mfma_f32_16x16x32_bf16 v[116:119], v[200:203], v[252:255], v[116:119]\n"
        "s_waitcnt lgkmcnt(7)\n"
        "v_mfma_f32_16x16x32_bf16 v[68:71], v[224:227], v[232:235], v[68:71]\n"
        "s_waitcnt lgkmcnt(6)\n"
        "v_mfma_f32_16x16x32_bf16 v[68:71], v[228:231], v[236:239], v[68:71]\n"
        "v_mfma_f32_16x16x32_bf16 v[120:123], v[196:199], v[232:235], v[120:123]\n"
        "v_mfma_f32_16x16x32_bf16 v[120:123], v[200:203], v[236:239], v[120:123]\n"
        "s_waitcnt lgkmcnt(5)\n"
        "v_mfma_f32_16x16x32_bf16 v[92:95], v[224:227], v[240:243], v[92:95]\n"
        "s_waitcnt lgkmcnt(4)\n"
        "ds_read_b128 v[224:227], v1 offset:28672\n"
        "v_mfma_f32_16x16x32_bf16 v[92:95], v[228:231], v[244:247], v[92:95]\n"
        "ds_read_b128 v[228:231], v1 offset:33792\n"
        "v_mfma_f32_16x16x32_bf16 v[124:127], v[196:199], v[240:243], v[124:127]\n"
        "ds_read_b128 v[196:199], v1 offset:34816\n"
        "v_mfma_f32_16x16x32_bf16 v[124:127], v[200:203], v[244:247], v[124:127]\n"
        "ds_read_b128 v[200:203], v1 offset:39936\n"
        "v_mfma_f32_16x16x32_bf16 v[48:51], v[204:207], v[248:251], v[48:51]\n"
        "s_waitcnt lgkmcnt(7)\n"
        "v_mfma_f32_16x16x32_bf16 v[48:51], v[208:211], v[252:255], v[48:51]\n"
        "v_mfma_f32_16x16x32_bf16 v[72:75], v[204:207], v[232:235], v[72:75]\n"
        "v_mfma_f32_16x16x32_bf16 v[72:75], v[208:211], v[236:239], v[72:75]\n"
        "v_mfma_f32_16x16x32_bf16 v[96:99], v[204:207], v[240:243], v[96:99]\n"
        "ds_read_b128 v[204:207], v1 offset:40960\n"
        "v_mfma_f32_16x16x32_bf16 v[96:99], v[208:211], v[244:247], v[96:99]\n"
        "s_waitcnt lgkmcnt(7)\n"
        "ds_read_b128 v[208:211], v1 offset:46080\n"
        "v_mfma_f32_16x16x32_bf16 v[144:147], v[212:215], v[248:251], v[144:147]\n"
        "s_waitcnt lgkmcnt(7)\n"
        "v_mfma_f32_16x16x32_bf16 v[144:147], v[216:219], v[252:255], v[144:147]\n"
        "v_mfma_f32_16x16x32_bf16 v[128:131], v[212:215], v[232:235], v[128:131]\n"
        "v_mfma_f32_16x16x32_bf16 v[128:131], v[216:219], v[236:239], v[128:131]\n"
        "v_mfma_f32_16x16x32_bf16 v[148:151], v[212:215], v[240:243], v[148:151]\n"
        "ds_read_b128 v[212:215], v1 offset:47104\n"
        "v_mfma_f32_16x16x32_bf16 v[148:151], v[216:219], v[244:247], v[148:151]\n"
        "s_waitcnt lgkmcnt(7)\n"
        "ds_read_b128 v[216:219], v1 offset:29696\n"
        "v_mfma_f32_16x16x32_bf16 v[52:55], v[220:223], v[248:251], v[52:55]\n"
        "s_waitcnt lgkmcnt(7)\n"
        "v_mfma_f32_16x16x32_bf16 v[52:55], v[224:227], v[252:255], v[52:55]\n"
        "v_mfma_f32_16x16x32_bf16 v[76:79], v[220:223], v[232:235], v[76:79]\n"
        "v_mfma_f32_16x16x32_bf16 v[76:79], v[224:227], v[236:239], v[76:79]\n"
        "v_mfma_f32_16x16x32_bf16 v[100:103], v[220:223], v[240:243], v[100:103]\n"
        "ds_read_b128 v[220:223], v1 offset:30720\n"
        "v_mfma_f32_16x16x32_bf16 v[100:103], v[224:227], v[244:247], v[100:103]\n"
        "s_waitcnt lgkmcnt(7)\n"
        "ds_read_b128 v[224:227], v1 offset:35840\n"
        "v_mfma_f32_16x16x32_bf16 v[132:135], v[228:231], v[248:251], v[132:135]\n"
        "s_waitcnt lgkmcnt(7)\n"
        "v_mfma_f32_16x16x32_bf16 v[132:135], v[196:199], v[252:255], v[132:135]\n"
        "v_mfma_f32_16x16x32_bf16 v[160:163], v[228:231], v[232:235], v[160:163]\n"
        "v_mfma_f32_16x16x32_bf16 v[160:163], v[196:199], v[236:239], v[160:163]\n"
        "v_mfma_f32_16x16x32_bf16 v[136:139], v[228:231], v[240:243], v[136:139]\n"
        "ds_read_b128 v[228:231], v1 offset:36864\n"
        "v_mfma_f32_16x16x32_bf16 v[136:139], v[196:199], v[244:247], v[136:139]\n"
        "s_waitcnt lgkmcnt(7)\n"
        "ds_read_b128 v[196:199], v1 offset:41984\n"
        "v_mfma_f32_16x16x32_bf16 v[56:59], v[200:203], v[248:251], v[56:59]\n"
        "s_waitcnt lgkmcnt(7)\n"
        "v_mfma_f32_16x16x32_bf16 v[56:59], v[204:207], v[252:255], v[56:59]\n"
        "v_mfma_f32_16x16x32_bf16 v[80:83], v[200:203], v[232:235], v[80:83]\n"
        "v_mfma_f32_16x16x32_bf16 v[80:83], v[204:207], v[236:239], v[80:83]\n"
        "v_mfma_f32_16x16x32_bf16 v[104:107], v[200:203], v[240:243], v[104:107]\n"
        "ds_read_b128 v[200:203], v1 offset:43008\n"
        "v_mfma_f32_16x16x32_bf16 v[104:107], v[204:207], v[244:247], v[104:107]\n"
        "s_waitcnt lgkmcnt(7)\n"
        "ds_read_b128 v[204:207], v1 offset:48128\n"
        "v_mfma_f32_16x16x32_bf16 v[164:167], v[208:211], v[248:251], v[164:167]\n"
        "s_waitcnt lgkmcnt(7)\n"
        "v_mfma_f32_16x16x32_bf16 v[164:167], v[212:215], v[252:255], v[164:167]\n"
        "v_mfma_f32_16x16x32_bf16 v[140:143], v[208:211], v[232:235], v[140:143]\n"
        "ds_read_b32 v5, v2 offset:4\n"
        "v_mfma_f32_16x16x32_bf16 v[140:143], v[212:215], v[236:239], v[140:143]\n"
        "v_mfma_f32_16x16x32_bf16 v[168:171], v[208:211], v[240:243], v[168:171]\n"
        "ds_read_b128 v[208:211], v1 offset:49152\n"
        "v_mfma_f32_16x16x32_bf16 v[168:171], v[212:215], v[244:247], v[168:171]\n"
        "s_waitcnt lgkmcnt(8)\n"
        "ds_write_b8 v3, v4 offset:12\n"
        "v_mfma_f32_16x16x32_bf16 v[60:63], v[216:219], v[248:251], v[60:63]\n"
        "s_waitcnt lgkmcnt(8)\n"
        "v_mfma_f32_16x16x32_bf16 v[60:63], v[220:223], v[252:255], v[60:63]\n"
        "v_mfma_f32_16x16x32_bf16 v[84:87], v[216:219], v[232:235], v[84:87]\n"
        "s_waitcnt lgkmcnt(2)\n"
        "v_readfirstlane_b32 s86, v5\n"
        "v_mfma_f32_16x16x32_bf16 v[84:87], v[220:223], v[236:239], v[84:87]\n"
        "v_mfma_f32_16x16x32_bf16 v[108:111], v[216:219], v[240:243], v[108:111]\n"
        "v_mfma_f32_16x16x32_bf16 v[108:111], v[220:223], v[244:247], v[108:111]\n"
        "s_cmp_eq_u32 s86, 0x1010101\n"
        "v_mfma_f32_16x16x32_bf16 v[152:155], v[224:227], v[248:251], v[152:155]\n"
        "s_cbranch_scc0 Lsl7_%=\n"
        "Lrt8_%=:\n"
        "v_mfma_f32_16x16x32_bf16 v[152:155], v[228:231], v[252:255], v[152:155]\n"
        "ds_read_b128 v[212:215], v9 offset:0\n"
        "v_mfma_f32_16x16x32_bf16 v[156:159], v[224:227], v[232:235], v[156:159]\n"
        "ds_read_b128 v[216:219], v9 offset:1024\n"
        "v_mfma_f32_16x16x32_bf16 v[156:159], v[228:231], v[236:239], v[156:159]\n"
        "ds_read_b128 v[188:191], v31 offset:51200\n"
        "v_mfma_f32_16x16x32_bf16 v[172:175], v[224:227], v[240:243], v[172:175]\n"
        "ds_read_b128 v[192:195], v33 offset:51200\n"
        "v_xor_b32 v31, 64, v30\n"
        "v_mfma_f32_16x16x32_bf16 v[172:175], v[228:231], v[244:247], v[172:175]\n"
        "ds_read_b128 v[220:223], v9 offset:6144\n"
        "v_xor_b32 v33, 64, v32\n"
        "v_mfma_f32_16x16x32_bf16 v[64:67], v[196:199], v[248:251], v[64:67]\n"
        "ds_read_b128 v[224:227], v9 offset:7168\n"
        "v_mfma_f32_16x16x32_bf16 v[64:67], v[200:203], v[252:255], v[64:67]\n"
        "ds_read_b128 v[228:231], v9 offset:12288\n"
        "v_mfma_f32_16x16x32_bf16 v[176:179], v[204:207], v[248:251], v[176:179]\n"
        "s_waitcnt lgkmcnt(8)\n"
        "v_mfma_f32_16x16x32_bf16 v[176:179], v[208:211], v[252:255], v[176:179]\n"
        "ds_read_b128 v[248:251], v35 offset:51200\n"
        "v_mfma_f32_16x16x32_bf16 v[88:91], v[196:199], v[232:235], v[88:91]\n"
        "ds_read_b128 v[252:255], v37 offset:51200\n"
        "v_xor_b32 v35, 64, v34\n"
        "v_mfma_f32_16x16x32_bf16 v[88:91], v[200:203], v[236:239], v[88:91]\n"
        "v_xor_b32 v37, 64, v36\n"
        "v_mfma_f32_16x16x32_bf16 v[180:183], v[204:207], v[232:235], v[180:183]\n"
        "v_mfma_f32_16x16x32_bf16 v[180:183], v[208:211], v[236:239], v[180:183]\n"
        "ds_read_b128 v[232:235], v41 offset:51200\n"
        "v_mfma_f32_16x16x32_bf16 v[112:115], v[196:199], v[240:243], v[112:115]\n"
        "ds_read_b128 v[236:239], v43 offset:51200\n"
        "v_xor_b32 v41, 64, v40\n"
        "v_mfma_f32_16x16x32_bf16 v[112:115], v[200:203], v[244:247], v[112:115]\n"
        "ds_read_b128 v[196:199], v9 offset:13312\n"
        "v_xor_b32 v43, 64, v42\n"
        "v_mfma_f32_16x16x32_bf16 v[184:187], v[204:207], v[240:243], v[184:187]\n"
        "ds_read_b128 v[200:203], v9 offset:18432\n"
        "v_mfma_f32_16x16x32_bf16 v[184:187], v[208:211], v[244:247], v[184:187]\n"
        "s_waitcnt lgkmcnt(10)\n"
        "ds_read_b128 v[204:207], v9 offset:19456\n"
        "v_mfma_f32_16x16x32_bf16 v[44:47], v[212:215], v[188:191], v[44:47]\n"
        "s_waitcnt lgkmcnt(10)\n"
        "ds_read_b128 v[208:211], v9 offset:2048\n"
        "v_mfma_f32_16x16x32_bf16 v[44:47], v[216:219], v[192:195], v[44:47]\n"
        "s_waitcnt lgkmcnt(10)\n"
        "ds_read_b128 v[240:243], v9 offset:3072\n"
        "v_mfma_f32_16x16x32_bf16 v[116:119], v[220:223], v[188:191], v[116:119]\n"
        "s_waitcnt lgkmcnt(10)\n"
        "ds_read_b128 v[244:247], v9 offset:8192\n"
        "v_mfma_f32_16x16x32_bf16 v[116:119], v[224:227], v[192:195], v[116:119]\n"
        "s_waitcnt lgkmcnt(9)\n"
        "v_mfma_f32_16x16x32_bf16 v[68:71], v[212:215], v[248:251], v[68:71]\n"
        "s_waitcnt lgkmcnt(8)\n"
        "v_cvt_pk_bf16_f32 v44, v44, v45\n"
        "v_cvt_pk_bf16_f32 v45, v46, v47\n"
        "v_mfma_f32_16x16x32_bf16 v[68:71], v[216:219], v[252:255], v[68:71]\n"
        "v_mfma_f32_16x16x32_bf16 v[120:123], v[220:223], v[248:251], v[120:123]\n"
        "v_mfma_f32_16x16x32_bf16 v[120:123], v[224:227], v[252:255], v[120:123]\n"
        "s_waitcnt lgkmcnt(7)\n"
        "v_cvt_pk_bf16_f32 v46, v116, v117\n"
        "v_cvt_pk_bf16_f32 v47, v118, v119\n"
        "v_mfma_f32_16x16x32_bf16 v[92:95], v[212:215], v[232:235], v[92:95]\n"
        "s_waitcnt lgkmcnt(6)\n"
        "ds_read_b128 v[212:215], v9 offset:9216\n"
        "v_mfma_f32_16x16x32_bf16 v[92:95], v[216:219], v[236:239], v[92:95]\n"
        "v_cvt_pk_bf16_f32 v68, v68, v69\n"
        "v_cvt_pk_bf16_f32 v69, v70, v71\n"
        "v_mfma_f32_16x16x32_bf16 v[124:127], v[220:223], v[232:235], v[124:127]\n"
        "v_cvt_pk_bf16_f32 v70, v120, v121\n"
        "v_cvt_pk_bf16_f32 v71, v122, v123\n"
        "v_mfma_f32_16x16x32_bf16 v[124:127], v[224:227], v[236:239], v[124:127]\n"
        "ds_read_b128 v[216:219], v9 offset:14336\n"
        "v_mfma_f32_16x16x32_bf16 v[48:51], v[228:231], v[188:191], v[48:51]\n"
        "s_waitcnt lgkmcnt(7)\n"
        "v_cvt_pk_bf16_f32 v92, v92, v93\n"
        "v_cvt_pk_bf16_f32 v93, v94, v95\n"
        "v_mfma_f32_16x16x32_bf16 v[48:51], v[196:199], v[192:195], v[48:51]\n"
        "ds_read_b128 v[220:223], v9 offset:15360\n"
        "v_mfma_f32_16x16x32_bf16 v[72:75], v[228:231], v[248:251], v[72:75]\n"
        "v_cvt_pk_bf16_f32 v94, v124, v125\n"
        "v_cvt_pk_bf16_f32 v95, v126, v127\n"
        "v_mfma_f32_16x16x32_bf16 v[72:75], v[196:199], v[252:255], v[72:75]\n"
        "ds_read_b128 v[224:227], v9 offset:20480\n"
        "v_mfma_f32_16x16x32_bf16 v[96:99], v[228:231], v[232:235], v[96:99]\n"
        "ds_read_b128 v[116:119], v9 offset:21504\n"
        "v_mfma_f32_16x16x32_bf16 v[96:99], v[196:199], v[236:239], v[96:99]\n"
        "s_waitcnt lgkmcnt(9)\n"
        "v_cvt_pk_bf16_f32 v48, v48, v49\n"
        "v_cvt_pk_bf16_f32 v49, v50, v51\n"
        "v_mfma_f32_16x16x32_bf16 v[144:147], v[200:203], v[188:191], v[144:147]\n"
        "s_waitcnt lgkmcnt(8)\n"
        "v_cvt_pk_bf16_f32 v72, v72, v73\n"
        "v_cvt_pk_bf16_f32 v73, v74, v75\n"
        "v_mfma_f32_16x16x32_bf16 v[144:147], v[204:207], v[192:195], v[144:147]\n"
        "v_cvt_pk_bf16_f32 v96, v96, v97\n"
        "v_cvt_pk_bf16_f32 v97, v98, v99\n"
        "v_mfma_f32_16x16x32_bf16 v[128:131], v[200:203], v[248:251], v[128:131]\n"
        "ds_read_b128 v[228:231], v9 offset:4096\n"
        "v_mfma_f32_16x16x32_bf16 v[128:131], v[204:207], v[252:255], v[128:131]\n"
        "ds_read_b128 v[196:199], v9 offset:5120\n"
        "v_mfma_f32_16x16x32_bf16 v[148:151], v[200:203], v[232:235], v[148:151]\n"
        "ds_read_b128 v[120:123], v9 offset:10240\n"
        "v_mfma_f32_16x16x32_bf16 v[148:151], v[204:207], v[236:239], v[148:151]\n"
        "s_waitcnt lgkmcnt(10)\n"
        "v_cvt_pk_bf16_f32 v50, v144, v145\n"
        "v_cvt_pk_bf16_f32 v51, v146, v147\n"
        "v_mfma_f32_16x16x32_bf16 v[52:55], v[208:211], v[188:191], v[52:55]\n"
        "s_waitcnt lgkmcnt(9)\n"
        "v_cvt_pk_bf16_f32 v74, v128, v129\n"
        "v_cvt_pk_bf16_f32 v75, v130, v131\n"
        "v_mfma_f32_16x16x32_bf16 v[52:55], v[240:243], v[192:195], v[52:55]\n"
        "v_cvt_pk_bf16_f32 v98, v148, v149\n"
        "v_cvt_pk_bf16_f32 v99, v150, v151\n"
        "v_mfma_f32_16x16x32_bf16 v[76:79], v[208:211], v[248:251], v[76:79]\n"
        "ds_read_b128 v[200:203], v9 offset:11264\n"
        "v_mfma_f32_16x16x32_bf16 v[76:79], v[240:243], v[252:255], v[76:79]\n"
        "ds_read_b128 v[204:207], v9 offset:16384\n"
        "v_mfma_f32_16x16x32_bf16 v[100:103], v[208:211], v[232:235], v[100:103]\n"
        "ds_read_b128 v[124:127], v9 offset:17408\n"
        "v_mfma_f32_16x16x32_bf16 v[100:103], v[240:243], v[236:239], v[100:103]\n"
        "s_waitcnt lgkmcnt(11)\n"
        "v_cvt_pk_bf16_f32 v52, v52, v53\n"
        "v_cvt_pk_bf16_f32 v53, v54, v55\n"
        "v_mfma_f32_16x16x32_bf16 v[132:135], v[244:247], v[188:191], v[132:135]\n"
        "s_waitcnt lgkmcnt(10)\n"
        "v_cvt_pk_bf16_f32 v76, v76, v77\n"
        "v_cvt_pk_bf16_f32 v77, v78, v79\n"
        "v_mfma_f32_16x16x32_bf16 v[132:135], v[212:215], v[192:195], v[132:135]\n"
        "v_cvt_pk_bf16_f32 v100, v100, v101\n"
        "v_cvt_pk_bf16_f32 v101, v102, v103\n"
        "v_mfma_f32_16x16x32_bf16 v[160:163], v[244:247], v[248:251], v[160:163]\n"
        "ds_read_b128 v[208:211], v9 offset:22528\n"
        "v_mfma_f32_16x16x32_bf16 v[160:163], v[212:215], v[252:255], v[160:163]\n"
        "ds_read_b128 v[240:243], v9 offset:23552\n"
        "v_mfma_f32_16x16x32_bf16 v[136:139], v[244:247], v[232:235], v[136:139]\n"
        "ds_write_b8 v3, v4 offset:16\n"
        "v_mfma_f32_16x16x32_bf16 v[136:139], v[212:215], v[236:239], v[136:139]\n"
        "s_waitcnt lgkmcnt(12)\n"
        "v_cvt_pk_bf16_f32 v54, v132, v133\n"
        "v_cvt_pk_bf16_f32 v55, v134, v135\n"
        "v_mfma_f32_16x16x32_bf16 v[56:59], v[216:219], v[188:191], v[56:59]\n"
        "s_waitcnt lgkmcnt(11)\n"
        "v_cvt_pk_bf16_f32 v78, v160, v161\n"
        "v_cvt_pk_bf16_f32 v79, v162, v163\n"
        "v_mfma_f32_16x16x32_bf16 v[56:59], v[220:223], v[192:195], v[56:59]\n"
        "v_cvt_pk_bf16_f32 v102, v136, v137\n"
        "v_cvt_pk_bf16_f32 v103, v138, v139\n"
        "v_mfma_f32_16x16x32_bf16 v[80:83], v[216:219], v[248:251], v[80:83]\n"
        "v_mfma_f32_16x16x32_bf16 v[80:83], v[220:223], v[252:255], v[80:83]\n"
        "v_mfma_f32_16x16x32_bf16 v[104:107], v[216:219], v[232:235], v[104:107]\n"
        "v_mfma_f32_16x16x32_bf16 v[104:107], v[220:223], v[236:239], v[104:107]\n"
        "s_waitcnt lgkmcnt(10)\n"
        "v_mfma_f32_16x16x32_bf16 v[164:167], v[224:227], v[188:191], v[164:167]\n"
        "s_waitcnt lgkmcnt(9)\n"
        "v_cvt_pk_bf16_f32 v56, v56, v57\n"
        "v_cvt_pk_bf16_f32 v57, v58, v59\n"
        "v_mfma_f32_16x16x32_bf16 v[164:167], v[116:119], v[192:195], v[164:167]\n"
        "v_cvt_pk_bf16_f32 v80, v80, v81\n"
        "v_cvt_pk_bf16_f32 v81, v82, v83\n"
        "v_mfma_f32_16x16x32_bf16 v[140:143], v[224:227], v[248:251], v[140:143]\n"
        "v_cvt_pk_bf16_f32 v104, v104, v105\n"
        "v_cvt_pk_bf16_f32 v105, v106, v107\n"
        "v_mfma_f32_16x16x32_bf16 v[140:143], v[116:119], v[252:255], v[140:143]\n"
        "v_mfma_f32_16x16x32_bf16 v[168:171], v[224:227], v[232:235], v[168:171]\n"
        "v_mfma_f32_16x16x32_bf16 v[168:171], v[116:119], v[236:239], v[168:171]\n"
        "s_waitcnt lgkmcnt(8)\n"
        "v_cvt_pk_bf16_f32 v58, v164, v165\n"
        "v_cvt_pk_bf16_f32 v59, v166, v167\n"
        "v_mfma_f32_16x16x32_bf16 v[60:63], v[228:231], v[188:191], v[60:63]\n"
        "s_waitcnt lgkmcnt(7)\n"
        "v_mfma_f32_16x16x32_bf16 v[60:63], v[196:199], v[192:195], v[60:63]\n"
        "v_cvt_pk_bf16_f32 v82, v140, v141\n"
        "v_cvt_pk_bf16_f32 v83, v142, v143\n"
        "v_mfma_f32_16x16x32_bf16 v[84:87], v[228:231], v[248:251], v[84:87]\n"
        "ds_read_b32 v5, v2 offset:20\n"
        "v_cvt_pk_bf16_f32 v106, v168, v169\n"
        "v_mfma_f32_16x16x32_bf16 v[84:87], v[196:199], v[252:255], v[84:87]\n"
        "v_cvt_pk_bf16_f32 v107, v170, v171\n"
        "v_mfma_f32_16x16x32_bf16 v[108:111], v[228:231], v[232:235], v[108:111]\n"
        "v_cvt_pk_bf16_f32 v60, v60, v61\n"
        "v_cvt_pk_bf16_f32 v61, v62, v63\n"
        "v_mfma_f32_16x16x32_bf16 v[108:111], v[196:199], v[236:239], v[108:111]\n"
        "s_waitcnt lgkmcnt(7)\n"
        "v_mfma_f32_16x16x32_bf16 v[152:155], v[120:123], v[188:191], v[152:155]\n"
        "s_waitcnt lgkmcnt(6)\n"
        "v_mfma_f32_16x16x32_bf16 v[152:155], v[200:203], v[192:195], v[152:155]\n"
        "v_cvt_pk_bf16_f32 v84, v84, v85\n"
        "v_cvt_pk_bf16_f32 v85, v86, v87\n"
        "v_mfma_f32_16x16x32_bf16 v[156:159], v[120:123], v[248:251], v[156:159]\n"
        "s_waitcnt lgkmcnt(0)\n"
        "v_readfirstlane_b32 s86, v5\n"
        "v_mfma_f32_16x16x32_bf16 v[156:159], v[200:203], v[252:255], v[156:159]\n"
        "v_cvt_pk_bf16_f32 v108, v108, v109\n"
        "v_cvt_pk_bf16_f32 v109, v110, v111\n"
        "v_mfma_f32_16x16x32_bf16 v[172:175], v[120:123], v[232:235], v[172:175]\n"
        "v_cvt_pk_bf16_f32 v62, v152, v153\n"
        "v_cvt_pk_bf16_f32 v63, v154, v155\n"
        "v_mfma_f32_16x16x32_bf16 v[172:175], v[200:203], v[236:239], v[172:175]\n"
        "s_cmp_eq_u32 s86, 0x1010101\n"
        "v_mfma_f32_16x16x32_bf16 v[64:67], v[204:207], v[188:191], v[64:67]\n"
        "s_cbranch_scc0 Lsl10_%=\n"
        "Lrt11_%=:\n"
        "v_mfma_f32_16x16x32_bf16 v[64:67], v[124:127], v[192:195], v[64:67]\n"
        "v_cvt_pk_bf16_f32 v86, v156, v157\n"
        "v_cvt_pk_bf16_f32 v87, v158, v159\n"
        "v_mfma_f32_16x16x32_bf16 v[176:179], v[208:211], v[188:191], v[176:179]\n"
        "v_mfma_f32_16x16x32_bf16 v[176:179], v[240:243], v[192:195], v[176:179]\n"
        "ds_read_b128 v[216:219], v22 offset:0\n"
        "v_cvt_pk_bf16_f32 v110, v172, v173\n"
        "v_mfma_f32_16x16x32_bf16 v[88:91], v[204:207], v[248:251], v[88:91]\n"
        "ds_read_b128 v[220:223], v23 offset:0\n"
        "v_cvt_pk_bf16_f32 v111, v174, v175\n"
        "v_mfma_f32_16x16x32_bf16 v[88:91], v[124:127], v[252:255], v[88:91]\n"
        "ds_read_b128 v[224:227], v24 offset:0\n"
        "v_cvt_pk_bf16_f32 v64, v64, v65\n"
        "v_mfma_f32_16x16x32_bf16 v[180:183], v[208:211], v[248:251], v[180:183]\n"
        "ds_read_b128 v[228:231], v25 offset:0\n"
        "v_cvt_pk_bf16_f32 v65, v66, v67\n"
        "v_mfma_f32_16x16x32_bf16 v[180:183], v[240:243], v[252:255], v[180:183]\n"
        "v_cvt_pk_bf16_f32 v66, v176, v177\n"
        "v_cvt_pk_bf16_f32 v67, v178, v179\n"
        "v_mfma_f32_16x16x32_bf16 v[112:115], v[204:207], v[232:235], v[112:115]\n"
        "ds_read_b128 v[200:203], v26 offset:0\n"
        "v_cvt_pk_bf16_f32 v88, v88, v89\n"
        "v_mfma_f32_16x16x32_bf16 v[112:115], v[124:127], v[236:239], v[112:115]\n"
        "ds_read_b128 v[204:207], v27 offset:0\n"
        "v_cvt_pk_bf16_f32 v89, v90, v91\n"
        "v_mfma_f32_16x16x32_bf16 v[184:187], v[208:211], v[232:235], v[184:187]\n"
        "v_cvt_pk_bf16_f32 v90, v180, v181\n"
        "v_cvt_pk_bf16_f32 v91, v182, v183\n"
        "v_mfma_f32_16x16x32_bf16 v[184:187], v[240:243], v[236:239], v[184:187]\n"
        "s_waitcnt lgkmcnt(5)\n"
        "ds_read_b128 v[208:211], v28 offset:0\n"
        "v_mfma_f32_16x16x32_bf16 v[116:119], v[216:219], v[44:47], 0\n"
        "s_waitcnt lgkmcnt(5)\n"
        "ds_read_b128 v[212:215], v29 offset:0\n"
        "v_cvt_pk_bf16_f32 v112, v112, v113\n"
        "v_mfma_f32_16x16x32_bf16 v[116:119], v[220:223], v[48:51], v[116:119]\n"
        "s_waitcnt lgkmcnt(5)\n"
        "v_cvt_pk_bf16_f32 v113, v114, v115\n"
        "v_cvt_pk_bf16_f32 v114, v184, v185\n"
        "v_mfma_f32_16x16x32_bf16 v[120:123], v[224:227], v[44:47], 0\n"
        "s_waitcnt lgkmcnt(4)\n"
        "v_cvt_pk_bf16_f32 v115, v186, v187\n"
        "v_mfma_f32_16x16x32_bf16 v[120:123], v[228:231], v[48:51], v[120:123]\n"
        "s_waitcnt lgkmcnt(3)\n"
        "ds_read_b128 v[232:235], v38 offset:0\n"
        "v_cndmask_b32 v116, v116, v8, s[50:51]\n"
        "v_mfma_f32_16x16x32_bf16 v[124:127], v[200:203], v[44:47], 0\n"
        "s_waitcnt lgkmcnt(3)\n"
        "ds_read_b128 v[236:239], v39 offset:0\n"
        "v_cndmask_b32 v118, v118, v8, s[52:53]\n"
        "v_mfma_f32_16x16x32_bf16 v[124:127], v[204:207], v[48:51], v[124:127]\n"
        "v_cndmask_b32 v120, v120, v8, s[50:51]\n"
        "v_cndmask_b32 v122, v122, v8, s[52:53]\n"
        "v_mfma_f32_16x16x32_bf16 v[144:147], v[200:203], v[68:71], 0\n"
        "v_mfma_f32_16x16x32_bf16 v[144:147], v[204:207], v[72:75], v[144:147]\n"
        "s_waitcnt lgkmcnt(3)\n"
        "v_mfma_f32_16x16x32_bf16 v[128:131], v[208:211], v[44:47], 0\n"
        "s_waitcnt lgkmcnt(2)\n"
        "ds_read_b32 v5, v2 offset:24\n"
        "v_mfma_f32_16x16x32_bf16 v[128:131], v[212:215], v[48:51], v[128:131]\n"
        "ds_read_b128 v[240:243], v40 offset:0\n"
        "v_cndmask_b32 v127, v127, v8, s[54:55]\n"
        "v_mfma_f32_16x16x32_bf16 v[148:151], v[208:211], v[68:71], 0\n"
        "ds_read_b128 v[244:247], v41 offset:0\n"
        "v_cndmask_b32 v144, v144, v8, s[58:59]\n"
        "v_mfma_f32_16x16x32_bf16 v[148:151], v[212:215], v[72:75], v[148:151]\n"
        "s_waitcnt lgkmcnt(4)\n"
        "v_cndmask_b32 v146, v146, v8, s[60:61]\n"
        "v_mfma_f32_16x16x32_bf16 v[132:135], v[232:235], v[44:47], 0\n"
        "s_waitcnt lgkmcnt(3)\n"
        "v_cndmask_b32 v131, v131, v8, s[54:55]\n"
        "v_mfma_f32_16x16x32_bf16 v[132:135], v[236:239], v[48:51], v[132:135]\n"
        "v_mfma_f32_16x16x32_bf16 v[160:163], v[232:235], v[68:71], 0\n"
        "s_waitcnt lgkmcnt(2)\n"
        "v_readfirstlane_b32 s86, v5\n"
        "v_cndmask_b32 v148, v148, v8, s[58:59]\n"
        "v_mfma_f32_16x16x32_bf16 v[160:163], v[236:239], v[72:75], v[160:163]\n"
        "ds_read_b128 v[216:219], v42 offset:0\n"
        "v_cndmask_b32 v150, v150, v8, s[60:61]\n"
        "v_mfma_f32_16x16x32_bf16 v[188:191], v[232:235], v[92:95], 0\n"
        "ds_read_b128 v[220:223], v43 offset:0\n"
        "v_cndmask_b32 v133, v133, v8, s[56:57]\n"
        "v_mfma_f32_16x16x32_bf16 v[188:191], v[236:239], v[96:99], v[188:191]\n"
        "s_waitcnt lgkmcnt(3)\n"
        "s_cmp_eq_u32 s86, 0x1010101\n"
        "v_cndmask_b32 v135, v135, v8, s[56:57]\n"
        "v_mfma_f32_16x16x32_bf16 v[136:139], v[240:243], v[44:47], 0\n"
        "s_waitcnt lgkmcnt(2)\n"
        "v_mfma_f32_16x16x32_bf16 v[136:139], v[244:247], v[48:51], v[136:139]\n"
        "s_cbranch_scc0 Lsl13_%=\n"
        "Lrt14_%=:\n"
        "v_mfma_f32_16x16x32_bf16 v[164:167], v[240:243], v[68:71], 0\n"
        "ds_read_b128 v[224:227], v22 offset:25600\n"
        "v_cndmask_b32 v188, v188, v8, s[76:77]\n"
        "v_mfma_f32_16x16x32_bf16 v[164:167], v[244:247], v[72:75], v[164:167]\n"
        "ds_read_b128 v[228:231], v23 offset:25600\n"
        "v_cndmask_b32 v190, v190, v8, s[76:77]\n"
        "v_mfma_f32_16x16x32_bf16 v[192:195], v[240:243], v[92:95], 0\n"
        "ds_read_b128 v[200:203], v24 offset:25600\n"
        "v_cndmask_b32 v137, v137, v8, s[56:57]\n"
        "v_mfma_f32_16x16x32_bf16 v[192:195], v[244:247], v[96:99], v[192:195]\n"
        "s_waitcnt lgkmcnt(4)\n"
        "ds_read_b128 v[204:207], v25 offset:25600\n"
        "v_cndmask_b32 v139, v139, v8, s[56:57]\n"
        "v_mfma_f32_16x16x32_bf16 v[140:143], v[216:219], v[44:47], 0\n"
        "s_waitcnt lgkmcnt(4)\n"
        "ds_read_b128 v[208:211], v26 offset:25600\n"
        "v_mfma_f32_16x16x32_bf16 v[140:143], v[220:223], v[48:51], v[140:143]\n"
        "ds_read_b128 v[212:215], v27 offset:25600\n"
        "v_cndmask_b32 v192, v192, v8, s[76:77]\n"
        "v_mfma_f32_16x16x32_bf16 v[168:171], v[216:219], v[68:71], 0\n"
        "v_cndmask_b32 v194, v194, v8, s[76:77]\n"
        "v_mfma_f32_16x16x32_bf16 v[168:171], v[220:223], v[72:75], v[168:171]\n"
        "v_mfma_f32_16x16x32_bf16 v[196:199], v[216:219], v[92:95], 0\n"
        "ds_read_b128 v[232:235], v28 offset:25600\n"
        "v_mfma_f32_16x16x32_bf16 v[196:199], v[220:223], v[96:99], v[196:199]\n"
        "s_waitcnt lgkmcnt(6)\n"
        "ds_read_b128 v[236:239], v29 offset:25600\n"
        "v_cndmask_b32 v141, v141, v8, s[56:57]\n"
        "v_mfma_f32_16x16x32_bf16 v[116:119], v[224:227], v[52:55], v[116:119]\n"
        "s_waitcnt lgkmcnt(6)\n"
        "v_cndmask_b32 v143, v143, v8, s[56:57]\n"
        "v_mov_b32 v152, 0\n"
        "v_mfma_f32_16x16x32_bf16 v[116:119], v[228:231], v[56:59], v[116:119]\n"
        "s_waitcnt lgkmcnt(5)\n"
        "v_cndmask_b32 v196, v196, v8, s[76:77]\n"
        "v_cndmask_b32 v198, v198, v8, s[76:77]\n"
        "v_mfma_f32_16x16x32_bf16 v[120:123], v[200:203], v[52:55], v[120:123]\n"
        "s_waitcnt lgkmcnt(4)\n"
        "ds_read_b128 v[240:243], v38 offset:25600\n"
        "v_cndmask_b32 v153, 0, v8, s[62:63]\n"
        "v_mfma_f32_16x16x32_bf16 v[120:123], v[204:207], v[56:59], v[120:123]\n"
        "s_waitcnt lgkmcnt(4)\n"
        "ds_read_b128 v[244:247], v39 offset:25600\n"
        "v_mov_b32 v154, 0\n"
        "v_mfma_f32_16x16x32_bf16 v[124:127], v[208:211], v[52:55], v[124:127]\n"
        "s_waitcnt lgkmcnt(4)\n"
        "v_cndmask_b32 v155, 0, v8, s[64:65]\n"
        "v_mov_b32 v156, 0\n"
        "v_mfma_f32_16x16x32_bf16 v[124:127], v[212:215], v[56:59], v[124:127]\n"
        "v_cndmask_b32 v157, 0, v8, s[62:63]\n"
        "v_mov_b32 v158, 0\n"
        "v_mfma_f32_16x16x32_bf16 v[144:147], v[208:211], v[76:79], v[144:147]\n"
        "v_cndmask_b32 v159, 0, v8, s[64:65]\n"
        "v_mov_b32 v172, 0\n"
        "v_mfma_f32_16x16x32_bf16 v[144:147], v[212:215], v[80:83], v[144:147]\n"
        "s_waitcnt lgkmcnt(3)\n"
        "v_cndmask_b32 v173, 0, v8, s[66:67]\n"
        "v_cndmask_b32 v174, 0, v8, s[68:69]\n"
        "v_mfma_f32_16x16x32_bf16 v[128:131], v[232:235], v[52:55], v[128:131]\n"
        "s_waitcnt lgkmcnt(2)\n"
        "ds_read_b32 v5, v2 offset:28\n"
        "v_cndmask_b32 v175, 0, v8, s[70:71]\n"
        "v_mfma_f32_16x16x32_bf16 v[128:131], v[236:239], v[56:59], v[128:131]\n"
        "ds_read_b128 v[216:219], v40 offset:25600\n"
        "v_mov_b32 v176, 0\n"
        "v_mfma_f32_16x16x32_bf16 v[148:151], v[232:235], v[76:79], v[148:151]\n"
        "ds_read_b128 v[220:223], v41 offset:25600\n"
        "v_cndmask_b32 v177, 0, v8, s[66:67]\n"
        "v_mfma_f32_16x16x32_bf16 v[148:151], v[236:239], v[80:83], v[148:151]\n"
        "s_waitcnt lgkmcnt(4)\n"
        "v_cndmask_b32 v178, 0, v8, s[68:69]\n"
        "v_cndmask_b32 v179, 0, v8, s[70:71]\n"
        "v_mfma_f32_16x16x32_bf16 v[132:135], v[240:243], v[52:55], v[132:135]\n"
        "s_waitcnt lgkmcnt(3)\n"
        "v_mov_b32 v180, 0\n"
        "v_cndmask_b32 v181, 0, v8, s[72:73]\n"
        "v_mfma_f32_16x16x32_bf16 v[132:135], v[244:247], v[56:59], v[132:135]\n"
        "v_mov_b32 v182, 0\n"
        "v_cndmask_b32 v183, 0, v8, s[74:75]\n"
        "v_mfma_f32_16x16x32_bf16 v[160:163], v[240:243], v[76:79], v[160:163]\n"
        "s_waitcnt lgkmcnt(2)\n"
        "ds_read_b128 v[224:227], v42 offset:25600\n"
        "v_readfirstlane_b32 s86, v5\n"
        "v_mfma_f32_16x16x32_bf16 v[160:163], v[244:247], v[80:83], v[160:163]\n"
        "ds_read_b128 v[228:231], v43 offset:25600\n"
        "v_mov_b32 v184, 0\n"
        "v_mfma_f32_16x16x32_bf16 v[188:191], v[240:243], v[100:103], v[188:191]\n"
        "v_cndmask_b32 v185, 0, v8, s[72:73]\n"
        "v_mov_b32 v186, 0\n"
        "v_mfma_f32_16x16x32_bf16 v[188:191], v[244:247], v[104:107], v[188:191]\n"
        "s_waitcnt lgkmcnt(3)\n"
        "s_cmp_eq_u32 s86, 0x1010101\n"
        "v_cndmask_b32 v187, 0, v8, s[74:75]\n"
        "v_mfma_f32_16x16x32_bf16 v[136:139], v[216:219], v[52:55], v[136:139]\n"
        "s_waitcnt lgkmcnt(2)\n"
        "v_xor_b32 v44, 32, v10\n"
        "v_xor_b32 v45, 32, v11\n"
        "v_mfma_f32_16x16x32_bf16 v[136:139], v[220:223], v[56:59], v[136:139]\n"
        "s_cbranch_scc0 Lsl16_%=\n"
        "Lrt17_%=:\n"
        "v_mfma_f32_16x16x32_bf16 v[164:167], v[216:219], v[76:79], v[164:167]\n"
        "ds_read_b128 v[200:203], v22 offset:51200\n"
        "v_xor_b32 v46, 32, v12\n"
        "v_mfma_f32_16x16x32_bf16 v[164:167], v[220:223], v[80:83], v[164:167]\n"
        "ds_read_b128 v[204:207], v23 offset:51200\n"
        "v_xor_b32 v47, 32, v13\n"
        "v_mfma_f32_16x16x32_bf16 v[192:195], v[216:219], v[100:103], v[192:195]\n"
        "ds_read_b128 v[208:211], v24 offset:51200\n"
        "v_xor_b32 v48, 32, v14\n"
        "v_mfma_f32_16x16x32_bf16 v[192:195], v[220:223], v[104:107], v[192:195]\n"
        "s_waitcnt lgkmcnt(4)\n"
        "ds_read_b128 v[212:215], v25 offset:51200\n"
        "v_xor_b32 v49, 32, v15\n"
        "v_mfma_f32_16x16x32_bf16 v[140:143], v[224:227], v[52:55], v[140:143]\n"
        "s_waitcnt lgkmcnt(4)\n"
        "ds_read_b128 v[232:235], v26 offset:51200\n"
        "v_xor_b32 v50, 32, v16\n"
        "v_mfma_f32_16x16x32_bf16 v[140:143], v[228:231], v[56:59], v[140:143]\n"
        "ds_read_b128 v[236:239], v27 offset:51200\n"
        "v_xor_b32 v51, 32, v17\n"
        "v_mfma_f32_16x16x32_bf16 v[168:171], v[224:227], v[76:79], v[168:171]\n"
        "ds_read_b128 v[240:243], v28 offset:51200\n"
        "v_xor_b32 v21, 32, v18\n"
        "v_mfma_f32_16x16x32_bf16 v[168:171], v[228:231], v[80:83], v[168:171]\n"
        "ds_read_b128 v[244:247], v29 offset:51200\n"
        "v_xor_b32 v252, 32, v19\n"
        "v_mfma_f32_16x16x32_bf16 v[196:199], v[224:227], v[100:103], v[196:199]\n"
        "ds_read_b128 v[216:219], v38 offset:51200\n"
        "v_xor_b32 v253, 32, v20\n"
        "v_mfma_f32_16x16x32_bf16 v[196:199], v[228:231], v[104:107], v[196:199]\n"
        "s_waitcnt lgkmcnt(8)\n"
        "ds_read_b128 v[220:223], v39 offset:51200\n"
        "v_mfma_f32_16x16x32_bf16 v[116:119], v[200:203], v[60:63], v[116:119]\n"
        "s_waitcnt lgkmcnt(8)\n"
        "ds_read_b128 v[224:227], v40 offset:51200\n"
        "v_mfma_f32_16x16x32_bf16 v[116:119], v[204:207], v[64:67], v[116:119]\n"
        "s_waitcnt lgkmcnt(8)\n"
        "ds_read_b128 v[228:231], v41 offset:51200\n"
        "v_mfma_f32_16x16x32_bf16 v[120:123], v[208:211], v[60:63], v[120:123]\n"
        "s_waitcnt lgkmcnt(8)\n"
        "ds_read_b128 v[200:203], v42 offset:51200\n"
        "v_mfma_f32_16x16x32_bf16 v[120:123], v[212:215], v[64:67], v[120:123]\n"
        "s_waitcnt lgkmcnt(8)\n"
        "ds_read_b128 v[204:207], v43 offset:51200\n"
        "v_mfma_f32_16x16x32_bf16 v[124:127], v[232:235], v[60:63], v[124:127]\n"
        "s_waitcnt lgkmcnt(8)\n"
        "v_mfma_f32_16x16x32_bf16 v[124:127], v[236:239], v[64:67], v[124:127]\n"
        "s_waitcnt lgkmcnt(7)\n"
        "v_mfma_f32_16x16x32_bf16 v[128:131], v[240:243], v[60:63], v[128:131]\n"
        "s_waitcnt lgkmcnt(6)\n"
        "v_mfma_f32_16x16x32_bf16 v[128:131], v[244:247], v[64:67], v[128:131]\n"
        "s_waitcnt lgkmcnt(5)\n"
        "v_cndmask_b32 v116, v120, v116, s[40:41]\n"
        "v_cndmask_b32 v117, v121, v117, s[40:41]\n"
        "v_mfma_f32_16x16x32_bf16 v[132:135], v[216:219], v[60:63], v[132:135]\n"
        "s_waitcnt lgkmcnt(4)\n"
        "v_cndmask_b32 v118, v122, v118, s[42:43]\n"
        "v_cndmask_b32 v119, v123, v119, s[42:43]\n"
        "v_mfma_f32_16x16x32_bf16 v[132:135], v[220:223], v[64:67], v[132:135]\n"
        "s_waitcnt lgkmcnt(3)\n"
        "v_cndmask_b32 v124, v128, v124, s[40:41]\n"
        "v_cndmask_b32 v125, v129, v125, s[40:41]\n"
        "v_mfma_f32_16x16x32_bf16 v[136:139], v[224:227], v[60:63], v[136:139]\n"
        "s_waitcnt lgkmcnt(2)\n"
        "v_cndmask_b32 v126, v130, v126, s[42:43]\n"
        "v_cndmask_b32 v127, v131, v127, s[42:43]\n"
        "v_mfma_f32_16x16x32_bf16 v[136:139], v[228:231], v[64:67], v[136:139]\n"
        "s_waitcnt lgkmcnt(1)\n"
        "v_max3_f32 v120, v116, v117, v118\n"
        "v_max3_f32 v120, v120, v119, v124\n"
        "v_mfma_f32_16x16x32_bf16 v[140:143], v[200:203], v[60:63], v[140:143]\n"
        "s_waitcnt lgkmcnt(0)\n"
        "v_max3_f32 v120, v120, v125, v126\n"
        "v_mfma_f32_16x16x32_bf16 v[140:143], v[204:207], v[64:67], v[140:143]\n"
        "v_mov_b64 v[66:67], 0\n"
        "v_mfma_f32_16x16x32_bf16 v[144:147], v[232:235], v[84:87], v[144:147]\n"
        "v_cndmask_b32 v132, v136, v132, s[44:45]\n"
        "v_cndmask_b32 v133, v137, v133, s[44:45]\n"
        "v_mfma_f32_16x16x32_bf16 v[144:147], v[236:239], v[88:91], v[144:147]\n"
        "v_cndmask_b32 v134, v138, v134, s[46:47]\n"
        "v_cndmask_b32 v135, v139, v135, s[46:47]\n"
        "v_mfma_f32_16x16x32_bf16 v[148:151], v[240:243], v[84:87], v[148:151]\n"
        "v_cndmask_b32 v140, v142, v140, s[48:49]\n"
        "v_cndmask_b32 v141, v143, v141, s[48:49]\n"
        "v_mfma_f32_16x16x32_bf16 v[148:151], v[244:247], v[88:91], v[148:151]\n"
        "v_max3_f32 v120, v120, v127, v132\n"
        "v_max3_f32 v120, v120, v133, v134\n"
        "v_mfma_f32_16x16x32_bf16 v[160:163], v[216:219], v[84:87], v[160:163]\n"
        "v_max3_f32 v120, v120, v135, v140\n"
        "v_max_f32 v120, v120, v141\n"
        "v_mfma_f32_16x16x32_bf16 v[160:163], v[220:223], v[88:91], v[160:163]\n"
        "v_mov_b32 v121, v120\n"
        "v_mov_b64 v[136:137], s[90:91]\n"
        "v_mfma_f32_16x16x32_bf16 v[188:191], v[216:219], v[108:111], v[188:191]\n"
        "ds_read_b128 v[208:211], v30 offset:0\n"
        "v_mov_b64 v[138:139], s[90:91]\n"
        "v_mfma_f32_16x16x32_bf16 v[188:191], v[220:223], v[112:115], v[188:191]\n"
        "ds_read_b128 v[212:215], v31 offset:0\n"
        "v_mfma_f32_16x16x32_bf16 v[164:167], v[224:227], v[84:87], v[164:167]\n"
        "v_permlane16_swap_b32 v120, v121\n"
        "v_mfma_f32_16x16x32_bf16 v[164:167], v[228:231], v[88:91], v[164:167]\n"
        "v_max_f32 v120, v120, v121\n"
        "v_mov_b32 v121, v120\n"
        "v_mfma_f32_16x16x32_bf16 v[192:195], v[224:227], v[108:111], v[192:195]\n"
        "ds_read_b128 v[232:235], v32 offset:0\n"
        "v_mfma_f32_16x16x32_bf16 v[192:195], v[228:231], v[112:115], v[192:195]\n"
        "ds_read_b128 v[236:239], v33 offset:0\n"
        "v_mfma_f32_16x16x32_bf16 v[168:171], v[200:203], v[84:87], v[168:171]\n"
        "v_permlane32_swap_b32 v120, v121\n"
        "v_mfma_f32_16x16x32_bf16 v[168:171], v[204:207], v[88:91], v[168:171]\n"
        "v_max_f32 v120, v120, v121\n"
        "v_sub_f32 v116, v116, v120\n"
        "v_mfma_f32_16x16x32_bf16 v[196:199], v[200:203], v[108:111], v[196:199]\n"
        "ds_read_b128 v[240:243], v34 offset:0\n"
        "v_sub_f32 v117, v117, v120\n"
        "v_mfma_f32_16x16x32_bf16 v[196:199], v[204:207], v[112:115], v[196:199]\n"
        "s_waitcnt lgkmcnt(4)\n"
        "ds_read_b128 v[244:247], v35 offset:0\n"
        "v_sub_f32 v118, v118, v120\n"
        "v_mfma_f32_16x16x32_bf16 v[152:155], v[208:211], v[68:71], v[152:155]\n"
        "s_waitcnt lgkmcnt(4)\n"
        "ds_read_b128 v[216:219], v36 offset:0\n"
        "v_sub_f32 v119, v119, v120\n"
        "v_mfma_f32_16x16x32_bf16 v[152:155], v[212:215], v[72:75], v[152:155]\n"
        "ds_read_b128 v[220:223], v37 offset:0\n"
        "v_sub_f32 v124, v124, v120\n"
        "v_mfma_f32_16x16x32_bf16 v[172:175], v[208:211], v[92:95], v[172:175]\n"
        "ds_read_b128 v[224:227], v30 offset:25600\n"
        "v_sub_f32 v125, v125, v120\n"
        "v_mfma_f32_16x16x32_bf16 v[172:175], v[212:215], v[96:99], v[172:175]\n"
        "s_waitcnt lgkmcnt(6)\n"
        "ds_read_b128 v[228:231], v31 offset:25600\n"
        "v_sub_f32 v126, v126, v120\n"
        "v_mfma_f32_16x16x32_bf16 v[156:159], v[232:235], v[68:71], v[156:159]\n"
        "s_waitcnt lgkmcnt(6)\n"
        "v_exp_f32 v116, v116\n"
        "v_mfma_f32_16x16x32_bf16 v[156:159], v[236:239], v[72:75], v[156:159]\n"
        "v_exp_f32 v117, v117\n"
        "v_mfma_f32_16x16x32_bf16 v[176:179], v[232:235], v[92:95], v[176:179]\n"
        "ds_read_b128 v[200:203], v32 offset:25600\n"
        "v_sub_f32 v127, v127, v120\n"
        "v_mfma_f32_16x16x32_bf16 v[176:179], v[236:239], v[96:99], v[176:179]\n"
        "s_waitcnt lgkmcnt(6)\n"
        "ds_read_b128 v[204:207], v33 offset:25600\n"
        "v_sub_f32 v132, v132, v120\n"
        "v_mfma_f32_16x16x32_bf16 v[180:183], v[240:243], v[92:95], v[180:183]\n"
        "s_waitcnt lgkmcnt(6)\n"
        "v_exp_f32 v118, v118\n"
        "v_mfma_f32_16x16x32_bf16 v[180:183], v[244:247], v[96:99], v[180:183]\n"
        "s_waitcnt lgkmcnt(5)\n"
        "v_exp_f32 v119, v119\n"
        "v_mfma_f32_16x16x32_bf16 v[184:187], v[216:219], v[92:95], v[184:187]\n"
        "s_waitcnt lgkmcnt(4)\n"
        "ds_read_b128 v[208:211], v34 offset:25600\n"
        "v_sub_f32 v133, v133, v120\n"
        "v_mfma_f32_16x16x32_bf16 v[184:187], v[220:223], v[96:99], v[184:187]\n"
        "s_waitcnt lgkmcnt(4)\n"
        "ds_read_b128 v[212:215], v35 offset:25600\n"
        "v_sub_f32 v134, v134, v120\n"
        "v_mfma_f32_16x16x32_bf16 v[152:155], v[224:227], v[76:79], v[152:155]\n"
        "s_waitcnt lgkmcnt(4)\n"
        "ds_read_b128 v[232:235], v36 offset:25600\n"
        "v_sub_f32 v135, v135, v120\n"
        "v_mfma_f32_16x16x32_bf16 v[152:155], v[228:231], v[80:83], v[152:155]\n"
        "ds_read_b128 v[236:239], v37 offset:25600\n"
        "v_sub_f32 v140, v140, v120\n"
        "v_mfma_f32_16x16x32_bf16 v[172:175], v[224:227], v[100:103], v[172:175]\n"
        "ds_read_b128 v[240:243], v30 offset:51200\n"
        "v_sub_f32 v141, v141, v120\n"
        "v_mfma_f32_16x16x32_bf16 v[172:175], v[228:231], v[104:107], v[172:175]\n"
        "s_waitcnt lgkmcnt(6)\n"
        "ds_read_b128 v[244:247], v31 offset:51200\n"
        "v_cvt_pk_bf16_f32 v54, v116, v117\n"
        "v_mfma_f32_16x16x32_bf16 v[156:159], v[200:203], v[76:79], v[156:159]\n"
        "s_waitcnt lgkmcnt(6)\n"
        "ds_read_b128 v[216:219], v32 offset:51200\n"
        "v_cndmask_b32 v52, 0, v54, s[40:41]\n"
        "v_mfma_f32_16x16x32_bf16 v[156:159], v[204:207], v[80:83], v[156:159]\n"
        "ds_read_b128 v[220:223], v33 offset:51200\n"
        "v_xor_b32 v54, v54, v52\n"
        "v_mfma_f32_16x16x32_bf16 v[176:179], v[200:203], v[100:103], v[176:179]\n"
        "v_exp_f32 v124, v124\n"
        "v_mfma_f32_16x16x32_bf16 v[176:179], v[204:207], v[104:107], v[176:179]\n"
        "s_waitcnt lgkmcnt(7)\n"
        "v_exp_f32 v125, v125\n"
        "v_mfma_f32_16x16x32_bf16 v[180:183], v[208:211], v[100:103], v[180:183]\n"
        "s_waitcnt lgkmcnt(6)\n"
        "v_exp_f32 v126, v126\n"
        "v_mfma_f32_16x16x32_bf16 v[180:183], v[212:215], v[104:107], v[180:183]\n"
        "s_waitcnt lgkmcnt(5)\n"
        "v_exp_f32 v127, v127\n"
        "v_mfma_f32_16x16x32_bf16 v[184:187], v[232:235], v[100:103], v[184:187]\n"
        "s_waitcnt lgkmcnt(4)\n"
        "ds_read_b128 v[224:227], v34 offset:51200\n"
        "v_cvt_pk_bf16_f32 v55, v118, v119\n"
        "v_mfma_f32_16x16x32_bf16 v[184:187], v[236:239], v[104:107], v[184:187]\n"
        "s_waitcnt lgkmcnt(4)\n"
        "ds_read_b128 v[228:231], v35 offset:51200\n"
        "v_cndmask_b32 v53, 0, v55, s[42:43]\n"
        "v_mfma_f32_16x16x32_bf16 v[152:155], v[240:243], v[84:87], v[152:155]\n"
        "s_waitcnt lgkmcnt(4)\n"
        "ds_read_b128 v[200:203], v36 offset:51200\n"
        "v_xor_b32 v55, v55, v53\n"
        "v_mfma_f32_16x16x32_bf16 v[152:155], v[244:247], v[88:91], v[152:155]\n"
        "s_waitcnt lgkmcnt(4)\n"
        "ds_read_b128 v[204:207], v37 offset:51200\n"
        "v_cvt_pk_bf16_f32 v58, v124, v125\n"
        "v_mfma_f32_16x16x32_bf16 v[156:159], v[216:219], v[84:87], v[156:159]\n"
        "s_waitcnt lgkmcnt(4)\n"
        "v_exp_f32 v132, v132\n"
        "v_mfma_f32_16x16x32_bf16 v[156:159], v[220:223], v[88:91], v[156:159]\n"
        "v_exp_f32 v133, v133\n"
        "v_mfma_f32_16x16x32_bf16 v[172:175], v[240:243], v[108:111], v[172:175]\n"
        "v_exp_f32 v134, v134\n"
        "v_mfma_f32_16x16x32_bf16 v[172:175], v[244:247], v[112:115], v[172:175]\n"
        "v_exp_f32 v135, v135\n"
        "v_mfma_f32_16x16x32_bf16 v[176:179], v[216:219], v[108:111], v[176:179]\n"
        "v_exp_f32 v140, v140\n"
        "v_mfma_f32_16x16x32_bf16 v[176:179], v[220:223], v[112:115], v[176:179]\n"
        "s_waitcnt lgkmcnt(3)\n"
        "v_exp_f32 v141, v141\n"
        "v_mfma_f32_16x16x32_bf16 v[180:183], v[224:227], v[108:111], v[180:183]\n"
        "s_waitcnt lgkmcnt(2)\n"
        "v_cndmask_b32 v56, 0, v58, s[40:41]\n"
        "v_xor_b32 v58, v58, v56\n"
        "v_mfma_f32_16x16x32_bf16 v[180:183], v[228:231], v[112:115], v[180:183]\n"
        "s_waitcnt lgkmcnt(1)\n"
        "v_cvt_pk_bf16_f32 v59, v126, v127\n"
        "v_cndmask_b32 v57, 0, v59, s[42:43]\n"
        "v_mfma_f32_16x16x32_bf16 v[184:187], v[200:203], v[108:111], v[184:187]\n"
        "s_waitcnt lgkmcnt(0)\n"
        "v_xor_b32 v59, v59, v57\n"
        "v_cvt_pk_bf16_f32 v62, v132, v133\n"
        "v_mfma_f32_16x16x32_bf16 v[184:187], v[204:207], v[112:115], v[184:187]\n"
        "ds_read_b64_tr_b16 v[200:201], v10 offset:0\n"
        "ds_read_b64_tr_b16 v[202:203], v11 offset:0\n"
        "ds_read_b64_tr_b16 v[204:205], v12 offset:0\n"
        "ds_read_b64_tr_b16 v[206:207], v13 offset:0\n"
        "ds_read_b64_tr_b16 v[216:217], v18 offset:0\n"
        "ds_read_b64_tr_b16 v[218:219], v19 offset:0\n"
        "ds_read_b64_tr_b16 v[220:221], v20 offset:0\n"
        "ds_read_b64_tr_b16 v[208:209], v14 offset:0\n"
        "ds_read_b64_tr_b16 v[210:211], v15 offset:0\n"
        "ds_read_b64_tr_b16 v[212:213], v16 offset:0\n"
        "ds_read_b64_tr_b16 v[214:215], v17 offset:0\n"
        "v_cndmask_b32 v60, 0, v62, s[44:45]\n"
        "v_xor_b32 v62, v62, v60\n"
        "v_cvt_pk_bf16_f32 v63, v134, v135\n"
        "v_cndmask_b32 v61, 0, v63, s[46:47]\n"
        "v_xor_b32 v63, v63, v61\n"
        "v_cvt_pk_bf16_f32 v65, v140, v141\n"
        "v_cndmask_b32 v64, 0, v65, s[48:49]\n"
        "v_xor_b32 v65, v65, v64\n"
        "s_not_b64 exec, s[44:45]\n"
        "v_mov_b64 v[160:161], v[164:165]\n"
        "v_mov_b64 v[188:189], v[192:193]\n"
        "s_not_b64 exec, s[46:47]\n"
        "v_mov_b64 v[162:163], v[166:167]\n"
        "v_mov_b64 v[190:191], v[194:195]\n"
        "s_not_b64 exec, s[48:49]\n"
        "v_mov_b64 v[168:169], v[170:171]\n"
        "v_mov_b64 v[196:197], v[198:199]\n"
        "s_not_b64 exec, s[40:41]\n"
        "v_mov_b64 v[144:145], v[148:149]\n"
        "v_mov_b64 v[152:153], v[156:157]\n"
        "v_mov_b64 v[172:173], v[176:177]\n"
        "v_mov_b64 v[180:181], v[184:185]\n"
        "s_not_b64 exec, s[42:43]\n"
        "v_mov_b64 v[146:147], v[150:151]\n"
        "v_mov_b64 v[154:155], v[158:159]\n"
        "v_mov_b64 v[174:175], v[178:179]\n"
        "v_mov_b64 v[182:183], v[186:187]\n"
        "s_mov_b64 exec, -1\n"
        "v_max3_f32 v148, v144, v145, v146\n"
        "v_max3_f32 v176, v172, v173, v174\n"
        "v_max3_f32 v148, v148, v147, v152\n"
        "v_max3_f32 v176, v176, v175, v180\n"
        "v_max3_f32 v148, v148, v153, v154\n"
        "v_max3_f32 v176, v176, v181, v182\n"
        "v_max3_f32 v148, v148, v155, v160\n"
        "v_max3_f32 v176, v176, v183, v188\n"
        "v_max3_f32 v148, v148, v161, v162\n"
        "v_max3_f32 v176, v176, v189, v190\n"
        "v_max3_f32 v148, v148, v163, v168\n"
        "v_max3_f32 v176, v176, v191, v196\n"
        "v_max_f32 v148, v148, v169\n"
        "v_max_f32 v176, v176, v197\n"
        "v_mov_b32 v149, v148\n"
        "v_mov_b32 v177, v176\n"
        "v_mov_b64 v[82:83], 0\n"
        "v_permlane16_swap_b32 v148, v149\n"
        "v_permlane16_swap_b32 v176, v177\n"
        "v_max_f32 v148, v148, v149\n"
        "v_max_f32 v176, v176, v177\n"
        "v_mov_b32 v149, v148\n"
        "v_mov_b32 v177, v176\n"
        "v_mov_b64 v[106:107], 0\n"
        "v_permlane32_swap_b32 v148, v149\n"
        "v_permlane32_swap_b32 v176, v177\n"
        "v_max_f32 v148, v148, v149\n"
        "v_max_f32 v176, v176, v177\n"
        "v_pk_add_f32 v[144:145], v[144:145], v[148:149] op_sel_hi:[1,0] neg_lo:[0,1] neg_hi:[0,1]\n"
        "v_pk_add_f32 v[146:147], v[146:147], v[148:149] op_sel_hi:[1,0] neg_lo:[0,1] neg_hi:[0,1]\n"
        "v_pk_add_f32 v[152:153], v[152:153], v[148:149] op_sel_hi:[1,0] neg_lo:[0,1] neg_hi:[0,1]\n"
        "v_pk_add_f32 v[154:155], v[154:155], v[148:149] op_sel_hi:[1,0] neg_lo:[0,1] neg_hi:[0,1]\n"
        "v_pk_add_f32 v[160:161], v[160:161], v[148:149] op_sel_hi:[1,0] neg_lo:[0,1] neg_hi:[0,1]\n"
        "v_pk_add_f32 v[162:163], v[162:163], v[148:149] op_sel_hi:[1,0] neg_lo:[0,1] neg_hi:[0,1]\n"
        "v_pk_add_f32 v[168:169], v[168:169], v[148:149] op_sel_hi:[1,0] neg_lo:[0,1] neg_hi:[0,1]\n"
        "v_pk_add_f32 v[172:173], v[172:173], v[176:177] op_sel_hi:[1,0] neg_lo:[0,1] neg_hi:[0,1]\n"
        "v_pk_add_f32 v[174:175], v[174:175], v[176:177] op_sel_hi:[1,0] neg_lo:[0,1] neg_hi:[0,1]\n"
        "v_pk_add_f32 v[180:181], v[180:181], v[176:177] op_sel_hi:[1,0] neg_lo:[0,1] neg_hi:[0,1]\n"
        "v_pk_add_f32 v[182:183], v[182:183], v[176:177] op_sel_hi:[1,0] neg_lo:[0,1] neg_hi:[0,1]\n"
        "v_pk_add_f32 v[188:189], v[188:189], v[176:177] op_sel_hi:[1,0] neg_lo:[0,1] neg_hi:[0,1]\n"
        "v_pk_add_f32 v[190:191], v[190:191], v[176:177] op_sel_hi:[1,0] neg_lo:[0,1] neg_hi:[0,1]\n"
        "v_pk_add_f32 v[196:197], v[196:197], v[176:177] op_sel_hi:[1,0] neg_lo:[0,1] neg_hi:[0,1]\n"
        "v_mfma_f32_16x16x32_bf16 v[108:111], v[52:55], v[136:139], 0\n"
        "ds_read_b64_tr_b16 v[224:225], v44 offset:0\n"
        "ds_read_b64_tr_b16 v[226:227], v45 offset:0\n"
        "ds_read_b64_tr_b16 v[228:229], v46 offset:0\n"
        "v_exp_f32 v144, v144\n"
        "v_mfma_f32_16x16x32_bf16 v[108:111], v[56:59], v[136:139], v[108:111]\n"
        "s_waitcnt lgkmcnt(12)\n"
        "ds_read_b64_tr_b16 v[230:231], v47 offset:0\n"
        "ds_read_b64_tr_b16 v[240:241], v21 offset:0\n"
        "ds_read_b64_tr_b16 v[242:243], v252 offset:0\n"
        "v_exp_f32 v145, v145\n"
        "v_mfma_f32_16x16x32_bf16 v[108:111], v[60:63], v[136:139], v[108:111]\n"
        "s_waitcnt lgkmcnt(12)\n"
        "ds_read_b64_tr_b16 v[244:245], v253 offset:0\n"
        "ds_read_b64_tr_b16 v[232:233], v48 offset:0\n"
        "ds_read_b64_tr_b16 v[234:235], v49 offset:0\n"
        "v_exp_f32 v146, v146\n"
        "v_mfma_f32_16x16x32_bf16 v[108:111], v[64:67], v[136:139], v[108:111]\n"
        "s_waitcnt lgkmcnt(13)\n"
        "ds_read_b64_tr_b16 v[236:237], v50 offset:0\n"
        "ds_read_b64_tr_b16 v[238:239], v51 offset:0\n"
        "v_exp_f32 v147, v147\n"
        "v_exp_f32 v152, v152\n"
        "v_mfma_f32_16x16x32_bf16 v[124:127], v[200:203], v[52:55], 0\n"
        "v_exp_f32 v153, v153\n"
        "v_exp_f32 v154, v154\n"
        "v_mfma_f32_16x16x32_bf16 v[124:127], v[204:207], v[56:59], v[124:127]\n"
        "v_exp_f32 v155, v155\n"
        "v_exp_f32 v160, v160\n"
        "v_exp_f32 v161, v161\n"
        "v_mfma_f32_16x16x32_bf16 v[124:127], v[216:219], v[60:63], v[124:127]\n"
        "v_exp_f32 v162, v162\n"
        "v_exp_f32 v163, v163\n"
        "v_mfma_f32_16x16x32_bf16 v[124:127], v[220:223], v[64:67], v[124:127]\n"
        "s_waitcnt lgkmcnt(9)\n"
        "v_exp_f32 v168, v168\n"
        "v_exp_f32 v169, v169\n"
        "v_cvt_pk_bf16_f32 v70, v144, v145\n"
        "v_mfma_f32_16x16x32_bf16 v[128:131], v[224:227], v[52:55], 0\n"
        "s_waitcnt lgkmcnt(7)\n"
        "v_cvt_pk_bf16_f32 v71, v146, v147\n"
        "v_cvt_pk_bf16_f32 v74, v152, v153\n"
        "v_cvt_pk_bf16_f32 v75, v154, v155\n"
        "v_cvt_pk_bf16_f32 v78, v160, v161\n"
        "v_cvt_pk_bf16_f32 v79, v162, v163\n"
        "v_mfma_f32_16x16x32_bf16 v[128:131], v[228:231], v[56:59], v[128:131]\n"
        "s_waitcnt lgkmcnt(5)\n"
        "v_cvt_pk_bf16_f32 v81, v168, v169\n"
        "v_cndmask_b32 v68, 0, v70, s[40:41]\n"
        "v_cndmask_b32 v69, 0, v71, s[42:43]\n"
        "v_cndmask_b32 v72, 0, v74, s[40:41]\n"
        "v_cndmask_b32 v73, 0, v75, s[42:43]\n"
        "v_mfma_f32_16x16x32_bf16 v[128:131], v[240:243], v[60:63], v[128:131]\n"
        "s_waitcnt lgkmcnt(4)\n"
        "v_cndmask_b32 v76, 0, v78, s[44:45]\n"
        "v_cndmask_b32 v77, 0, v79, s[46:47]\n"
        "v_cndmask_b32 v80, 0, v81, s[48:49]\n"
        "v_xor_b32 v70, v70, v68\n"
        "v_xor_b32 v71, v71, v69\n"
        "v_mfma_f32_16x16x32_bf16 v[128:131], v[244:247], v[64:67], v[128:131]\n"
        "v_xor_b32 v74, v74, v72\n"
        "v_xor_b32 v75, v75, v73\n"
        "v_xor_b32 v78, v78, v76\n"
        "v_xor_b32 v79, v79, v77\n"
        "v_xor_b32 v81, v81, v80\n"
        "v_mfma_f32_16x16x32_bf16 v[132:135], v[204:207], v[68:71], 0\n"
        "v_exp_f32 v172, v172\n"
        "v_exp_f32 v173, v173\n"
        "v_exp_f32 v174, v174\n"
        "v_mfma_f32_16x16x32_bf16 v[132:135], v[208:211], v[72:75], v[132:135]\n"
        "v_exp_f32 v175, v175\n"
        "v_exp_f32 v180, v180\n"
        "v_mfma_f32_16x16x32_bf16 v[132:135], v[216:219], v[76:79], v[132:135]\n"
        "v_exp_f32 v181, v181\n"
        "v_exp_f32 v182, v182\n"
        "v_mfma_f32_16x16x32_bf16 v[132:135], v[220:223], v[80:83], v[132:135]\n"
        "v_cvt_pk_bf16_f32 v116, v124, v125\n"
        "v_cvt_pk_bf16_f32 v117, v126, v127\n"
        "v_exp_f32 v183, v183\n"
        "v_exp_f32 v188, v188\n"
        "v_mfma_f32_16x16x32_bf16 v[124:127], v[228:231], v[68:71], 0\n"
        "s_waitcnt lgkmcnt(2)\n"
        "v_exp_f32 v189, v189\n"
        "v_exp_f32 v190, v190\n"
        "v_mfma_f32_16x16x32_bf16 v[124:127], v[232:235], v[72:75], v[124:127]\n"
        "v_exp_f32 v191, v191\n"
        "v_exp_f32 v196, v196\n"
        "v_mfma_f32_16x16x32_bf16 v[124:127], v[240:243], v[76:79], v[124:127]\n"
        "v_exp_f32 v197, v197\n"
        "v_cvt_pk_bf16_f32 v94, v172, v173\n"
        "v_mfma_f32_16x16x32_bf16 v[124:127], v[244:247], v[80:83], v[124:127]\n"
        "v_cvt_pk_bf16_f32 v118, v128, v129\n"
        "v_cvt_pk_bf16_f32 v119, v130, v131\n"
        "v_cvt_pk_bf16_f32 v95, v174, v175\n"
        "v_cvt_pk_bf16_f32 v98, v180, v181\n"
        "v_cvt_pk_bf16_f32 v99, v182, v183\n"
        "v_cvt_pk_bf16_f32 v102, v188, v189\n"
        "v_mfma_f32_16x16x32_bf16 v[112:115], v[68:71], v[136:139], 0\n"
        "v_cvt_pk_bf16_f32 v103, v190, v191\n"
        "v_cvt_pk_bf16_f32 v105, v196, v197\n"
        "v_cndmask_b32 v92, 0, v94, s[40:41]\n"
        "v_cndmask_b32 v93, 0, v95, s[42:43]\n"
        "v_mfma_f32_16x16x32_bf16 v[112:115], v[72:75], v[136:139], v[112:115]\n"
        "v_cndmask_b32 v96, 0, v98, s[40:41]\n"
        "v_cndmask_b32 v97, 0, v99, s[42:43]\n"
        "v_cndmask_b32 v100, 0, v102, s[44:45]\n"
        "v_cndmask_b32 v101, 0, v103, s[46:47]\n"
        "v_mfma_f32_16x16x32_bf16 v[112:115], v[76:79], v[136:139], v[112:115]\n"
        "v_cndmask_b32 v104, 0, v105, s[48:49]\n"
        "v_xor_b32 v94, v94, v92\n"
        "v_xor_b32 v95, v95, v93\n"
        "v_xor_b32 v98, v98, v96\n"
        "v_mfma_f32_16x16x32_bf16 v[112:115], v[80:83], v[136:139], v[112:115]\n"
        "v_cvt_pk_bf16_f32 v140, v132, v133\n"
        "v_cvt_pk_bf16_f32 v141, v134, v135\n"
        "v_xor_b32 v99, v99, v97\n"
        "v_xor_b32 v102, v102, v100\n"
        "v_xor_b32 v103, v103, v101\n"
        "v_xor_b32 v105, v105, v104\n"
        "v_xor_b32 v22, 64, v10\n"
        "v_xor_b32 v23, 64, v11\n"
        "v_mfma_f32_16x16x32_bf16 v[128:131], v[208:211], v[92:95], 0\n"
        "v_xor_b32 v24, 64, v12\n"
        "v_xor_b32 v25, 64, v13\n"
        "v_mfma_f32_16x16x32_bf16 v[128:131], v[212:215], v[96:99], v[128:131]\n"
        "v_xor_b32 v38, 64, v18\n"
        "v_xor_b32 v39, 64, v19\n"
        "v_mfma_f32_16x16x32_bf16 v[128:131], v[216:219], v[100:103], v[128:131]\n"
        "v_cvt_pk_bf16_f32 v142, v124, v125\n"
        "v_xor_b32 v40, 64, v20\n"
        "v_mfma_f32_16x16x32_bf16 v[128:131], v[220:223], v[104:107], v[128:131]\n"
        "v_cvt_pk_bf16_f32 v143, v126, v127\n"
        "v_xor_b32 v26, 64, v14\n"
        "v_mfma_f32_16x16x32_bf16 v[132:135], v[232:235], v[92:95], 0\n"
        "s_waitcnt lgkmcnt(0)\n"
        "ds_read_b64_tr_b16 v[200:201], v22 offset:0\n"
        "v_xor_b32 v27, 64, v15\n"
        "v_mfma_f32_16x16x32_bf16 v[132:135], v[236:239], v[96:99], v[132:135]\n"
        "ds_read_b64_tr_b16 v[202:203], v23 offset:0\n"
        "v_xor_b32 v28, 64, v16\n"
        "v_mfma_f32_16x16x32_bf16 v[132:135], v[240:243], v[100:103], v[132:135]\n"
        "ds_read_b64_tr_b16 v[204:205], v24 offset:0\n"
        "v_xor_b32 v29, 64, v17\n"
        "v_mfma_f32_16x16x32_bf16 v[132:135], v[244:247], v[104:107], v[132:135]\n"
        "ds_read_b64_tr_b16 v[206:207], v25 offset:0\n"
        "v_cvt_pk_bf16_f32 v164, v128, v129\n"
        "v_mfma_f32_16x16x32_bf16 v[248:251], v[92:95], v[136:139], 0\n"
        "ds_read_b64_tr_b16 v[216:217], v38 offset:0\n"
        "ds_read_b64_tr_b16 v[218:219], v39 offset:0\n"
        "v_mfma_f32_16x16x32_bf16 v[248:251], v[96:99], v[136:139], v[248:251]\n"
        "ds_read_b64_tr_b16 v[220:221], v40 offset:0\n"
        "v_cvt_pk_bf16_f32 v165, v130, v131\n"
        "v_mfma_f32_16x16x32_bf16 v[248:251], v[100:103], v[136:139], v[248:251]\n"
        "v_xor_b32 v41, 96, v10\n"
        "v_xor_b32 v42, 96, v11\n"
        "v_mfma_f32_16x16x32_bf16 v[248:251], v[104:107], v[136:139], v[248:251]\n"
        "s_waitcnt lgkmcnt(5)\n"
        "ds_read_b64_tr_b16 v[208:209], v26 offset:0\n"
        "ds_read_b64_tr_b16 v[210:211], v27 offset:0\n"
        "v_mfma_f32_16x16x32_bf16 v[188:191], v[200:203], v[52:55], 0\n"
        "s_waitcnt lgkmcnt(5)\n"
        "v_xor_b32 v43, 96, v12\n"
        "v_xor_b32 v30, 96, v13\n"
        "v_mfma_f32_16x16x32_bf16 v[188:191], v[204:207], v[56:59], v[188:191]\n"
        "s_waitcnt lgkmcnt(3)\n"
        "v_xor_b32 v35, 96, v18\n"
        "v_xor_b32 v36, 96, v19\n"
        "v_mfma_f32_16x16x32_bf16 v[188:191], v[216:219], v[60:63], v[188:191]\n"
        "s_waitcnt lgkmcnt(2)\n"
        "v_cvt_pk_bf16_f32 v166, v132, v133\n"
        "v_xor_b32 v37, 96, v20\n"
        "v_mfma_f32_16x16x32_bf16 v[188:191], v[220:223], v[64:67], v[188:191]\n"
        "ds_read_b64_tr_b16 v[212:213], v28 offset:0\n"
        "ds_read_b64_tr_b16 v[214:215], v29 offset:0\n"
        "v_cvt_pk_bf16_f32 v167, v134, v135\n"
        "v_mfma_f32_16x16x32_bf16 v[192:195], v[204:207], v[68:71], 0\n"
        "s_waitcnt lgkmcnt(2)\n"
        "v_xor_b32 v31, 96, v14\n"
        "v_xor_b32 v32, 96, v15\n"
        "v_mfma_f32_16x16x32_bf16 v[192:195], v[208:211], v[72:75], v[192:195]\n"
        "v_rcp_f32 v108, v108\n"
        "v_mfma_f32_16x16x32_bf16 v[192:195], v[216:219], v[76:79], v[192:195]\n"
        "ds_read_b64_tr_b16 v[224:225], v41 offset:0\n"
        "ds_read_b64_tr_b16 v[226:227], v42 offset:0\n"
        "v_mfma_f32_16x16x32_bf16 v[192:195], v[220:223], v[80:83], v[192:195]\n"
        "ds_read_b64_tr_b16 v[228:229], v43 offset:0\n"
        "ds_read_b64_tr_b16 v[230:231], v30 offset:0\n"
        "v_mfma_f32_16x16x32_bf16 v[196:199], v[208:211], v[92:95], 0\n"
        "s_waitcnt lgkmcnt(4)\n"
        "ds_read_b64_tr_b16 v[240:241], v35 offset:0\n"
        "ds_read_b64_tr_b16 v[242:243], v36 offset:0\n"
        "v_xor_b32 v33, 96, v16\n"
        "v_xor_b32 v34, 96, v17\n"
        "v_mfma_f32_16x16x32_bf16 v[196:199], v[212:215], v[96:99], v[196:199]\n"
        "ds_read_b64_tr_b16 v[244:245], v37 offset:0\n"
        "v_cvt_pk_bf16_f32 v120, v188, v189\n"
        "v_mfma_f32_16x16x32_bf16 v[196:199], v[216:219], v[100:103], v[196:199]\n"
        "v_rcp_f32 v109, v109\n"
        "v_mfma_f32_16x16x32_bf16 v[196:199], v[220:223], v[104:107], v[196:199]\n"
        "s_waitcnt lgkmcnt(5)\n"
        "ds_read_b64_tr_b16 v[232:233], v31 offset:0\n"
        "ds_read_b64_tr_b16 v[234:235], v32 offset:0\n"
        "v_cvt_pk_bf16_f32 v121, v190, v191\n"
        "v_mfma_f32_16x16x32_bf16 v[188:191], v[224:227], v[52:55], 0\n"
        "s_waitcnt lgkmcnt(5)\n"
        "v_rcp_f32 v110, v110\n"
        "v_mfma_f32_16x16x32_bf16 v[188:191], v[228:231], v[56:59], v[188:191]\n"
        "s_waitcnt lgkmcnt(3)\n"
        "v_rcp_f32 v111, v111\n"
        "v_mfma_f32_16x16x32_bf16 v[188:191], v[240:243], v[60:63], v[188:191]\n"
        "s_waitcnt lgkmcnt(2)\n"
        "v_cvt_pk_bf16_f32 v144, v192, v193\n"
        "v_cvt_pk_bf16_f32 v145, v194, v195\n"
        "v_mfma_f32_16x16x32_bf16 v[188:191], v[244:247], v[64:67], v[188:191]\n"
        "ds_read_b64_tr_b16 v[236:237], v33 offset:0\n"
        "ds_read_b64_tr_b16 v[238:239], v34 offset:0\n"
        "v_mfma_f32_16x16x32_bf16 v[192:195], v[228:231], v[68:71], 0\n"
        "s_waitcnt lgkmcnt(2)\n"
        "ds_write_b8 v3, v4 offset:32\n"
        "v_mfma_f32_16x16x32_bf16 v[192:195], v[232:235], v[72:75], v[192:195]\n"
        "ds_read_b64_tr_b16 v[200:201], v10 offset:25600\n"
        "v_cvt_pk_bf16_f32 v168, v196, v197\n"
        "v_mfma_f32_16x16x32_bf16 v[192:195], v[240:243], v[76:79], v[192:195]\n"
        "ds_read_b64_tr_b16 v[202:203], v11 offset:25600\n"
        "v_cvt_pk_bf16_f32 v169, v198, v199\n"
        "v_mfma_f32_16x16x32_bf16 v[192:195], v[244:247], v[80:83], v[192:195]\n"
        "ds_read_b64_tr_b16 v[204:205], v12 offset:25600\n"
        "ds_read_b64_tr_b16 v[206:207], v13 offset:25600\n"
        "v_mfma_f32_16x16x32_bf16 v[196:199], v[232:235], v[92:95], 0\n"
        "s_waitcnt lgkmcnt(5)\n"
        "ds_read_b64_tr_b16 v[216:217], v18 offset:25600\n"
        "ds_read_b64_tr_b16 v[218:219], v19 offset:25600\n"
        "v_mfma_f32_16x16x32_bf16 v[196:199], v[236:239], v[96:99], v[196:199]\n"
        "ds_read_b64_tr_b16 v[220:221], v20 offset:25600\n"
        "v_cvt_pk_bf16_f32 v122, v188, v189\n"
        "v_mfma_f32_16x16x32_bf16 v[196:199], v[240:243], v[100:103], v[196:199]\n"
        "ds_read_b64_tr_b16 v[208:209], v14 offset:25600\n"
        "v_cvt_pk_bf16_f32 v123, v190, v191\n"
        "v_mfma_f32_16x16x32_bf16 v[196:199], v[244:247], v[104:107], v[196:199]\n"
        "s_waitcnt lgkmcnt(6)\n"
        "ds_read_b64_tr_b16 v[210:211], v15 offset:25600\n"
        "v_cvt_pk_bf16_f32 v146, v192, v193\n"
        "v_mfma_f32_16x16x32_bf16 v[188:191], v[200:203], v[52:55], 0\n"
        "s_waitcnt lgkmcnt(5)\n"
        "v_rcp_f32 v112, v112\n"
        "v_mfma_f32_16x16x32_bf16 v[188:191], v[204:207], v[56:59], v[188:191]\n"
        "s_waitcnt lgkmcnt(3)\n"
        "v_rcp_f32 v113, v113\n"
        "v_mfma_f32_16x16x32_bf16 v[188:191], v[216:219], v[60:63], v[188:191]\n"
        "s_waitcnt lgkmcnt(2)\n"
        "v_rcp_f32 v114, v114\n"
        "v_mfma_f32_16x16x32_bf16 v[188:191], v[220:223], v[64:67], v[188:191]\n"
        "ds_read_b64_tr_b16 v[212:213], v16 offset:25600\n"
        "ds_read_b64_tr_b16 v[214:215], v17 offset:25600\n"
        "v_cvt_pk_bf16_f32 v147, v194, v195\n"
        "v_mfma_f32_16x16x32_bf16 v[192:195], v[204:207], v[68:71], 0\n"
        "s_waitcnt lgkmcnt(2)\n"
        "v_rcp_f32 v115, v115\n"
        "v_mfma_f32_16x16x32_bf16 v[192:195], v[208:211], v[72:75], v[192:195]\n"
        "ds_read_b64_tr_b16 v[224:225], v44 offset:25600\n"
        "v_cvt_pk_bf16_f32 v170, v196, v197\n"
        "v_mfma_f32_16x16x32_bf16 v[192:195], v[216:219], v[76:79], v[192:195]\n"
        "ds_read_b64_tr_b16 v[226:227], v45 offset:25600\n"
        "v_cvt_pk_bf16_f32 v171, v198, v199\n"
        "v_mfma_f32_16x16x32_bf16 v[192:195], v[220:223], v[80:83], v[192:195]\n"
        "ds_read_b64_tr_b16 v[228:229], v46 offset:25600\n"
        "ds_read_b64_tr_b16 v[230:231], v47 offset:25600\n"
        "v_mfma_f32_16x16x32_bf16 v[196:199], v[208:211], v[92:95], 0\n"
        "s_waitcnt lgkmcnt(4)\n"
        "ds_read_b64_tr_b16 v[240:241], v21 offset:25600\n"
        "ds_read_b64_tr_b16 v[242:243], v252 offset:25600\n"
        "v_mfma_f32_16x16x32_bf16 v[196:199], v[212:215], v[96:99], v[196:199]\n"
        "ds_read_b64_tr_b16 v[244:245], v253 offset:25600\n"
        "v_cvt_pk_bf16_f32 v124, v188, v189\n"
        "v_mfma_f32_16x16x32_bf16 v[196:199], v[216:219], v[100:103], v[196:199]\n"
        "ds_read_b64_tr_b16 v[232:233], v48 offset:25600\n"
        "v_cvt_pk_bf16_f32 v125, v190, v191\n"
        "v_mfma_f32_16x16x32_bf16 v[196:199], v[220:223], v[104:107], v[196:199]\n"
        "s_waitcnt lgkmcnt(6)\n"
        "ds_read_b64_tr_b16 v[234:235], v49 offset:25600\n"
        "v_cvt_pk_bf16_f32 v148, v192, v193\n"
        "v_mfma_f32_16x16x32_bf16 v[188:191], v[224:227], v[52:55], 0\n"
        "s_waitcnt lgkmcnt(5)\n"
        "v_rcp_f32 v248, v248\n"
        "v_mfma_f32_16x16x32_bf16 v[188:191], v[228:231], v[56:59], v[188:191]\n"
        "s_waitcnt lgkmcnt(3)\n"
        "v_rcp_f32 v249, v249\n"
        "v_mfma_f32_16x16x32_bf16 v[188:191], v[240:243], v[60:63], v[188:191]\n"
        "s_waitcnt lgkmcnt(2)\n"
        "v_rcp_f32 v250, v250\n"
        "v_mfma_f32_16x16x32_bf16 v[188:191], v[244:247], v[64:67], v[188:191]\n"
        "ds_read_b64_tr_b16 v[236:237], v50 offset:25600\n"
        "ds_read_b64_tr_b16 v[238:239], v51 offset:25600\n"
        "v_cvt_pk_bf16_f32 v149, v194, v195\n"
        "v_mfma_f32_16x16x32_bf16 v[192:195], v[228:231], v[68:71], 0\n"
        "s_waitcnt lgkmcnt(2)\n"
        "v_rcp_f32 v251, v251\n"
        "v_mfma_f32_16x16x32_bf16 v[192:195], v[232:235], v[72:75], v[192:195]\n"
        "ds_read_b64_tr_b16 v[200:201], v22 offset:25600\n"
        "v_cvt_pk_bf16_f32 v172, v196, v197\n"
        "v_mfma_f32_16x16x32_bf16 v[192:195], v[240:243], v[76:79], v[192:195]\n"
        "ds_read_b64_tr_b16 v[202:203], v23 offset:25600\n"
        "v_cvt_pk_bf16_f32 v173, v198, v199\n"
        "v_mfma_f32_16x16x32_bf16 v[192:195], v[244:247], v[80:83], v[192:195]\n"
        "ds_read_b64_tr_b16 v[204:205], v24 offset:25600\n"
        "ds_read_b64_tr_b16 v[206:207], v25 offset:25600\n"
        "v_mfma_f32_16x16x32_bf16 v[196:199], v[232:235], v[92:95], 0\n"
        "s_waitcnt lgkmcnt(4)\n"
        "ds_read_b64_tr_b16 v[216:217], v38 offset:25600\n"
        "ds_read_b64_tr_b16 v[218:219], v39 offset:25600\n"
        "v_mfma_f32_16x16x32_bf16 v[196:199], v[236:239], v[96:99], v[196:199]\n"
        "ds_read_b64_tr_b16 v[220:221], v40 offset:25600\n"
        "v_cvt_pk_bf16_f32 v126, v188, v189\n"
        "v_mfma_f32_16x16x32_bf16 v[196:199], v[240:243], v[100:103], v[196:199]\n"
        "ds_read_b64_tr_b16 v[208:209], v26 offset:25600\n"
        "v_cvt_pk_bf16_f32 v127, v190, v191\n"
        "v_mfma_f32_16x16x32_bf16 v[196:199], v[244:247], v[104:107], v[196:199]\n"
        "s_waitcnt lgkmcnt(6)\n"
        "ds_read_b64_tr_b16 v[210:211], v27 offset:25600\n"
        "v_cvt_pk_bf16_f32 v150, v192, v193\n"
        "v_mfma_f32_16x16x32_bf16 v[188:191], v[200:203], v[52:55], 0\n"
        "s_waitcnt lgkmcnt(5)\n"
        "ds_read_b64_tr_b16 v[212:213], v28 offset:25600\n"
        "v_cvt_pk_bf16_f32 v151, v194, v195\n"
        "v_mfma_f32_16x16x32_bf16 v[188:191], v[204:207], v[56:59], v[188:191]\n"
        "s_waitcnt lgkmcnt(4)\n"
        "ds_read_b64_tr_b16 v[214:215], v29 offset:25600\n"
        "v_cvt_pk_bf16_f32 v174, v196, v197\n"
        "v_mfma_f32_16x16x32_bf16 v[188:191], v[216:219], v[60:63], v[188:191]\n"
        "s_waitcnt lgkmcnt(4)\n"
        "ds_read_b64_tr_b16 v[224:225], v41 offset:25600\n"
        "v_cvt_pk_bf16_f32 v175, v198, v199\n"
        "v_mfma_f32_16x16x32_bf16 v[188:191], v[220:223], v[64:67], v[188:191]\n"
        "ds_read_b64_tr_b16 v[226:227], v42 offset:25600\n"
        "v_mfma_f32_16x16x32_bf16 v[192:195], v[204:207], v[68:71], 0\n"
        "s_waitcnt lgkmcnt(4)\n"
        "ds_read_b64_tr_b16 v[228:229], v43 offset:25600\n"
        "v_mfma_f32_16x16x32_bf16 v[192:195], v[208:211], v[72:75], v[192:195]\n"
        "ds_read_b64_tr_b16 v[230:231], v30 offset:25600\n"
        "v_mfma_f32_16x16x32_bf16 v[192:195], v[216:219], v[76:79], v[192:195]\n"
        "ds_read_b64_tr_b16 v[240:241], v35 offset:25600\n"
        "v_cvt_pk_bf16_f32 v128, v188, v189\n"
        "v_mfma_f32_16x16x32_bf16 v[192:195], v[220:223], v[80:83], v[192:195]\n"
        "ds_read_b64_tr_b16 v[242:243], v36 offset:25600\n"
        "v_cvt_pk_bf16_f32 v129, v190, v191\n"
        "v_mfma_f32_16x16x32_bf16 v[196:199], v[208:211], v[92:95], 0\n"
        "s_waitcnt lgkmcnt(6)\n"
        "ds_read_b64_tr_b16 v[244:245], v37 offset:25600\n"
        "v_mfma_f32_16x16x32_bf16 v[196:199], v[212:215], v[96:99], v[196:199]\n"
        "ds_read_b64_tr_b16 v[232:233], v31 offset:25600\n"
        "v_mfma_f32_16x16x32_bf16 v[196:199], v[216:219], v[100:103], v[196:199]\n"
        "ds_read_b64_tr_b16 v[234:235], v32 offset:25600\n"
        "v_cvt_pk_bf16_f32 v152, v192, v193\n"
        "v_mfma_f32_16x16x32_bf16 v[196:199], v[220:223], v[104:107], v[196:199]\n"
        "s_waitcnt lgkmcnt(7)\n"
        "ds_read_b64_tr_b16 v[236:237], v33 offset:25600\n"
        "v_cvt_pk_bf16_f32 v153, v194, v195\n"
        "v_mfma_f32_16x16x32_bf16 v[188:191], v[224:227], v[52:55], 0\n"
        "s_waitcnt lgkmcnt(6)\n"
        "ds_read_b64_tr_b16 v[238:239], v34 offset:25600\n"
        "v_mfma_f32_16x16x32_bf16 v[188:191], v[228:231], v[56:59], v[188:191]\n"
        "s_waitcnt lgkmcnt(5)\n"
        "ds_write_b8 v3, v4 offset:36\n"
        "v_mfma_f32_16x16x32_bf16 v[188:191], v[240:243], v[60:63], v[188:191]\n"
        "s_waitcnt lgkmcnt(5)\n"
        "ds_read_b64_tr_b16 v[200:201], v10 offset:51200\n"
        "v_cvt_pk_bf16_f32 v176, v196, v197\n"
        "v_mfma_f32_16x16x32_bf16 v[188:191], v[244:247], v[64:67], v[188:191]\n"
        "ds_read_b64_tr_b16 v[202:203], v11 offset:51200\n"
        "v_cvt_pk_bf16_f32 v177, v198, v199\n"
        "v_mfma_f32_16x16x32_bf16 v[192:195], v[228:231], v[68:71], 0\n"
        "s_waitcnt lgkmcnt(5)\n"
        "ds_read_b64_tr_b16 v[204:205], v12 offset:51200\n"
        "v_mfma_f32_16x16x32_bf16 v[192:195], v[232:235], v[72:75], v[192:195]\n"
        "ds_read_b64_tr_b16 v[206:207], v13 offset:51200\n"
        "v_mfma_f32_16x16x32_bf16 v[192:195], v[240:243], v[76:79], v[192:195]\n"
        "ds_read_b64_tr_b16 v[216:217], v18 offset:51200\n"
        "v_cvt_pk_bf16_f32 v130, v188, v189\n"
        "v_mfma_f32_16x16x32_bf16 v[192:195], v[244:247], v[80:83], v[192:195]\n"
        "ds_read_b64_tr_b16 v[218:219], v19 offset:51200\n"
        "v_cvt_pk_bf16_f32 v131, v190, v191\n"
        "v_mfma_f32_16x16x32_bf16 v[196:199], v[232:235], v[92:95], 0\n"
        "s_waitcnt lgkmcnt(7)\n"
        "ds_read_b64_tr_b16 v[220:221], v20 offset:51200\n"
        "v_mfma_f32_16x16x32_bf16 v[196:199], v[236:239], v[96:99], v[196:199]\n"
        "ds_read_b64_tr_b16 v[208:209], v14 offset:51200\n"
        "v_mfma_f32_16x16x32_bf16 v[196:199], v[240:243], v[100:103], v[196:199]\n"
        "ds_read_b64_tr_b16 v[210:211], v15 offset:51200\n"
        "v_cvt_pk_bf16_f32 v154, v192, v193\n"
        "v_mfma_f32_16x16x32_bf16 v[196:199], v[244:247], v[104:107], v[196:199]\n"
        "s_waitcnt lgkmcnt(7)\n"
        "ds_read_b64_tr_b16 v[212:213], v16 offset:51200\n"
        "v_cvt_pk_bf16_f32 v155, v194, v195\n"
        "v_mfma_f32_16x16x32_bf16 v[188:191], v[200:203], v[52:55], 0\n"
        "s_waitcnt lgkmcnt(6)\n"
        "ds_read_b64_tr_b16 v[214:215], v17 offset:51200\n"
        "v_mfma_f32_16x16x32_bf16 v[188:191], v[204:207], v[56:59], v[188:191]\n"
        "s_waitcnt lgkmcnt(5)\n"
        "ds_read_b64_tr_b16 v[224:225], v44 offset:51200\n"
        "v_cvt_pk_bf16_f32 v178, v196, v197\n"
        "v_mfma_f32_16x16x32_bf16 v[188:191], v[216:219], v[60:63], v[188:191]\n"
        "s_waitcnt lgkmcnt(5)\n"
        "ds_read_b64_tr_b16 v[226:227], v45 offset:51200\n"
        "v_cvt_pk_bf16_f32 v179, v198, v199\n"
        "v_mfma_f32_16x16x32_bf16 v[188:191], v[220:223], v[64:67], v[188:191]\n"
        "ds_read_b64_tr_b16 v[228:229], v46 offset:51200\n"
        "v_mfma_f32_16x16x32_bf16 v[192:195], v[204:207], v[68:71], 0\n"
        "s_waitcnt lgkmcnt(5)\n"
        "ds_read_b64_tr_b16 v[230:231], v47 offset:51200\n"
        "v_mfma_f32_16x16x32_bf16 v[192:195], v[208:211], v[72:75], v[192:195]\n"
        "ds_read_b64_tr_b16 v[240:241], v21 offset:51200\n"
        "v_mfma_f32_16x16x32_bf16 v[192:195], v[216:219], v[76:79], v[192:195]\n"
        "ds_read_b64_tr_b16 v[242:243], v252 offset:51200\n"
        "v_cvt_pk_bf16_f32 v132, v188, v189\n"
        "v_mfma_f32_16x16x32_bf16 v[192:195], v[220:223], v[80:83], v[192:195]\n"
        "ds_read_b64_tr_b16 v[244:245], v253 offset:51200\n"
        "v_cvt_pk_bf16_f32 v133, v190, v191\n"
        "v_mfma_f32_16x16x32_bf16 v[196:199], v[208:211], v[92:95], 0\n"
        "s_waitcnt lgkmcnt(7)\n"
        "ds_read_b32 v5, v2 offset:44\n"
        "v_mfma_f32_16x16x32_bf16 v[196:199], v[212:215], v[96:99], v[196:199]\n"
        "ds_read_b64_tr_b16 v[232:233], v48 offset:51200\n"
        "v_mfma_f32_16x16x32_bf16 v[196:199], v[216:219], v[100:103], v[196:199]\n"
        "ds_read_b64_tr_b16 v[234:235], v49 offset:51200\n"
        "v_cvt_pk_bf16_f32 v156, v192, v193\n"
        "v_mfma_f32_16x16x32_bf16 v[196:199], v[220:223], v[104:107], v[196:199]\n"
        "s_waitcnt lgkmcnt(8)\n"
        "ds_read_b64_tr_b16 v[236:237], v50 offset:51200\n"
        "v_cvt_pk_bf16_f32 v157, v194, v195\n"
        "v_mfma_f32_16x16x32_bf16 v[188:191], v[224:227], v[52:55], 0\n"
        "s_waitcnt lgkmcnt(3)\n"
        "ds_read_b64_tr_b16 v[238:239], v51 offset:51200\n"
        "v_readfirstlane_b32 s86, v5\n"
        "v_mfma_f32_16x16x32_bf16 v[188:191], v[228:231], v[56:59], v[188:191]\n"
        "ds_read_b64_tr_b16 v[200:201], v22 offset:51200\n"
        "v_cvt_pk_bf16_f32 v180, v196, v197\n"
        "v_mfma_f32_16x16x32_bf16 v[188:191], v[240:243], v[60:63], v[188:191]\n"
        "ds_read_b64_tr_b16 v[202:203], v23 offset:51200\n"
        "v_cvt_pk_bf16_f32 v181, v198, v199\n"
        "v_mfma_f32_16x16x32_bf16 v[188:191], v[244:247], v[64:67], v[188:191]\n"
        "s_cmp_eq_u32 s86, 0x1010100\n"
        "ds_read_b64_tr_b16 v[204:205], v24 offset:51200\n"
        "v_mfma_f32_16x16x32_bf16 v[192:195], v[228:231], v[68:71], 0\n"
        "s_waitcnt lgkmcnt(5)\n"
        "ds_read_b64_tr_b16 v[206:207], v25 offset:51200\n"
        "v_mfma_f32_16x16x32_bf16 v[192:195], v[232:235], v[72:75], v[192:195]\n"
        "s_cbranch_scc0 Lsl19_%=\n"
        "Lrt20_%=:\n"
        "v_mfma_f32_16x16x32_bf16 v[192:195], v[240:243], v[76:79], v[192:195]\n"
        "ds_read_b64_tr_b16 v[216:217], v38 offset:51200\n"
        "v_cvt_pk_bf16_f32 v134, v188, v189\n"
        "v_mfma_f32_16x16x32_bf16 v[192:195], v[244:247], v[80:83], v[192:195]\n"
        "ds_read_b64_tr_b16 v[218:219], v39 offset:51200\n"
        "v_cvt_pk_bf16_f32 v135, v190, v191\n"
        "v_mfma_f32_16x16x32_bf16 v[196:199], v[232:235], v[92:95], 0\n"
        "s_waitcnt lgkmcnt(6)\n"
        "ds_read_b64_tr_b16 v[220:221], v40 offset:51200\n"
        "v_mfma_f32_16x16x32_bf16 v[196:199], v[236:239], v[96:99], v[196:199]\n"
        "ds_read_b64_tr_b16 v[224:225], v41 offset:51200\n"
        "v_mfma_f32_16x16x32_bf16 v[196:199], v[240:243], v[100:103], v[196:199]\n"
        "ds_read_b64_tr_b16 v[226:227], v42 offset:51200\n"
        "v_cvt_pk_bf16_f32 v158, v192, v193\n"
        "v_mfma_f32_16x16x32_bf16 v[196:199], v[244:247], v[104:107], v[196:199]\n"
        "s_waitcnt lgkmcnt(7)\n"
        "ds_read_b64_tr_b16 v[228:229], v43 offset:51200\n"
        "ds_read_b64_tr_b16 v[230:231], v30 offset:51200\n"
        "v_mfma_f32_16x16x32_bf16 v[188:191], v[200:203], v[52:55], 0\n"
        "s_waitcnt lgkmcnt(7)\n"
        "ds_read_b64_tr_b16 v[84:85], v35 offset:51200\n"
        "ds_read_b64_tr_b16 v[86:87], v36 offset:51200\n"
        "v_mfma_f32_16x16x32_bf16 v[188:191], v[204:207], v[56:59], v[188:191]\n"
        "s_waitcnt lgkmcnt(7)\n"
        "ds_read_b64_tr_b16 v[88:89], v37 offset:51200\n"
        "v_cvt_pk_bf16_f32 v159, v194, v195\n"
        "v_mfma_f32_16x16x32_bf16 v[188:191], v[216:219], v[60:63], v[188:191]\n"
        "s_waitcnt lgkmcnt(7)\n"
        "ds_read_b128 v[44:47], v1 offset:0\n"
        "v_cvt_pk_bf16_f32 v182, v196, v197\n"
        "v_mfma_f32_16x16x32_bf16 v[188:191], v[220:223], v[64:67], v[188:191]\n"
        "s_waitcnt lgkmcnt(6)\n"
        "ds_read_b128 v[48:51], v1 offset:1024\n"
        "v_cvt_pk_bf16_f32 v183, v198, v199\n"
        "v_mfma_f32_16x16x32_bf16 v[192:195], v[224:227], v[52:55], 0\n"
        "s_waitcnt lgkmcnt(5)\n"
        "ds_read_b128 v[240:243], v1 offset:2048\n"
        "v_mfma_f32_16x16x32_bf16 v[192:195], v[228:231], v[56:59], v[192:195]\n"
        "s_waitcnt lgkmcnt(4)\n"
        "ds_read_b128 v[244:247], v1 offset:3072\n"
        "v_cvt_pk_bf16_f32 v136, v188, v189\n"
        "v_mfma_f32_16x16x32_bf16 v[192:195], v[84:87], v[60:63], v[192:195]\n"
        "s_waitcnt lgkmcnt(4)\n"
        "ds_read_b128 v[252:255], v1 offset:4096\n"
        "v_cvt_pk_bf16_f32 v137, v190, v191\n"
        "v_mfma_f32_16x16x32_bf16 v[192:195], v[88:91], v[64:67], v[192:195]\n"
        "s_waitcnt lgkmcnt(4)\n"
        "ds_read_b128 v[200:203], v1 offset:5120\n"
        "v_mfma_f32_16x16x32_bf16 v[12:15], v[116:119], v[44:47], 0\n"
        "s_waitcnt lgkmcnt(4)\n"
        "ds_read_b128 v[224:227], v1 offset:6144\n"
        "v_mfma_f32_16x16x32_bf16 v[12:15], v[120:123], v[48:51], v[12:15]\n"
        "s_waitcnt lgkmcnt(4)\n"
        "ds_read_b128 v[52:55], v1 offset:7168\n"
        "v_mfma_f32_16x16x32_bf16 v[12:15], v[124:127], v[240:243], v[12:15]\n"
        "s_waitcnt lgkmcnt(4)\n"
        "ds_read_b128 v[56:59], v1 offset:8192\n"
        "v_cvt_pk_bf16_f32 v138, v192, v193\n"
        "v_cvt_pk_bf16_f32 v139, v194, v195\n"
        "v_mfma_f32_16x16x32_bf16 v[12:15], v[128:131], v[244:247], v[12:15]\n"
        "s_waitcnt lgkmcnt(4)\n"
        "ds_read_b128 v[44:47], v1 offset:9216\n"
        "v_mfma_f32_16x16x32_bf16 v[12:15], v[132:135], v[252:255], v[12:15]\n"
        "s_waitcnt lgkmcnt(4)\n"
        "ds_read_b128 v[48:51], v1 offset:10240\n"
        "v_mfma_f32_16x16x32_bf16 v[12:15], v[136:139], v[200:203], v[12:15]\n"
        "s_waitcnt lgkmcnt(4)\n"
        "ds_read_b128 v[240:243], v1 offset:11264\n"
        "v_mfma_f32_16x16x32_bf16 v[16:19], v[116:119], v[224:227], 0\n"
        "s_waitcnt lgkmcnt(4)\n"
        "ds_read_b128 v[244:247], v1 offset:12288\n"
        "v_mfma_f32_16x16x32_bf16 v[16:19], v[120:123], v[52:55], v[16:19]\n"
        "s_waitcnt lgkmcnt(4)\n"
        "ds_read_b128 v[252:255], v1 offset:13312\n"
        "v_mfma_f32_16x16x32_bf16 v[16:19], v[124:127], v[56:59], v[16:19]\n"
        "s_waitcnt lgkmcnt(4)\n"
        "ds_read_b128 v[200:203], v1 offset:14336\n"
        "v_mul_f32 v12, v12, v108\n"
        "v_mfma_f32_16x16x32_bf16 v[16:19], v[128:131], v[44:47], v[16:19]\n"
        "s_waitcnt lgkmcnt(4)\n"
        "ds_read_b128 v[60:63], v1 offset:15360\n"
        "v_mul_f32 v13, v13, v109\n"
        "v_mfma_f32_16x16x32_bf16 v[16:19], v[132:135], v[48:51], v[16:19]\n"
        "s_waitcnt lgkmcnt(4)\n"
        "ds_read_b128 v[64:67], v1 offset:16384\n"
        "v_mul_f32 v14, v14, v110\n"
        "v_mfma_f32_16x16x32_bf16 v[16:19], v[136:139], v[240:243], v[16:19]\n"
        "s_waitcnt lgkmcnt(4)\n"
        "ds_read_b128 v[224:227], v1 offset:17408\n"
        "v_mul_f32 v15, v15, v111\n"
        "v_mfma_f32_16x16x32_bf16 v[20:23], v[116:119], v[244:247], 0\n"
        "s_waitcnt lgkmcnt(4)\n"
        "ds_read_b128 v[44:47], v1 offset:18432\n"
        "v_mfma_f32_16x16x32_bf16 v[20:23], v[120:123], v[252:255], v[20:23]\n"
        "s_waitcnt lgkmcnt(4)\n"
        "ds_read_b128 v[48:51], v1 offset:19456\n"
        "v_mfma_f32_16x16x32_bf16 v[20:23], v[124:127], v[200:203], v[20:23]\n"
        "s_waitcnt lgkmcnt(4)\n"
        "ds_read_b128 v[52:55], v1 offset:20480\n"
        "v_mul_f32 v16, v16, v108\n"
        "v_mfma_f32_16x16x32_bf16 v[20:23], v[128:131], v[60:63], v[20:23]\n"
        "s_waitcnt lgkmcnt(4)\n"
        "ds_read_b128 v[56:59], v1 offset:21504\n"
        "v_cvt_pk_bf16_f32 v36, v12, v16\n"
        "v_mfma_f32_16x16x32_bf16 v[20:23], v[132:135], v[64:67], v[20:23]\n"
        "s_waitcnt lgkmcnt(4)\n"
        "ds_read_b64_tr_b16 v[208:209], v26 offset:51200\n"
        "ds_read_b64_tr_b16 v[210:211], v27 offset:51200\n"
        "ds_read_b128 v[60:63], v1 offset:22528\n"
        "v_mfma_f32_16x16x32_bf16 v[20:23], v[136:139], v[224:227], v[20:23]\n"
        "s_waitcnt lgkmcnt(6)\n"
        "ds_read_b128 v[64:67], v1 offset:23552\n"
        "v_mul_f32 v17, v17, v109\n"
        "v_mfma_f32_16x16x32_bf16 v[24:27], v[116:119], v[44:47], 0\n"
        "s_waitcnt lgkmcnt(6)\n"
        "ds_read_b64_tr_b16 v[212:213], v28 offset:51200\n"
        "v_cvt_pk_bf16_f32 v38, v13, v17\n"
        "v_mfma_f32_16x16x32_bf16 v[24:27], v[120:123], v[48:51], v[24:27]\n"
        "s_waitcnt lgkmcnt(6)\n"
        "ds_read_b64_tr_b16 v[214:215], v29 offset:51200\n"
        "v_mul_f32 v18, v18, v110\n"
        "v_mfma_f32_16x16x32_bf16 v[24:27], v[124:127], v[52:55], v[24:27]\n"
        "s_waitcnt lgkmcnt(6)\n"
        "ds_read_b64_tr_b16 v[232:233], v31 offset:51200\n"
        "v_cvt_pk_bf16_f32 v40, v14, v18\n"
        "v_mfma_f32_16x16x32_bf16 v[24:27], v[128:131], v[56:59], v[24:27]\n"
        "s_waitcnt lgkmcnt(4)\n"
        "ds_read_b64_tr_b16 v[234:235], v32 offset:51200\n"
        "v_mul_f32 v19, v19, v111\n"
        "v_mfma_f32_16x16x32_bf16 v[24:27], v[132:135], v[60:63], v[24:27]\n"
        "s_waitcnt lgkmcnt(4)\n"
        "ds_read_b64_tr_b16 v[236:237], v33 offset:51200\n"
        "v_cvt_pk_bf16_f32 v42, v15, v19\n"
        "v_mfma_f32_16x16x32_bf16 v[24:27], v[136:139], v[64:67], v[24:27]\n"
        "ds_read_b64_tr_b16 v[238:239], v34 offset:51200\n"
        "v_mul_f32 v20, v20, v108\n"
        "v_mfma_f32_16x16x32_bf16 v[196:199], v[204:207], v[68:71], 0\n"
        "ds_write_b8 v3, v4 offset:40\n"
        "v_mfma_f32_16x16x32_bf16 v[196:199], v[208:211], v[72:75], v[196:199]\n"
        "v_mul_f32 v21, v21, v109\n"
        "v_mul_f32 v22, v22, v110\n"
        "v_mfma_f32_16x16x32_bf16 v[196:199], v[216:219], v[76:79], v[196:199]\n"
        "v_mul_f32 v23, v23, v111\n"
        "v_mul_f32 v24, v24, v108\n"
        "v_mfma_f32_16x16x32_bf16 v[196:199], v[220:223], v[80:83], v[196:199]\n"
        "v_mul_f32 v25, v25, v109\n"
        "v_cvt_pk_bf16_f32 v37, v20, v24\n"
        "v_mfma_f32_16x16x32_bf16 v[188:191], v[208:211], v[92:95], 0\n"
        "s_waitcnt lgkmcnt(5)\n"
        "buffer_store_dwordx2 v[36:37], v6, s[28:31], s80 offen offset:0 sc1\n"
        "v_cvt_pk_bf16_f32 v39, v21, v25\n"
        "v_mfma_f32_16x16x32_bf16 v[188:191], v[212:215], v[96:99], v[188:191]\n"
        "buffer_store_dwordx2 v[38:39], v6, s[28:31], s81 offen offset:0 sc1\n"
        "v_mul_f32 v26, v26, v110\n"
        "v_mfma_f32_16x16x32_bf16 v[188:191], v[216:219], v[100:103], v[188:191]\n"
        "v_cvt_pk_bf16_f32 v160, v196, v197\n"
        "v_cvt_pk_bf16_f32 v161, v198, v199\n"
        "v_mfma_f32_16x16x32_bf16 v[188:191], v[220:223], v[104:107], v[188:191]\n"
        "ds_read_b128 v[200:203], v1 offset:0\n"
        "v_cvt_pk_bf16_f32 v41, v22, v26\n"
        "v_mfma_f32_16x16x32_bf16 v[192:195], v[228:231], v[68:71], 0\n"
        "s_waitcnt lgkmcnt(4)\n"
        "buffer_store_dwordx2 v[40:41], v7, s[28:31], s80 offen offset:0 sc1\n"
        "ds_read_b128 v[204:207], v1 offset:1024\n"
        "v_mfma_f32_16x16x32_bf16 v[192:195], v[232:235], v[72:75], v[192:195]\n"
        "ds_read_b128 v[208:211], v1 offset:2048\n"
        "v_mul_f32 v27, v27, v111\n"
        "v_mfma_f32_16x16x32_bf16 v[192:195], v[84:87], v[76:79], v[192:195]\n"
        "v_cvt_pk_bf16_f32 v184, v188, v189\n"
        "v_cvt_pk_bf16_f32 v185, v190, v191\n"
        "v_mfma_f32_16x16x32_bf16 v[192:195], v[88:91], v[80:83], v[192:195]\n"
        "ds_read_b128 v[212:215], v1 offset:3072\n"
        "v_cvt_pk_bf16_f32 v43, v23, v27\n"
        "v_mfma_f32_16x16x32_bf16 v[196:199], v[232:235], v[92:95], 0\n"
        "s_waitcnt lgkmcnt(5)\n"
        "buffer_store_dwordx2 v[42:43], v7, s[28:31], s81 offen offset:0 sc1\n"
        "ds_read_b128 v[216:219], v1 offset:4096\n"
        "v_mfma_f32_16x16x32_bf16 v[196:199], v[236:239], v[96:99], v[196:199]\n"
        "ds_read_b128 v[220:223], v1 offset:5120\n"
        "v_mfma_f32_16x16x32_bf16 v[196:199], v[84:87], v[100:103], v[196:199]\n"
        "v_cvt_pk_bf16_f32 v162, v192, v193\n"
        "v_cvt_pk_bf16_f32 v163, v194, v195\n"
        "v_mfma_f32_16x16x32_bf16 v[196:199], v[88:91], v[104:107], v[196:199]\n"
        "ds_read_b128 v[68:71], v1 offset:6144\n"
        "v_mfma_f32_16x16x32_bf16 v[104:107], v[140:143], v[44:47], 0\n"
        "ds_read_b128 v[72:75], v1 offset:7168\n"
        "v_mfma_f32_16x16x32_bf16 v[104:107], v[144:147], v[48:51], v[104:107]\n"
        "ds_read_b128 v[76:79], v1 offset:8192\n"
        "v_mfma_f32_16x16x32_bf16 v[104:107], v[148:151], v[52:55], v[104:107]\n"
        "ds_read_b128 v[80:83], v1 offset:9216\n"
        "v_mfma_f32_16x16x32_bf16 v[104:107], v[152:155], v[56:59], v[104:107]\n"
        "v_cvt_pk_bf16_f32 v186, v196, v197\n"
        "v_cvt_pk_bf16_f32 v187, v198, v199\n"
        "v_mfma_f32_16x16x32_bf16 v[104:107], v[156:159], v[60:63], v[104:107]\n"
        "ds_read_b128 v[84:87], v1 offset:10240\n"
        "v_mfma_f32_16x16x32_bf16 v[104:107], v[160:163], v[64:67], v[104:107]\n"
        "s_waitcnt lgkmcnt(10)\n"
        "ds_read_b128 v[88:91], v1 offset:11264\n"
        "v_mfma_f32_16x16x32_bf16 v[92:95], v[140:143], v[200:203], 0\n"
        "s_waitcnt lgkmcnt(10)\n"
        "ds_read_b128 v[224:227], v1 offset:12288\n"
        "v_mfma_f32_16x16x32_bf16 v[92:95], v[144:147], v[204:207], v[92:95]\n"
        "s_waitcnt lgkmcnt(10)\n"
        "ds_read_b128 v[228:231], v1 offset:13312\n"
        "v_mfma_f32_16x16x32_bf16 v[92:95], v[148:151], v[208:211], v[92:95]\n"
        "s_waitcnt lgkmcnt(10)\n"
        "ds_read_b128 v[232:235], v1 offset:14336\n"
        "v_mul_f32 v104, v104, v112\n"
        "v_mfma_f32_16x16x32_bf16 v[92:95], v[152:155], v[212:215], v[92:95]\n"
        "s_waitcnt lgkmcnt(10)\n"
        "ds_read_b128 v[236:239], v1 offset:15360\n"
        "v_mul_f32 v105, v105, v113\n"
        "v_mfma_f32_16x16x32_bf16 v[92:95], v[156:159], v[216:219], v[92:95]\n"
        "s_waitcnt lgkmcnt(10)\n"
        "ds_read_b128 v[240:243], v1 offset:16384\n"
        "v_mul_f32 v106, v106, v114\n"
        "v_mfma_f32_16x16x32_bf16 v[92:95], v[160:163], v[220:223], v[92:95]\n"
        "s_waitcnt lgkmcnt(10)\n"
        "ds_read_b32 v5, v2 offset:48\n"
        "v_mul_f32 v107, v107, v115\n"
        "v_mfma_f32_16x16x32_bf16 v[96:99], v[140:143], v[68:71], 0\n"
        "s_waitcnt lgkmcnt(10)\n"
        "ds_read_b128 v[244:247], v1 offset:17408\n"
        "v_mfma_f32_16x16x32_bf16 v[96:99], v[144:147], v[72:75], v[96:99]\n"
        "s_waitcnt lgkmcnt(10)\n"
        "v_mfma_f32_16x16x32_bf16 v[96:99], v[148:151], v[76:79], v[96:99]\n"
        "s_waitcnt lgkmcnt(9)\n"
        "v_mul_f32 v92, v92, v112\n"
        "v_mul_f32 v93, v93, v113\n"
        "v_mfma_f32_16x16x32_bf16 v[96:99], v[152:155], v[80:83], v[96:99]\n"
        "s_waitcnt lgkmcnt(8)\n"
        "v_mul_f32 v94, v94, v114\n"
        "v_mul_f32 v95, v95, v115\n"
        "v_mfma_f32_16x16x32_bf16 v[96:99], v[156:159], v[84:87], v[96:99]\n"
        "s_waitcnt lgkmcnt(7)\n"
        "v_mfma_f32_16x16x32_bf16 v[96:99], v[160:163], v[88:91], v[96:99]\n"
        "s_waitcnt lgkmcnt(1)\n"
        "v_readfirstlane_b32 s86, v5\n"
        "v_mfma_f32_16x16x32_bf16 v[100:103], v[140:143], v[224:227], 0\n"
        "v_mfma_f32_16x16x32_bf16 v[100:103], v[144:147], v[228:231], v[100:103]\n"
        "v_mfma_f32_16x16x32_bf16 v[100:103], v[148:151], v[232:235], v[100:103]\n"
        "s_cmp_eq_u32 s86, 0x1010100\n"
        "v_mfma_f32_16x16x32_bf16 v[100:103], v[152:155], v[236:239], v[100:103]\n"
        "s_cbranch_scc0 Lsl22_%=\n"
        "Lrt23_%=:\n"
        "v_mfma_f32_16x16x32_bf16 v[100:103], v[156:159], v[240:243], v[100:103]\n"
        "s_waitcnt lgkmcnt(0)\n"
        "v_mul_f32 v96, v96, v112\n"
        "v_cvt_pk_bf16_f32 v28, v92, v96\n"
        "v_mfma_f32_16x16x32_bf16 v[100:103], v[160:163], v[244:247], v[100:103]\n"
        "v_mul_f32 v97, v97, v113\n"
        "v_cvt_pk_bf16_f32 v30, v93, v97\n"
        "v_mfma_f32_16x16x32_bf16 v[24:27], v[164:167], v[44:47], 0\n"
        "v_mul_f32 v98, v98, v114\n"
        "v_cvt_pk_bf16_f32 v32, v94, v98\n"
        "v_mfma_f32_16x16x32_bf16 v[24:27], v[168:171], v[48:51], v[24:27]\n"
        "v_mul_f32 v99, v99, v115\n"
        "v_cvt_pk_bf16_f32 v34, v95, v99\n"
        "v_mfma_f32_16x16x32_bf16 v[24:27], v[172:175], v[52:55], v[24:27]\n"
        "v_mul_f32 v100, v100, v112\n"
        "v_mul_f32 v101, v101, v113\n"
        "v_mfma_f32_16x16x32_bf16 v[24:27], v[176:179], v[56:59], v[24:27]\n"
        "v_cvt_pk_bf16_f32 v29, v100, v104\n"
        "v_cvt_pk_bf16_f32 v31, v101, v105\n"
        "v_mfma_f32_16x16x32_bf16 v[24:27], v[180:183], v[60:63], v[24:27]\n"
        "buffer_store_dwordx2 v[28:29], v6, s[28:31], s82 offen offset:0 sc1\n"
        "ds_read_b128 v[44:47], v1 offset:44032\n"
        "v_mfma_f32_16x16x32_bf16 v[24:27], v[184:187], v[64:67], v[24:27]\n"
        "buffer_store_dwordx2 v[30:31], v6, s[28:31], s83 offen offset:0 sc1\n"
        "v_mul_f32 v102, v102, v114\n"
        "v_mfma_f32_16x16x32_bf16 v[12:15], v[164:167], v[200:203], 0\n"
        "ds_read_b128 v[48:51], v1 offset:45056\n"
        "v_cvt_pk_bf16_f32 v33, v102, v106\n"
        "v_mfma_f32_16x16x32_bf16 v[12:15], v[168:171], v[204:207], v[12:15]\n"
        "buffer_store_dwordx2 v[32:33], v7, s[28:31], s82 offen offset:0 sc1\n"
        "v_mul_f32 v103, v103, v115\n"
        "v_mfma_f32_16x16x32_bf16 v[12:15], v[172:175], v[208:211], v[12:15]\n"
        "ds_read_b128 v[52:55], v1 offset:46080\n"
        "v_cvt_pk_bf16_f32 v35, v103, v107\n"
        "v_mfma_f32_16x16x32_bf16 v[12:15], v[176:179], v[212:215], v[12:15]\n"
        "buffer_store_dwordx2 v[34:35], v7, s[28:31], s83 offen offset:0 sc1\n"
        "ds_read_b128 v[56:59], v1 offset:47104\n"
        "v_mfma_f32_16x16x32_bf16 v[12:15], v[180:183], v[216:219], v[12:15]\n"
        "ds_read_b128 v[60:63], v1 offset:48128\n"
        "v_mul_f32 v24, v24, v248\n"
        "v_mfma_f32_16x16x32_bf16 v[12:15], v[184:187], v[220:223], v[12:15]\n"
        "ds_read_b128 v[64:67], v1 offset:49152\n"
        "v_mul_f32 v25, v25, v249\n"
        "v_mfma_f32_16x16x32_bf16 v[16:19], v[164:167], v[68:71], 0\n"
        "ds_read_b128 v[200:203], v1 offset:25600\n"
        "v_mul_f32 v26, v26, v250\n"
        "v_mfma_f32_16x16x32_bf16 v[16:19], v[168:171], v[72:75], v[16:19]\n"
        "ds_read_b128 v[204:207], v1 offset:26624\n"
        "v_mul_f32 v27, v27, v251\n"
        "v_mfma_f32_16x16x32_bf16 v[16:19], v[172:175], v[76:79], v[16:19]\n"
        "ds_read_b128 v[208:211], v1 offset:27648\n"
        "v_mul_f32 v12, v12, v248\n"
        "v_mfma_f32_16x16x32_bf16 v[16:19], v[176:179], v[80:83], v[16:19]\n"
        "ds_read_b128 v[212:215], v1 offset:28672\n"
        "v_mul_f32 v13, v13, v249\n"
        "v_mfma_f32_16x16x32_bf16 v[16:19], v[180:183], v[84:87], v[16:19]\n"
        "ds_read_b128 v[216:219], v1 offset:29696\n"
        "v_mul_f32 v14, v14, v250\n"
        "v_mfma_f32_16x16x32_bf16 v[16:19], v[184:187], v[88:91], v[16:19]\n"
        "ds_read_b128 v[220:223], v1 offset:30720\n"
        "v_mul_f32 v15, v15, v251\n"
        "v_mfma_f32_16x16x32_bf16 v[20:23], v[164:167], v[224:227], 0\n"
        "ds_read_b128 v[68:71], v1 offset:31744\n"
        "v_mfma_f32_16x16x32_bf16 v[20:23], v[168:171], v[228:231], v[20:23]\n"
        "ds_read_b128 v[72:75], v1 offset:32768\n"
        "v_mfma_f32_16x16x32_bf16 v[20:23], v[172:175], v[232:235], v[20:23]\n"
        "ds_read_b128 v[76:79], v1 offset:33792\n"
        "v_mul_f32 v16, v16, v248\n"
        "v_mfma_f32_16x16x32_bf16 v[20:23], v[176:179], v[236:239], v[20:23]\n"
        "s_waitcnt lgkmcnt(14)\n"
        "ds_read_b128 v[80:83], v1 offset:34816\n"
        "v_cvt_pk_bf16_f32 v36, v12, v16\n"
        "v_mfma_f32_16x16x32_bf16 v[20:23], v[180:183], v[240:243], v[20:23]\n"
        "s_waitcnt lgkmcnt(14)\n"
        "ds_read_b128 v[84:87], v1 offset:35840\n"
        "v_mul_f32 v17, v17, v249\n"
        "v_mfma_f32_16x16x32_bf16 v[20:23], v[184:187], v[244:247], v[20:23]\n"
        "s_waitcnt lgkmcnt(14)\n"
        "ds_read_b128 v[88:91], v1 offset:36864\n"
        "v_cvt_pk_bf16_f32 v38, v13, v17\n"
        "v_mfma_f32_16x16x32_bf16 v[104:107], v[116:119], v[44:47], 0\n"
        "s_waitcnt lgkmcnt(14)\n"
        "ds_read_b128 v[224:227], v1 offset:37888\n"
        "v_mul_f32 v18, v18, v250\n"
        "v_mfma_f32_16x16x32_bf16 v[104:107], v[120:123], v[48:51], v[104:107]\n"
        "s_waitcnt lgkmcnt(14)\n"
        "ds_read_b128 v[228:231], v1 offset:38912\n"
        "v_cvt_pk_bf16_f32 v40, v14, v18\n"
        "v_mfma_f32_16x16x32_bf16 v[104:107], v[124:127], v[52:55], v[104:107]\n"
        "v_mul_f32 v19, v19, v251\n"
        "v_cvt_pk_bf16_f32 v42, v15, v19\n"
        "v_mfma_f32_16x16x32_bf16 v[104:107], v[128:131], v[56:59], v[104:107]\n"
        "s_waitcnt lgkmcnt(14)\n"
        "ds_read_b128 v[232:235], v1 offset:39936\n"
        "v_mul_f32 v20, v20, v248\n"
        "v_mfma_f32_16x16x32_bf16 v[104:107], v[132:135], v[60:63], v[104:107]\n"
        "s_waitcnt lgkmcnt(14)\n"
        "ds_read_b128 v[236:239], v1 offset:40960\n"
        "v_cvt_pk_bf16_f32 v37, v20, v24\n"
        "v_mfma_f32_16x16x32_bf16 v[104:107], v[136:139], v[64:67], v[104:107]\n"
        "s_waitcnt lgkmcnt(14)\n"
        "buffer_store_dwordx2 v[36:37], v6, s[28:31], s84 offen offset:0 sc1\n"
        "ds_read_b128 v[240:243], v1 offset:41984\n"
        "v_mfma_f32_16x16x32_bf16 v[92:95], v[116:119], v[200:203], 0\n"
        "s_waitcnt lgkmcnt(14)\n"
        "ds_read_b128 v[244:247], v1 offset:43008\n"
        "v_mul_f32 v21, v21, v249\n"
        "v_mfma_f32_16x16x32_bf16 v[92:95], v[120:123], v[204:207], v[92:95]\n"
        "v_mul_f32 v22, v22, v250\n"
        "v_cvt_pk_bf16_f32 v39, v21, v25\n"
        "v_mfma_f32_16x16x32_bf16 v[92:95], v[124:127], v[208:211], v[92:95]\n"
        "s_waitcnt lgkmcnt(14)\n"
        "buffer_store_dwordx2 v[38:39], v6, s[28:31], s85 offen offset:0 sc1\n"
        "v_cvt_pk_bf16_f32 v41, v22, v26\n"
        "v_mfma_f32_16x16x32_bf16 v[92:95], v[128:131], v[212:215], v[92:95]\n"
        "s_waitcnt lgkmcnt(13)\n"
        "buffer_store_dwordx2 v[40:41], v7, s[28:31], s84 offen offset:0 sc1\n"
        "v_mul_f32 v23, v23, v251\n"
        "v_mfma_f32_16x16x32_bf16 v[92:95], v[132:135], v[216:219], v[92:95]\n"
        "s_waitcnt lgkmcnt(12)\n"
        "v_cvt_pk_bf16_f32 v43, v23, v27\n"
        "v_mul_f32 v104, v104, v108\n"
        "v_mfma_f32_16x16x32_bf16 v[92:95], v[136:139], v[220:223], v[92:95]\n"
        "s_waitcnt lgkmcnt(11)\n"
        "buffer_store_dwordx2 v[42:43], v7, s[28:31], s85 offen offset:0 sc1\n"
        "v_mul_f32 v105, v105, v109\n"
        "v_mfma_f32_16x16x32_bf16 v[96:99], v[116:119], v[68:71], 0\n"
        "s_waitcnt lgkmcnt(10)\n"
        "v_mul_f32 v106, v106, v110\n"
        "v_mul_f32 v107, v107, v111\n"
        "v_mfma_f32_16x16x32_bf16 v[96:99], v[120:123], v[72:75], v[96:99]\n"
        "s_waitcnt lgkmcnt(9)\n"
        "v_mfma_f32_16x16x32_bf16 v[96:99], v[124:127], v[76:79], v[96:99]\n"
        "s_waitcnt lgkmcnt(8)\n"
        "v_mul_f32 v92, v92, v108\n"
        "v_mul_f32 v93, v93, v109\n"
        "v_mfma_f32_16x16x32_bf16 v[96:99], v[128:131], v[80:83], v[96:99]\n"
        "s_waitcnt lgkmcnt(7)\n"
        "v_mul_f32 v94, v94, v110\n"
        "v_mul_f32 v95, v95, v111\n"
        "v_mfma_f32_16x16x32_bf16 v[96:99], v[132:135], v[84:87], v[96:99]\n"
        "s_waitcnt lgkmcnt(6)\n"
        "v_mfma_f32_16x16x32_bf16 v[96:99], v[136:139], v[88:91], v[96:99]\n"
        "s_waitcnt lgkmcnt(5)\n"
        "v_mfma_f32_16x16x32_bf16 v[100:103], v[116:119], v[224:227], 0\n"
        "s_waitcnt lgkmcnt(4)\n"
        "v_mfma_f32_16x16x32_bf16 v[100:103], v[120:123], v[228:231], v[100:103]\n"
        "s_waitcnt lgkmcnt(3)\n"
        "v_mfma_f32_16x16x32_bf16 v[100:103], v[124:127], v[232:235], v[100:103]\n"
        "s_waitcnt lgkmcnt(2)\n"
        "v_mfma_f32_16x16x32_bf16 v[100:103], v[128:131], v[236:239], v[100:103]\n"
        "s_waitcnt lgkmcnt(1)\n"
        "v_mul_f32 v96, v96, v108\n"
        "v_cvt_pk_bf16_f32 v28, v92, v96\n"
        "v_mfma_f32_16x16x32_bf16 v[100:103], v[132:135], v[240:243], v[100:103]\n"
        "s_waitcnt lgkmcnt(0)\n"
        "v_mul_f32 v97, v97, v109\n"
        "v_cvt_pk_bf16_f32 v30, v93, v97\n"
        "v_mfma_f32_16x16x32_bf16 v[100:103], v[136:139], v[244:247], v[100:103]\n"
        "v_mul_f32 v98, v98, v110\n"
        "v_cvt_pk_bf16_f32 v32, v94, v98\n"
        "v_mfma_f32_16x16x32_bf16 v[24:27], v[140:143], v[44:47], 0\n"
        "v_mul_f32 v99, v99, v111\n"
        "v_cvt_pk_bf16_f32 v34, v95, v99\n"
        "v_mfma_f32_16x16x32_bf16 v[24:27], v[144:147], v[48:51], v[24:27]\n"
        "v_mfma_f32_16x16x32_bf16 v[24:27], v[148:151], v[52:55], v[24:27]\n"
        "v_mfma_f32_16x16x32_bf16 v[24:27], v[152:155], v[56:59], v[24:27]\n"
        "v_mul_f32 v100, v100, v108\n"
        "v_cvt_pk_bf16_f32 v29, v100, v104\n"
        "v_mfma_f32_16x16x32_bf16 v[24:27], v[156:159], v[60:63], v[24:27]\n"
        "buffer_store_dwordx2 v[28:29], v6, s[28:31], s80 offen offset:128 sc1\n"
        "v_mul_f32 v101, v101, v109\n"
        "v_mfma_f32_16x16x32_bf16 v[24:27], v[160:163], v[64:67], v[24:27]\n"
        "v_mul_f32 v102, v102, v110\n"
        "v_cvt_pk_bf16_f32 v31, v101, v105\n"
        "v_mfma_f32_16x16x32_bf16 v[12:15], v[140:143], v[200:203], 0\n"
        "buffer_store_dwordx2 v[30:31], v6, s[28:31], s81 offen offset:128 sc1\n"
        "v_cvt_pk_bf16_f32 v33, v102, v106\n"
        "v_mfma_f32_16x16x32_bf16 v[12:15], v[144:147], v[204:207], v[12:15]\n"
        "buffer_store_dwordx2 v[32:33], v7, s[28:31], s80 offen offset:128 sc1\n"
        "v_mul_f32 v103, v103, v111\n"
        "v_mfma_f32_16x16x32_bf16 v[12:15], v[148:151], v[208:211], v[12:15]\n"
        "v_cvt_pk_bf16_f32 v35, v103, v107\n"
        "v_mul_f32 v24, v24, v112\n"
        "v_mfma_f32_16x16x32_bf16 v[12:15], v[152:155], v[212:215], v[12:15]\n"
        "buffer_store_dwordx2 v[34:35], v7, s[28:31], s81 offen offset:128 sc1\n"
        "v_mul_f32 v25, v25, v113\n"
        "v_mfma_f32_16x16x32_bf16 v[12:15], v[156:159], v[216:219], v[12:15]\n"
        "v_mul_f32 v26, v26, v114\n"
        "v_mul_f32 v27, v27, v115\n"
        "v_mfma_f32_16x16x32_bf16 v[12:15], v[160:163], v[220:223], v[12:15]\n"
        "ds_read_b32 v5, v2 offset:52\n"
        "v_mfma_f32_16x16x32_bf16 v[16:19], v[140:143], v[68:71], 0\n"
        "v_mfma_f32_16x16x32_bf16 v[16:19], v[144:147], v[72:75], v[16:19]\n"
        "v_mfma_f32_16x16x32_bf16 v[16:19], v[148:151], v[76:79], v[16:19]\n"
        "v_mfma_f32_16x16x32_bf16 v[16:19], v[152:155], v[80:83], v[16:19]\n"
        "v_mfma_f32_16x16x32_bf16 v[16:19], v[156:159], v[84:87], v[16:19]\n"
        "v_mfma_f32_16x16x32_bf16 v[16:19], v[160:163], v[88:91], v[16:19]\n"
        "s_waitcnt lgkmcnt(0)\n"
        "v_readfirstlane_b32 s86, v5\n"
        "v_mfma_f32_16x16x32_bf16 v[20:23], v[140:143], v[224:227], 0\n"
        "v_mul_f32 v12, v12, v112\n"
        "v_mul_f32 v13, v13, v113\n"
        "v_mfma_f32_16x16x32_bf16 v[20:23], v[144:147], v[228:231], v[20:23]\n"
        "v_mul_f32 v14, v14, v114\n"
        "v_mul_f32 v15, v15, v115\n"
        "v_mfma_f32_16x16x32_bf16 v[20:23], v[148:151], v[232:235], v[20:23]\n"
        "s_cmp_eq_u32 s86, 0x1010100\n"
        "v_mul_f32 v16, v16, v112\n"
        "v_mul_f32 v17, v17, v113\n"
        "v_mfma_f32_16x16x32_bf16 v[20:23], v[152:155], v[236:239], v[20:23]\n"
        "s_cbranch_scc0 Lsl25_%=\n"
        "Lrt26_%=:\n"
        "v_mfma_f32_16x16x32_bf16 v[20:23], v[156:159], v[240:243], v[20:23]\n"
        "v_cvt_pk_bf16_f32 v36, v12, v16\n"
        "v_cvt_pk_bf16_f32 v38, v13, v17\n"
        "v_mfma_f32_16x16x32_bf16 v[20:23], v[160:163], v[244:247], v[20:23]\n"
        "v_mul_f32 v18, v18, v114\n"
        "v_cvt_pk_bf16_f32 v40, v14, v18\n"
        "v_mfma_f32_16x16x32_bf16 v[104:107], v[164:167], v[44:47], 0\n"
        "v_mul_f32 v19, v19, v115\n"
        "v_cvt_pk_bf16_f32 v42, v15, v19\n"
        "v_mfma_f32_16x16x32_bf16 v[104:107], v[168:171], v[48:51], v[104:107]\n"
        "ds_read_b128 v[44:47], v9 offset:18432\n"
        "v_mfma_f32_16x16x32_bf16 v[104:107], v[172:175], v[52:55], v[104:107]\n"
        "v_mul_f32 v20, v20, v112\n"
        "v_mul_f32 v21, v21, v113\n"
        "v_mfma_f32_16x16x32_bf16 v[104:107], v[176:179], v[56:59], v[104:107]\n"
        "v_cvt_pk_bf16_f32 v37, v20, v24\n"
        "v_cvt_pk_bf16_f32 v39, v21, v25\n"
        "v_mfma_f32_16x16x32_bf16 v[104:107], v[180:183], v[60:63], v[104:107]\n"
        "buffer_store_dwordx2 v[36:37], v6, s[28:31], s82 offen offset:128 sc1\n"
        "v_mul_f32 v22, v22, v114\n"
        "v_mfma_f32_16x16x32_bf16 v[104:107], v[184:187], v[64:67], v[104:107]\n"
        "buffer_store_dwordx2 v[38:39], v6, s[28:31], s83 offen offset:128 sc1\n"
        "v_cvt_pk_bf16_f32 v41, v22, v26\n"
        "v_mfma_f32_16x16x32_bf16 v[92:95], v[164:167], v[200:203], 0\n"
        "buffer_store_dwordx2 v[40:41], v7, s[28:31], s82 offen offset:128 sc1\n"
        "ds_read_b128 v[48:51], v9 offset:19456\n"
        "v_mfma_f32_16x16x32_bf16 v[92:95], v[168:171], v[204:207], v[92:95]\n"
        "v_mul_f32 v23, v23, v115\n"
        "v_cvt_pk_bf16_f32 v43, v23, v27\n"
        "v_mfma_f32_16x16x32_bf16 v[92:95], v[172:175], v[208:211], v[92:95]\n"
        "buffer_store_dwordx2 v[42:43], v7, s[28:31], s83 offen offset:128 sc1\n"
        "ds_read_b128 v[52:55], v9 offset:20480\n"
        "v_mfma_f32_16x16x32_bf16 v[92:95], v[176:179], v[212:215], v[92:95]\n"
        "ds_read_b128 v[56:59], v9 offset:21504\n"
        "v_mul_f32 v104, v104, v248\n"
        "v_mfma_f32_16x16x32_bf16 v[92:95], v[180:183], v[216:219], v[92:95]\n"
        "ds_read_b128 v[60:63], v9 offset:22528\n"
        "v_mul_f32 v105, v105, v249\n"
        "v_mfma_f32_16x16x32_bf16 v[92:95], v[184:187], v[220:223], v[92:95]\n"
        "ds_read_b128 v[64:67], v9 offset:23552\n"
        "v_mul_f32 v106, v106, v250\n"
        "v_mfma_f32_16x16x32_bf16 v[96:99], v[164:167], v[68:71], 0\n"
        "ds_read_b128 v[200:203], v9 offset:0\n"
        "v_mul_f32 v107, v107, v251\n"
        "v_mfma_f32_16x16x32_bf16 v[96:99], v[168:171], v[72:75], v[96:99]\n"
        "ds_read_b128 v[204:207], v9 offset:1024\n"
        "v_mfma_f32_16x16x32_bf16 v[96:99], v[172:175], v[76:79], v[96:99]\n"
        "ds_read_b128 v[208:211], v9 offset:2048\n"
        "v_mul_f32 v92, v92, v248\n"
        "v_mfma_f32_16x16x32_bf16 v[96:99], v[176:179], v[80:83], v[96:99]\n"
        "ds_read_b128 v[212:215], v9 offset:3072\n"
        "v_mul_f32 v93, v93, v249\n"
        "v_mfma_f32_16x16x32_bf16 v[96:99], v[180:183], v[84:87], v[96:99]\n"
        "ds_read_b128 v[216:219], v9 offset:4096\n"
        "v_mul_f32 v94, v94, v250\n"
        "v_mfma_f32_16x16x32_bf16 v[96:99], v[184:187], v[88:91], v[96:99]\n"
        "ds_read_b128 v[220:223], v9 offset:5120\n"
        "v_mul_f32 v95, v95, v251\n"
        "v_mfma_f32_16x16x32_bf16 v[100:103], v[164:167], v[224:227], 0\n"
        "ds_read_b128 v[68:71], v9 offset:6144\n"
        "v_mfma_f32_16x16x32_bf16 v[100:103], v[168:171], v[228:231], v[100:103]\n"
        "ds_read_b128 v[72:75], v9 offset:7168\n"
        "v_mfma_f32_16x16x32_bf16 v[100:103], v[172:175], v[232:235], v[100:103]\n"
        "ds_read_b128 v[76:79], v9 offset:8192\n"
        "v_mul_f32 v96, v96, v248\n"
        "v_mfma_f32_16x16x32_bf16 v[100:103], v[176:179], v[236:239], v[100:103]\n"
        "s_waitcnt lgkmcnt(14)\n"
        "ds_read_b128 v[80:83], v9 offset:9216\n"
        "v_cvt_pk_bf16_f32 v28, v92, v96\n"
        "v_mfma_f32_16x16x32_bf16 v[100:103], v[180:183], v[240:243], v[100:103]\n"
        "s_waitcnt lgkmcnt(14)\n"
        "ds_read_b128 v[84:87], v9 offset:10240\n"
        "v_mul_f32 v97, v97, v249\n"
        "v_mfma_f32_16x16x32_bf16 v[100:103], v[184:187], v[244:247], v[100:103]\n"
        "s_waitcnt lgkmcnt(14)\n"
        "ds_read_b128 v[88:91], v9 offset:11264\n"
        "v_cvt_pk_bf16_f32 v30, v93, v97\n"
        "v_mfma_f32_16x16x32_bf16 v[24:27], v[116:119], v[44:47], 0\n"
        "s_waitcnt lgkmcnt(14)\n"
        "ds_read_b128 v[224:227], v9 offset:12288\n"
        "v_mul_f32 v98, v98, v250\n"
        "v_mfma_f32_16x16x32_bf16 v[24:27], v[120:123], v[48:51], v[24:27]\n"
        "s_waitcnt lgkmcnt(14)\n"
        "ds_read_b128 v[228:231], v9 offset:13312\n"
        "v_cvt_pk_bf16_f32 v32, v94, v98\n"
        "v_mfma_f32_16x16x32_bf16 v[24:27], v[124:127], v[52:55], v[24:27]\n"
        "v_mul_f32 v99, v99, v251\n"
        "v_cvt_pk_bf16_f32 v34, v95, v99\n"
        "v_mfma_f32_16x16x32_bf16 v[24:27], v[128:131], v[56:59], v[24:27]\n"
        "s_waitcnt lgkmcnt(14)\n"
        "ds_read_b128 v[232:235], v9 offset:14336\n"
        "v_mul_f32 v100, v100, v248\n"
        "v_mfma_f32_16x16x32_bf16 v[24:27], v[132:135], v[60:63], v[24:27]\n"
        "s_waitcnt lgkmcnt(14)\n"
        "ds_read_b128 v[236:239], v9 offset:15360\n"
        "v_cvt_pk_bf16_f32 v29, v100, v104\n"
        "v_mfma_f32_16x16x32_bf16 v[24:27], v[136:139], v[64:67], v[24:27]\n"
        "s_waitcnt lgkmcnt(14)\n"
        "buffer_store_dwordx2 v[28:29], v6, s[28:31], s84 offen offset:128 sc1\n"
        "ds_read_b128 v[240:243], v9 offset:16384\n"
        "v_mfma_f32_16x16x32_bf16 v[12:15], v[116:119], v[200:203], 0\n"
        "s_waitcnt lgkmcnt(14)\n"
        "ds_read_b128 v[244:247], v9 offset:17408\n"
        "v_mul_f32 v101, v101, v249\n"
        "v_mfma_f32_16x16x32_bf16 v[12:15], v[120:123], v[204:207], v[12:15]\n"
        "v_mul_f32 v102, v102, v250\n"
        "v_cvt_pk_bf16_f32 v31, v101, v105\n"
        "v_mfma_f32_16x16x32_bf16 v[12:15], v[124:127], v[208:211], v[12:15]\n"
        "s_waitcnt lgkmcnt(14)\n"
        "buffer_store_dwordx2 v[30:31], v6, s[28:31], s85 offen offset:128 sc1\n"
        "v_cvt_pk_bf16_f32 v33, v102, v106\n"
        "v_mfma_f32_16x16x32_bf16 v[12:15], v[128:131], v[212:215], v[12:15]\n"
        "s_waitcnt lgkmcnt(13)\n"
        "buffer_store_dwordx2 v[32:33], v7, s[28:31], s84 offen offset:128 sc1\n"
        "v_mul_f32 v103, v103, v251\n"
        "v_mfma_f32_16x16x32_bf16 v[12:15], v[132:135], v[216:219], v[12:15]\n"
        "s_waitcnt lgkmcnt(12)\n"
        "v_cvt_pk_bf16_f32 v35, v103, v107\n"
        "v_mul_f32 v24, v24, v108\n"
        "v_mfma_f32_16x16x32_bf16 v[12:15], v[136:139], v[220:223], v[12:15]\n"
        "s_waitcnt lgkmcnt(11)\n"
        "buffer_store_dwordx2 v[34:35], v7, s[28:31], s85 offen offset:128 sc1\n"
        "v_mul_f32 v25, v25, v109\n"
        "v_mfma_f32_16x16x32_bf16 v[16:19], v[116:119], v[68:71], 0\n"
        "s_waitcnt lgkmcnt(10)\n"
        "v_mul_f32 v26, v26, v110\n"
        "v_mul_f32 v27, v27, v111\n"
        "v_mfma_f32_16x16x32_bf16 v[16:19], v[120:123], v[72:75], v[16:19]\n"
        "s_waitcnt lgkmcnt(9)\n"
        "v_mfma_f32_16x16x32_bf16 v[16:19], v[124:127], v[76:79], v[16:19]\n"
        "s_waitcnt lgkmcnt(8)\n"
        "v_mul_f32 v12, v12, v108\n"
        "v_mul_f32 v13, v13, v109\n"
        "v_mfma_f32_16x16x32_bf16 v[16:19], v[128:131], v[80:83], v[16:19]\n"
        "s_waitcnt lgkmcnt(7)\n"
        "v_mul_f32 v14, v14, v110\n"
        "v_mul_f32 v15, v15, v111\n"
        "v_mfma_f32_16x16x32_bf16 v[16:19], v[132:135], v[84:87], v[16:19]\n"
        "s_waitcnt lgkmcnt(6)\n"
        "v_mfma_f32_16x16x32_bf16 v[16:19], v[136:139], v[88:91], v[16:19]\n"
        "s_waitcnt lgkmcnt(5)\n"
        "v_mfma_f32_16x16x32_bf16 v[20:23], v[116:119], v[224:227], 0\n"
        "s_waitcnt lgkmcnt(4)\n"
        "v_mfma_f32_16x16x32_bf16 v[20:23], v[120:123], v[228:231], v[20:23]\n"
        "s_waitcnt lgkmcnt(3)\n"
        "v_mfma_f32_16x16x32_bf16 v[20:23], v[124:127], v[232:235], v[20:23]\n"
        "s_waitcnt lgkmcnt(2)\n"
        "v_mfma_f32_16x16x32_bf16 v[20:23], v[128:131], v[236:239], v[20:23]\n"
        "s_waitcnt lgkmcnt(1)\n"
        "v_mul_f32 v16, v16, v108\n"
        "v_cvt_pk_bf16_f32 v36, v12, v16\n"
        "v_mfma_f32_16x16x32_bf16 v[20:23], v[132:135], v[240:243], v[20:23]\n"
        "s_waitcnt lgkmcnt(0)\n"
        "v_mul_f32 v17, v17, v109\n"
        "v_cvt_pk_bf16_f32 v38, v13, v17\n"
        "v_mfma_f32_16x16x32_bf16 v[20:23], v[136:139], v[244:247], v[20:23]\n"
        "v_mul_f32 v18, v18, v110\n"
        "v_cvt_pk_bf16_f32 v40, v14, v18\n"
        "v_mfma_f32_16x16x32_bf16 v[104:107], v[140:143], v[44:47], 0\n"
        "v_mul_f32 v19, v19, v111\n"
        "v_cvt_pk_bf16_f32 v42, v15, v19\n"
        "v_mfma_f32_16x16x32_bf16 v[104:107], v[144:147], v[48:51], v[104:107]\n"
        "v_mfma_f32_16x16x32_bf16 v[104:107], v[148:151], v[52:55], v[104:107]\n"
        "v_mfma_f32_16x16x32_bf16 v[104:107], v[152:155], v[56:59], v[104:107]\n"
        "v_mul_f32 v20, v20, v108\n"
        "v_cvt_pk_bf16_f32 v37, v20, v24\n"
        "v_mfma_f32_16x16x32_bf16 v[104:107], v[156:159], v[60:63], v[104:107]\n"
        "buffer_store_dwordx2 v[36:37], v6, s[28:31], s80 offen offset:256 sc1\n"
        "v_mul_f32 v21, v21, v109\n"
        "v_mfma_f32_16x16x32_bf16 v[104:107], v[160:163], v[64:67], v[104:107]\n"
        "v_mul_f32 v22, v22, v110\n"
        "v_cvt_pk_bf16_f32 v39, v21, v25\n"
        "v_mfma_f32_16x16x32_bf16 v[92:95], v[140:143], v[200:203], 0\n"
        "buffer_store_dwordx2 v[38:39], v6, s[28:31], s81 offen offset:256 sc1\n"
        "v_cvt_pk_bf16_f32 v41, v22, v26\n"
        "v_mfma_f32_16x16x32_bf16 v[92:95], v[144:147], v[204:207], v[92:95]\n"
        "buffer_store_dwordx2 v[40:41], v7, s[28:31], s80 offen offset:256 sc1\n"
        "v_mul_f32 v23, v23, v111\n"
        "v_mfma_f32_16x16x32_bf16 v[92:95], v[148:151], v[208:211], v[92:95]\n"
        "v_cvt_pk_bf16_f32 v43, v23, v27\n"
        "v_mul_f32 v104, v104, v112\n"
        "v_mfma_f32_16x16x32_bf16 v[92:95], v[152:155], v[212:215], v[92:95]\n"
        "buffer_store_dwordx2 v[42:43], v7, s[28:31], s81 offen offset:256 sc1\n"
        "v_mul_f32 v105, v105, v113\n"
        "v_mfma_f32_16x16x32_bf16 v[92:95], v[156:159], v[216:219], v[92:95]\n"
        "v_mul_f32 v106, v106, v114\n"
        "v_mul_f32 v107, v107, v115\n"
        "v_mfma_f32_16x16x32_bf16 v[92:95], v[160:163], v[220:223], v[92:95]\n"
        "v_mfma_f32_16x16x32_bf16 v[96:99], v[140:143], v[68:71], 0\n"
        "v_mfma_f32_16x16x32_bf16 v[96:99], v[144:147], v[72:75], v[96:99]\n"
        "v_mfma_f32_16x16x32_bf16 v[96:99], v[148:151], v[76:79], v[96:99]\n"
        "v_mfma_f32_16x16x32_bf16 v[96:99], v[152:155], v[80:83], v[96:99]\n"
        "v_mfma_f32_16x16x32_bf16 v[96:99], v[156:159], v[84:87], v[96:99]\n"
        "v_mfma_f32_16x16x32_bf16 v[96:99], v[160:163], v[88:91], v[96:99]\n"
        "v_mfma_f32_16x16x32_bf16 v[100:103], v[140:143], v[224:227], 0\n"
        "v_mfma_f32_16x16x32_bf16 v[100:103], v[144:147], v[228:231], v[100:103]\n"
        "v_mul_f32 v92, v92, v112\n"
        "v_mul_f32 v93, v93, v113\n"
        "v_mfma_f32_16x16x32_bf16 v[100:103], v[148:151], v[232:235], v[100:103]\n"
        "v_mul_f32 v94, v94, v114\n"
        "v_mul_f32 v95, v95, v115\n"
        "v_mfma_f32_16x16x32_bf16 v[100:103], v[152:155], v[236:239], v[100:103]\n"
        "v_mul_f32 v96, v96, v112\n"
        "v_cvt_pk_bf16_f32 v28, v92, v96\n"
        "v_mfma_f32_16x16x32_bf16 v[100:103], v[156:159], v[240:243], v[100:103]\n"
        "v_mul_f32 v97, v97, v113\n"
        "v_cvt_pk_bf16_f32 v30, v93, v97\n"
        "v_mfma_f32_16x16x32_bf16 v[100:103], v[160:163], v[244:247], v[100:103]\n"
        "v_mul_f32 v98, v98, v114\n"
        "v_cvt_pk_bf16_f32 v32, v94, v98\n"
        "v_mfma_f32_16x16x32_bf16 v[24:27], v[164:167], v[44:47], 0\n"
        "v_mul_f32 v99, v99, v115\n"
        "v_cvt_pk_bf16_f32 v34, v95, v99\n"
        "v_mfma_f32_16x16x32_bf16 v[24:27], v[168:171], v[48:51], v[24:27]\n"
        "v_mfma_f32_16x16x32_bf16 v[24:27], v[172:175], v[52:55], v[24:27]\n"
        "v_mfma_f32_16x16x32_bf16 v[24:27], v[176:179], v[56:59], v[24:27]\n"
        "v_mul_f32 v100, v100, v112\n"
        "v_cvt_pk_bf16_f32 v29, v100, v104\n"
        "v_mfma_f32_16x16x32_bf16 v[24:27], v[180:183], v[60:63], v[24:27]\n"
        "buffer_store_dwordx2 v[28:29], v6, s[28:31], s82 offen offset:256 sc1\n"
        "v_mul_f32 v101, v101, v113\n"
        "v_mfma_f32_16x16x32_bf16 v[24:27], v[184:187], v[64:67], v[24:27]\n"
        "v_mul_f32 v102, v102, v114\n"
        "v_cvt_pk_bf16_f32 v31, v101, v105\n"
        "v_mfma_f32_16x16x32_bf16 v[12:15], v[164:167], v[200:203], 0\n"
        "buffer_store_dwordx2 v[30:31], v6, s[28:31], s83 offen offset:256 sc1\n"
        "v_cvt_pk_bf16_f32 v33, v102, v106\n"
        "v_mfma_f32_16x16x32_bf16 v[12:15], v[168:171], v[204:207], v[12:15]\n"
        "buffer_store_dwordx2 v[32:33], v7, s[28:31], s82 offen offset:256 sc1\n"
        "v_mul_f32 v103, v103, v115\n"
        "v_mfma_f32_16x16x32_bf16 v[12:15], v[172:175], v[208:211], v[12:15]\n"
        "v_cvt_pk_bf16_f32 v35, v103, v107\n"
        "v_mul_f32 v24, v24, v248\n"
        "v_mfma_f32_16x16x32_bf16 v[12:15], v[176:179], v[212:215], v[12:15]\n"
        "buffer_store_dwordx2 v[34:35], v7, s[28:31], s83 offen offset:256 sc1\n"
        "v_mul_f32 v25, v25, v249\n"
        "v_mfma_f32_16x16x32_bf16 v[12:15], v[180:183], v[216:219], v[12:15]\n"
        "v_mul_f32 v26, v26, v250\n"
        "v_mul_f32 v27, v27, v251\n"
        "v_mfma_f32_16x16x32_bf16 v[12:15], v[184:187], v[220:223], v[12:15]\n"
        "v_mfma_f32_16x16x32_bf16 v[16:19], v[164:167], v[68:71], 0\n"
        "v_mfma_f32_16x16x32_bf16 v[16:19], v[168:171], v[72:75], v[16:19]\n"
        "v_mfma_f32_16x16x32_bf16 v[16:19], v[172:175], v[76:79], v[16:19]\n"
        "v_mfma_f32_16x16x32_bf16 v[16:19], v[176:179], v[80:83], v[16:19]\n"
        "v_mfma_f32_16x16x32_bf16 v[16:19], v[180:183], v[84:87], v[16:19]\n"
        "v_mfma_f32_16x16x32_bf16 v[16:19], v[184:187], v[88:91], v[16:19]\n"
        "v_mfma_f32_16x16x32_bf16 v[20:23], v[164:167], v[224:227], 0\n"
        "v_mfma_f32_16x16x32_bf16 v[20:23], v[168:171], v[228:231], v[20:23]\n"
        "v_mul_f32 v12, v12, v248\n"
        "v_mul_f32 v13, v13, v249\n"
        "v_mfma_f32_16x16x32_bf16 v[20:23], v[172:175], v[232:235], v[20:23]\n"
        "v_mul_f32 v14, v14, v250\n"
        "v_mul_f32 v15, v15, v251\n"
        "v_mfma_f32_16x16x32_bf16 v[20:23], v[176:179], v[236:239], v[20:23]\n"
        "v_mul_f32 v16, v16, v248\n"
        "v_cvt_pk_bf16_f32 v36, v12, v16\n"
        "v_mfma_f32_16x16x32_bf16 v[20:23], v[180:183], v[240:243], v[20:23]\n"
        "v_mul_f32 v17, v17, v249\n"
        "v_cvt_pk_bf16_f32 v38, v13, v17\n"
        "v_mfma_f32_16x16x32_bf16 v[20:23], v[184:187], v[244:247], v[20:23]\n"
        "v_mul_f32 v18, v18, v250\n"
        "v_mul_f32 v19, v19, v251\n"
        "s_nop 5\n"
        "v_mul_f32 v20, v20, v248\n"
        "v_mul_f32 v21, v21, v249\n"
        "v_mul_f32 v22, v22, v250\n"
        "v_mul_f32 v23, v23, v251\n"
        "v_cvt_pk_bf16_f32 v37, v20, v24\n"
        "buffer_store_dwordx2 v[36:37], v6, s[28:31], s84 offen offset:256 sc1\n"
        "v_cvt_pk_bf16_f32 v39, v21, v25\n"
        "buffer_store_dwordx2 v[38:39], v6, s[28:31], s85 offen offset:256 sc1\n"
        "v_cvt_pk_bf16_f32 v40, v14, v18\n"
        "v_cvt_pk_bf16_f32 v41, v22, v26\n"
        "buffer_store_dwordx2 v[40:41], v7, s[28:31], s84 offen offset:256 sc1\n"
        "v_cvt_pk_bf16_f32 v42, v15, v19\n"
        "v_cvt_pk_bf16_f32 v43, v23, v27\n"
        "buffer_store_dwordx2 v[42:43], v7, s[28:31], s85 offen offset:256 sc1\n"
        "s_endpgm\n"
        "Lwave4_%=:\n"
        "s_waitcnt lgkmcnt(0)\n"
        "s_bitcmp1_b32 s18, 31\n"
        "s_cbranch_scc1 Ltg28_%=\n"
        "s_bitset1_b32 s18, 31\n"
        "s_mul_i32 s7, s7, 0x4c957f2d\n"
        "s_mul_i32 s86, s6, 0x5851f42d\n"
        "s_add_u32 s7, s7, s86\n"
        "s_mul_hi_u32 s86, s6, 0x4c957f2d\n"
        "s_add_u32 s7, s7, s86\n"
        "s_mul_i32 s6, s6, 0x4c957f2d\n"
        "s_add_u32 s6, s6, 0xf767814f\n"
        "s_addc_u32 s7, s7, 0x14057b7e\n"
        "Ltg28_%=:\n"
        "s_mov_b32 s88, s46\n"
        "s_mov_b32 s89, s47\n"
        "v_mov_b32 v2, 0x27000\n"
        "s_and_b32 s86, s3, 3\n"
        "v_mov_b32 v4, 1\n"
        "v_add_u32 v3, s86, v2\n"
        "s_mov_b32 s30, 0x1260000\n"
        "s_mov_b32 s31, 0x20000\n"
        "s_branch Lhjoin_%=\n"
        "Lhelper_%=:\n"
        "s_add_u32 m0, s91, 0x7f80\n"
        "s_nop 0\n"
        "buffer_load_dwordx4 v249, s[20:23], 0 offen offset:128 lds\n"
        "s_add_u32 m0, s91, 0x9b80\n"
        "s_nop 0\n"
        "buffer_load_dwordx4 v250, s[20:23], 0 offen offset:128 lds\n"
        "v_bfe_u32 v10, v0, 5, 1\n"
        "v_bfe_u32 v11, v0, 4, 1\n"
        "s_add_u32 s86, s12, 18\n"
        "s_add_u32 s87, s13, 18\n"
        "v_lshl_add_u32 v10, v10, 1, s86\n"
        "v_lshl_add_u32 v11, v11, 1, s87\n"
        "v_mad_i32_i24 v29, v10, s14, v9\n"
        "v_mad_i32_i24 v29, v11, s15, v29\n"
        "v_and_b32 v10, 15, v0\n"
        "v_lshl_add_u32 v29, v10, 3, v29\n"
        "s_cmp_eq_u32 s80, 0x1000\n"
        "s_cbranch_scc1 Lhjoin_%=\n"
        "s_waitcnt vmcnt(5)\n"
        "Lhjoin_%=:\n"
        "v_bfe_u32 v145, v212, 0, 8\n"
        "v_bfe_u32 v144, v212, 8, 8\n"
        "v_lshl_add_u32 v146, v145, 1, s12\n"
        "v_add_u32 v147, 7, v146\n"
        "v_min_u32 v146, v146, v147\n"
        "v_mad_i32_i24 v22, v146, s14, v9\n"
        "v_lshl_add_u32 v146, v144, 1, s13\n"
        "v_add_u32 v147, 7, v146\n"
        "v_min_u32 v146, v146, v147\n"
        "v_mad_i32_i24 v22, v146, s15, v22\n"
        "v_xor_b32 v146, v145, v144\n"
        "v_and_b32 v146, 2, v146\n"
        "v_and_b32 v147, 1, v145\n"
        "v_lshl_or_b32 v146, v147, 2, v146\n"
        "v_xor_b32 v146, v146, v7\n"
        "v_lshl_add_u32 v22, v146, 4, v22\n"
        "v_bfe_u32 v145, v212, 16, 8\n"
        "v_bfe_u32 v144, v212, 24, 8\n"
        "v_lshl_add_u32 v146, v145, 1, s12\n"
        "v_add_u32 v147, 7, v146\n"
        "v_min_u32 v146, v146, v147\n"
        "v_mad_i32_i24 v23, v146, s14, v9\n"
        "v_lshl_add_u32 v146, v144, 1, s13\n"
        "v_add_u32 v147, 7, v146\n"
        "v_min_u32 v146, v146, v147\n"
        "v_mad_i32_i24 v23, v146, s15, v23\n"
        "v_xor_b32 v146, v145, v144\n"
        "v_and_b32 v146, 2, v146\n"
        "v_and_b32 v147, 1, v145\n"
        "v_lshl_or_b32 v146, v147, 2, v146\n"
        "v_xor_b32 v146, v146, v7\n"
        "v_lshl_add_u32 v23, v146, 4, v23\n"
        "v_bfe_u32 v145, v213, 0, 8\n"
        "v_bfe_u32 v144, v213, 8, 8\n"
        "v_lshl_add_u32 v146, v145, 1, s12\n"
        "v_add_u32 v147, 7, v146\n"
        "v_min_u32 v146, v146, v147\n"
        "v_mad_i32_i24 v24, v146, s14, v9\n"
        "v_lshl_add_u32 v146, v144, 1, s13\n"
        "v_add_u32 v147, 7, v146\n"
        "v_min_u32 v146, v146, v147\n"
        "v_mad_i32_i24 v24, v146, s15, v24\n"
        "v_xor_b32 v146, v145, v144\n"
        "v_and_b32 v146, 2, v146\n"
        "v_and_b32 v147, 1, v145\n"
        "v_lshl_or_b32 v146, v147, 2, v146\n"
        "v_xor_b32 v146, v146, v7\n"
        "v_lshl_add_u32 v24, v146, 4, v24\n"
        "v_bfe_u32 v145, v213, 16, 8\n"
        "v_bfe_u32 v144, v213, 24, 8\n"
        "v_lshl_add_u32 v146, v145, 1, s12\n"
        "v_add_u32 v147, 7, v146\n"
        "v_min_u32 v146, v146, v147\n"
        "v_mad_i32_i24 v25, v146, s14, v9\n"
        "v_lshl_add_u32 v146, v144, 1, s13\n"
        "v_add_u32 v147, 7, v146\n"
        "v_min_u32 v146, v146, v147\n"
        "v_mad_i32_i24 v25, v146, s15, v25\n"
        "v_xor_b32 v146, v145, v144\n"
        "v_and_b32 v146, 2, v146\n"
        "v_and_b32 v147, 1, v145\n"
        "v_lshl_or_b32 v146, v147, 2, v146\n"
        "v_xor_b32 v146, v146, v7\n"
        "v_lshl_add_u32 v25, v146, 4, v25\n"
        "v_bfe_u32 v145, v214, 0, 8\n"
        "v_bfe_u32 v144, v214, 8, 8\n"
        "v_lshl_add_u32 v146, v145, 1, s12\n"
        "v_add_u32 v147, 7, v146\n"
        "v_min_u32 v146, v146, v147\n"
        "v_mad_i32_i24 v26, v146, s14, v9\n"
        "v_lshl_add_u32 v146, v144, 1, s13\n"
        "v_add_u32 v147, 7, v146\n"
        "v_min_u32 v146, v146, v147\n"
        "v_mad_i32_i24 v26, v146, s15, v26\n"
        "v_xor_b32 v146, v145, v144\n"
        "v_and_b32 v146, 2, v146\n"
        "v_and_b32 v147, 1, v145\n"
        "v_lshl_or_b32 v146, v147, 2, v146\n"
        "v_xor_b32 v146, v146, v7\n"
        "v_lshl_add_u32 v26, v146, 4, v26\n"
        "v_bfe_u32 v145, v214, 16, 8\n"
        "v_bfe_u32 v144, v214, 24, 8\n"
        "v_lshl_add_u32 v146, v145, 1, s12\n"
        "v_add_u32 v147, 7, v146\n"
        "v_min_u32 v146, v146, v147\n"
        "v_mad_i32_i24 v27, v146, s14, v9\n"
        "v_lshl_add_u32 v146, v144, 1, s13\n"
        "v_add_u32 v147, 7, v146\n"
        "v_min_u32 v146, v146, v147\n"
        "v_mad_i32_i24 v27, v146, s15, v27\n"
        "v_xor_b32 v146, v145, v144\n"
        "v_and_b32 v146, 2, v146\n"
        "v_and_b32 v147, 1, v145\n"
        "v_lshl_or_b32 v146, v147, 2, v146\n"
        "v_xor_b32 v146, v146, v7\n"
        "v_lshl_add_u32 v27, v146, 4, v27\n"
        "v_bfe_u32 v145, v215, 0, 8\n"
        "v_bfe_u32 v144, v215, 8, 8\n"
        "v_lshl_add_u32 v146, v145, 1, s12\n"
        "v_add_u32 v147, 7, v146\n"
        "v_min_u32 v146, v146, v147\n"
        "v_mad_i32_i24 v28, v146, s14, v9\n"
        "v_lshl_add_u32 v146, v144, 1, s13\n"
        "v_add_u32 v147, 7, v146\n"
        "v_min_u32 v146, v146, v147\n"
        "v_mad_i32_i24 v28, v146, s15, v28\n"
        "v_xor_b32 v146, v145, v144\n"
        "v_and_b32 v146, 2, v146\n"
        "v_and_b32 v147, 1, v145\n"
        "v_lshl_or_b32 v146, v147, 2, v146\n"
        "v_xor_b32 v146, v146, v7\n"
        "v_lshl_add_u32 v28, v146, 4, v28\n"
        "v_mov_b32 v18, 5\n"
        "v_and_b32 v19, 48, v0\n"
        "v_lshlrev_b32_sdwa v16, v18, v202 dst_sel:DWORD dst_unused:UNUSED_PAD src0_sel:DWORD src1_sel:WORD_0\n"
        "v_lshlrev_b32 v144, 1, v19\n"
        "v_xor_b32 v16, v16, v144\n"
        "v_and_b32 v144, 1, v0\n"
        "v_lshl_add_u32 v16, v144, 2, v16\n"
        "s_mov_b32 s89, 0x9e3779b1\n"
        "v_add_u32 v33, 0x51ed27, v8\n"
        "v_mul_lo_u32 v30, v33, s89\n"
        "v_mul_lo_u32 v31, v30, s89\n"
        "v_mul_lo_u32 v32, v31, s89\n"
        "v_mul_lo_u32 v33, v32, s89\n"
        "v_or_b32 v30, 1, v30\n"
        "v_or_b32 v31, 1, v31\n"
        "v_or_b32 v32, 1, v32\n"
        "v_or_b32 v33, 1, v33\n"
        "v_lshlrev_b32 v1, 4, v8\n"
        "s_and_b32 s88, s3, 3\n"
        "v_lshrrev_b32 v14, 3, v8\n"
        "v_lshl_add_u32 v14, s88, 3, v14\n"
        "v_add_u32 v10, 42, v14\n"
        "v_mul_u32_u24 v11, 293, v10\n"
        "v_lshrrev_b32 v11, 12, v11\n"
        "v_mad_i32_i24 v10, v11, -14, v10\n"
        "v_add_u32 v10, 3, v10\n"
        "v_lshl_add_u32 v12, v11, 1, s12\n"
        "v_add_u32 v13, 7, v12\n"
        "v_min_u32 v12, v12, v13\n"
        "v_mad_i32_i24 v218, v12, s14, v9\n"
        "v_lshl_add_u32 v12, v10, 1, s13\n"
        "v_add_u32 v13, 7, v12\n"
        "v_min_u32 v12, v12, v13\n"
        "v_mad_i32_i24 v218, v12, s15, v218\n"
        "v_xor_b32 v12, v11, v10\n"
        "v_and_b32 v12, 2, v12\n"
        "v_and_b32 v13, 1, v11\n"
        "v_lshl_or_b32 v12, v13, 2, v12\n"
        "v_xor_b32 v12, v12, v7\n"
        "v_lshl_add_u32 v218, v12, 4, v218\n"
        "v_add_u32 v10, 74, v14\n"
        "v_mul_u32_u24 v11, 293, v10\n"
        "v_lshrrev_b32 v11, 12, v11\n"
        "v_mad_i32_i24 v10, v11, -14, v10\n"
        "v_add_u32 v10, 3, v10\n"
        "v_lshl_add_u32 v12, v11, 1, s12\n"
        "v_add_u32 v13, 7, v12\n"
        "v_min_u32 v12, v12, v13\n"
        "v_mad_i32_i24 v219, v12, s14, v9\n"
        "v_lshl_add_u32 v12, v10, 1, s13\n"
        "v_add_u32 v13, 7, v12\n"
        "v_min_u32 v12, v12, v13\n"
        "v_mad_i32_i24 v219, v12, s15, v219\n"
        "v_xor_b32 v12, v11, v10\n"
        "v_and_b32 v12, 2, v12\n"
        "v_and_b32 v13, 1, v11\n"
        "v_lshl_or_b32 v12, v13, 2, v12\n"
        "v_xor_b32 v12, v12, v7\n"
        "v_lshl_add_u32 v219, v12, 4, v219\n"
        "v_add_u32 v10, 106, v14\n"
        "v_mul_u32_u24 v11, 293, v10\n"
        "v_lshrrev_b32 v11, 12, v11\n"
        "v_mad_i32_i24 v10, v11, -14, v10\n"
        "v_add_u32 v10, 3, v10\n"
        "v_lshl_add_u32 v12, v11, 1, s12\n"
        "v_add_u32 v13, 7, v12\n"
        "v_min_u32 v12, v12, v13\n"
        "v_mad_i32_i24 v220, v12, s14, v9\n"
        "v_lshl_add_u32 v12, v10, 1, s13\n"
        "v_add_u32 v13, 7, v12\n"
        "v_min_u32 v12, v12, v13\n"
        "v_mad_i32_i24 v220, v12, s15, v220\n"
        "v_xor_b32 v12, v11, v10\n"
        "v_and_b32 v12, 2, v12\n"
        "v_and_b32 v13, 1, v11\n"
        "v_lshl_or_b32 v12, v13, 2, v12\n"
        "v_xor_b32 v12, v12, v7\n"
        "v_lshl_add_u32 v220, v12, 4, v220\n"
        "v_add_u32 v10, 138, v14\n"
        "v_mul_u32_u24 v11, 293, v10\n"
        "v_lshrrev_b32 v11, 12, v11\n"
        "v_mad_i32_i24 v10, v11, -14, v10\n"
        "v_add_u32 v10, 3, v10\n"
        "v_lshl_add_u32 v12, v11, 1, s12\n"
        "v_add_u32 v13, 7, v12\n"
        "v_min_u32 v12, v12, v13\n"
        "v_mad_i32_i24 v221, v12, s14, v9\n"
        "v_lshl_add_u32 v12, v10, 1, s13\n"
        "v_add_u32 v13, 7, v12\n"
        "v_min_u32 v12, v12, v13\n"
        "v_mad_i32_i24 v221, v12, s15, v221\n"
        "v_xor_b32 v12, v11, v10\n"
        "v_and_b32 v12, 2, v12\n"
        "v_and_b32 v13, 1, v11\n"
        "v_lshl_or_b32 v12, v13, 2, v12\n"
        "v_xor_b32 v12, v12, v7\n"
        "v_lshl_add_u32 v221, v12, 4, v221\n"
        "v_add_u32 v10, 170, v14\n"
        "v_mul_u32_u24 v11, 293, v10\n"
        "v_lshrrev_b32 v11, 12, v11\n"
        "v_mad_i32_i24 v10, v11, -14, v10\n"
        "v_add_u32 v10, 3, v10\n"
        "v_lshl_add_u32 v12, v11, 1, s12\n"
        "v_add_u32 v13, 7, v12\n"
        "v_min_u32 v12, v12, v13\n"
        "v_mad_i32_i24 v222, v12, s14, v9\n"
        "v_lshl_add_u32 v12, v10, 1, s13\n"
        "v_add_u32 v13, 7, v12\n"
        "v_min_u32 v12, v12, v13\n"
        "v_mad_i32_i24 v222, v12, s15, v222\n"
        "v_xor_b32 v12, v11, v10\n"
        "v_and_b32 v12, 2, v12\n"
        "v_and_b32 v13, 1, v11\n"
        "v_lshl_or_b32 v12, v13, 2, v12\n"
        "v_xor_b32 v12, v12, v7\n"
        "v_lshl_add_u32 v222, v12, 4, v222\n"
        "v_add_u32 v10, 202, v14\n"
        "v_mul_u32_u24 v11, 293, v10\n"
        "v_lshrrev_b32 v11, 12, v11\n"
        "v_mad_i32_i24 v10, v11, -14, v10\n"
        "v_add_u32 v10, 3, v10\n"
        "v_lshl_add_u32 v12, v11, 1, s12\n"
        "v_add_u32 v13, 7, v12\n"
        "v_min_u32 v12, v12, v13\n"
        "v_mad_i32_i24 v223, v12, s14, v9\n"
        "v_lshl_add_u32 v12, v10, 1, s13\n"
        "v_add_u32 v13, 7, v12\n"
        "v_min_u32 v12, v12, v13\n"
        "v_mad_i32_i24 v223, v12, s15, v223\n"
        "v_xor_b32 v12, v11, v10\n"
        "v_and_b32 v12, 2, v12\n"
        "v_and_b32 v13, 1, v11\n"
        "v_lshl_or_b32 v12, v13, 2, v12\n"
        "v_xor_b32 v12, v12, v7\n"
        "v_lshl_add_u32 v223, v12, 4, v223\n"
        "v_add_u32 v10, 234, v14\n"
        "v_mul_u32_u24 v11, 293, v10\n"
        "v_lshrrev_b32 v11, 12, v11\n"
        "v_mad_i32_i24 v10, v11, -14, v10\n"
        "v_add_u32 v10, 3, v10\n"
        "v_lshl_add_u32 v12, v11, 1, s12\n"
        "v_add_u32 v13, 7, v12\n"
        "v_min_u32 v12, v12, v13\n"
        "v_mad_i32_i24 v224, v12, s14, v9\n"
        "v_lshl_add_u32 v12, v10, 1, s13\n"
        "v_add_u32 v13, 7, v12\n"
        "v_min_u32 v12, v12, v13\n"
        "v_mad_i32_i24 v224, v12, s15, v224\n"
        "v_xor_b32 v12, v11, v10\n"
        "v_and_b32 v12, 2, v12\n"
        "v_and_b32 v13, 1, v11\n"
        "v_lshl_or_b32 v12, v13, 2, v12\n"
        "v_xor_b32 v12, v12, v7\n"
        "v_lshl_add_u32 v224, v12, 4, v224\n"
        "s_lshl_b32 s86, s88, 10\n"
        "s_add_u32 m0, s86, 0xc700\n"
        "s_nop 0\n"
        "buffer_load_dwordx4 v218, s[20:23], 0 offen offset:256 lds\n"
        "v_and_b32 v15, 63, v0\n"
        "v_min_u32 v15, 17, v15\n"
        "v_mul_u32_u24 v11, 43, v15\n"
        "v_lshrrev_b32 v11, 7, v11\n"
        "v_mul_u32_u24 v11, 12752, v11\n"
        "v_lshl_add_u32 v15, v15, 4, v11\n"
        "s_and_b32 s11, s2, 0\n"
        "s_mul_i32 s11, s11, 76800\n"
        "s_cmp_lg_u32 s19, 0\n"
        "s_cselect_b32 s11, s19, s11\n"
        "s_add_u32 s11, s11, 81920\n"
        "s_mul_i32 s10, s88, 3200\n"
        "s_add_u32 s10, s10, s11\n"
        "s_mul_i32 s11, s88, 6144\n"
        "v_add_u32 v14, s11, v1\n"
        "v_add_u32 v14, 0x12c00, v14\n"
        "v_add_u32 v13, s10, v1\n"
        "v_mov_b32 v10, s6\n"
        "v_mov_b32 v11, s7\n"
        "s_waitcnt lgkmcnt(0)\n"
        "Lt129_%=:\n"
        "s_memtime s[86:87]\n"
        "s_waitcnt lgkmcnt(0)\n"
        "s_sub_u32 s86, s86, s8\n"
        "s_cmp_lt_u32 s86, 5700\n"
        "s_cbranch_scc1 Lt129_%=\n"
        "Lhre_%=:\n"
        "s_lshl_b32 s86, s88, 10\n"
        "buffer_load_dwordx2 v[36:37], v15, s[24:27], s10 offen offset:3072 sc1\n"
        "s_mov_b32 s87, 0\n"
        "s_add_u32 m0, s11, 0x12c00\n"
        "s_nop 0\n"
        "buffer_load_dwordx4 v13, s[24:27], s87 offen sc1 lds\n"
        "s_mov_b32 s87, 12800\n"
        "s_add_u32 m0, s11, 0x13000\n"
        "s_nop 0\n"
        "buffer_load_dwordx4 v13, s[24:27], s87 offen sc1 lds\n"
        "s_mov_b32 s87, 1024\n"
        "s_add_u32 m0, s11, 0x13400\n"
        "s_nop 0\n"
        "buffer_load_dwordx4 v13, s[24:27], s87 offen sc1 lds\n"
        "s_mov_b32 s87, 13824\n"
        "s_add_u32 m0, s11, 0x13800\n"
        "s_nop 0\n"
        "buffer_load_dwordx4 v13, s[24:27], s87 offen sc1 lds\n"
        "s_mov_b32 s87, 2048\n"
        "s_add_u32 m0, s11, 0x13c00\n"
        "s_nop 0\n"
        "buffer_load_dwordx4 v13, s[24:27], s87 offen sc1 lds\n"
        "s_mov_b32 s87, 14848\n"
        "s_add_u32 m0, s11, 0x14000\n"
        "s_nop 0\n"
        "buffer_load_dwordx4 v13, s[24:27], s87 offen sc1 lds\n"
        "s_sleep 2\n"
        "s_add_u32 m0, s86, 0xd700\n"
        "s_nop 0\n"
        "buffer_load_dwordx4 v219, s[20:23], 0 offen offset:256 lds\n"
        "s_add_u32 m0, s86, 0xe700\n"
        "s_nop 0\n"
        "buffer_load_dwordx4 v220, s[20:23], 0 offen offset:256 lds\n"
        "s_mov_b32 s87, 25600\n"
        "s_add_u32 m0, s11, 0x19000\n"
        "s_nop 0\n"
        "buffer_load_dwordx4 v13, s[24:27], s87 offen sc1 lds\n"
        "s_mov_b32 s87, 38400\n"
        "s_add_u32 m0, s11, 0x19400\n"
        "s_nop 0\n"
        "buffer_load_dwordx4 v13, s[24:27], s87 offen sc1 lds\n"
        "s_mov_b32 s87, 26624\n"
        "s_add_u32 m0, s11, 0x19800\n"
        "s_nop 0\n"
        "buffer_load_dwordx4 v13, s[24:27], s87 offen sc1 lds\n"
        "s_mov_b32 s87, 39424\n"
        "s_add_u32 m0, s11, 0x19c00\n"
        "s_nop 0\n"
        "buffer_load_dwordx4 v13, s[24:27], s87 offen sc1 lds\n"
        "s_mov_b32 s87, 27648\n"
        "s_add_u32 m0, s11, 0x1a000\n"
        "s_nop 0\n"
        "buffer_load_dwordx4 v13, s[24:27], s87 offen sc1 lds\n"
        "s_mov_b32 s87, 40448\n"
        "s_add_u32 m0, s11, 0x1a400\n"
        "s_nop 0\n"
        "buffer_load_dwordx4 v13, s[24:27], s87 offen sc1 lds\n"
        "s_add_u32 m0, s86, 0xf700\n"
        "s_nop 0\n"
        "buffer_load_dwordx4 v221, s[20:23], 0 offen offset:256 lds\n"
        "s_add_u32 m0, s86, 0x10700\n"
        "s_nop 0\n"
        "buffer_load_dwordx4 v222, s[20:23], 0 offen offset:256 lds\n"
        "s_mov_b32 s90, 40\n"
        "s_waitcnt vmcnt(16)\n"
        "v_cmp_ne_u32 vcc, s7, v37\n"
        "s_nop 4\n"
        "s_cbranch_vccnz Lgret_%=\n"
        "Lgok_%=:\n"
        "s_waitcnt vmcnt(10)\n"
        "s_barrier\n"
        "s_load_dwordx8 s[40:47], s[4:5], 512\n"
        "s_add_u32 m0, s86, 0x11700\n"
        "s_nop 0\n"
        "buffer_load_dwordx4 v223, s[20:23], 0 offen offset:256 lds\n"
        "s_waitcnt vmcnt(3)\n"
        "ds_write_b8 v3, v4 offset:0\n"
        "s_cmp_lg_u32 s3, 4\n"
        "s_cbranch_scc1 Lw4x_%=\n"
        "s_add_u32 m0, s86, 0x12700\n"
        "s_nop 0\n"
        "buffer_load_dwordx4 v224, s[20:23], 0 offen offset:256 lds\n"
        "Lw4x_%=:\n"
        "s_mov_b32 s87, 51200\n"
        "s_add_u32 m0, s11, 0x1f400\n"
        "s_nop 0\n"
        "buffer_load_dwordx4 v13, s[24:27], s87 offen sc1 lds\n"
        "s_mov_b32 s87, 64000\n"
        "s_add_u32 m0, s11, 0x1f800\n"
        "s_nop 0\n"
        "buffer_load_dwordx4 v13, s[24:27], s87 offen sc1 lds\n"
        "s_mov_b32 s87, 52224\n"
        "s_add_u32 m0, s11, 0x1fc00\n"
        "s_nop 0\n"
        "buffer_load_dwordx4 v13, s[24:27], s87 offen sc1 lds\n"
        "s_mov_b32 s87, 65024\n"
        "s_add_u32 m0, s11, 0x20000\n"
        "s_nop 0\n"
        "buffer_load_dwordx4 v13, s[24:27], s87 offen sc1 lds\n"
        "s_mov_b32 s87, 53248\n"
        "s_add_u32 m0, s11, 0x20400\n"
        "s_nop 0\n"
        "buffer_load_dwordx4 v13, s[24:27], s87 offen sc1 lds\n"
        "s_mov_b32 s87, 66048\n"
        "s_add_u32 m0, s11, 0x20800\n"
        "s_nop 0\n"
        "buffer_load_dwordx4 v13, s[24:27], s87 offen sc1 lds\n"
        "ds_read_b128 v[40:43], v14 offset:0\n"
        "ds_read_b128 v[44:47], v14 offset:1024\n"
        "ds_read_b128 v[48:51], v14 offset:2048\n"
        "ds_read_b128 v[52:55], v14 offset:3072\n"
        "ds_read_b128 v[56:59], v14 offset:4096\n"
        "ds_read_b128 v[60:63], v14 offset:5120\n"
        "ds_read_b128 v[64:67], v14 offset:25600\n"
        "ds_read_b128 v[68:71], v14 offset:26624\n"
        "ds_read_b128 v[72:75], v14 offset:27648\n"
        "ds_read_b128 v[76:79], v14 offset:28672\n"
        "ds_read_b128 v[80:83], v14 offset:29696\n"
        "ds_read_b128 v[84:87], v14 offset:30720\n"
        "v_cmp_gt_u32 vcc, 18, v8\n"
        "v_subrev_u32 v35, s6, v36\n"
        "s_nop 0\n"
        "v_cndmask_b32 v35, 0, v35, vcc\n"
        "v_cmp_gt_u32 vcc, 12, v8\n"
        "s_nop 1\n"
        "v_cndmask_b32 v12, 0, v35, vcc\n"
        "v_sub_u32 v35, v35, v12\n"
        "v_cmp_ne_u32 vcc, s7, v37\n"
        "s_waitcnt lgkmcnt(10)\n"
        "v_add_u32 v124, v40, v44\n"
        "v_add_u32 v125, v41, v45\n"
        "v_add_u32 v126, v42, v46\n"
        "v_add_u32 v127, v43, v47\n"
        "s_waitcnt lgkmcnt(8)\n"
        "v_add3_u32 v124, v124, v48, v52\n"
        "v_add3_u32 v125, v125, v49, v53\n"
        "v_add3_u32 v126, v126, v50, v54\n"
        "v_add3_u32 v127, v127, v51, v55\n"
        "s_waitcnt lgkmcnt(6)\n"
        "v_add3_u32 v124, v124, v56, v60\n"
        "v_add3_u32 v125, v125, v57, v61\n"
        "v_add3_u32 v126, v126, v58, v62\n"
        "v_add3_u32 v127, v127, v59, v63\n"
        "s_waitcnt lgkmcnt(4)\n"
        "v_add3_u32 v124, v124, v64, v68\n"
        "v_add3_u32 v125, v125, v65, v69\n"
        "v_add3_u32 v126, v126, v66, v70\n"
        "v_add3_u32 v127, v127, v67, v71\n"
        "s_waitcnt lgkmcnt(2)\n"
        "v_add3_u32 v124, v124, v72, v76\n"
        "v_add3_u32 v125, v125, v73, v77\n"
        "v_add3_u32 v126, v126, v74, v78\n"
        "v_add3_u32 v127, v127, v75, v79\n"
        "s_waitcnt lgkmcnt(0)\n"
        "v_add3_u32 v124, v124, v80, v84\n"
        "v_add3_u32 v125, v125, v81, v85\n"
        "v_add3_u32 v126, v126, v82, v86\n"
        "v_add3_u32 v127, v127, v83, v87\n"
        "v_mul_lo_u32 v124, v124, v30\n"
        "v_mul_lo_u32 v125, v125, v31\n"
        "v_mul_lo_u32 v126, v126, v32\n"
        "v_mul_lo_u32 v127, v127, v33\n"
        "v_add3_u32 v34, v124, v125, v126\n"
        "v_add_u32 v34, v34, v127\n"
        "v_sub_u32 v34, v34, v12\n"
        "s_nop 1\n"
        "v_add_u32_dpp v34, v34, v34 row_shr:1 row_mask:0xf bank_mask:0xf\n"
        "s_nop 1\n"
        "v_add_u32_dpp v34, v34, v34 row_shr:2 row_mask:0xf bank_mask:0xf\n"
        "s_nop 1\n"
        "v_add_u32_dpp v34, v34, v34 row_shr:4 row_mask:0xf bank_mask:0xf\n"
        "s_nop 1\n"
        "v_add_u32_dpp v34, v34, v34 row_shr:8 row_mask:0xf bank_mask:0xf\n"
        "s_nop 1\n"
        "v_add_u32_dpp v34, v34, v34 row_bcast:15 row_mask:0xa bank_mask:0xf\n"
        "s_nop 1\n"
        "v_add_u32_dpp v34, v34, v34 row_bcast:31 row_mask:0xc bank_mask:0xf\n"
        "s_nop 0\n"
        "v_readlane_b32 s89, v34, 63\n"
        "s_or_b32 s89, s89, vcc_lo\n"
        "s_or_b32 s89, s89, vcc_hi\n"
        "s_cmp_eq_u32 s89, 0\n"
        "s_cselect_b32 s89, 1, 2\n"
        "v_mov_b32 v12, s89\n"
        "s_mov_b32 s87, 0x200000\n"
        "Lpl31_%=:\n"
        "ds_read_b32 v5, v2 offset:8\n"
        "s_waitcnt lgkmcnt(0)\n"
        "v_readfirstlane_b32 s86, v5\n"
        "s_cmp_eq_u32 s86, 0x1010101\n"
        "s_cbranch_scc1 Lrt30_%=\n"
        "s_sub_u32 s87, s87, 1\n"
        "s_cbranch_scc1 Lrt30_%=\n"
        "s_sleep 1\n"
        "s_branch Lpl31_%=\n"
        "Lrt30_%=:\n"
        "s_add_u32 m0, s11, 0x12c00\n"
        "s_nop 0\n"
        "buffer_load_dwordx4 v22, s[20:23], 0 offen lds\n"
        "s_add_u32 m0, s11, 0x13000\n"
        "s_nop 0\n"
        "buffer_load_dwordx4 v23, s[20:23], 0 offen lds\n"
        "s_add_u32 m0, s11, 0x13400\n"
        "s_nop 0\n"
        "buffer_load_dwordx4 v24, s[20:23], 0 offen lds\n"
        "s_add_u32 m0, s11, 0x13800\n"
        "s_nop 0\n"
        "buffer_load_dwordx4 v25, s[20:23], 0 offen lds\n"
        "s_add_u32 m0, s11, 0x13c00\n"
        "s_nop 0\n"
        "buffer_load_dwordx4 v26, s[20:23], 0 offen lds\n"
        "s_add_u32 m0, s11, 0x14000\n"
        "s_nop 0\n"
        "buffer_load_dwordx4 v27, s[20:23], 0 offen lds\n"
        "s_waitcnt vmcnt(6)\n"
        "ds_write_b8 v3, v12 offset:4\n"
        "s_cmp_lg_u32 s88, 1\n"
        "s_cbranch_scc1 Lhk32_%=\n"
        "s_mov_b32 m0, 0x18c00\n"
        "s_nop 0\n"
        "buffer_load_dwordx4 v28, s[20:23], 0 offen lds\n"
        "Lhk32_%=:\n"
        "ds_read_b128 v[88:91], v14 offset:51200\n"
        "ds_read_b128 v[92:95], v14 offset:52224\n"
        "ds_read_b128 v[96:99], v14 offset:53248\n"
        "ds_read_b128 v[100:103], v14 offset:54272\n"
        "ds_read_b128 v[104:107], v14 offset:55296\n"
        "ds_read_b128 v[108:111], v14 offset:56320\n"
        "s_mov_b32 s87, 0x200000\n"
        "Lpl34_%=:\n"
        "ds_read_b32 v5, v2 offset:4\n"
        "s_waitcnt lgkmcnt(0)\n"
        "v_readfirstlane_b32 s86, v5\n"
        "s_cmp_eq_u32 s86, 0x1010101\n"
        "s_cbranch_scc1 Lrt33_%=\n"
        "s_and_b32 s86, s86, 0x02020202\n"
        "s_cbranch_scc1 Lrelight_%=\n"
        "s_sub_u32 s87, s87, 1\n"
        "s_cbranch_scc1 Lrt33_%=\n"
        "s_sleep 1\n"
        "s_branch Lpl34_%=\n"
        "Lrt33_%=:\n"
        "s_waitcnt vmcnt(0)\n"
        "ds_write_b8 v3, v4 offset:20\n"
        "s_cmp_lg_u32 s3, 4\n"
        "s_cbranch_scc1 Lh0_%=\n"
        "s_sub_u32 s86, s2, 24\n"
        "s_cmp_ge_u32 s86, 36\n"
        "s_cbranch_scc1 Lh0_%=\n"
        "s_cmp_lg_u32 s19, 0\n"
        "s_cbranch_scc1 Lh0_%=\n"
        "buffer_store_dwordx4 v[232:235], v140, s[24:27], s19 offen offset:0 sc1\n"
        "buffer_store_dwordx4 v[236:239], v140, s[24:27], s19 offen offset:64 sc1\n"
        "Lh0_%=:\n"
        "s_cmp_lg_u32 s3, 7\n"
        "s_cbranch_scc1 Lh2_%=\n"
        "s_lshl_b32 s86, s2, 3\n"
        "s_add_u32 s86, s86, 0xc4000\n"
        "v_mov_b32 v12, 0\n"
        "s_mov_b64 exec, 1\n"
        "buffer_store_dwordx2 v[10:11], v12, s[24:27], s86 offen sc1\n"
        "s_mov_b64 exec, -1\n"
        "Lh2_%=:\n"
        "s_mov_b32 s87, 0x200000\n"
        "Lpl36_%=:\n"
        "ds_read_b32 v5, v2 offset:12\n"
        "s_waitcnt lgkmcnt(0)\n"
        "v_readfirstlane_b32 s86, v5\n"
        "s_cmp_eq_u32 s86, 0x1010101\n"
        "s_cbranch_scc1 Lrt35_%=\n"
        "s_sub_u32 s87, s87, 1\n"
        "s_cbranch_scc1 Lrt35_%=\n"
        "s_sleep 1\n"
        "s_branch Lpl36_%=\n"
        "Lrt35_%=:\n"
        "s_add_u32 m0, s11, 0x18f80\n"
        "s_nop 0\n"
        "buffer_load_dwordx4 v22, s[20:23], 0 offen offset:128 lds\n"
        "s_add_u32 m0, s11, 0x19380\n"
        "s_nop 0\n"
        "buffer_load_dwordx4 v23, s[20:23], 0 offen offset:128 lds\n"
        "s_add_u32 m0, s11, 0x19780\n"
        "s_nop 0\n"
        "buffer_load_dwordx4 v24, s[20:23], 0 offen offset:128 lds\n"
        "s_add_u32 m0, s11, 0x19b80\n"
        "s_nop 0\n"
        "buffer_load_dwordx4 v25, s[20:23], 0 offen offset:128 lds\n"
        "s_add_u32 m0, s11, 0x19f80\n"
        "s_nop 0\n"
        "buffer_load_dwordx4 v26, s[20:23], 0 offen offset:128 lds\n"
        "s_add_u32 m0, s11, 0x1a380\n"
        "s_nop 0\n"
        "buffer_load_dwordx4 v27, s[20:23], 0 offen offset:128 lds\n"
        "s_cmp_lg_u32 s88, 2\n"
        "s_cbranch_scc1 Lhk37_%=\n"
        "s_mov_b32 m0, 0x1ef80\n"
        "s_nop 0\n"
        "buffer_load_dwordx4 v28, s[20:23], 0 offen offset:128 lds\n"
        "Lhk37_%=:\n"
        "v_add_u32 v124, v88, v92\n"
        "v_add_u32 v125, v89, v93\n"
        "v_add_u32 v126, v90, v94\n"
        "v_add_u32 v127, v91, v95\n"
        "v_add3_u32 v124, v124, v96, v100\n"
        "v_add3_u32 v125, v125, v97, v101\n"
        "v_add3_u32 v126, v126, v98, v102\n"
        "v_add3_u32 v127, v127, v99, v103\n"
        "v_add3_u32 v124, v124, v104, v108\n"
        "v_add3_u32 v125, v125, v105, v109\n"
        "v_add3_u32 v126, v126, v106, v110\n"
        "v_add3_u32 v127, v127, v107, v111\n"
        "v_mul_lo_u32 v124, v124, v30\n"
        "v_mul_lo_u32 v125, v125, v31\n"
        "v_mul_lo_u32 v126, v126, v32\n"
        "v_mul_lo_u32 v127, v127, v33\n"
        "v_add3_u32 v34, v124, v125, v126\n"
        "v_add_u32 v34, v34, v127\n"
        "v_sub_u32 v34, v34, v35\n"
        "s_nop 1\n"
        "v_add_u32_dpp v34, v34, v34 row_shr:1 row_mask:0xf bank_mask:0xf\n"
        "s_nop 1\n"
        "v_add_u32_dpp v34, v34, v34 row_shr:2 row_mask:0xf bank_mask:0xf\n"
        "s_nop 1\n"
        "v_add_u32_dpp v34, v34, v34 row_shr:4 row_mask:0xf bank_mask:0xf\n"
        "s_nop 1\n"
        "v_add_u32_dpp v34, v34, v34 row_shr:8 row_mask:0xf bank_mask:0xf\n"
        "s_nop 1\n"
        "v_add_u32_dpp v34, v34, v34 row_bcast:15 row_mask:0xa bank_mask:0xf\n"
        "s_nop 1\n"
        "v_add_u32_dpp v34, v34, v34 row_bcast:31 row_mask:0xc bank_mask:0xf\n"
        "s_nop 0\n"
        "v_readlane_b32 s91, v34, 63\n"
        "s_waitcnt vmcnt(0)\n"
        "ds_write_b8 v3, v4 offset:24\n"
        "s_cmp_lg_u32 s3, 4\n"
        "s_cbranch_scc1 Lh1_%=\n"
        "s_sub_u32 s86, s2, 24\n"
        "s_cmp_ge_u32 s86, 36\n"
        "s_cbranch_scc1 Lh1_%=\n"
        "s_cmp_lg_u32 s19, 0\n"
        "s_cbranch_scc1 Lh1_%=\n"
        "s_lshl_b32 s86, s86, 3\n"
        "v_lshlrev_b32 v12, 10, v8\n"
        "s_add_u32 s86, s86, 0x12000\n"
        "s_mov_b64 exec, 0xff\n"
        "buffer_store_dwordx2 v[10:11], v12, s[24:27], s86 offen sc1\n"
        "s_mov_b64 exec, -1\n"
        "Lh1_%=:\n"
        "s_mov_b32 s87, 0x200000\n"
        "Lpl39_%=:\n"
        "ds_read_b32 v5, v2 offset:16\n"
        "s_waitcnt lgkmcnt(0)\n"
        "v_readfirstlane_b32 s86, v5\n"
        "s_cmp_eq_u32 s86, 0x1010101\n"
        "s_cbranch_scc1 Lrt38_%=\n"
        "s_sub_u32 s87, s87, 1\n"
        "s_cbranch_scc1 Lrt38_%=\n"
        "s_sleep 1\n"
        "s_branch Lpl39_%=\n"
        "Lrt38_%=:\n"
        "s_add_u32 m0, s11, 0x1f300\n"
        "s_nop 0\n"
        "buffer_load_dwordx4 v22, s[20:23], 0 offen offset:256 lds\n"
        "s_add_u32 m0, s11, 0x1f700\n"
        "s_nop 0\n"
        "buffer_load_dwordx4 v23, s[20:23], 0 offen offset:256 lds\n"
        "s_add_u32 m0, s11, 0x1fb00\n"
        "s_nop 0\n"
        "buffer_load_dwordx4 v24, s[20:23], 0 offen offset:256 lds\n"
        "s_add_u32 m0, s11, 0x1ff00\n"
        "s_nop 0\n"
        "buffer_load_dwordx4 v25, s[20:23], 0 offen offset:256 lds\n"
        "s_add_u32 m0, s11, 0x20300\n"
        "s_nop 0\n"
        "buffer_load_dwordx4 v26, s[20:23], 0 offen offset:256 lds\n"
        "s_add_u32 m0, s11, 0x20700\n"
        "s_nop 0\n"
        "buffer_load_dwordx4 v27, s[20:23], 0 offen offset:256 lds\n"
        "s_cmp_lg_u32 s88, 1\n"
        "s_cbranch_scc1 Lhk40_%=\n"
        "s_mov_b32 m0, 0x25300\n"
        "s_nop 0\n"
        "buffer_load_dwordx4 v28, s[20:23], 0 offen offset:256 lds\n"
        "Lhk40_%=:\n"
        "v_mov_b32 v12, 0xbc000\n"
        "buffer_load_dwordx2 v[38:39], v12, s[24:27], 0 offen sc1\n"
        "ds_read2_b32 v[112:113], v16 offset0:0 offset1:2\n"
        "ds_read2_b32 v[114:115], v16 offset0:4 offset1:6\n"
        "v_add_u32 v16, 0x6400, v16\n"
        "ds_read2_b32 v[116:117], v16 offset0:0 offset1:2\n"
        "ds_read2_b32 v[118:119], v16 offset0:4 offset1:6\n"
        "v_add_u32 v16, 0x6400, v16\n"
        "ds_read2_b32 v[120:121], v16 offset0:0 offset1:2\n"
        "ds_read2_b32 v[122:123], v16 offset0:4 offset1:6\n"
        "s_setprio 3\n"
        "s_waitcnt vmcnt(0)\n"
        "ds_write_b8 v3, v4 offset:28\n"
        "s_cmp_lg_u32 s3, 7\n"
        "s_cbranch_scc1 Lh3_%=\n"
        "s_cmp_lg_u32 s2, 0\n"
        "s_cbranch_scc1 Lh3_%=\n"
        "s_mov_b64 exec, 1\n"
        "buffer_store_dwordx2 v[10:11], v12, s[24:27], 0 offen sc1\n"
        "s_mov_b64 exec, -1\n"
        "Lh3_%=:\n"
        "v_min_u32 v12, 17, v8\n"
        "v_lshlrev_b32 v12, 4, v12\n"
        "s_and_b32 s89, s2, 7\n"
        "s_lshl_b32 s89, s89, 10\n"
        "s_add_u32 s89, s89, 0x12000\n"
        "s_cmp_lg_u32 s3, 5\n"
        "s_cbranch_scc1 Lh7_%=\n"
        "s_sleep 12\n"
        "buffer_load_dwordx4 v[160:163], v12, s[24:27], s89 offen sc1\n"
        "Lh7_%=:\n"
        "s_mov_b32 s86, s91\n"
        "v_mov_b32 v15, 1\n"
        "s_cmp_lg_u32 s86, 0\n"
        "s_cbranch_scc1 Lh6_%=\n"
        "s_cmp_lg_u32 s3, 5\n"
        "s_cbranch_scc1 Lh5_%=\n"
        "s_cmp_lg_u32 s19, 0\n"
        "s_cbranch_scc1 Lh5_%=\n"
        "s_mov_b32 s87, 300\n"
        "Lm2p41_%=:\n"
        "s_waitcnt vmcnt(0)\n"
        "v_cmp_ne_u64 vcc, s[6:7], v[160:161]\n"
        "v_cmp_ne_u64 s[10:11], s[6:7], v[162:163]\n"
        "s_nop 1\n"
        "s_or_b64 vcc, vcc, s[10:11]\n"
        "s_cbranch_scc0 Lh5_%=\n"
        "s_sleep 8\n"
        "buffer_load_dwordx4 v[160:163], v12, s[24:27], s89 offen sc1\n"
        "s_sub_u32 s87, s87, 1\n"
        "s_cbranch_scc0 Lm2p41_%=\n"
        "Lh6_%=:\n"
        "v_mov_b32 v15, 2\n"
        "ds_write_b8 v3, v15 offset:44\n"
        "Lh5_%=:\n"
        "ds_write_b8 v3, v15 offset:56\n"
        "s_waitcnt vmcnt(0)\n"
        "s_setprio 0\n"
        "v_and_b32 v12, 1, v0\n"
        "v_mul_u32_u24 v17, 0xaaaa, v12\n"
        "v_add_u32 v17, 0x4444, v17\n"
        "v_bfe_u32 v15, v0, 2, 2\n"
        "v_mul_u32_u24 v16, 384, v15\n"
        "v_lshl_add_u32 v16, v12, 6, v16\n"
        "v_add_u32 v16, v16, v19\n"
        "v_add_u32 v16, 0x25800, v16\n"
        "v_mul_u32_u24 v13, 24, v19\n"
        "v_lshl_add_u32 v13, v15, 3, v13\n"
        "v_lshl_add_u32 v13, v12, 1, v13\n"
        "v_and_b32 v12, 2, v0\n"
        "v_lshl_add_u32 v13, v12, 5, v13\n"
        "v_add_u32 v13, 0x25800, v13\n"
        "s_and_b32 s86, s3, 2\n"
        "s_lshl_b32 s86, s86, 4\n"
        "s_and_b32 s87, s3, 1\n"
        "s_lshl2_add_u32 s86, s87, s86\n"
        "v_add_u32 v15, s86, v13\n"
        "v_mov_b64 v[124:125], 0\n"
        "v_mov_b64 v[128:129], 0\n"
        "v_mov_b64 v[132:133], 0\n"
        "s_cmp_lg_u32 s3, 4\n"
        "s_cbranch_scc1 Lhm2_%=\n"
        "v_and_b32 v20, 3, v0\n"
        "v_lshlrev_b32 v20, 3, v20\n"
        "v_lshlrev_b32_sdwa v152, v18, v200 dst_sel:DWORD dst_unused:UNUSED_PAD src0_sel:DWORD src1_sel:WORD_0\n"
        "v_xor_b32 v152, v152, v19\n"
        "v_xor_b32 v153, 64, v152\n"
        "v_lshlrev_b32_sdwa v154, v18, v200 dst_sel:DWORD dst_unused:UNUSED_PAD src0_sel:DWORD src1_sel:WORD_1\n"
        "v_xor_b32 v154, v154, v19\n"
        "v_xor_b32 v155, 64, v154\n"
        "v_lshlrev_b32_sdwa v156, v18, v201 dst_sel:DWORD dst_unused:UNUSED_PAD src0_sel:DWORD src1_sel:WORD_0\n"
        "v_xor_b32 v156, v156, v19\n"
        "v_xor_b32 v157, 64, v156\n"
        "v_lshlrev_b32_sdwa v158, v18, v201 dst_sel:DWORD dst_unused:UNUSED_PAD src0_sel:DWORD src1_sel:WORD_1\n"
        "v_xor_b32 v158, v158, v19\n"
        "v_xor_b32 v159, 64, v158\n"
        "v_lshlrev_b32_sdwa v136, v18, v208 dst_sel:DWORD dst_unused:UNUSED_PAD src0_sel:DWORD src1_sel:WORD_0\n"
        "v_or_b32 v136, v136, v20\n"
        "v_xor_b32 v140, 32, v136\n"
        "v_xor_b32 v144, 64, v136\n"
        "v_xor_b32 v148, 96, v136\n"
        "v_lshlrev_b32_sdwa v137, v18, v208 dst_sel:DWORD dst_unused:UNUSED_PAD src0_sel:DWORD src1_sel:WORD_1\n"
        "v_or_b32 v137, v137, v20\n"
        "v_xor_b32 v141, 32, v137\n"
        "v_xor_b32 v145, 64, v137\n"
        "v_xor_b32 v149, 96, v137\n"
        "v_lshlrev_b32_sdwa v138, v18, v209 dst_sel:DWORD dst_unused:UNUSED_PAD src0_sel:DWORD src1_sel:WORD_0\n"
        "v_or_b32 v138, v138, v20\n"
        "v_xor_b32 v142, 32, v138\n"
        "v_xor_b32 v146, 64, v138\n"
        "v_xor_b32 v150, 96, v138\n"
        "v_lshlrev_b32_sdwa v139, v18, v209 dst_sel:DWORD dst_unused:UNUSED_PAD src0_sel:DWORD src1_sel:WORD_1\n"
        "v_or_b32 v139, v139, v20\n"
        "v_xor_b32 v143, 32, v139\n"
        "v_xor_b32 v147, 64, v139\n"
        "v_xor_b32 v151, 96, v139\n"
        "s_setprio 0\n"
        "ds_read_b64_tr_b16 v[160:161], v136 offset:0\n"
        "ds_read_b64_tr_b16 v[162:163], v137 offset:0\n"
        "ds_read_b64_tr_b16 v[164:165], v138 offset:0\n"
        "ds_read_b64_tr_b16 v[166:167], v139 offset:0\n"
        "ds_read_b64_tr_b16 v[168:169], v140 offset:0\n"
        "ds_read_b64_tr_b16 v[170:171], v141 offset:0\n"
        "s_waitcnt lgkmcnt(12)\n"
        "ds_read_b64_tr_b16 v[172:173], v142 offset:0\n"
        "ds_read_b64_tr_b16 v[174:175], v143 offset:0\n"
        "ds_read_b64_tr_b16 v[176:177], v144 offset:0\n"
        "s_waitcnt lgkmcnt(12)\n"
        "ds_read_b64_tr_b16 v[178:179], v145 offset:0\n"
        "ds_read_b64_tr_b16 v[180:181], v146 offset:0\n"
        "ds_read_b64_tr_b16 v[182:183], v147 offset:0\n"
        "s_waitcnt lgkmcnt(12)\n"
        "ds_read_b64_tr_b16 v[184:185], v148 offset:0\n"
        "ds_read_b64_tr_b16 v[186:187], v149 offset:0\n"
        "ds_read_b64_tr_b16 v[188:189], v150 offset:0\n"
        "s_waitcnt lgkmcnt(12)\n"
        "ds_read_b64_tr_b16 v[190:191], v151 offset:0\n"
        "ds_read_b64_tr_b16 v[192:193], v136 offset:25600\n"
        "ds_read_b64_tr_b16 v[194:195], v137 offset:25600\n"
        "s_waitcnt lgkmcnt(12)\n"
        "ds_read_b64_tr_b16 v[196:197], v138 offset:25600\n"
        "ds_read_b64_tr_b16 v[198:199], v139 offset:25600\n"
        "ds_read_b64_tr_b16 v[200:201], v140 offset:25600\n"
        "s_waitcnt lgkmcnt(12)\n"
        "ds_read_b64_tr_b16 v[202:203], v141 offset:25600\n"
        "ds_read_b64_tr_b16 v[204:205], v142 offset:25600\n"
        "ds_read_b64_tr_b16 v[206:207], v143 offset:25600\n"
        "s_waitcnt lgkmcnt(12)\n"
        "ds_read_b64_tr_b16 v[208:209], v144 offset:25600\n"
        "ds_read_b64_tr_b16 v[210:211], v145 offset:25600\n"
        "ds_read_b64_tr_b16 v[212:213], v146 offset:25600\n"
        "s_waitcnt lgkmcnt(12)\n"
        "ds_read_b64_tr_b16 v[214:215], v147 offset:25600\n"
        "ds_read_b64_tr_b16 v[216:217], v148 offset:25600\n"
        "ds_read_b64_tr_b16 v[218:219], v149 offset:25600\n"
        "s_waitcnt lgkmcnt(12)\n"
        "ds_read_b64_tr_b16 v[220:221], v150 offset:25600\n"
        "ds_read_b64_tr_b16 v[222:223], v151 offset:25600\n"
        "ds_read_b64_tr_b16 v[224:225], v136 offset:51200\n"
        "s_waitcnt lgkmcnt(12)\n"
        "ds_read_b64_tr_b16 v[226:227], v137 offset:51200\n"
        "ds_read_b64_tr_b16 v[228:229], v138 offset:51200\n"
        "ds_read_b64_tr_b16 v[230:231], v139 offset:51200\n"
        "s_waitcnt lgkmcnt(12)\n"
        "ds_read_b64_tr_b16 v[232:233], v140 offset:51200\n"
        "ds_read_b64_tr_b16 v[234:235], v141 offset:51200\n"
        "ds_read_b64_tr_b16 v[236:237], v142 offset:51200\n"
        "s_waitcnt lgkmcnt(12)\n"
        "ds_read_b64_tr_b16 v[238:239], v143 offset:51200\n"
        "ds_read_b64_tr_b16 v[240:241], v144 offset:51200\n"
        "ds_read_b64_tr_b16 v[242:243], v145 offset:51200\n"
        "s_waitcnt lgkmcnt(12)\n"
        "ds_read_b64_tr_b16 v[244:245], v146 offset:51200\n"
        "ds_read_b64_tr_b16 v[246:247], v147 offset:51200\n"
        "ds_read_b64_tr_b16 v[248:249], v148 offset:51200\n"
        "s_waitcnt lgkmcnt(12)\n"
        "ds_read_b64_tr_b16 v[250:251], v149 offset:51200\n"
        "ds_read_b64_tr_b16 v[252:253], v150 offset:51200\n"
        "ds_read_b64_tr_b16 v[254:255], v151 offset:51200\n"
        "v_mov_b32 v12, 0xf149f2ca\n"
        "v_cndmask_b32 v136, 0, v12, s[40:41]\n"
        "v_mov_b32 v137, 0\n"
        "v_cndmask_b32 v140, 0, v12, s[42:43]\n"
        "v_mov_b32 v141, 0\n"
        "v_cndmask_b32 v144, 0, v12, s[44:45]\n"
        "v_mov_b32 v145, 0\n"
        "v_cndmask_b32 v148, 0, v12, s[46:47]\n"
        "v_mov_b32 v149, 0\n"
        "s_setprio 3\n"
        "s_sleep 36\n"
        "s_mov_b32 s87, 0x200000\n"
        "Lpl43_%=:\n"
        "ds_read_b32 v5, v2 offset:56\n"
        "s_waitcnt lgkmcnt(0)\n"
        "v_readfirstlane_b32 s86, v5\n"
        "s_cmp_eq_u32 s86, 0x1010101\n"
        "s_cbranch_scc1 Lrt42_%=\n"
        "s_and_b32 s86, s86, 0x02020202\n"
        "s_cbranch_scc1 Lfallback_%=\n"
        "s_sub_u32 s87, s87, 1\n"
        "s_cbranch_scc1 Lrt42_%=\n"
        "s_sleep 4\n"
        "s_branch Lpl43_%=\n"
        "Lrt42_%=:\n"
        "s_mov_b32 s87, 0x200000\n"
        "Lpl45_%=:\n"
        "ds_read_b32 v5, v2 offset:40\n"
        "s_waitcnt lgkmcnt(0)\n"
        "v_readfirstlane_b32 s86, v5\n"
        "s_cmp_eq_u32 s86, 0x1010101\n"
        "s_cbranch_scc1 Lrt44_%=\n"
        "s_sub_u32 s87, s87, 1\n"
        "s_cbranch_scc1 Lrt44_%=\n"
        "s_sleep 3\n"
        "s_branch Lpl45_%=\n"
        "Lrt44_%=:\n"
        "v_smfmac_f32_16x16x64_bf16 v[124:127], v[112:115], v[40:47], v17\n"
        "v_smfmac_f32_16x16x64_bf16 v[124:127], v[116:119], v[64:71], v17\n"
        "v_smfmac_f32_16x16x64_bf16 v[124:127], v[120:123], v[88:95], v17\n"
        "v_smfmac_f32_16x16x64_bf16 v[128:131], v[112:115], v[48:55], v17\n"
        "v_smfmac_f32_16x16x64_bf16 v[128:131], v[116:119], v[72:79], v17\n"
        "v_smfmac_f32_16x16x64_bf16 v[128:131], v[120:123], v[96:103], v17\n"
        "v_smfmac_f32_16x16x64_bf16 v[132:135], v[112:115], v[56:63], v17\n"
        "v_smfmac_f32_16x16x64_bf16 v[132:135], v[116:119], v[80:87], v17\n"
        "v_smfmac_f32_16x16x64_bf16 v[132:135], v[120:123], v[104:111], v17\n"
        "s_nop 1\n"
        "v_add_f32 v124, v124, v125\n"
        "s_nop 1\n"
        "v_add_f32 v128, v128, v129\n"
        "s_nop 1\n"
        "v_add_f32 v132, v132, v133\n"
        "v_cvt_pk_bf16_f32 v124, v124, v128\n"
        "v_cvt_pk_bf16_f32 v132, v132, v132\n"
        "ds_write_b16 v15, v124\n"
        "ds_write_b16_d16_hi v15, v124 offset:128\n"
        "ds_write_b16 v15, v132 offset:256\n"
        "ds_write_b8 v3, v4 offset:60\n"
        "ds_read_b128 v[40:43], v152 offset:0\n"
        "ds_read_b128 v[44:47], v153 offset:0\n"
        "ds_read_b128 v[48:51], v152 offset:25600\n"
        "ds_read_b128 v[52:55], v153 offset:25600\n"
        "ds_read_b128 v[56:59], v152 offset:51200\n"
        "ds_read_b128 v[60:63], v153 offset:51200\n"
        "ds_read_b128 v[64:67], v154 offset:0\n"
        "ds_read_b128 v[68:71], v155 offset:0\n"
        "ds_read_b128 v[72:75], v154 offset:25600\n"
        "ds_read_b128 v[76:79], v155 offset:25600\n"
        "ds_read_b128 v[80:83], v154 offset:51200\n"
        "s_waitcnt lgkmcnt(12)\n"
        "ds_read_b128 v[84:87], v155 offset:51200\n"
        "ds_read_b128 v[88:91], v156 offset:0\n"
        "ds_read_b128 v[92:95], v157 offset:0\n"
        "s_waitcnt lgkmcnt(12)\n"
        "ds_read_b128 v[96:99], v156 offset:25600\n"
        "ds_read_b128 v[100:103], v157 offset:25600\n"
        "ds_read_b128 v[104:107], v156 offset:51200\n"
        "s_waitcnt lgkmcnt(12)\n"
        "ds_read_b128 v[108:111], v157 offset:51200\n"
        "ds_read_b128 v[112:115], v158 offset:0\n"
        "ds_read_b128 v[116:119], v159 offset:0\n"
        "s_waitcnt lgkmcnt(12)\n"
        "ds_read_b128 v[120:123], v158 offset:25600\n"
        "ds_read_b128 v[124:127], v159 offset:25600\n"
        "ds_read_b128 v[128:131], v158 offset:51200\n"
        "s_waitcnt lgkmcnt(12)\n"
        "ds_read_b128 v[132:135], v159 offset:51200\n"
        "s_mov_b32 s87, 0x200000\n"
        "Lpl47_%=:\n"
        "ds_read_b32 v5, v2 offset:60\n"
        "s_waitcnt lgkmcnt(0)\n"
        "v_readfirstlane_b32 s86, v5\n"
        "s_cmp_eq_u32 s86, 0x1010101\n"
        "s_cbranch_scc1 Lrt46_%=\n"
        "s_sub_u32 s87, s87, 1\n"
        "s_cbranch_scc1 Lrt46_%=\n"
        "s_sleep 1\n"
        "s_branch Lpl47_%=\n"
        "Lrt46_%=:\n"
        "ds_read_b128 v[22:25], v16 offset:0\n"
        "ds_read_b128 v[26:29], v16 offset:128\n"
        "ds_read_b128 v[30:33], v16 offset:256\n"
        "s_waitcnt lgkmcnt(2)\n"
        "v_smfmac_f32_16x16x64_bf16 v[136:139], v[22:25], v[40:47], v17\n"
        "s_waitcnt lgkmcnt(1)\n"
        "v_smfmac_f32_16x16x64_bf16 v[136:139], v[26:29], v[48:55], v17\n"
        "s_waitcnt lgkmcnt(0)\n"
        "v_smfmac_f32_16x16x64_bf16 v[136:139], v[30:33], v[56:63], v17\n"
        "v_smfmac_f32_16x16x64_bf16 v[140:143], v[22:25], v[64:71], v17\n"
        "v_smfmac_f32_16x16x64_bf16 v[140:143], v[26:29], v[72:79], v17\n"
        "v_smfmac_f32_16x16x64_bf16 v[140:143], v[30:33], v[80:87], v17\n"
        "v_smfmac_f32_16x16x64_bf16 v[144:147], v[22:25], v[88:95], v17\n"
        "v_smfmac_f32_16x16x64_bf16 v[144:147], v[26:29], v[96:103], v17\n"
        "v_smfmac_f32_16x16x64_bf16 v[144:147], v[30:33], v[104:111], v17\n"
        "v_smfmac_f32_16x16x64_bf16 v[148:151], v[22:25], v[112:119], v17\n"
        "v_smfmac_f32_16x16x64_bf16 v[148:151], v[26:29], v[120:127], v17\n"
        "v_smfmac_f32_16x16x64_bf16 v[148:151], v[30:33], v[128:135], v17\n"
        "s_setprio 0\n"
        "v_mov_b64 v[40:41], 0\n"
        "v_mov_b64 v[44:45], 0\n"
        "v_mov_b64 v[48:49], 0\n"
        "v_mov_b64 v[52:53], 0\n"
        "v_mov_b64 v[56:57], 0\n"
        "v_mov_b64 v[60:61], 0\n"
        "v_mov_b64 v[64:65], 0\n"
        "v_mov_b64 v[68:69], 0\n"
        "v_add_f32 v136, v136, v137\n"
        "v_add_f32 v140, v140, v141\n"
        "v_add_f32 v144, v144, v145\n"
        "v_add_f32 v148, v148, v149\n"
        "v_max3_f32 v10, v136, v140, v144\n"
        "v_max_f32 v10, v10, v148\n"
        "v_mov_b64 v[72:73], 0\n"
        "v_mov_b64 v[76:77], 0\n"
        "v_max_f32_dpp v10, v10, v10 row_ror:8 row_mask:0xf bank_mask:0xf\n"
        "v_mov_b64 v[80:81], 0\n"
        "v_mov_b64 v[84:85], 0\n"
        "v_max_f32_dpp v10, v10, v10 row_ror:4 row_mask:0xf bank_mask:0xf\n"
        "s_nop 1\n"
        "v_max_f32_dpp v10, v10, v10 row_ror:2 row_mask:0xf bank_mask:0xf\n"
        "s_nop 1\n"
        "v_max_f32_dpp v10, v10, v10 row_ror:1 row_mask:0xf bank_mask:0xf\n"
        "v_sub_f32 v136, v136, v10\n"
        "v_sub_f32 v140, v140, v10\n"
        "v_sub_f32 v144, v144, v10\n"
        "v_sub_f32 v148, v148, v10\n"
        "v_exp_f32 v136, v136\n"
        "v_exp_f32 v140, v140\n"
        "v_exp_f32 v144, v144\n"
        "v_exp_f32 v148, v148\n"
        "v_add_f32 v10, v136, v140\n"
        "v_add_f32 v11, v144, v148\n"
        "v_add_f32 v10, v10, v11\n"
        "s_nop 1\n"
        "v_add_f32_dpp v10, v10, v10 row_ror:8 row_mask:0xf bank_mask:0xf\n"
        "s_nop 1\n"
        "v_add_f32_dpp v10, v10, v10 row_ror:4 row_mask:0xf bank_mask:0xf\n"
        "s_nop 1\n"
        "v_add_f32_dpp v10, v10, v10 row_ror:2 row_mask:0xf bank_mask:0xf\n"
        "s_nop 1\n"
        "v_add_f32_dpp v10, v10, v10 row_ror:1 row_mask:0xf bank_mask:0xf\n"
        "v_rcp_f32 v10, v10\n"
        "s_nop 0\n"
        "v_mul_f32 v136, v136, v10\n"
        "v_mul_f32 v140, v140, v10\n"
        "v_mul_f32 v144, v144, v10\n"
        "v_mul_f32 v148, v148, v10\n"
        "v_cvt_pk_bf16_f32 v10, v136, v140\n"
        "v_cvt_pk_bf16_f32 v11, v144, v148\n"
        "ds_write_b16 v13, v10 offset:2048\n"
        "ds_write_b16_d16_hi v13, v10 offset:2052\n"
        "ds_write_b16 v13, v11 offset:2080\n"
        "ds_write_b16_d16_hi v13, v11 offset:2084\n"
        "ds_read_b128 v[22:25], v16 offset:2048\n"
        "s_setprio 3\n"
        "s_waitcnt lgkmcnt(0)\n"
        "v_smfmac_f32_16x16x64_bf16 v[40:43], v[22:25], v[160:167], v17\n"
        "v_smfmac_f32_16x16x64_bf16 v[44:47], v[22:25], v[168:175], v17\n"
        "v_smfmac_f32_16x16x64_bf16 v[48:51], v[22:25], v[176:183], v17\n"
        "v_smfmac_f32_16x16x64_bf16 v[52:55], v[22:25], v[184:191], v17\n"
        "v_smfmac_f32_16x16x64_bf16 v[56:59], v[22:25], v[192:199], v17\n"
        "v_smfmac_f32_16x16x64_bf16 v[60:63], v[22:25], v[200:207], v17\n"
        "v_smfmac_f32_16x16x64_bf16 v[64:67], v[22:25], v[208:215], v17\n"
        "v_smfmac_f32_16x16x64_bf16 v[68:71], v[22:25], v[216:223], v17\n"
        "v_smfmac_f32_16x16x64_bf16 v[72:75], v[22:25], v[224:231], v17\n"
        "v_smfmac_f32_16x16x64_bf16 v[76:79], v[22:25], v[232:239], v17\n"
        "v_smfmac_f32_16x16x64_bf16 v[80:83], v[22:25], v[240:247], v17\n"
        "v_smfmac_f32_16x16x64_bf16 v[84:87], v[22:25], v[248:255], v17\n"
        "s_setprio 0\n"
        "v_add_f32 v40, v40, v41\n"
        "v_add_f32 v44, v44, v45\n"
        "v_add_f32 v48, v48, v49\n"
        "v_add_f32 v52, v52, v53\n"
        "v_add_f32 v56, v56, v57\n"
        "v_add_f32 v60, v60, v61\n"
        "v_add_f32 v64, v64, v65\n"
        "v_add_f32 v68, v68, v69\n"
        "v_add_f32 v72, v72, v73\n"
        "v_add_f32 v76, v76, v77\n"
        "v_add_f32 v80, v80, v81\n"
        "v_add_f32 v84, v84, v85\n"
        "v_cvt_pk_bf16_f32 v40, v40, v44\n"
        "v_cvt_pk_bf16_f32 v48, v48, v52\n"
        "v_cvt_pk_bf16_f32 v56, v56, v60\n"
        "v_cvt_pk_bf16_f32 v64, v64, v68\n"
        "v_cvt_pk_bf16_f32 v72, v72, v76\n"
        "v_cvt_pk_bf16_f32 v80, v80, v84\n"
        "ds_write_b16 v13, v40 offset:4096\n"
        "ds_write_b16_d16_hi v13, v40 offset:4100\n"
        "ds_write_b16 v13, v48 offset:4128\n"
        "ds_write_b16_d16_hi v13, v48 offset:4132\n"
        "ds_write_b16 v13, v56 offset:4224\n"
        "ds_write_b16_d16_hi v13, v56 offset:4228\n"
        "ds_write_b16 v13, v64 offset:4256\n"
        "ds_write_b16_d16_hi v13, v64 offset:4260\n"
        "ds_write_b16 v13, v72 offset:4352\n"
        "ds_write_b16_d16_hi v13, v72 offset:4356\n"
        "ds_write_b16 v13, v80 offset:4384\n"
        "ds_write_b16_d16_hi v13, v80 offset:4388\n"
        "ds_write_b8 v2, v4 offset:64\n"
        "s_branch Lhend_%=\n"
        "Lhm2_%=:\n"
        "v_mov_b64 v[136:137], 0\n"
        "v_mov_b64 v[140:141], 0\n"
        "v_mov_b64 v[144:145], 0\n"
        "v_mov_b64 v[148:149], 0\n"
        "s_setprio 3\n"
        "s_sleep 10\n"
        "s_mov_b32 s87, 0x200000\n"
        "Lpl49_%=:\n"
        "ds_read_b32 v5, v2 offset:56\n"
        "s_waitcnt lgkmcnt(0)\n"
        "v_readfirstlane_b32 s86, v5\n"
        "s_cmp_eq_u32 s86, 0x1010101\n"
        "s_cbranch_scc1 Lrt48_%=\n"
        "s_and_b32 s86, s86, 0x02020202\n"
        "s_cbranch_scc1 Lfallback_%=\n"
        "s_sub_u32 s87, s87, 1\n"
        "s_cbranch_scc1 Lrt48_%=\n"
        "s_sleep 2\n"
        "s_branch Lpl49_%=\n"
        "Lrt48_%=:\n"
        "s_sub_u32 s88, s88, 1\n"
        "s_lshl_b32 s11, s88, 13\n"
        "s_add_u32 s10, s11, s19\n"
        "v_add_u32 v14, s10, v1\n"
        "v_lshlrev_b32 v12, 3, v1\n"
        "s_add_u32 s87, s10, 0\n"
        "buffer_load_dword v244, v12, s[24:27], s87 offen sc1\n"
        "s_mov_b32 s87, 0x200000\n"
        "Lpl51_%=:\n"
        "ds_read_b32 v5, v2 offset:32\n"
        "s_waitcnt lgkmcnt(0)\n"
        "v_readfirstlane_b32 s86, v5\n"
        "s_cmp_eq_u32 s86, 0x1010101\n"
        "s_cbranch_scc1 Lrt50_%=\n"
        "s_sub_u32 s87, s87, 1\n"
        "s_cbranch_scc1 Lrt50_%=\n"
        "s_sleep 2\n"
        "s_branch Lpl51_%=\n"
        "Lrt50_%=:\n"
        "s_mov_b32 s87, 0x0\n"
        "s_add_u32 m0, s11, 0x12c00\n"
        "s_nop 0\n"
        "buffer_load_dwordx4 v14, s[24:27], s87 offen offset:0 sc1 lds\n"
        "s_add_u32 m0, s11, 0x12c00\n"
        "s_nop 0\n"
        "buffer_load_dwordx4 v14, s[24:27], s87 offen offset:1024 sc1 lds\n"
        "s_add_u32 m0, s11, 0x12c00\n"
        "s_nop 0\n"
        "buffer_load_dwordx4 v14, s[24:27], s87 offen offset:2048 sc1 lds\n"
        "s_add_u32 m0, s11, 0x12c00\n"
        "s_nop 0\n"
        "buffer_load_dwordx4 v14, s[24:27], s87 offen offset:3072 sc1 lds\n"
        "s_mov_b32 s87, 0x1000\n"
        "s_add_u32 m0, s11, 0x13c00\n"
        "s_nop 0\n"
        "buffer_load_dwordx4 v14, s[24:27], s87 offen offset:0 sc1 lds\n"
        "s_add_u32 m0, s11, 0x13c00\n"
        "s_nop 0\n"
        "buffer_load_dwordx4 v14, s[24:27], s87 offen offset:1024 sc1 lds\n"
        "s_add_u32 m0, s11, 0x13c00\n"
        "s_nop 0\n"
        "buffer_load_dwordx4 v14, s[24:27], s87 offen offset:2048 sc1 lds\n"
        "s_add_u32 m0, s11, 0x13c00\n"
        "s_nop 0\n"
        "buffer_load_dwordx4 v14, s[24:27], s87 offen offset:3072 sc1 lds\n"
        "s_waitcnt vmcnt(0)\n"
        "ds_write_b8 v3, v4 offset:44\n"
        "s_mov_b32 s87, 0x200000\n"
        "Lpl53_%=:\n"
        "ds_read_b32 v5, v2 offset:36\n"
        "s_waitcnt lgkmcnt(0)\n"
        "v_readfirstlane_b32 s86, v5\n"
        "s_cmp_eq_u32 s86, 0x1010101\n"
        "s_cbranch_scc1 Lrt52_%=\n"
        "s_sub_u32 s87, s87, 1\n"
        "s_cbranch_scc1 Lrt52_%=\n"
        "s_sleep 2\n"
        "s_branch Lpl53_%=\n"
        "Lrt52_%=:\n"
        "s_mov_b32 s87, 0x6000\n"
        "s_add_u32 m0, s11, 0x19000\n"
        "s_nop 0\n"
        "buffer_load_dwordx4 v14, s[24:27], s87 offen offset:0 sc1 lds\n"
        "s_add_u32 m0, s11, 0x19000\n"
        "s_nop 0\n"
        "buffer_load_dwordx4 v14, s[24:27], s87 offen offset:1024 sc1 lds\n"
        "s_add_u32 m0, s11, 0x19000\n"
        "s_nop 0\n"
        "buffer_load_dwordx4 v14, s[24:27], s87 offen offset:2048 sc1 lds\n"
        "s_add_u32 m0, s11, 0x19000\n"
        "s_nop 0\n"
        "buffer_load_dwordx4 v14, s[24:27], s87 offen offset:3072 sc1 lds\n"
        "s_mov_b32 s87, 0x7000\n"
        "s_add_u32 m0, s11, 0x1a000\n"
        "s_nop 0\n"
        "buffer_load_dwordx4 v14, s[24:27], s87 offen offset:0 sc1 lds\n"
        "s_add_u32 m0, s11, 0x1a000\n"
        "s_nop 0\n"
        "buffer_load_dwordx4 v14, s[24:27], s87 offen offset:1024 sc1 lds\n"
        "s_add_u32 m0, s11, 0x1a000\n"
        "s_nop 0\n"
        "buffer_load_dwordx4 v14, s[24:27], s87 offen offset:2048 sc1 lds\n"
        "s_add_u32 m0, s11, 0x1a000\n"
        "s_nop 0\n"
        "buffer_load_dwordx4 v14, s[24:27], s87 offen offset:3072 sc1 lds\n"
        "s_waitcnt vmcnt(0)\n"
        "ds_write_b8 v3, v4 offset:48\n"
        "s_mov_b32 s87, 0x200000\n"
        "Lpl55_%=:\n"
        "ds_read_b32 v5, v2 offset:40\n"
        "s_waitcnt lgkmcnt(0)\n"
        "v_readfirstlane_b32 s86, v5\n"
        "s_cmp_eq_u32 s86, 0x1010101\n"
        "s_cbranch_scc1 Lrt54_%=\n"
        "s_sub_u32 s87, s87, 1\n"
        "s_cbranch_scc1 Lrt54_%=\n"
        "s_sleep 2\n"
        "s_branch Lpl55_%=\n"
        "Lrt54_%=:\n"
        "s_mov_b32 s87, 0xc000\n"
        "s_add_u32 m0, s11, 0x1f400\n"
        "s_nop 0\n"
        "buffer_load_dwordx4 v14, s[24:27], s87 offen offset:0 sc1 lds\n"
        "s_add_u32 m0, s11, 0x1f400\n"
        "s_nop 0\n"
        "buffer_load_dwordx4 v14, s[24:27], s87 offen offset:1024 sc1 lds\n"
        "s_add_u32 m0, s11, 0x1f400\n"
        "s_nop 0\n"
        "buffer_load_dwordx4 v14, s[24:27], s87 offen offset:2048 sc1 lds\n"
        "s_add_u32 m0, s11, 0x1f400\n"
        "s_nop 0\n"
        "buffer_load_dwordx4 v14, s[24:27], s87 offen offset:3072 sc1 lds\n"
        "s_mov_b32 s87, 0xd000\n"
        "s_add_u32 m0, s11, 0x20400\n"
        "s_nop 0\n"
        "buffer_load_dwordx4 v14, s[24:27], s87 offen offset:0 sc1 lds\n"
        "s_add_u32 m0, s11, 0x20400\n"
        "s_nop 0\n"
        "buffer_load_dwordx4 v14, s[24:27], s87 offen offset:1024 sc1 lds\n"
        "s_add_u32 m0, s11, 0x20400\n"
        "s_nop 0\n"
        "buffer_load_dwordx4 v14, s[24:27], s87 offen offset:2048 sc1 lds\n"
        "s_add_u32 m0, s11, 0x20400\n"
        "s_nop 0\n"
        "buffer_load_dwordx4 v14, s[24:27], s87 offen offset:3072 sc1 lds\n"
        "v_smfmac_f32_16x16x64_bf16 v[124:127], v[112:115], v[40:47], v17\n"
        "v_smfmac_f32_16x16x64_bf16 v[124:127], v[116:119], v[64:71], v17\n"
        "v_smfmac_f32_16x16x64_bf16 v[124:127], v[120:123], v[88:95], v17\n"
        "v_smfmac_f32_16x16x64_bf16 v[128:131], v[112:115], v[48:55], v17\n"
        "v_smfmac_f32_16x16x64_bf16 v[128:131], v[116:119], v[72:79], v17\n"
        "v_smfmac_f32_16x16x64_bf16 v[128:131], v[120:123], v[96:103], v17\n"
        "v_smfmac_f32_16x16x64_bf16 v[132:135], v[112:115], v[56:63], v17\n"
        "v_smfmac_f32_16x16x64_bf16 v[132:135], v[116:119], v[80:87], v17\n"
        "v_smfmac_f32_16x16x64_bf16 v[132:135], v[120:123], v[104:111], v17\n"
        "s_nop 1\n"
        "v_add_f32 v124, v124, v125\n"
        "s_nop 1\n"
        "v_add_f32 v128, v128, v129\n"
        "s_nop 1\n"
        "v_add_f32 v132, v132, v133\n"
        "v_cvt_pk_bf16_f32 v124, v124, v128\n"
        "v_cvt_pk_bf16_f32 v132, v132, v132\n"
        "ds_write_b16 v15, v124\n"
        "ds_write_b16_d16_hi v15, v124 offset:128\n"
        "ds_write_b16 v15, v132 offset:256\n"
        "ds_write_b8 v3, v4 offset:60\n"
        "s_waitcnt vmcnt(0)\n"
        "ds_write_b8 v3, v4 offset:52\n"
        "s_cmp_lg_u32 s3, 7\n"
        "s_cbranch_scc1 Lhcx_%=\n"
        "v_cmp_eq_u64 vcc, s[6:7], v[38:39]\n"
        "s_cmp_eq_u32 s2, 0\n"
        "s_cbranch_scc1 Lhcl_%=\n"
        "s_nop 4\n"
        "s_cbranch_vccz Lhcx_%=\n"
        "Lhcl_%=:\n"
        "s_mov_b32 s86, 0xc4000\n"
        "buffer_load_dwordx4 v[160:163], v1, s[24:27], s86 offen sc1\n"
        "buffer_load_dwordx4 v[164:167], v1, s[24:27], s86 offen offset:1024 sc1\n"
        "s_waitcnt vmcnt(0)\n"
        "v_cmp_ne_u64 vcc, s[6:7], v[160:161]\n"
        "v_cmp_ne_u64 s[10:11], s[6:7], v[162:163]\n"
        "v_cmp_ne_u64 s[40:41], s[6:7], v[164:165]\n"
        "v_cmp_ne_u64 s[42:43], s[6:7], v[166:167]\n"
        "s_nop 1\n"
        "s_or_b64 vcc, vcc, s[10:11]\n"
        "s_or_b64 vcc, vcc, s[40:41]\n"
        "s_or_b64 vcc, vcc, s[42:43]\n"
        "s_cbranch_scc1 Lhcx_%=\n"
        "v_mov_b32 v12, 0xb4000\n"
        "s_mov_b64 exec, 1\n"
        "buffer_store_dwordx2 v[10:11], v12, s[24:27], 0 offen sc1\n"
        "s_mov_b64 exec, -1\n"
        "Lhcx_%=:\n"
        "s_mul_i32 s89, s88, 25600\n"
        "v_add_u32 v14, s89, v1\n"
        "v_add_u32 v14, 0x12c00, v14\n"
        "s_lshl_b32 s89, s88, 7\n"
        "s_setprio 0\n"
        "s_mov_b32 s87, 0x200000\n"
        "Lpl57_%=:\n"
        "ds_read_b32 v5, v2 offset:52\n"
        "s_waitcnt lgkmcnt(0)\n"
        "v_readfirstlane_b32 s86, v5\n"
        "s_cmp_eq_u32 s86, 0x1010100\n"
        "s_cbranch_scc1 Lrt56_%=\n"
        "s_sub_u32 s87, s87, 1\n"
        "s_cbranch_scc1 Lrt56_%=\n"
        "s_sleep 1\n"
        "s_branch Lpl57_%=\n"
        "Lrt56_%=:\n"
        "ds_read_b128 v[40:43], v14 offset:0\n"
        "ds_read_b128 v[44:47], v14 offset:1024\n"
        "ds_read_b128 v[48:51], v14 offset:2048\n"
        "ds_read_b128 v[52:55], v14 offset:3072\n"
        "ds_read_b128 v[56:59], v14 offset:4096\n"
        "ds_read_b128 v[60:63], v14 offset:5120\n"
        "ds_read_b128 v[64:67], v14 offset:6144\n"
        "ds_read_b128 v[68:71], v14 offset:7168\n"
        "ds_read_b128 v[72:75], v14 offset:8192\n"
        "ds_read_b128 v[76:79], v14 offset:9216\n"
        "ds_read_b128 v[80:83], v14 offset:10240\n"
        "ds_read_b128 v[84:87], v14 offset:11264\n"
        "ds_read_b128 v[88:91], v14 offset:12288\n"
        "ds_read_b128 v[92:95], v14 offset:13312\n"
        "ds_read_b128 v[96:99], v14 offset:14336\n"
        "s_waitcnt lgkmcnt(12)\n"
        "ds_read_b128 v[100:103], v14 offset:15360\n"
        "ds_read_b128 v[104:107], v14 offset:16384\n"
        "ds_read_b128 v[108:111], v14 offset:17408\n"
        "s_waitcnt lgkmcnt(12)\n"
        "ds_read_b128 v[112:115], v14 offset:18432\n"
        "ds_read_b128 v[116:119], v14 offset:19456\n"
        "ds_read_b128 v[120:123], v14 offset:20480\n"
        "s_waitcnt lgkmcnt(12)\n"
        "ds_read_b128 v[124:127], v14 offset:21504\n"
        "ds_read_b128 v[128:131], v14 offset:22528\n"
        "ds_read_b128 v[132:135], v14 offset:23552\n"
        "s_mov_b32 s87, 0x200000\n"
        "Lpl59_%=:\n"
        "ds_read_b32 v5, v2 offset:64\n"
        "s_waitcnt lgkmcnt(0)\n"
        "v_readfirstlane_b32 s86, v5\n"
        "s_cmp_eq_u32 s86, 0x1\n"
        "s_cbranch_scc1 Lrt58_%=\n"
        "s_sub_u32 s87, s87, 1\n"
        "s_cbranch_scc1 Lrt58_%=\n"
        "s_sleep 2\n"
        "s_branch Lpl59_%=\n"
        "Lrt58_%=:\n"
        "s_setprio 3\n"
        "ds_read_b128 v[160:163], v16 offset:4096\n"
        "ds_read_b128 v[164:167], v16 offset:4224\n"
        "ds_read_b128 v[168:171], v16 offset:4352\n"
        "s_waitcnt lgkmcnt(2)\n"
        "v_smfmac_f32_16x16x64_bf16 v[136:139], v[160:163], v[40:47], v17\n"
        "s_waitcnt lgkmcnt(1)\n"
        "v_smfmac_f32_16x16x64_bf16 v[136:139], v[164:167], v[48:55], v17\n"
        "s_waitcnt lgkmcnt(0)\n"
        "v_smfmac_f32_16x16x64_bf16 v[136:139], v[168:171], v[56:63], v17\n"
        "v_smfmac_f32_16x16x64_bf16 v[140:143], v[160:163], v[64:71], v17\n"
        "v_smfmac_f32_16x16x64_bf16 v[140:143], v[164:167], v[72:79], v17\n"
        "v_smfmac_f32_16x16x64_bf16 v[140:143], v[168:171], v[80:87], v17\n"
        "v_smfmac_f32_16x16x64_bf16 v[144:147], v[160:163], v[88:95], v17\n"
        "v_smfmac_f32_16x16x64_bf16 v[144:147], v[164:167], v[96:103], v17\n"
        "v_smfmac_f32_16x16x64_bf16 v[144:147], v[168:171], v[104:111], v17\n"
        "v_smfmac_f32_16x16x64_bf16 v[148:151], v[160:163], v[112:119], v17\n"
        "v_smfmac_f32_16x16x64_bf16 v[148:151], v[164:167], v[120:127], v17\n"
        "v_smfmac_f32_16x16x64_bf16 v[148:151], v[168:171], v[128:135], v17\n"
        "v_add_f32 v136, v136, v137\n"
        "s_nop 0\n"
        "v_add_f32 v140, v140, v141\n"
        "s_nop 1\n"
        "v_add_f32 v144, v144, v145\n"
        "s_nop 1\n"
        "v_add_f32 v148, v148, v149\n"
        "v_cvt_pk_bf16_f32 v16, v136, v140\n"
        "v_cvt_pk_bf16_f32 v17, v144, v148\n"
        "buffer_store_dwordx2 v[16:17], v29, s[28:31], s89 offen sc1\n"
        "Lhend_%=:\n"
        "s_endpgm\n"
        "Lpriv_%=:\n"
        "s_mov_b32 s40, -1\n"
        "s_cmp_lg_u32 s3, 4\n"
        "s_cbranch_scc1 Lp2e_%=\n"
        "s_branch Lpnx_%=\n"
        "Lprn_%=:\n"
        "s_waitcnt vmcnt(0)\n"
        "s_add_u32 s80, s80, 1\n"
        "s_cmp_lt_u32 s80, 3\n"
        "s_cbranch_scc1 Lprl_%=\n"
        "s_branch Lpnx_%=\n"
        "Lp2_%=:\n"
        "buffer_store_dwordx4 v[232:235], v140, s[24:27], s19 offen offset:0 sc1\n"
        "buffer_store_dwordx4 v[236:239], v140, s[24:27], s19 offen offset:64 sc1\n"
        "s_waitcnt vmcnt(0)\n"
        "Lpnx_%=:\n"
        "s_add_u32 s40, s40, 1\n"
        "s_cmp_lt_u32 s40, 24\n"
        "s_cselect_b32 s47, 3, 2\n"
        "v_bfe_u32 v10, v0, 3, 2\n"
        "v_bfe_u32 v11, v0, 5, 1\n"
        "v_lshl_add_u32 v10, v11, s47, v10\n"
        "s_lshl_b32 s47, 2304, s47\n"
        "s_lshr_b32 s42, s40, 2\n"
        "v_lshrrev_b32 v11, 3, v0\n"
        "s_mul_i32 s42, s42, 36864\n"
        "v_and_b32 v11, 6, v11\n"
        "s_bfe_u32 s44, s40, 0x10001\n"
        "v_xor_b32 v11, v11, v0\n"
        "s_mul_i32 s44, s44, 36864\n"
        "v_and_b32 v11, 7, v11\n"
        "s_bitcmp1_b32 s40, 0\n"
        "v_mul_u32_u24 v10, 1152, v10\n"
        "s_cselect_b32 s45, 12, 0\n"
        "v_lshl_add_u32 v10, v11, 4, v10\n"
        "s_add_u32 s45, s45, 1\n"
        "v_add_u32 v22, s47, v10\n"
        "s_mul_i32 s45, s45, 384\n"
        "v_mov_b32 v12, v10\n"
        "s_add_u32 s44, s44, s45\n"
        "v_mov_b32 v13, v22\n"
        "s_add_u32 s43, s42, 4608\n"
        "s_add_u32 s45, s44, 73728\n"
        "s_add_u32 s46, s44, 147456\n"
        "s_mov_b32 s47, 0\n"
        "s_cmp_lt_u32 s40, 24\n"
        "s_cbranch_scc1 Lpa60_%=\n"
        "s_mul_i32 s48, s40, 43\n"
        "v_bfe_u32 v12, v0, 2, 4\n"
        "s_lshr_b32 s48, s48, 8\n"
        "v_and_b32 v14, 3, v0\n"
        "s_mul_i32 s49, s48, 6\n"
        "v_mul_u32_u24 v12, 384, v12\n"
        "s_sub_u32 s49, s40, s49\n"
        "v_lshl_add_u32 v12, v14, 4, v12\n"
        "s_sub_u32 s48, s48, 4\n"
        "v_add_u32 v13, 6144, v12\n"
        "s_mul_i32 s42, s49, 36864\n"
        "s_lshl_b32 s44, s48, 6\n"
        "s_add_u32 s42, s42, 768\n"
        "s_add_u32 s45, s44, 12288\n"
        "s_add_u32 s43, s42, 18432\n"
        "s_mov_b32 s47, 24448\n"
        "Lpa60_%=:\n"
        "s_cmp_lt_u32 s40, 60\n"
        "s_cbranch_scc1 Lp1_%=\n"
        "Lp2e_%=:\n"
        "s_waitcnt vmcnt(0)\n"
        "s_barrier\n"
        "s_branch Lpdone_%=\n"
        "Lrelight_%=:\n"
        "s_nop 7\n"
        "s_waitcnt vmcnt(0) lgkmcnt(0)\n"
        "s_barrier\n"
        "s_add_u32 s18, s18, 0x100\n"
        "s_bitcmp1_b32 s18, 10\n"
        "s_cbranch_scc1 Lfallback_%=\n"
        "s_cmp_lg_u32 s3, 1\n"
        "s_cbranch_scc1 Lrl1_%=\n"
        "v_mov_b32 v200, 0\n"
        "v_and_b32 v201, 63, v0\n"
        "v_lshl_add_u32 v201, v201, 2, v2\n"
        "ds_write_b32 v201, v200\n"
        "s_waitcnt lgkmcnt(0)\n"
        "Lrl1_%=:\n"
        "s_barrier\n"
        "s_cmp_ge_u32 s3, 4\n"
        "s_cbranch_scc1 Lhre_%=\n"
        "s_branch Lcre_%=\n"
        "Lfallback_%=:\n"
        "s_nop 7\n"
        "s_setprio 0\n"
        "s_waitcnt vmcnt(0) lgkmcnt(0)\n"
        "s_barrier\n"
        "s_andn2_b32 s18, s18, 0xff00\n"
        "s_add_u32 s18, s18, 1\n"
        "s_and_b32 s86, s18, 0xff\n"
        "s_cmp_lt_u32 s86, 3\n"
        "s_cbranch_scc1 Ltop_%=\n"
        "s_mul_i32 s19, s2, 196608\n"
        "s_add_u32 s19, s19, 0x100000\n"
        "s_cmp_lt_u32 s86, 100\n"
        "s_cbranch_scc1 Ltop_%=\n"
        "s_endpgm\n"
        "Lsl4_%=:\n"
        "s_mov_b32 s87, 0x200000\n"
        "Lpl6_%=:\n"
        "ds_read_b32 v5, v2 offset:0\n"
        "s_waitcnt lgkmcnt(0)\n"
        "v_readfirstlane_b32 s86, v5\n"
        "s_cmp_eq_u32 s86, 0x1010101\n"
        "s_cbranch_scc1 Lrt5_%=\n"
        "s_sub_u32 s87, s87, 1\n"
        "s_cbranch_scc1 Lrt5_%=\n"
        "s_branch Lpl6_%=\n"
        "Lsl7_%=:\n"
        "s_mov_b32 s87, 0x200000\n"
        "Lpl9_%=:\n"
        "ds_read_b32 v5, v2 offset:4\n"
        "s_waitcnt lgkmcnt(0)\n"
        "v_readfirstlane_b32 s86, v5\n"
        "s_cmp_eq_u32 s86, 0x1010101\n"
        "s_cbranch_scc1 Lrt8_%=\n"
        "s_and_b32 s86, s86, 0x02020202\n"
        "s_cbranch_scc1 Lrelight_%=\n"
        "s_sub_u32 s87, s87, 1\n"
        "s_cbranch_scc1 Lrt8_%=\n"
        "s_branch Lpl9_%=\n"
        "Lsl10_%=:\n"
        "s_mov_b32 s87, 0x200000\n"
        "Lpl12_%=:\n"
        "ds_read_b32 v5, v2 offset:20\n"
        "s_waitcnt lgkmcnt(0)\n"
        "v_readfirstlane_b32 s86, v5\n"
        "s_cmp_eq_u32 s86, 0x1010101\n"
        "s_cbranch_scc1 Lrt11_%=\n"
        "s_sub_u32 s87, s87, 1\n"
        "s_cbranch_scc1 Lrt11_%=\n"
        "s_branch Lpl12_%=\n"
        "Lsl13_%=:\n"
        "s_mov_b32 s87, 0x200000\n"
        "Lpl15_%=:\n"
        "ds_read_b32 v5, v2 offset:24\n"
        "s_waitcnt lgkmcnt(0)\n"
        "v_readfirstlane_b32 s86, v5\n"
        "s_cmp_eq_u32 s86, 0x1010101\n"
        "s_cbranch_scc1 Lrt14_%=\n"
        "s_sub_u32 s87, s87, 1\n"
        "s_cbranch_scc1 Lrt14_%=\n"
        "s_branch Lpl15_%=\n"
        "Lsl16_%=:\n"
        "s_mov_b32 s87, 0x200000\n"
        "Lpl18_%=:\n"
        "ds_read_b32 v5, v2 offset:28\n"
        "s_waitcnt lgkmcnt(0)\n"
        "v_readfirstlane_b32 s86, v5\n"
        "s_cmp_eq_u32 s86, 0x1010101\n"
        "s_cbranch_scc1 Lrt17_%=\n"
        "s_sub_u32 s87, s87, 1\n"
        "s_cbranch_scc1 Lrt17_%=\n"
        "s_branch Lpl18_%=\n"
        "Lsl19_%=:\n"
        "s_mov_b32 s87, 0x200000\n"
        "Lpl21_%=:\n"
        "ds_read_b32 v5, v2 offset:44\n"
        "s_waitcnt lgkmcnt(0)\n"
        "v_readfirstlane_b32 s86, v5\n"
        "s_cmp_eq_u32 s86, 0x1010100\n"
        "s_cbranch_scc1 Lrt20_%=\n"
        "s_and_b32 s86, s86, 0x02020202\n"
        "s_cbranch_scc1 Lfallback_%=\n"
        "s_sub_u32 s87, s87, 1\n"
        "s_cbranch_scc1 Lrt20_%=\n"
        "s_branch Lpl21_%=\n"
        "Lsl22_%=:\n"
        "s_mov_b32 s87, 0x200000\n"
        "Lpl24_%=:\n"
        "ds_read_b32 v5, v2 offset:48\n"
        "s_waitcnt lgkmcnt(0)\n"
        "v_readfirstlane_b32 s86, v5\n"
        "s_cmp_eq_u32 s86, 0x1010100\n"
        "s_cbranch_scc1 Lrt23_%=\n"
        "s_sub_u32 s87, s87, 1\n"
        "s_cbranch_scc1 Lrt23_%=\n"
        "s_branch Lpl24_%=\n"
        "Lsl25_%=:\n"
        "s_mov_b32 s87, 0x200000\n"
        "Lpl27_%=:\n"
        "ds_read_b32 v5, v2 offset:52\n"
        "s_waitcnt lgkmcnt(0)\n"
        "v_readfirstlane_b32 s86, v5\n"
        "s_cmp_eq_u32 s86, 0x1010100\n"
        "s_cbranch_scc1 Lrt26_%=\n"
        "s_sub_u32 s87, s87, 1\n"
        "s_cbranch_scc1 Lrt26_%=\n"
        "s_branch Lpl27_%=\n"
        "Lgret_%=:\n"
        "s_sub_u32 s90, s90, 1\n"
        "s_cbranch_scc1 Lgok_%=\n"
        "s_sleep 4\n"
        "buffer_load_dwordx2 v[36:37], v15, s[24:27], s10 offen offset:3072 sc1\n"
        "s_waitcnt vmcnt(0)\n"
        "v_cmp_ne_u32 vcc, s7, v37\n"
        "s_nop 4\n"
        "s_cbranch_vccnz Lgret_%=\n"
        "s_mov_b32 s87, 0\n"
        "s_add_u32 m0, s11, 0x12c00\n"
        "s_nop 0\n"
        "buffer_load_dwordx4 v13, s[24:27], s87 offen sc1 lds\n"
        "s_mov_b32 s87, 12800\n"
        "s_add_u32 m0, s11, 0x13000\n"
        "s_nop 0\n"
        "buffer_load_dwordx4 v13, s[24:27], s87 offen sc1 lds\n"
        "s_mov_b32 s87, 1024\n"
        "s_add_u32 m0, s11, 0x13400\n"
        "s_nop 0\n"
        "buffer_load_dwordx4 v13, s[24:27], s87 offen sc1 lds\n"
        "s_mov_b32 s87, 13824\n"
        "s_add_u32 m0, s11, 0x13800\n"
        "s_nop 0\n"
        "buffer_load_dwordx4 v13, s[24:27], s87 offen sc1 lds\n"
        "s_mov_b32 s87, 2048\n"
        "s_add_u32 m0, s11, 0x13c00\n"
        "s_nop 0\n"
        "buffer_load_dwordx4 v13, s[24:27], s87 offen sc1 lds\n"
        "s_mov_b32 s87, 14848\n"
        "s_add_u32 m0, s11, 0x14000\n"
        "s_nop 0\n"
        "buffer_load_dwordx4 v13, s[24:27], s87 offen sc1 lds\n"
        "s_mov_b32 s87, 25600\n"
        "s_add_u32 m0, s11, 0x19000\n"
        "s_nop 0\n"
        "buffer_load_dwordx4 v13, s[24:27], s87 offen sc1 lds\n"
        "s_mov_b32 s87, 38400\n"
        "s_add_u32 m0, s11, 0x19400\n"
        "s_nop 0\n"
        "buffer_load_dwordx4 v13, s[24:27], s87 offen sc1 lds\n"
        "s_mov_b32 s87, 26624\n"
        "s_add_u32 m0, s11, 0x19800\n"
        "s_nop 0\n"
        "buffer_load_dwordx4 v13, s[24:27], s87 offen sc1 lds\n"
        "s_mov_b32 s87, 39424\n"
        "s_add_u32 m0, s11, 0x19c00\n"
        "s_nop 0\n"
        "buffer_load_dwordx4 v13, s[24:27], s87 offen sc1 lds\n"
        "s_mov_b32 s87, 27648\n"
        "s_add_u32 m0, s11, 0x1a000\n"
        "s_nop 0\n"
        "buffer_load_dwordx4 v13, s[24:27], s87 offen sc1 lds\n"
        "s_mov_b32 s87, 40448\n"
        "s_add_u32 m0, s11, 0x1a400\n"
        "s_nop 0\n"
        "buffer_load_dwordx4 v13, s[24:27], s87 offen sc1 lds\n"
        "s_waitcnt vmcnt(0)\n"
        "s_branch Lgok_%=\n"
        :
        : "{s[0:1]}"(__builtin_amdgcn_kernarg_segment_ptr()), "{s2}"(blockIdx.x), "{v0}"(threadIdx.x), "{s[4:5]}"(na_tab), "{m0}"((unsigned)(size_t)smem)
        : "memory", "scc", "vcc", "v1", "v2", "v3", "v4", "v5", "v6", "v7", "v8", "v9", "v10", "v11", "v12", "v13", "v14", "v15", "v16", "v17", "v18", "v19", "v20", "v21", "v22", "v23", "v24", "v25", "v26", "v27", "v28", "v29", "v30", "v31", "v32", "v33", "v34", "v35", "v36", "v37", "v38", "v39", "v40", "v41", "v42", "v43", "v44", "v45", "v46", "v47", "v48", "v49", "v50", "v51", "v52", "v53", "v54", "v55", "v56", "v57", "v58", "v59", "v60", "v61", "v62", "v63", "v64", "v65", "v66", "v67", "v68", "v69", "v70", "v71", "v72", "v73", "v74", "v75", "v76", "v77", "v78", "v79", "v80", "v81", "v82", "v83", "v84", "v85", "v86", "v87", "v88", "v89", "v90", "v91", "v92", "v93", "v94", "v95", "v96", "v97", "v98", "v99", "v100", "v101", "v102", "v103", "v104", "v105", "v106", "v107", "v108", "v109", "v110", "v111", "v112", "v113", "v114", "v115", "v116", "v117", "v118", "v119", "v120", "v121", "v122", "v123", "v124", "v125", "v126", "v127", "v128", "v129", "v130", "v131", "v132", "v133", "v134", "v135", "v136", "v137", "v138", "v139", "v140", "v141", "v142", "v143", "v144", "v145", "v146", "v147", "v148", "v149", "v150", "v151", "v152", "v153", "v154", "v155", "v156", "v157", "v158", "v159", "v160", "v161", "v162", "v163", "v164", "v165", "v166", "v167", "v168", "v169", "v170", "v171", "v172", "v173", "v174", "v175", "v176", "v177", "v178", "v179", "v180", "v181", "v182", "v183", "v184", "v185", "v186", "v187", "v188", "v189", "v190", "v191", "v192", "v193", "v194", "v195", "v196", "v197", "v198", "v199", "v200", "v201", "v202", "v203", "v204", "v205", "v206", "v207", "v208", "v209", "v210", "v211", "v212", "v213", "v214", "v215", "v216", "v217", "v218", "v219", "v220", "v221", "v222", "v223", "v224", "v225", "v226", "v227", "v228", "v229", "v230", "v231", "v232", "v233", "v234", "v235", "v236", "v237", "v238", "v239", "v240", "v241", "v242", "v243", "v244", "v245", "v246", "v247", "v248", "v249", "v250", "v251", "v252", "v253", "v254", "v255", "s3", "s6", "s7", "s8", "s9", "s10", "s11", "s12", "s13", "s14", "s15", "s16", "s17", "s18", "s19", "s20", "s21", "s22", "s23", "s24", "s25", "s26", "s27", "s28", "s29", "s30", "s31", "s32", "s33", "s34", "s35", "s36", "s37", "s38", "s39", "s40", "s41", "s42", "s43", "s44", "s45", "s46", "s47", "s48", "s49", "s50", "s51", "s52", "s53", "s54", "s55", "s56", "s57", "s58", "s59", "s60", "s61", "s62", "s63", "s64", "s65", "s66", "s67", "s68", "s69", "s70", "s71", "s72", "s73", "s74", "s75", "s76", "s77", "s78", "s79", "s80", "s81", "s82", "s83", "s84", "s85", "s86", "s87", "s88", "s89", "s90", "s91", "s92", "s93", "s94", "s95", "s96", "s97", "s98", "s99", "s100", "s101");
}

extern "C" void kernel_launch(void* const* d_in, const int* in_sizes, int n_in,
                              void* d_out, int out_size, void* d_ws, size_t ws_size,
                              hipStream_t stream) {
    na_main<<<256, 512, 0, stream>>>(d_in[0], d_in[1], d_in[2], d_ws, d_out);
}
```
